```python
import torch

def _softmax(x, axis=-1):
    return torch.softmax(x, dim=axis)
def _gelu(x):
    return torch.nn.functional.gelu(x, approximate="tanh")
def _silu(x):
    return torch.nn.functional.silu(x)
def _sigmoid(x):
    return torch.sigmoid(x)
def _layernorm(x, axis=-1, eps=1e-5):
    m = x.mean(dim=axis, keepdim=True)
    v = x.var(dim=axis, keepdim=True, unbiased=False)
    return (x - m) / torch.sqrt(v + eps)
def _scatter_add(tgt, idx, src, axis=0):
    return tgt.clone().index_add_(axis, idx, src)
def _segment_sum(src, seg, n):
    out = torch.zeros((n,) + src.shape[1:], dtype=src.dtype)
    return out.index_add_(0, seg, src)
def _pad(x, pw):
    flat = tuple(v for pair in reversed(pw) for v in pair)
    return torch.nn.functional.pad(x, flat)

kernel_name = 'concat_heads__mha_block_cached__mha_block_cached__near_ridge'

def setup_inputs():

    _w = (lambda a: (lambda t: t.to(torch.bfloat16) if t.is_floating_point() else t)(torch.as_tensor(a)))
    def _A_setup():
        B, Sq, D = 8, 256, 576
        H, Sk, Dh = 32, 1024, 128
        return {
            "x": _w((lambda *s: torch.randn(*s, dtype=torch.bfloat16) * 0.8)(B, Sq, D)),
            "k_cache": _w((lambda *s: torch.randn(*s, dtype=torch.bfloat16) * 0.8)(B, H, Sk, Dh)),
            "v_cache": _w((lambda *s: torch.randn(*s, dtype=torch.bfloat16) * 0.05)(B, H, Sk, Dh)),
            "wq": _w((lambda *s: torch.randn(*s, dtype=torch.bfloat16) * 0.8)(D, H * Dh) * (D ** -0.5)),
            "wo": _w((lambda *s: torch.randn(*s, dtype=torch.bfloat16) * 0.05)(H * Dh, D) * ((H * Dh) ** -0.5)),
        }
    _A_in = _A_setup()

    def _B_setup():
        B, Sq, D = 8, 256, 576
        H, Sk, Dh = 32, 1024, 128
        return {
            "x": _w((lambda *s: torch.randn(*s, dtype=torch.bfloat16) * 0.8)(B, Sq, D)),
            "k_cache": _w((lambda *s: torch.randn(*s, dtype=torch.bfloat16) * 0.8)(B, H, Sk, Dh)),
            "v_cache": _w((lambda *s: torch.randn(*s, dtype=torch.bfloat16) * 0.05)(B, H, Sk, Dh)),
            "wq": _w((lambda *s: torch.randn(*s, dtype=torch.bfloat16) * 0.8)(D, H * Dh) * (D ** -0.5)),
            "wo": _w((lambda *s: torch.randn(*s, dtype=torch.bfloat16) * 0.05)(H * Dh, D) * ((H * Dh) ** -0.5)),
        }
    _B_in = _B_setup()

    return {
        "A_x": _A_in["x"],
        "A_k_cache": _A_in["k_cache"],
        "A_v_cache": _A_in["v_cache"],
        "A_wq": _A_in["wq"],
        "A_wo": _A_in["wo"],
        "B_x": _B_in["x"],
        "B_k_cache": _B_in["k_cache"],
        "B_v_cache": _B_in["v_cache"],
        "B_wq": _B_in["wq"],
        "B_wo": _B_in["wo"],
    }

def reference(A_x, A_k_cache, A_v_cache, A_wq, A_wo, B_x, B_k_cache, B_v_cache, B_wq, B_wo):
    def _A_ref(x, k_cache, v_cache, wq, wo):
        B, Sq, D = x.shape
        H, Dh = 32, 128
        q = (lambda x, *d: x.permute(*d))((x @ wq).reshape(B, Sq, H, Dh), 0, 2, 1, 3)
        scores = torch.einsum("bhqd,bhkd->bhqk", q, k_cache) * (Dh ** -0.5)
        p = _softmax(scores, axis=-1)
        o = torch.einsum("bhqk,bhkd->bhqd", p, v_cache)
        return (lambda x, *d: x.permute(*d))(o, 0, 2, 1, 3).reshape(B, Sq, H * Dh) @ wo

    def _B_ref(x, k_cache, v_cache, wq, wo):
        B, Sq, D = x.shape
        H, Dh = 32, 128
        q = (lambda x, *d: x.permute(*d))((x @ wq).reshape(B, Sq, H, Dh), 0, 2, 1, 3)
        scores = torch.einsum("bhqd,bhkd->bhqk", q, k_cache) * (Dh ** -0.5)
        p = _softmax(scores, axis=-1)
        o = torch.einsum("bhqk,bhkd->bhqd", p, v_cache)
        return (lambda x, *d: x.permute(*d))(o, 0, 2, 1, 3).reshape(B, Sq, H * Dh) @ wo

    a = _A_ref(A_x, A_k_cache, A_v_cache, A_wq, A_wo)
    b = _B_ref(B_x, B_k_cache, B_v_cache, B_wq, B_wo)
    return torch.concatenate([a, b], axis=-1)
```

```cpp
#include <hip/hip_runtime.h>
#include <hip/hip_bf16.h>

struct Args {
  const char* in[10];
  char* ws;
  char* out;
};

__global__ __launch_bounds__(256, 1) void mha2_fused(const Args p) {
  __shared__ __attribute__((aligned(16))) char lds[163840];
  const unsigned w = __builtin_amdgcn_readfirstlane(threadIdx.x >> 6);
  asm volatile(
"s_bfe_u32 s25, %1, 0x10002\n"
"s_mul_i32 s26, s25, 40\n"
"s_load_dwordx8 s[4:11], %0, s26 offset:0x0\n"
"s_load_dwordx2 s[12:13], %0, s26 offset:0x20\n"
"s_load_dwordx4 s[16:19], %0, 0x50\n"
"s_bfe_u32 s27, %1, 0x20000\n"
"s_bfe_u32 s23, %1, 0x20003\n"
"s_lshl2_add_u32 s23, s27, s23\n"
"s_bfe_u32 s24, %1, 0x30005\n"
"s_mov_b32 s69, %2\n"
"v_mbcnt_lo_u32_b32 v236, -1, 0\n"
"v_mbcnt_hi_u32_b32 v236, -1, v236\n"
"s_getpc_b64 s[72:73]\n"
"L_pc%=:\n"
"s_and_b32 s73, s73, 0xffff\n"
"s_mov_b32 s74, L_slow%=-L_pc%=\n"
"s_mov_b32 s75, 0x20000\n"
"s_and_b32 s77, s23, 3\n"
"s_lshl2_add_u32 s76, s24, s77\n"
"s_lshl2_add_u32 s76, s76, s69\n"
"s_lshl_b32 s76, s76, 7\n"
"v_min_u32 v239, 6, v236\n"
"v_lshlrev_b32 v239, 14, v239\n"
"buffer_load_dword v246, v239, s[72:75], s76 offen\n"
"s_lshl_b32 s61, s69, 12\n"
"s_mul_i32 s62, s69, 4112\n"
"s_add_u32 s62, s62, 65536\n"
"v_lshrrev_b32 v238, 4, v236\n"
"v_lshrrev_b32 v239, 3, v236\n"
"v_lshl_add_u32 v239, s69, 6, v239\n"
"v_mul_u32_u24 v239, 1152, v239\n"
"v_and_b32 v240, 7, v236\n"
"v_xor_b32 v240, v240, v238\n"
"v_lshl_add_u32 v226, v240, 4, v239\n"
"v_xor_b32 v227, 64, v226\n"
"v_add_u32 v227, 9216, v227\n"
"v_bfe_u32 v239, v236, 2, 2\n"
"v_lshl_add_u32 v239, s69, 4, v239\n"
"v_and_b32 v240, 3, v236\n"
"v_lshl_add_u32 v240, v238, 2, v240\n"
"v_lshlrev_b32 v240, 4, v240\n"
"v_mov_b32 v224, v240\n"
"v_lshl_add_u32 v225, v239, 13, v240\n"
"s_mov_b32 s59, 0\n"
"s_lshr_b32 s72, s23, 2\n"
"s_add_u32 s84, s72, 0\n"
"s_and_b32 s84, s84, 3\n"
"s_mul_i32 s84, s84, 18432\n"
"s_add_u32 s85, s72, 1\n"
"s_and_b32 s85, s85, 3\n"
"s_mul_i32 s85, s85, 18432\n"
"s_add_u32 s87, s72, 2\n"
"s_and_b32 s87, s87, 3\n"
"s_mul_i32 s87, s87, 18432\n"
"s_add_u32 s68, s72, 3\n"
"s_and_b32 s68, s68, 3\n"
"s_mul_i32 s68, s68, 18432\n"
"s_mov_b32 s46, 294912\n"
"s_mov_b32 s47, 0x20000\n"
"s_mul_i32 s26, s24, 294912\n"
"s_lshl_b32 s27, s23, 9\n"
"s_sub_u32 s50, 4718592, s27\n"
"s_mov_b32 s51, 0x20000\n"
"s_waitcnt lgkmcnt(0)\n"
"s_add_u32 s44, s4, s26\n"
"s_addc_u32 s45, s5, 0\n"
"s_and_b32 s45, s45, 0xffff\n"
"s_add_u32 s48, s10, s27\n"
"s_addc_u32 s49, s11, 0\n"
"s_and_b32 s49, s49, 0xffff\n"
"s_add_u32 m0, s61, 65536\n"
"s_add_u32 s58, s84, 0\n"
"buffer_load_dwordx4 v226, s[44:47], s58 offen lds\n"
"s_add_u32 m0, s61, 66560\n"
"s_nop 0\n"
"buffer_load_dwordx4 v227, s[44:47], s58 offen lds\n"
"s_add_u32 m0, s61, 67584\n"
"s_add_u32 s58, s85, 0\n"
"buffer_load_dwordx4 v226, s[44:47], s58 offen lds\n"
"s_add_u32 m0, s61, 68608\n"
"s_nop 0\n"
"buffer_load_dwordx4 v227, s[44:47], s58 offen lds\n"
"s_add_u32 m0, s61, 81984\n"
"s_add_u32 s58, s87, 0\n"
"buffer_load_dwordx4 v226, s[44:47], s58 offen lds\n"
"s_add_u32 m0, s61, 83008\n"
"s_nop 0\n"
"buffer_load_dwordx4 v227, s[44:47], s58 offen lds\n"
"s_add_u32 m0, s61, 84032\n"
"s_add_u32 s58, s68, 0\n"
"buffer_load_dwordx4 v226, s[44:47], s58 offen lds\n"
"s_add_u32 m0, s61, 85056\n"
"s_nop 0\n"
"buffer_load_dwordx4 v227, s[44:47], s58 offen lds\n"
"s_add_u32 m0, s62, 65792\n"
"s_add_u32 s60, s59, 0\n"
"buffer_load_dwordx4 v225, s[48:51], s60 offen lds\n"
"s_add_u32 m0, s62, 66816\n"
"s_add_u32 s60, s59, 32768\n"
"buffer_load_dwordx4 v225, s[48:51], s60 offen lds\n"
"s_add_u32 m0, s62, 67848\n"
"s_add_u32 s60, s59, 65536\n"
"buffer_load_dwordx4 v225, s[48:51], s60 offen lds\n"
"s_add_u32 m0, s62, 68872\n"
"s_add_u32 s60, s59, 98304\n"
"buffer_load_dwordx4 v225, s[48:51], s60 offen lds\n"
"v_and_b32 v237, 15, v236\n"
"v_lshrrev_b32 v239, 1, v237\n"
"v_xor_b32 v239, v239, v238\n"
"v_lshlrev_b32 v240, 7, v237\n"
"v_add_u32 v240, s61, v240\n"
"v_lshl_add_u32 v228, v239, 4, v240\n"
"v_xor_b32 v229, 64, v228\n"
"v_add_u32 v230, 65536, v228\n"
"v_add_u32 v231, 65536, v229\n"
"v_mul_u32_u24 v239, 2056, v238\n"
"v_lshl_add_u32 v239, v237, 4, v239\n"
"v_add_u32 v220, 65536, v239\n"
"v_add_u32 v221, 98304, v239\n"
"s_mov_b32 s98, 139264\n"
"s_mov_b32 s99, 0x20000\n"
"s_mov_b32 s38, 262144\n"
"s_mov_b32 s39, 0x20000\n"
"s_mov_b32 s42, 262144\n"
"s_mov_b32 s43, 0x20000\n"
"s_mov_b32 s54, 0\n"
"s_mov_b32 s55, 0x20000\n"
"s_mov_b32 s94, 1179648\n"
"s_mov_b32 s95, 0x20000\n"
"s_lshl4_add_u32 s26, s24, s23\n"
"s_lshl_b32 s26, s26, 19\n"
"s_add_u32 s36, s6, s26\n"
"s_addc_u32 s37, s7, 0\n"
"s_and_b32 s37, s37, 0xffff\n"
"s_add_u32 s40, s8, s26\n"
"s_addc_u32 s41, s9, 0\n"
"s_and_b32 s41, s41, 0xffff\n"
"s_lshr_b32 s27, s23, 2\n"
"s_mul_i32 s26, s27, 1179648\n"
"s_add_u32 s92, s12, s26\n"
"s_addc_u32 s93, s13, 0\n"
"s_and_b32 s93, s93, 0xffff\n"
"s_lshl4_add_u32 s27, s24, s27\n"
"s_mul_i32 s27, s27, 36864\n"
"s_and_b32 s26, s23, 3\n"
"s_lshl2_add_u32 s26, s25, s26\n"
"s_mul_i32 s26, s26, 288\n"
"s_add_u32 s26, s26, s27\n"
"s_add_u32 s18, s18, s26\n"
"s_addc_u32 s19, s19, 0\n"
"s_and_b32 s19, s19, 0xffff\n"
"s_lshl3_add_u32 s27, s25, s24\n"
"s_mul_i32 s26, s27, 3145728\n"
"s_add_u32 s52, s16, s26\n"
"s_addc_u32 s53, s17, 0\n"
"s_and_b32 s53, s53, 0xffff\n"
"s_mul_i32 s26, s27, 147456\n"
"s_add_u32 s26, s26, 50855936\n"
"s_add_u32 s96, s16, s26\n"
"s_addc_u32 s97, s17, 0\n"
"s_and_b32 s97, s97, 0xffff\n"
"s_lshl_b32 s72, s23, 8\n"
"s_add_u32 s72, s72, 256\n"
"v_lshl_add_u32 v239, v236, 2, s72\n"
"v_accvgpr_write_b32 a221, v239\n"
"s_lshl1_add_u32 s56, s23, s24\n"
"s_lshl_b32 s56, s56, 12\n"
"s_and_b32 s56, s56, 159744\n"
"s_mov_b32 s57, s56\n"
"s_mov_b32 s63, 0\n"
"v_and_b32 v239, 12, v237\n"
"v_add_u32 v239, v239, v237\n"
"v_lshlrev_b32 v239, 8, v239\n"
"v_xor_b32 v240, v238, v237\n"
"v_lshl_add_u32 v216, v240, 4, v239\n"
"v_xor_b32 v217, 64, v216\n"
"v_xor_b32 v218, 128, v216\n"
"v_xor_b32 v219, 192, v216\n"
"s_and_b32 s72, s69, 1\n"
"s_lshl_b32 s72, s72, 3\n"
"v_xor_b32 v240, s72, v240\n"
"v_lshlrev_b32 v222, 4, v240\n"
"v_bfe_u32 v242, v236, 4, 1\n"
"v_lshl_add_u32 v222, v242, 8, v222\n"
"v_bfe_u32 v242, v236, 5, 1\n"
"v_lshl_add_u32 v222, v242, 9, v222\n"
"v_bfe_u32 v242, s69, 0, 1\n"
"v_lshl_add_u32 v222, v242, 15, v222\n"
"v_bfe_u32 v242, s69, 1, 1\n"
"v_lshl_add_u32 v222, v242, 16, v222\n"
"v_bfe_u32 v242, v236, 2, 1\n"
"v_lshl_add_u32 v224, v242, 8, v224\n"
"v_bfe_u32 v242, v236, 3, 1\n"
"v_lshl_add_u32 v224, v242, 9, v224\n"
"v_bfe_u32 v242, s69, 0, 1\n"
"v_lshl_add_u32 v224, v242, 15, v224\n"
"v_bfe_u32 v242, s69, 1, 1\n"
"v_lshl_add_u32 v224, v242, 16, v224\n"
"v_xor_b32 v223, 64, v222\n"
"v_lshlrev_b32 v232, 4, v236\n"
"s_lshl2_add_u32 s72, s69, 147776\n"
"v_mov_b32 v233, s72\n"
"v_mov_b32 v234, 147776\n"
"v_and_b32 v211, 3, v236\n"
"v_lshl_add_u32 v211, v211, 2, v234\n"
"v_add_u32 v211, 32, v211\n"
"v_mov_b32 v209, 0\n"
"ds_write_b32 v233, v209 offset:32\n"
"s_mov_b32 s65, 0\n"
"s_lshl3_add_u32 s67, s23, s69\n"
"s_mul_i32 s67, s67, 16384\n"
"s_cmp_eq_u32 s69, 0\n"
"s_cselect_b32 s79, 139264, 0\n"
"s_mov_b32 s70, 0x3e0293ee\n"
"s_mov_b32 s71, 0x3e0293ee\n"
"s_mul_i32 s74, s24, 72\n"
"s_mul_i32 s73, s69, 18\n"
"s_add_u32 s74, s74, s73\n"
"v_lshrrev_b32 v239, 1, v236\n"
"v_min_u32 v239, 17, v239\n"
"v_add_u32 v239, s74, v239\n"
"v_lshlrev_b32 v239, 13, v239\n"
"v_and_b32 v240, 1, v236\n"
"v_lshl_add_u32 v239, v240, 7, v239\n"
"v_accvgpr_write_b32 a204, v239\n"
"s_and_b32 s72, s23, 3\n"
"s_lshl2_add_u32 s74, s24, s72\n"
"s_lshl2_add_u32 s74, s74, s69\n"
"s_bitcmp1_b32 s23, 2\n"
"s_cselect_b32 s78, 8, 12\n"
"s_cselect_b32 s73, 256, 0\n"
"s_cselect_b32 s94, s94, 1327104\n"
"s_mul_i32 s74, s74, s78\n"
"s_add_u32 s74, s74, s73\n"
"s_sub_u32 s73, s78, 1\n"
"v_min_u32 v239, s73, v237\n"
"v_add_u32 v239, s74, v239\n"
"v_lshrrev_b32 v240, 8, v239\n"
"v_lshl_add_u32 v240, v240, 2, v238\n"
"v_and_b32 v239, 255, v239\n"
"v_lshlrev_b32 v239, 7, v239\n"
"v_lshl_add_u32 v239, v240, 15, v239\n"
"v_accvgpr_write_b32 a207, v239\n"
"s_lshl_b32 s78, s78, 16\n"
"s_add_u32 m0, s61, 0\n"
"s_add_u32 s58, s84, 128\n"
"buffer_load_dwordx4 v226, s[44:47], s58 offen lds\n"
"s_add_u32 m0, s61, 1024\n"
"s_nop 0\n"
"buffer_load_dwordx4 v227, s[44:47], s58 offen lds\n"
"s_add_u32 m0, s61, 2048\n"
"s_add_u32 s58, s85, 128\n"
"buffer_load_dwordx4 v226, s[44:47], s58 offen lds\n"
"s_add_u32 m0, s62, 32896\n"
"s_add_u32 s60, s59, 524288\n"
"buffer_load_dwordx4 v225, s[48:51], s60 offen lds\n"
"s_waitcnt vmcnt(8)\n"
"ds_read_b128 v[128:131], v230\n"
"ds_read_b128 v[132:135], v231\n"
"ds_read_b128 v[136:139], v230 offset:2048\n"
"ds_read_b128 v[140:143], v231 offset:2048\n"
"ds_read_b128 v[144:147], v230 offset:16448\n"
"ds_read_b128 v[148:151], v231 offset:16448\n"
"ds_read_b128 v[152:155], v230 offset:18496\n"
"ds_read_b128 v[156:159], v231 offset:18496\n"
"s_add_u32 m0, s61, 3072\n"
"s_nop 0\n"
"buffer_load_dwordx4 v227, s[44:47], s58 offen lds\n"
"s_add_u32 m0, s62, 33920\n"
"s_add_u32 s60, s59, 557056\n"
"buffer_load_dwordx4 v225, s[48:51], s60 offen lds\n"
"s_add_u32 m0, s61, 16384\n"
"s_add_u32 s58, s87, 128\n"
"buffer_load_dwordx4 v226, s[44:47], s58 offen lds\n"
"s_add_u32 m0, s62, 34952\n"
"s_add_u32 s60, s59, 589824\n"
"buffer_load_dwordx4 v225, s[48:51], s60 offen lds\n"
"s_add_u32 m0, s61, 17408\n"
"s_nop 0\n"
"buffer_load_dwordx4 v227, s[44:47], s58 offen lds\n"
"s_waitcnt vmcnt(9) lgkmcnt(0)\n"
"s_barrier\n"
"ds_read_b64_tr_b16 a[160:161], v221 offset:33024\n"
"ds_read_b64_tr_b16 a[162:163], v221 offset:34048\n"
"ds_read_b64_tr_b16 a[164:165], v221 offset:41248\n"
"ds_read_b64_tr_b16 a[166:167], v221 offset:42272\n"
"ds_read_b64_tr_b16 a[168:169], v221 offset:33032\n"
"ds_read_b64_tr_b16 a[170:171], v221 offset:34056\n"
"ds_read_b64_tr_b16 a[172:173], v221 offset:41256\n"
"ds_read_b64_tr_b16 a[174:175], v221 offset:42280\n"
"ds_read_b64_tr_b16 a[176:177], v221 offset:33280\n"
"ds_read_b64_tr_b16 a[178:179], v221 offset:34304\n"
"ds_read_b64_tr_b16 a[180:181], v221 offset:41504\n"
"ds_read_b64_tr_b16 a[182:183], v221 offset:42528\n"
"s_waitcnt lgkmcnt(8)\n"
"v_mfma_f32_16x16x32_bf16 v[64:67], a[160:163], v[128:131], 0\n"
"ds_read_b64_tr_b16 a[184:185], v221 offset:33288\n"
"v_mfma_f32_16x16x32_bf16 v[64:67], a[164:167], v[132:135], v[64:67]\n"
"s_add_u32 s60, s59, 622592\n"
"v_mfma_f32_16x16x32_bf16 v[80:83], a[164:167], v[140:143], 0\n"
"s_add_u32 m0, s62, 35976\n"
"ds_read_b64_tr_b16 a[186:187], v221 offset:34312\n"
"v_mfma_f32_16x16x32_bf16 v[80:83], a[160:163], v[136:139], v[80:83]\n"
"s_mul_i32 s74, s72, 288\n"
"buffer_load_dwordx4 v225, s[48:51], s60 offen lds\n"
"v_mfma_f32_16x16x32_bf16 v[96:99], a[160:163], v[144:147], 0\n"
"s_add_u32 m0, s61, 18432\n"
"s_add_u32 s58, s68, 128\n"
"ds_read_b64_tr_b16 a[188:189], v221 offset:41512\n"
"v_mfma_f32_16x16x32_bf16 v[96:99], a[164:167], v[148:151], v[96:99]\n"
"s_mul_i32 s73, s69, 36864\n"
"buffer_load_dwordx4 v226, s[44:47], s58 offen lds\n"
"v_mfma_f32_16x16x32_bf16 v[112:115], a[164:167], v[156:159], 0\n"
"s_add_u32 s74, s74, s73\n"
"ds_read_b64_tr_b16 a[190:191], v221 offset:42536\n"
"v_add_u32 v239, 0, v236\n"
"v_mfma_f32_16x16x32_bf16 v[112:115], a[160:163], v[152:155], v[112:115]\n"
"s_waitcnt lgkmcnt(8)\n"
"v_mul_u32_u24 v240, 3641, v239\n"
"v_lshrrev_b32 v240, 16, v240\n"
"v_mfma_f32_16x16x32_bf16 v[12:15], a[168:171], v[152:155], 0\n"
"ds_read_b64_tr_b16 a[160:161], v221 offset:33536\n"
"v_mul_u32_u24 v241, 18, v240\n"
"v_mfma_f32_16x16x32_bf16 v[12:15], a[172:175], v[156:159], v[12:15]\n"
"v_sub_u32 v239, v239, v241\n"
"v_lshrrev_b32 v241, 1, v240\n"
"v_mfma_f32_16x16x32_bf16 v[8:11], a[172:175], v[148:151], 0\n"
"s_add_u32 m0, s61, 19456\n"
"ds_read_b64_tr_b16 a[162:163], v221 offset:34560\n"
"v_and_b32 v241, 4, v241\n"
"v_mfma_f32_16x16x32_bf16 v[8:11], a[168:171], v[144:147], v[8:11]\n"
"buffer_load_dwordx4 v227, s[44:47], s58 offen lds\n"
"v_mfma_f32_16x16x32_bf16 v[4:7], a[168:171], v[136:139], 0\n"
"ds_read_b64_tr_b16 a[164:165], v221 offset:41760\n"
"v_xor_b32 v240, v240, v241\n"
"v_mfma_f32_16x16x32_bf16 v[4:7], a[172:175], v[140:143], v[4:7]\n"
"s_add_u32 s58, s84, 256\n"
"v_mul_u32_u24 v240, 1152, v240\n"
"v_lshl_add_u32 v240, v239, 4, v240\n"
"v_mfma_f32_16x16x32_bf16 v[0:3], a[172:175], v[132:135], 0\n"
"s_add_u32 m0, s61, 32768\n"
"ds_read_b64_tr_b16 a[166:167], v221 offset:42784\n"
"v_subrev_u32 v240, 0, v240\n"
"v_mfma_f32_16x16x32_bf16 v[0:3], a[168:171], v[128:131], v[0:3]\n"
"s_waitcnt lgkmcnt(8)\n"
"buffer_load_dwordx4 v226, s[44:47], s58 offen lds\n"
"v_mfma_f32_16x16x32_bf16 v[68:71], a[176:179], v[128:131], 0\n"
"ds_read_b64_tr_b16 a[168:169], v221 offset:33544\n"
"v_add_u32 v240, s74, v240\n"
"v_mfma_f32_16x16x32_bf16 v[68:71], a[180:183], v[132:135], v[68:71]\n"
"v_accvgpr_write_b32 a208, v240\n"
"v_add_u32 v239, 64, v236\n"
"v_mfma_f32_16x16x32_bf16 v[84:87], a[180:183], v[140:143], 0\n"
"s_add_u32 m0, s61, 33792\n"
"ds_read_b64_tr_b16 a[170:171], v221 offset:34568\n"
"v_mul_u32_u24 v240, 3641, v239\n"
"v_mfma_f32_16x16x32_bf16 v[84:87], a[176:179], v[136:139], v[84:87]\n"
"buffer_load_dwordx4 v227, s[44:47], s58 offen lds\n"
"v_mfma_f32_16x16x32_bf16 v[100:103], a[176:179], v[144:147], 0\n"
"ds_read_b64_tr_b16 a[172:173], v221 offset:41768\n"
"v_lshrrev_b32 v240, 16, v240\n"
"v_mfma_f32_16x16x32_bf16 v[100:103], a[180:183], v[148:151], v[100:103]\n"
"v_mul_u32_u24 v241, 18, v240\n"
"v_sub_u32 v239, v239, v241\n"
"v_mfma_f32_16x16x32_bf16 v[116:119], a[180:183], v[156:159], 0\n"
"s_add_u32 s58, s85, 256\n"
"ds_read_b64_tr_b16 a[174:175], v221 offset:42792\n"
"v_lshrrev_b32 v241, 1, v240\n"
"v_mfma_f32_16x16x32_bf16 v[116:119], a[176:179], v[152:155], v[116:119]\n"
"s_waitcnt lgkmcnt(8)\n"
"v_and_b32 v241, 4, v241\n"
"v_xor_b32 v240, v240, v241\n"
"v_mfma_f32_16x16x32_bf16 v[28:31], a[184:187], v[152:155], 0\n"
"s_add_u32 m0, s61, 34816\n"
"ds_read_b64_tr_b16 a[176:177], v221 offset:33792\n"
"v_mul_u32_u24 v240, 1152, v240\n"
"v_mfma_f32_16x16x32_bf16 v[28:31], a[188:191], v[156:159], v[28:31]\n"
"buffer_load_dwordx4 v226, s[44:47], s58 offen lds\n"
"v_mfma_f32_16x16x32_bf16 v[24:27], a[188:191], v[148:151], 0\n"
"ds_read_b64_tr_b16 a[178:179], v221 offset:34816\n"
"v_lshl_add_u32 v240, v239, 4, v240\n"
"v_mfma_f32_16x16x32_bf16 v[24:27], a[184:187], v[144:147], v[24:27]\n"
"s_add_u32 s60, s59, 1048576\n"
"v_subrev_u32 v240, 1024, v240\n"
"v_add_u32 v240, s74, v240\n"
"v_mfma_f32_16x16x32_bf16 v[20:23], a[184:187], v[136:139], 0\n"
"s_add_u32 m0, s62, 49344\n"
"ds_read_b64_tr_b16 a[180:181], v221 offset:42016\n"
"v_accvgpr_write_b32 a209, v240\n"
"v_mfma_f32_16x16x32_bf16 v[20:23], a[188:191], v[140:143], v[20:23]\n"
"buffer_load_dwordx4 v225, s[48:51], s60 offen lds\n"
"v_mfma_f32_16x16x32_bf16 v[16:19], a[188:191], v[132:135], 0\n"
"ds_read_b64_tr_b16 a[182:183], v221 offset:43040\n"
"v_add_u32 v239, 128, v236\n"
"v_mfma_f32_16x16x32_bf16 v[16:19], a[184:187], v[128:131], v[16:19]\n"
"s_waitcnt lgkmcnt(8)\n"
"v_mul_u32_u24 v240, 3641, v239\n"
"v_lshrrev_b32 v240, 16, v240\n"
"v_mfma_f32_16x16x32_bf16 v[72:75], a[160:163], v[128:131], 0\n"
"s_waitcnt vmcnt(14)\n"
"ds_read_b64_tr_b16 a[184:185], v221 offset:33800\n"
"v_mul_u32_u24 v241, 18, v240\n"
"v_mfma_f32_16x16x32_bf16 v[72:75], a[164:167], v[132:135], v[72:75]\n"
"ds_read_b128 v[160:163], v228\n"
"ds_read_b128 v[164:167], v229\n"
"v_mfma_f32_16x16x32_bf16 v[88:91], a[164:167], v[140:143], 0\n"
"s_add_u32 m0, s61, 35840\n"
"ds_read_b64_tr_b16 a[186:187], v221 offset:34824\n"
"v_sub_u32 v239, v239, v241\n"
"v_mfma_f32_16x16x32_bf16 v[88:91], a[160:163], v[136:139], v[88:91]\n"
"buffer_load_dwordx4 v227, s[44:47], s58 offen lds\n"
"v_mfma_f32_16x16x32_bf16 v[104:107], a[160:163], v[144:147], 0\n"
"ds_read_b64_tr_b16 a[188:189], v221 offset:42024\n"
"v_lshrrev_b32 v241, 1, v240\n"
"v_mfma_f32_16x16x32_bf16 v[104:107], a[164:167], v[148:151], v[104:107]\n"
"s_add_u32 s60, s59, 1081344\n"
"v_and_b32 v241, 4, v241\n"
"v_xor_b32 v240, v240, v241\n"
"v_mfma_f32_16x16x32_bf16 v[120:123], a[164:167], v[156:159], 0\n"
"s_add_u32 m0, s62, 50368\n"
"ds_read_b64_tr_b16 a[190:191], v221 offset:43048\n"
"v_mul_u32_u24 v240, 1152, v240\n"
"v_mfma_f32_16x16x32_bf16 v[120:123], a[160:163], v[152:155], v[120:123]\n"
"s_waitcnt lgkmcnt(10)\n"
"buffer_load_dwordx4 v225, s[48:51], s60 offen lds\n"
"v_mfma_f32_16x16x32_bf16 v[44:47], a[168:171], v[152:155], 0\n"
"s_waitcnt vmcnt(13)\n"
"v_lshl_add_u32 v240, v239, 4, v240\n"
"v_subrev_u32 v240, 2048, v240\n"
"v_mfma_f32_16x16x32_bf16 v[44:47], a[172:175], v[156:159], v[44:47]\n"
"s_add_u32 s58, s87, 256\n"
"ds_read_b128 v[168:171], v228 offset:2048\n"
"ds_read_b128 v[172:175], v229 offset:2048\n"
"v_mfma_f32_16x16x32_bf16 v[40:43], a[172:175], v[148:151], 0\n"
"s_add_u32 m0, s61, 49152\n"
"v_add_u32 v240, s74, v240\n"
"v_accvgpr_write_b32 a210, v240\n"
"v_mfma_f32_16x16x32_bf16 v[40:43], a[168:171], v[144:147], v[40:43]\n"
"s_waitcnt vmcnt(8) lgkmcnt(2)\n"
"buffer_load_dwordx4 v226, s[44:47], s58 offen lds\n"
"v_mfma_f32_16x16x32_bf16 v[36:39], a[168:171], v[136:139], 0\n"
"s_barrier\n"
"v_mfma_f32_16x16x32_bf16 v[36:39], a[172:175], v[140:143], v[36:39]\n"
"ds_read_b64_tr_b16 a[160:161], v220 offset:32896\n"
"ds_read_b64_tr_b16 a[162:163], v220 offset:33920\n"
"v_mfma_f32_16x16x32_bf16 v[32:35], a[172:175], v[132:135], 0\n"
"ds_read_b64_tr_b16 a[164:165], v220 offset:41120\n"
"v_add_u32 v239, 192, v236\n"
"v_mfma_f32_16x16x32_bf16 v[32:35], a[168:171], v[128:131], v[32:35]\n"
"v_mul_u32_u24 v240, 3641, v239\n"
"v_lshrrev_b32 v240, 16, v240\n"
"v_mfma_f32_16x16x32_bf16 v[76:79], a[176:179], v[128:131], 0\n"
"s_waitcnt vmcnt(10)\n"
"ds_read_b64_tr_b16 a[166:167], v220 offset:42144\n"
"ds_read_b64_tr_b16 a[168:169], v220 offset:32904\n"
"v_mfma_f32_16x16x32_bf16 v[76:79], a[180:183], v[132:135], v[76:79]\n"
"s_add_u32 s60, s59, 1114112\n"
"ds_read_b128 v[176:179], v228 offset:16384\n"
"ds_read_b128 v[180:183], v229 offset:16384\n"
"v_mfma_f32_16x16x32_bf16 v[92:95], a[180:183], v[140:143], 0\n"
"s_add_u32 m0, s62, 51400\n"
"ds_read_b64_tr_b16 a[170:171], v220 offset:33928\n"
"v_mul_u32_u24 v241, 18, v240\n"
"v_mfma_f32_16x16x32_bf16 v[92:95], a[176:179], v[136:139], v[92:95]\n"
"buffer_load_dwordx4 v225, s[48:51], s60 offen lds\n"
"v_mfma_f32_16x16x32_bf16 v[108:111], a[176:179], v[144:147], 0\n"
"s_add_u32 m0, s61, 50176\n"
"ds_read_b64_tr_b16 a[172:173], v220 offset:41128\n"
"v_sub_u32 v239, v239, v241\n"
"v_mfma_f32_16x16x32_bf16 v[108:111], a[180:183], v[148:151], v[108:111]\n"
"buffer_load_dwordx4 v227, s[44:47], s58 offen lds\n"
"v_mfma_f32_16x16x32_bf16 v[124:127], a[180:183], v[156:159], 0\n"
"ds_read_b64_tr_b16 a[174:175], v220 offset:42152\n"
"v_lshrrev_b32 v241, 1, v240\n"
"v_mfma_f32_16x16x32_bf16 v[124:127], a[176:179], v[152:155], v[124:127]\n"
"v_and_b32 v241, 4, v241\n"
"v_xor_b32 v240, v240, v241\n"
"v_mfma_f32_16x16x32_bf16 v[60:63], a[184:187], v[152:155], 0\n"
"s_waitcnt vmcnt(9)\n"
"ds_read_b64_tr_b16 a[176:177], v220 offset:33152\n"
"v_mul_u32_u24 v240, 1152, v240\n"
"v_mfma_f32_16x16x32_bf16 v[60:63], a[188:191], v[156:159], v[60:63]\n"
"s_add_u32 s60, s59, 1146880\n"
"ds_read_b128 v[184:187], v228 offset:18432\n"
"ds_read_b128 v[188:191], v229 offset:18432\n"
"v_mfma_f32_16x16x32_bf16 v[56:59], a[188:191], v[148:151], 0\n"
"s_add_u32 m0, s62, 52424\n"
"ds_read_b64_tr_b16 a[178:179], v220 offset:34176\n"
"v_lshl_add_u32 v240, v239, 4, v240\n"
"v_mfma_f32_16x16x32_bf16 v[56:59], a[184:187], v[144:147], v[56:59]\n"
"buffer_load_dwordx4 v225, s[48:51], s60 offen lds\n"
"v_mfma_f32_16x16x32_bf16 v[52:55], a[184:187], v[136:139], 0\n"
"ds_read_b64_tr_b16 a[180:181], v220 offset:41376\n"
"v_subrev_u32 v240, 3072, v240\n"
"v_mfma_f32_16x16x32_bf16 v[52:55], a[188:191], v[140:143], v[52:55]\n"
"s_add_u32 s58, s68, 256\n"
"v_add_u32 v240, s74, v240\n"
"v_accvgpr_write_b32 a211, v240\n"
"v_mfma_f32_16x16x32_bf16 v[48:51], a[188:191], v[132:135], 0\n"
"s_add_u32 m0, s61, 51200\n"
"ds_read_b64_tr_b16 a[182:183], v220 offset:42400\n"
"v_add_u32 v239, 256, v236\n"
"v_mfma_f32_16x16x32_bf16 v[48:51], a[184:187], v[128:131], v[48:51]\n"
"s_waitcnt lgkmcnt(3)\n"
"buffer_load_dwordx4 v226, s[44:47], s58 offen lds\n"
"v_mfma_f32_16x16x32_bf16 v[64:67], a[160:163], v[160:163], v[64:67]\n"
"ds_read_b64_tr_b16 a[184:185], v220 offset:33160\n"
"v_mul_u32_u24 v240, 3641, v239\n"
"v_mfma_f32_16x16x32_bf16 v[64:67], a[164:167], v[164:167], v[64:67]\n"
"v_lshrrev_b32 v240, 16, v240\n"
"v_mul_u32_u24 v241, 18, v240\n"
"v_mfma_f32_16x16x32_bf16 v[80:83], a[164:167], v[172:175], v[80:83]\n"
"s_add_u32 m0, s61, 52224\n"
"ds_read_b64_tr_b16 a[186:187], v220 offset:34184\n"
"v_sub_u32 v239, v239, v241\n"
"v_mfma_f32_16x16x32_bf16 v[80:83], a[160:163], v[168:171], v[80:83]\n"
"buffer_load_dwordx4 v227, s[44:47], s58 offen lds\n"
"v_mfma_f32_16x16x32_bf16 v[96:99], a[160:163], v[176:179], v[96:99]\n"
"ds_read_b64_tr_b16 a[188:189], v220 offset:41384\n"
"v_lshrrev_b32 v241, 1, v240\n"
"v_mfma_f32_16x16x32_bf16 v[96:99], a[164:167], v[180:183], v[96:99]\n"
"v_and_b32 v241, 4, v241\n"
"v_xor_b32 v240, v240, v241\n"
"v_mfma_f32_16x16x32_bf16 v[112:115], a[164:167], v[188:191], v[112:115]\n"
"s_add_u32 s58, s84, 384\n"
"ds_read_b64_tr_b16 a[190:191], v220 offset:42408\n"
"v_mul_u32_u24 v240, 1152, v240\n"
"v_mfma_f32_16x16x32_bf16 v[112:115], a[160:163], v[184:187], v[112:115]\n"
"s_waitcnt lgkmcnt(10)\n"
"v_lshl_add_u32 v240, v239, 4, v240\n"
"v_subrev_u32 v240, 0, v240\n"
"v_mfma_f32_16x16x32_bf16 v[12:15], a[168:171], v[184:187], v[12:15]\n"
"s_add_u32 m0, s61, 0\n"
"ds_read_b64_tr_b16 a[160:161], v220 offset:33408\n"
"v_add_u32 v240, s74, v240\n"
"v_mfma_f32_16x16x32_bf16 v[12:15], a[172:175], v[188:191], v[12:15]\n"
"buffer_load_dwordx4 v226, s[44:47], s58 offen lds\n"
"v_mfma_f32_16x16x32_bf16 v[8:11], a[172:175], v[180:183], v[8:11]\n"
"ds_read_b64_tr_b16 a[162:163], v220 offset:34432\n"
"v_accvgpr_write_b32 a212, v240\n"
"v_mfma_f32_16x16x32_bf16 v[8:11], a[168:171], v[176:179], v[8:11]\n"
"v_add_u32 v239, 320, v236\n"
"v_mul_u32_u24 v240, 3641, v239\n"
"v_mfma_f32_16x16x32_bf16 v[4:7], a[168:171], v[168:171], v[4:7]\n"
"s_add_u32 m0, s61, 1024\n"
"ds_read_b64_tr_b16 a[164:165], v220 offset:41632\n"
"v_lshrrev_b32 v240, 16, v240\n"
"v_mfma_f32_16x16x32_bf16 v[4:7], a[172:175], v[172:175], v[4:7]\n"
"buffer_load_dwordx4 v227, s[44:47], s58 offen lds\n"
"v_mfma_f32_16x16x32_bf16 v[0:3], a[172:175], v[164:167], v[0:3]\n"
"s_add_u32 s58, s85, 384\n"
"ds_read_b64_tr_b16 a[166:167], v220 offset:42656\n"
"v_mul_u32_u24 v241, 18, v240\n"
"v_mfma_f32_16x16x32_bf16 v[0:3], a[168:171], v[160:163], v[0:3]\n"
"s_waitcnt lgkmcnt(8)\n"
"v_sub_u32 v239, v239, v241\n"
"v_lshrrev_b32 v241, 1, v240\n"
"v_mfma_f32_16x16x32_bf16 v[68:71], a[176:179], v[160:163], v[68:71]\n"
"s_add_u32 m0, s61, 2048\n"
"ds_read_b64_tr_b16 a[168:169], v220 offset:33416\n"
"v_and_b32 v241, 4, v241\n"
"v_mfma_f32_16x16x32_bf16 v[68:71], a[180:183], v[164:167], v[68:71]\n"
"buffer_load_dwordx4 v226, s[44:47], s58 offen lds\n"
"v_mfma_f32_16x16x32_bf16 v[84:87], a[180:183], v[172:175], v[84:87]\n"
"ds_read_b64_tr_b16 a[170:171], v220 offset:34440\n"
"v_xor_b32 v240, v240, v241\n"
"v_mfma_f32_16x16x32_bf16 v[84:87], a[176:179], v[168:171], v[84:87]\n"
"v_mul_u32_u24 v240, 1152, v240\n"
"v_lshl_add_u32 v240, v239, 4, v240\n"
"v_mfma_f32_16x16x32_bf16 v[100:103], a[176:179], v[176:179], v[100:103]\n"
"ds_read_b64_tr_b16 a[172:173], v220 offset:41640\n"
"v_subrev_u32 v240, 1024, v240\n"
"v_mfma_f32_16x16x32_bf16 v[100:103], a[180:183], v[180:183], v[100:103]\n"
"s_add_u32 s60, s59, 1572864\n"
"v_add_u32 v240, s74, v240\n"
"v_accvgpr_write_b32 a213, v240\n"
"v_mfma_f32_16x16x32_bf16 v[116:119], a[180:183], v[188:191], v[116:119]\n"
"s_add_u32 m0, s62, 0\n"
"ds_read_b64_tr_b16 a[174:175], v220 offset:42664\n"
"v_add_u32 v239, 384, v236\n"
"v_mfma_f32_16x16x32_bf16 v[116:119], a[176:179], v[184:187], v[116:119]\n"
"s_waitcnt lgkmcnt(8)\n"
"buffer_load_dwordx4 v225, s[48:51], s60 offen lds\n"
"v_mfma_f32_16x16x32_bf16 v[28:31], a[184:187], v[184:187], v[28:31]\n"
"ds_read_b64_tr_b16 a[176:177], v220 offset:33664\n"
"v_mul_u32_u24 v240, 3641, v239\n"
"v_mfma_f32_16x16x32_bf16 v[28:31], a[188:191], v[188:191], v[28:31]\n"
"v_lshrrev_b32 v240, 16, v240\n"
"v_mul_u32_u24 v241, 18, v240\n"
"v_mfma_f32_16x16x32_bf16 v[24:27], a[188:191], v[180:183], v[24:27]\n"
"s_add_u32 m0, s61, 3072\n"
"ds_read_b64_tr_b16 a[178:179], v220 offset:34688\n"
"v_sub_u32 v239, v239, v241\n"
"v_mfma_f32_16x16x32_bf16 v[24:27], a[184:187], v[176:179], v[24:27]\n"
"buffer_load_dwordx4 v227, s[44:47], s58 offen lds\n"
"v_mfma_f32_16x16x32_bf16 v[20:23], a[184:187], v[168:171], v[20:23]\n"
"ds_read_b64_tr_b16 a[180:181], v220 offset:41888\n"
"v_lshrrev_b32 v241, 1, v240\n"
"v_mfma_f32_16x16x32_bf16 v[20:23], a[188:191], v[172:175], v[20:23]\n"
"s_add_u32 s60, s59, 1605632\n"
"v_and_b32 v241, 4, v241\n"
"v_xor_b32 v240, v240, v241\n"
"v_mfma_f32_16x16x32_bf16 v[16:19], a[188:191], v[164:167], v[16:19]\n"
"s_add_u32 m0, s62, 1024\n"
"ds_read_b64_tr_b16 a[182:183], v220 offset:42912\n"
"v_mul_u32_u24 v240, 1152, v240\n"
"v_mfma_f32_16x16x32_bf16 v[16:19], a[184:187], v[160:163], v[16:19]\n"
"s_waitcnt lgkmcnt(8)\n"
"buffer_load_dwordx4 v225, s[48:51], s60 offen lds\n"
"v_mfma_f32_16x16x32_bf16 v[72:75], a[160:163], v[160:163], v[72:75]\n"
"s_waitcnt vmcnt(16)\n"
"ds_read_b64_tr_b16 a[184:185], v220 offset:33672\n"
"v_lshl_add_u32 v240, v239, 4, v240\n"
"v_mfma_f32_16x16x32_bf16 v[72:75], a[164:167], v[164:167], v[72:75]\n"
"ds_read_b128 v[128:131], v228 offset:32768\n"
"ds_read_b128 v[132:135], v229 offset:32768\n"
"v_mfma_f32_16x16x32_bf16 v[88:91], a[164:167], v[172:175], v[88:91]\n"
"ds_read_b64_tr_b16 a[186:187], v220 offset:34696\n"
"v_subrev_u32 v240, 2048, v240\n"
"v_mfma_f32_16x16x32_bf16 v[88:91], a[160:163], v[168:171], v[88:91]\n"
"s_add_u32 s58, s87, 384\n"
"v_add_u32 v240, s74, v240\n"
"v_accvgpr_write_b32 a214, v240\n"
"v_mfma_f32_16x16x32_bf16 v[104:107], a[160:163], v[176:179], v[104:107]\n"
"s_add_u32 m0, s61, 16384\n"
"ds_read_b64_tr_b16 a[188:189], v220 offset:41896\n"
"v_add_u32 v239, 448, v236\n"
"v_mfma_f32_16x16x32_bf16 v[104:107], a[164:167], v[180:183], v[104:107]\n"
"buffer_load_dwordx4 v226, s[44:47], s58 offen lds\n"
"v_mfma_f32_16x16x32_bf16 v[120:123], a[164:167], v[188:191], v[120:123]\n"
"ds_read_b64_tr_b16 a[190:191], v220 offset:42920\n"
"v_mul_u32_u24 v240, 3641, v239\n"
"v_mfma_f32_16x16x32_bf16 v[120:123], a[160:163], v[184:187], v[120:123]\n"
"s_waitcnt lgkmcnt(10)\n"
"v_lshrrev_b32 v240, 16, v240\n"
"v_mul_u32_u24 v241, 18, v240\n"
"v_mfma_f32_16x16x32_bf16 v[44:47], a[168:171], v[184:187], v[44:47]\n"
"s_waitcnt vmcnt(14)\n"
"v_sub_u32 v239, v239, v241\n"
"v_lshrrev_b32 v241, 1, v240\n"
"v_mfma_f32_16x16x32_bf16 v[44:47], a[172:175], v[188:191], v[44:47]\n"
"s_add_u32 s60, s59, 1638400\n"
"ds_read_b128 v[136:139], v228 offset:34816\n"
"ds_read_b128 v[140:143], v229 offset:34816\n"
"v_mfma_f32_16x16x32_bf16 v[40:43], a[172:175], v[180:183], v[40:43]\n"
"s_add_u32 m0, s62, 2056\n"
"v_and_b32 v241, 4, v241\n"
"v_xor_b32 v240, v240, v241\n"
"v_mfma_f32_16x16x32_bf16 v[40:43], a[168:171], v[176:179], v[40:43]\n"
"s_waitcnt vmcnt(9) lgkmcnt(2)\n"
"buffer_load_dwordx4 v225, s[48:51], s60 offen lds\n"
"v_mfma_f32_16x16x32_bf16 v[36:39], a[168:171], v[168:171], v[36:39]\n"
"s_barrier\n"
"v_mfma_f32_16x16x32_bf16 v[36:39], a[172:175], v[172:175], v[36:39]\n"
"ds_read_b64_tr_b16 a[160:161], v220 offset:49344\n"
"ds_read_b64_tr_b16 a[162:163], v220 offset:50368\n"
"v_mfma_f32_16x16x32_bf16 v[32:35], a[172:175], v[164:167], v[32:35]\n"
"s_add_u32 m0, s61, 17408\n"
"ds_read_b64_tr_b16 a[164:165], v220 offset:57568\n"
"v_mul_u32_u24 v240, 1152, v240\n"
"v_mfma_f32_16x16x32_bf16 v[32:35], a[168:171], v[160:163], v[32:35]\n"
"buffer_load_dwordx4 v227, s[44:47], s58 offen lds\n"
"v_mfma_f32_16x16x32_bf16 v[76:79], a[176:179], v[160:163], v[76:79]\n"
"s_waitcnt vmcnt(12)\n"
"ds_read_b64_tr_b16 a[166:167], v220 offset:58592\n"
"ds_read_b64_tr_b16 a[168:169], v220 offset:49352\n"
"v_mfma_f32_16x16x32_bf16 v[76:79], a[180:183], v[164:167], v[76:79]\n"
"s_add_u32 s60, s59, 1671168\n"
"ds_read_b128 v[144:147], v228 offset:49152\n"
"ds_read_b128 v[148:151], v229 offset:49152\n"
"v_mfma_f32_16x16x32_bf16 v[92:95], a[180:183], v[172:175], v[92:95]\n"
"s_add_u32 m0, s62, 3080\n"
"ds_read_b64_tr_b16 a[170:171], v220 offset:50376\n"
"v_lshl_add_u32 v240, v239, 4, v240\n"
"v_mfma_f32_16x16x32_bf16 v[92:95], a[176:179], v[168:171], v[92:95]\n"
"buffer_load_dwordx4 v225, s[48:51], s60 offen lds\n"
"v_mfma_f32_16x16x32_bf16 v[108:111], a[176:179], v[176:179], v[108:111]\n"
"ds_read_b64_tr_b16 a[172:173], v220 offset:57576\n"
"v_subrev_u32 v240, 3072, v240\n"
"v_mfma_f32_16x16x32_bf16 v[108:111], a[180:183], v[180:183], v[108:111]\n"
"s_add_u32 s58, s68, 384\n"
"v_add_u32 v240, s74, v240\n"
"v_accvgpr_write_b32 a215, v240\n"
"v_mfma_f32_16x16x32_bf16 v[124:127], a[180:183], v[188:191], v[124:127]\n"
"s_add_u32 m0, s61, 18432\n"
"ds_read_b64_tr_b16 a[174:175], v220 offset:58600\n"
"v_add_u32 v239, 512, v236\n"
"v_mfma_f32_16x16x32_bf16 v[124:127], a[176:179], v[184:187], v[124:127]\n"
"buffer_load_dwordx4 v226, s[44:47], s58 offen lds\n"
"v_mfma_f32_16x16x32_bf16 v[60:63], a[184:187], v[184:187], v[60:63]\n"
"s_waitcnt vmcnt(11)\n"
"ds_read_b64_tr_b16 a[176:177], v220 offset:49600\n"
"v_mul_u32_u24 v240, 3641, v239\n"
"v_mfma_f32_16x16x32_bf16 v[60:63], a[188:191], v[188:191], v[60:63]\n"
"ds_read_b128 v[152:155], v228 offset:51200\n"
"ds_read_b128 v[156:159], v229 offset:51200\n"
"v_mfma_f32_16x16x32_bf16 v[56:59], a[188:191], v[180:183], v[56:59]\n"
"ds_read_b64_tr_b16 a[178:179], v220 offset:50624\n"
"v_lshrrev_b32 v240, 16, v240\n"
"v_mfma_f32_16x16x32_bf16 v[56:59], a[184:187], v[176:179], v[56:59]\n"
"v_mul_u32_u24 v241, 18, v240\n"
"v_sub_u32 v239, v239, v241\n"
"v_mfma_f32_16x16x32_bf16 v[52:55], a[184:187], v[168:171], v[52:55]\n"
"s_add_u32 m0, s61, 19456\n"
"ds_read_b64_tr_b16 a[180:181], v220 offset:57824\n"
"v_lshrrev_b32 v241, 1, v240\n"
"v_mfma_f32_16x16x32_bf16 v[52:55], a[188:191], v[172:175], v[52:55]\n"
"buffer_load_dwordx4 v227, s[44:47], s58 offen lds\n"
"v_mfma_f32_16x16x32_bf16 v[48:51], a[188:191], v[164:167], v[48:51]\n"
"ds_read_b64_tr_b16 a[182:183], v220 offset:58848\n"
"v_and_b32 v241, 4, v241\n"
"v_mfma_f32_16x16x32_bf16 v[48:51], a[184:187], v[160:163], v[48:51]\n"
"s_waitcnt lgkmcnt(3)\n"
"v_xor_b32 v240, v240, v241\n"
"v_mul_u32_u24 v240, 1152, v240\n"
"v_mfma_f32_16x16x32_bf16 v[64:67], a[160:163], v[128:131], v[64:67]\n"
"s_add_u32 m0, s61, 32768\n"
"s_add_u32 s58, s84, 512\n"
"ds_read_b64_tr_b16 a[184:185], v220 offset:49608\n"
"v_lshl_add_u32 v240, v239, 4, v240\n"
"v_mfma_f32_16x16x32_bf16 v[64:67], a[164:167], v[132:135], v[64:67]\n"
"buffer_load_dwordx4 v226, s[44:47], s58 offen lds\n"
"v_mfma_f32_16x16x32_bf16 v[80:83], a[164:167], v[140:143], v[80:83]\n"
"ds_read_b64_tr_b16 a[186:187], v220 offset:50632\n"
"v_subrev_u32 v240, 0, v240\n"
"v_mfma_f32_16x16x32_bf16 v[80:83], a[160:163], v[136:139], v[80:83]\n"
"v_add_u32 v240, s74, v240\n"
"v_accvgpr_write_b32 a216, v240\n"
"v_mfma_f32_16x16x32_bf16 v[96:99], a[160:163], v[144:147], v[96:99]\n"
"s_add_u32 m0, s61, 33792\n"
"ds_read_b64_tr_b16 a[188:189], v220 offset:57832\n"
"v_bfe_u32 v240, v236, 2, 3\n"
"v_mfma_f32_16x16x32_bf16 v[96:99], a[164:167], v[148:151], v[96:99]\n"
"buffer_load_dwordx4 v227, s[44:47], s58 offen lds\n"
"v_mfma_f32_16x16x32_bf16 v[112:115], a[164:167], v[156:159], v[112:115]\n"
"ds_read_b64_tr_b16 a[190:191], v220 offset:58856\n"
"v_lshl_add_u32 v240, v238, 3, v240\n"
"v_mfma_f32_16x16x32_bf16 v[112:115], a[160:163], v[152:155], v[112:115]\n"
"s_waitcnt lgkmcnt(10)\n"
"v_and_b32 v241, 3, v236\n"
"v_lshlrev_b32 v241, 3, v241\n"
"v_mfma_f32_16x16x32_bf16 v[12:15], a[168:171], v[152:155], v[12:15]\n"
"ds_read_b64_tr_b16 a[160:161], v220 offset:49856\n"
"v_mul_u32_u24 v239, 288, v240\n"
"v_mfma_f32_16x16x32_bf16 v[12:15], a[172:175], v[156:159], v[12:15]\n"
"s_add_u32 s58, s85, 512\n"
"v_add_u32 v239, v239, v241\n"
"v_accvgpr_write_b32 a217, v239\n"
"v_mfma_f32_16x16x32_bf16 v[8:11], a[172:175], v[148:151], v[8:11]\n"
"s_add_u32 m0, s61, 34816\n"
"ds_read_b64_tr_b16 a[162:163], v220 offset:50880\n"
"v_xor_b32 v240, 4, v240\n"
"v_mfma_f32_16x16x32_bf16 v[8:11], a[168:171], v[144:147], v[8:11]\n"
"buffer_load_dwordx4 v226, s[44:47], s58 offen lds\n"
"v_mfma_f32_16x16x32_bf16 v[4:7], a[168:171], v[136:139], v[4:7]\n"
"ds_read_b64_tr_b16 a[164:165], v220 offset:58080\n"
"v_mul_u32_u24 v239, 288, v240\n"
"v_mfma_f32_16x16x32_bf16 v[4:7], a[172:175], v[140:143], v[4:7]\n"
"s_add_u32 s60, s59, 2097152\n"
"v_add_u32 v239, v239, v241\n"
"v_accvgpr_write_b32 a218, v239\n"
"v_mfma_f32_16x16x32_bf16 v[0:3], a[172:175], v[132:135], v[0:3]\n"
"s_add_u32 m0, s62, 16448\n"
"ds_read_b64_tr_b16 a[166:167], v220 offset:59104\n"
"v_xor_b32 v240, 4, v240\n"
"v_mfma_f32_16x16x32_bf16 v[0:3], a[168:171], v[128:131], v[0:3]\n"
"s_waitcnt lgkmcnt(8)\n"
"buffer_load_dwordx4 v225, s[48:51], s60 offen lds\n"
"v_mfma_f32_16x16x32_bf16 v[68:71], a[176:179], v[128:131], v[68:71]\n"
"s_lshl_b32 s74, s69, 6\n"
"ds_read_b64_tr_b16 a[168:169], v220 offset:49864\n"
"v_add_u32 v239, s74, v237\n"
"v_mfma_f32_16x16x32_bf16 v[68:71], a[180:183], v[132:135], v[68:71]\n"
"v_mul_u32_u24 v239, 2304, v239\n"
"v_lshl_add_u32 v239, v238, 3, v239\n"
"v_mfma_f32_16x16x32_bf16 v[84:87], a[180:183], v[140:143], v[84:87]\n"
"s_add_u32 m0, s61, 35840\n"
"ds_read_b64_tr_b16 a[170:171], v220 offset:50888\n"
"v_accvgpr_write_b32 a219, v239\n"
"v_mfma_f32_16x16x32_bf16 v[84:87], a[176:179], v[136:139], v[84:87]\n"
"s_lshl_b32 s74, s23, 2\n"
"buffer_load_dwordx4 v227, s[44:47], s58 offen lds\n"
"v_mfma_f32_16x16x32_bf16 v[100:103], a[176:179], v[144:147], v[100:103]\n"
"s_add_u32 s74, s74, 256\n"
"ds_read_b64_tr_b16 a[172:173], v220 offset:58088\n"
"v_lshl_add_u32 v239, v236, 8, s74\n"
"v_mfma_f32_16x16x32_bf16 v[100:103], a[180:183], v[148:151], v[100:103]\n"
"v_lshl_add_u32 v239, v238, 20, v239\n"
"v_accvgpr_write_b32 a220, v239\n"
"v_mfma_f32_16x16x32_bf16 v[116:119], a[180:183], v[156:159], v[116:119]\n"
"s_add_u32 s60, s59, 2129920\n"
"ds_read_b64_tr_b16 a[174:175], v220 offset:59112\n"
"v_mfma_f32_16x16x32_bf16 v[116:119], a[176:179], v[152:155], v[116:119]\n"
"s_waitcnt lgkmcnt(8)\n"
"v_mfma_f32_16x16x32_bf16 v[28:31], a[184:187], v[152:155], v[28:31]\n"
"s_add_u32 m0, s62, 17472\n"
"ds_read_b64_tr_b16 a[176:177], v220 offset:50112\n"
"v_mfma_f32_16x16x32_bf16 v[28:31], a[188:191], v[156:159], v[28:31]\n"
"s_lshl_b32 s74, s69, 11\n"
"buffer_load_dwordx4 v225, s[48:51], s60 offen lds\n"
"v_mfma_f32_16x16x32_bf16 v[24:27], a[188:191], v[148:151], v[24:27]\n"
"s_add_u32 s74, s74, 8192\n"
"ds_read_b64_tr_b16 a[178:179], v220 offset:51136\n"
"v_subrev_u32 v240, s23, v236\n"
"v_mfma_f32_16x16x32_bf16 v[24:27], a[184:187], v[144:147], v[24:27]\n"
"s_add_u32 s58, s87, 512\n"
"v_and_b32 v241, 12, v240\n"
"v_lshl_add_u32 v239, v236, 13, s74\n"
"v_mfma_f32_16x16x32_bf16 v[20:23], a[184:187], v[136:139], v[20:23]\n"
"s_add_u32 m0, s61, 49152\n"
"ds_read_b64_tr_b16 a[180:181], v220 offset:58336\n"
"v_lshl_add_u32 v239, v241, 5, v239\n"
"v_mfma_f32_16x16x32_bf16 v[20:23], a[188:191], v[140:143], v[20:23]\n"
"buffer_load_dwordx4 v226, s[44:47], s58 offen lds\n"
"v_mfma_f32_16x16x32_bf16 v[16:19], a[188:191], v[132:135], v[16:19]\n"
"ds_read_b64_tr_b16 a[182:183], v220 offset:59360\n"
"v_and_b32 v240, 3, v240\n"
"v_mfma_f32_16x16x32_bf16 v[16:19], a[184:187], v[128:131], v[16:19]\n"
"s_waitcnt lgkmcnt(8)\n"
"v_lshl_add_u32 v239, v240, 20, v239\n"
"v_accvgpr_write_b32 a223, v239\n"
"v_mfma_f32_16x16x32_bf16 v[72:75], a[160:163], v[128:131], v[72:75]\n"
"s_waitcnt vmcnt(17)\n"
"ds_read_b64_tr_b16 a[184:185], v220 offset:50120\n"
"v_mfma_f32_16x16x32_bf16 v[72:75], a[164:167], v[132:135], v[72:75]\n"
"s_add_u32 s60, s59, 2162688\n"
"ds_read_b128 v[160:163], v228\n"
"ds_read_b128 v[164:167], v229\n"
"v_mfma_f32_16x16x32_bf16 v[88:91], a[164:167], v[140:143], v[88:91]\n"
"s_add_u32 m0, s62, 18504\n"
"ds_read_b64_tr_b16 a[186:187], v220 offset:51144\n"
"v_mfma_f32_16x16x32_bf16 v[88:91], a[160:163], v[136:139], v[88:91]\n"
"s_lshl_b32 s73, s72, 7\n"
"buffer_load_dwordx4 v225, s[48:51], s60 offen lds\n"
"v_mfma_f32_16x16x32_bf16 v[104:107], a[160:163], v[144:147], v[104:107]\n"
"s_add_u32 s74, s74, s73\n"
"ds_read_b64_tr_b16 a[188:189], v220 offset:58344\n"
"v_mfma_f32_16x16x32_bf16 v[104:107], a[164:167], v[148:151], v[104:107]\n"
"s_add_u32 s74, s74, 1536\n"
"v_lshl_add_u32 v239, v236, 13, s74\n"
"v_xor_b32 v240, s23, v236\n"
"v_mfma_f32_16x16x32_bf16 v[120:123], a[164:167], v[156:159], v[120:123]\n"
"s_add_u32 m0, s61, 50176\n"
"ds_read_b64_tr_b16 a[190:191], v220 offset:59368\n"
"v_lshrrev_b32 v240, 2, v240\n"
"v_mfma_f32_16x16x32_bf16 v[120:123], a[160:163], v[152:155], v[120:123]\n"
"s_waitcnt lgkmcnt(10)\n"
"buffer_load_dwordx4 v227, s[44:47], s58 offen lds\n"
"v_mfma_f32_16x16x32_bf16 v[44:47], a[168:171], v[152:155], v[44:47]\n"
"s_waitcnt vmcnt(16)\n"
"v_lshl_add_u32 v239, v240, 20, v239\n"
"v_accvgpr_write_b32 a205, v239\n"
"v_mfma_f32_16x16x32_bf16 v[44:47], a[172:175], v[156:159], v[44:47]\n"
"s_add_u32 s60, s59, 2195456\n"
"ds_read_b128 v[168:171], v228 offset:2048\n"
"ds_read_b128 v[172:175], v229 offset:2048\n"
"v_mfma_f32_16x16x32_bf16 v[40:43], a[172:175], v[148:151], v[40:43]\n"
"s_add_u32 m0, s62, 19528\n"
"v_bfe_i32 v239, v236, 0, 1\n"
"v_and_b32 v239, 0xaaaaaaaa, v239\n"
"v_mfma_f32_16x16x32_bf16 v[40:43], a[168:171], v[144:147], v[40:43]\n"
"s_waitcnt vmcnt(11) lgkmcnt(2)\n"
"buffer_load_dwordx4 v225, s[48:51], s60 offen lds\n"
"v_mfma_f32_16x16x32_bf16 v[36:39], a[168:171], v[136:139], v[36:39]\n"
"s_barrier\n"
"v_mfma_f32_16x16x32_bf16 v[36:39], a[172:175], v[140:143], v[36:39]\n"
"ds_read_b64_tr_b16 a[160:161], v220\n"
"ds_read_b64_tr_b16 a[162:163], v220 offset:1024\n"
"v_mfma_f32_16x16x32_bf16 v[32:35], a[172:175], v[132:135], v[32:35]\n"
"ds_read_b64_tr_b16 a[164:165], v220 offset:8224\n"
"v_or_b32 v208, 0x44444444, v239\n"
"v_mfma_f32_16x16x32_bf16 v[32:35], a[168:171], v[128:131], v[32:35]\n"
"v_cmp_gt_u32 vcc, 2, v237\n"
"v_mov_b32 v239, 0x3f803f80\n"
"v_mfma_f32_16x16x32_bf16 v[76:79], a[176:179], v[128:131], v[76:79]\n"
"s_waitcnt vmcnt(13)\n"
"ds_read_b64_tr_b16 a[166:167], v220 offset:9248\n"
"ds_read_b64_tr_b16 a[168:169], v220 offset:8\n"
"v_mfma_f32_16x16x32_bf16 v[76:79], a[180:183], v[132:135], v[76:79]\n"
"s_add_u32 s58, s68, 512\n"
"ds_read_b128 v[176:179], v228 offset:16384\n"
"ds_read_b128 v[180:183], v229 offset:16384\n"
"v_mfma_f32_16x16x32_bf16 v[92:95], a[180:183], v[140:143], v[92:95]\n"
"s_add_u32 m0, s61, 51200\n"
"ds_read_b64_tr_b16 a[170:171], v220 offset:1032\n"
"v_cndmask_b32 v239, 0, v239, vcc\n"
"v_mfma_f32_16x16x32_bf16 v[92:95], a[176:179], v[136:139], v[92:95]\n"
"buffer_load_dwordx4 v226, s[44:47], s58 offen lds\n"
"v_mfma_f32_16x16x32_bf16 v[108:111], a[176:179], v[144:147], v[108:111]\n"
"s_add_u32 m0, s61, 52224\n"
"ds_read_b64_tr_b16 a[172:173], v220 offset:8232\n"
"v_accvgpr_write_b32 a200, v239\n"
"v_mfma_f32_16x16x32_bf16 v[108:111], a[180:183], v[148:151], v[108:111]\n"
"buffer_load_dwordx4 v227, s[44:47], s58 offen lds\n"
"v_mfma_f32_16x16x32_bf16 v[124:127], a[180:183], v[156:159], v[124:127]\n"
"ds_read_b64_tr_b16 a[174:175], v220 offset:9256\n"
"v_accvgpr_write_b32 a201, v239\n"
"v_mfma_f32_16x16x32_bf16 v[124:127], a[176:179], v[152:155], v[124:127]\n"
"s_add_u32 s58, s84, 640\n"
"v_accvgpr_write_b32 a202, v239\n"
"v_accvgpr_write_b32 a203, v239\n"
"v_mfma_f32_16x16x32_bf16 v[60:63], a[184:187], v[152:155], v[60:63]\n"
"s_waitcnt vmcnt(12)\n"
"ds_read_b64_tr_b16 a[176:177], v220 offset:256\n"
"v_mfma_f32_16x16x32_bf16 v[60:63], a[188:191], v[156:159], v[60:63]\n"
"s_add_u32 m0, s61, 0\n"
"ds_read_b128 v[184:187], v228 offset:18432\n"
"ds_read_b128 v[188:191], v229 offset:18432\n"
"v_mfma_f32_16x16x32_bf16 v[56:59], a[188:191], v[148:151], v[56:59]\n"
"s_waitcnt lgkmcnt(15)\n"
"ds_read_b64_tr_b16 a[178:179], v220 offset:1280\n"
"v_mfma_f32_16x16x32_bf16 v[56:59], a[184:187], v[144:147], v[56:59]\n"
"buffer_load_dwordx4 v226, s[44:47], s58 offen lds\n"
"v_mfma_f32_16x16x32_bf16 v[52:55], a[184:187], v[136:139], v[52:55]\n"
"ds_read_b64_tr_b16 a[180:181], v220 offset:8480\n"
"v_mfma_f32_16x16x32_bf16 v[52:55], a[188:191], v[140:143], v[52:55]\n"
"s_add_u32 m0, s61, 1024\n"
"v_mfma_f32_16x16x32_bf16 v[48:51], a[188:191], v[132:135], v[48:51]\n"
"s_waitcnt lgkmcnt(15)\n"
"ds_read_b64_tr_b16 a[182:183], v220 offset:9504\n"
"v_mfma_f32_16x16x32_bf16 v[48:51], a[184:187], v[128:131], v[48:51]\n"
"s_waitcnt lgkmcnt(3)\n"
"buffer_load_dwordx4 v227, s[44:47], s58 offen lds\n"
"v_mfma_f32_16x16x32_bf16 v[64:67], a[160:163], v[160:163], v[64:67]\n"
"ds_read_b64_tr_b16 a[184:185], v220 offset:264\n"
"v_mfma_f32_16x16x32_bf16 v[64:67], a[164:167], v[164:167], v[64:67]\n"
"s_add_u32 s58, s85, 640\n"
"v_mfma_f32_16x16x32_bf16 v[80:83], a[164:167], v[172:175], v[80:83]\n"
"s_add_u32 m0, s61, 2048\n"
"ds_read_b64_tr_b16 a[186:187], v220 offset:1288\n"
"v_mfma_f32_16x16x32_bf16 v[80:83], a[160:163], v[168:171], v[80:83]\n"
"buffer_load_dwordx4 v226, s[44:47], s58 offen lds\n"
"v_mfma_f32_16x16x32_bf16 v[96:99], a[160:163], v[176:179], v[96:99]\n"
"ds_read_b64_tr_b16 a[188:189], v220 offset:8488\n"
"v_mfma_f32_16x16x32_bf16 v[96:99], a[164:167], v[180:183], v[96:99]\n"
"s_add_u32 s60, s59, 2621440\n"
"v_mfma_f32_16x16x32_bf16 v[112:115], a[164:167], v[188:191], v[112:115]\n"
"s_add_u32 m0, s62, 65792\n"
"ds_read_b64_tr_b16 a[190:191], v220 offset:9512\n"
"v_mfma_f32_16x16x32_bf16 v[112:115], a[160:163], v[184:187], v[112:115]\n"
"s_waitcnt lgkmcnt(10)\n"
"buffer_load_dwordx4 v225, s[48:51], s60 offen lds\n"
"v_mfma_f32_16x16x32_bf16 v[12:15], a[168:171], v[184:187], v[12:15]\n"
"ds_read_b64_tr_b16 a[160:161], v220 offset:512\n"
"v_mfma_f32_16x16x32_bf16 v[12:15], a[172:175], v[188:191], v[12:15]\n"
"v_mfma_f32_16x16x32_bf16 v[8:11], a[172:175], v[180:183], v[8:11]\n"
"ds_read_b64_tr_b16 a[162:163], v220 offset:1536\n"
"v_mfma_f32_16x16x32_bf16 v[8:11], a[168:171], v[176:179], v[8:11]\n"
"v_mfma_f32_16x16x32_bf16 v[4:7], a[168:171], v[168:171], v[4:7]\n"
"s_add_u32 m0, s61, 3072\n"
"ds_read_b64_tr_b16 a[164:165], v220 offset:8736\n"
"v_mfma_f32_16x16x32_bf16 v[4:7], a[172:175], v[172:175], v[4:7]\n"
"buffer_load_dwordx4 v227, s[44:47], s58 offen lds\n"
"v_mfma_f32_16x16x32_bf16 v[0:3], a[172:175], v[164:167], v[0:3]\n"
"ds_read_b64_tr_b16 a[166:167], v220 offset:9760\n"
"v_mfma_f32_16x16x32_bf16 v[0:3], a[168:171], v[160:163], v[0:3]\n"
"s_waitcnt lgkmcnt(8)\n"
"v_mfma_f32_16x16x32_bf16 v[68:71], a[176:179], v[160:163], v[68:71]\n"
"s_waitcnt vmcnt(15)\n"
"ds_read_b64_tr_b16 a[168:169], v220 offset:520\n"
"v_mfma_f32_16x16x32_bf16 v[68:71], a[180:183], v[164:167], v[68:71]\n"
"s_add_u32 s60, s59, 2654208\n"
"ds_read_b128 v[128:131], v228 offset:32768\n"
"ds_read_b128 v[132:135], v229 offset:32768\n"
"v_mfma_f32_16x16x32_bf16 v[84:87], a[180:183], v[172:175], v[84:87]\n"
"s_add_u32 m0, s62, 66816\n"
"ds_read_b64_tr_b16 a[170:171], v220 offset:1544\n"
"v_mfma_f32_16x16x32_bf16 v[84:87], a[176:179], v[168:171], v[84:87]\n"
"buffer_load_dwordx4 v225, s[48:51], s60 offen lds\n"
"v_mfma_f32_16x16x32_bf16 v[100:103], a[176:179], v[176:179], v[100:103]\n"
"ds_read_b64_tr_b16 a[172:173], v220 offset:8744\n"
"v_mfma_f32_16x16x32_bf16 v[100:103], a[180:183], v[180:183], v[100:103]\n"
"s_add_u32 s58, s87, 640\n"
"v_mfma_f32_16x16x32_bf16 v[116:119], a[180:183], v[188:191], v[116:119]\n"
"s_add_u32 m0, s61, 16384\n"
"ds_read_b64_tr_b16 a[174:175], v220 offset:9768\n"
"v_mfma_f32_16x16x32_bf16 v[116:119], a[176:179], v[184:187], v[116:119]\n"
"s_waitcnt lgkmcnt(10)\n"
"buffer_load_dwordx4 v226, s[44:47], s58 offen lds\n"
"v_mfma_f32_16x16x32_bf16 v[28:31], a[184:187], v[184:187], v[28:31]\n"
"ds_read_b64_tr_b16 a[176:177], v220 offset:768\n"
"v_mfma_f32_16x16x32_bf16 v[28:31], a[188:191], v[188:191], v[28:31]\n"
"s_add_u32 s60, s59, 2686976\n"
"v_mfma_f32_16x16x32_bf16 v[24:27], a[188:191], v[180:183], v[24:27]\n"
"s_add_u32 m0, s62, 67848\n"
"ds_read_b64_tr_b16 a[178:179], v220 offset:1792\n"
"v_mfma_f32_16x16x32_bf16 v[24:27], a[184:187], v[176:179], v[24:27]\n"
"buffer_load_dwordx4 v225, s[48:51], s60 offen lds\n"
"v_mfma_f32_16x16x32_bf16 v[20:23], a[184:187], v[168:171], v[20:23]\n"
"ds_read_b64_tr_b16 a[180:181], v220 offset:8992\n"
"v_mfma_f32_16x16x32_bf16 v[20:23], a[188:191], v[172:175], v[20:23]\n"
"v_mfma_f32_16x16x32_bf16 v[16:19], a[188:191], v[164:167], v[16:19]\n"
"s_add_u32 m0, s61, 17408\n"
"ds_read_b64_tr_b16 a[182:183], v220 offset:10016\n"
"v_mfma_f32_16x16x32_bf16 v[16:19], a[184:187], v[160:163], v[16:19]\n"
"s_waitcnt lgkmcnt(10)\n"
"buffer_load_dwordx4 v227, s[44:47], s58 offen lds\n"
"v_mfma_f32_16x16x32_bf16 v[72:75], a[160:163], v[160:163], v[72:75]\n"
"s_waitcnt vmcnt(16)\n"
"ds_read_b64_tr_b16 a[184:185], v220 offset:776\n"
"v_mfma_f32_16x16x32_bf16 v[72:75], a[164:167], v[164:167], v[72:75]\n"
"ds_read_b128 v[136:139], v228 offset:34816\n"
"ds_read_b128 v[140:143], v229 offset:34816\n"
"v_mfma_f32_16x16x32_bf16 v[88:91], a[164:167], v[172:175], v[88:91]\n"
"ds_read_b64_tr_b16 a[186:187], v220 offset:1800\n"
"v_mfma_f32_16x16x32_bf16 v[88:91], a[160:163], v[168:171], v[88:91]\n"
"s_add_u32 s60, s59, 2719744\n"
"v_mfma_f32_16x16x32_bf16 v[104:107], a[160:163], v[176:179], v[104:107]\n"
"s_add_u32 m0, s62, 68872\n"
"ds_read_b64_tr_b16 a[188:189], v220 offset:9000\n"
"v_mfma_f32_16x16x32_bf16 v[104:107], a[164:167], v[180:183], v[104:107]\n"
"buffer_load_dwordx4 v225, s[48:51], s60 offen lds\n"
"v_mfma_f32_16x16x32_bf16 v[120:123], a[164:167], v[188:191], v[120:123]\n"
"ds_read_b64_tr_b16 a[190:191], v220 offset:10024\n"
"v_mfma_f32_16x16x32_bf16 v[120:123], a[160:163], v[184:187], v[120:123]\n"
"s_waitcnt lgkmcnt(10)\n"
"v_mfma_f32_16x16x32_bf16 v[44:47], a[168:171], v[184:187], v[44:47]\n"
"s_waitcnt vmcnt(13)\n"
"v_mfma_f32_16x16x32_bf16 v[44:47], a[172:175], v[188:191], v[44:47]\n"
"s_add_u32 s58, s68, 640\n"
"ds_read_b128 v[144:147], v228 offset:49152\n"
"ds_read_b128 v[148:151], v229 offset:49152\n"
"v_mfma_f32_16x16x32_bf16 v[40:43], a[172:175], v[180:183], v[40:43]\n"
"s_add_u32 m0, s61, 18432\n"
"v_mfma_f32_16x16x32_bf16 v[40:43], a[168:171], v[176:179], v[40:43]\n"
"s_waitcnt vmcnt(12) lgkmcnt(2)\n"
"buffer_load_dwordx4 v226, s[44:47], s58 offen lds\n"
"v_mfma_f32_16x16x32_bf16 v[36:39], a[168:171], v[168:171], v[36:39]\n"
"s_barrier\n"
"v_mfma_f32_16x16x32_bf16 v[36:39], a[172:175], v[172:175], v[36:39]\n"
"ds_read_b64_tr_b16 a[160:161], v220 offset:16448\n"
"ds_read_b64_tr_b16 a[162:163], v220 offset:17472\n"
"v_mfma_f32_16x16x32_bf16 v[32:35], a[172:175], v[164:167], v[32:35]\n"
"s_add_u32 m0, s61, 19456\n"
"ds_read_b64_tr_b16 a[164:165], v220 offset:24672\n"
"v_mfma_f32_16x16x32_bf16 v[32:35], a[168:171], v[160:163], v[32:35]\n"
"buffer_load_dwordx4 v227, s[44:47], s58 offen lds\n"
"v_mfma_f32_16x16x32_bf16 v[76:79], a[176:179], v[160:163], v[76:79]\n"
"s_add_u32 s58, s84, 768\n"
"ds_read_b64_tr_b16 a[166:167], v220 offset:25696\n"
"ds_read_b64_tr_b16 a[168:169], v220 offset:16456\n"
"v_mfma_f32_16x16x32_bf16 v[76:79], a[180:183], v[164:167], v[76:79]\n"
"s_add_u32 m0, s61, 32768\n"
"v_mfma_f32_16x16x32_bf16 v[92:95], a[180:183], v[172:175], v[92:95]\n"
"s_waitcnt lgkmcnt(15)\n"
"ds_read_b64_tr_b16 a[170:171], v220 offset:17480\n"
"v_mfma_f32_16x16x32_bf16 v[92:95], a[176:179], v[168:171], v[92:95]\n"
"buffer_load_dwordx4 v226, s[44:47], s58 offen lds\n"
"v_mfma_f32_16x16x32_bf16 v[108:111], a[176:179], v[176:179], v[108:111]\n"
"ds_read_b64_tr_b16 a[172:173], v220 offset:24680\n"
"v_mfma_f32_16x16x32_bf16 v[108:111], a[180:183], v[180:183], v[108:111]\n"
"s_add_u32 m0, s61, 33792\n"
"v_mfma_f32_16x16x32_bf16 v[124:127], a[180:183], v[188:191], v[124:127]\n"
"s_waitcnt lgkmcnt(15)\n"
"ds_read_b64_tr_b16 a[174:175], v220 offset:25704\n"
"v_mfma_f32_16x16x32_bf16 v[124:127], a[176:179], v[184:187], v[124:127]\n"
"buffer_load_dwordx4 v227, s[44:47], s58 offen lds\n"
"v_mfma_f32_16x16x32_bf16 v[60:63], a[184:187], v[184:187], v[60:63]\n"
"s_waitcnt vmcnt(14)\n"
"ds_read_b64_tr_b16 a[176:177], v220 offset:16704\n"
"v_mfma_f32_16x16x32_bf16 v[60:63], a[188:191], v[188:191], v[60:63]\n"
"s_add_u32 m0, s61, 34816\n"
"ds_read_b128 v[152:155], v228 offset:51200\n"
"ds_read_b128 v[156:159], v229 offset:51200\n"
"v_mfma_f32_16x16x32_bf16 v[56:59], a[188:191], v[180:183], v[56:59]\n"
"s_waitcnt lgkmcnt(15)\n"
"s_add_u32 s58, s85, 768\n"
"ds_read_b64_tr_b16 a[178:179], v220 offset:17728\n"
"v_mfma_f32_16x16x32_bf16 v[56:59], a[184:187], v[176:179], v[56:59]\n"
"buffer_load_dwordx4 v226, s[44:47], s58 offen lds\n"
"v_mfma_f32_16x16x32_bf16 v[52:55], a[184:187], v[168:171], v[52:55]\n"
"ds_read_b64_tr_b16 a[180:181], v220 offset:24928\n"
"v_mfma_f32_16x16x32_bf16 v[52:55], a[188:191], v[172:175], v[52:55]\n"
"v_mfma_f32_16x16x32_bf16 v[48:51], a[188:191], v[164:167], v[48:51]\n"
"ds_read_b64_tr_b16 a[182:183], v220 offset:25952\n"
"v_mfma_f32_16x16x32_bf16 v[48:51], a[184:187], v[160:163], v[48:51]\n"
"s_waitcnt lgkmcnt(3)\n"
"v_mfma_f32_16x16x32_bf16 v[64:67], a[160:163], v[128:131], v[64:67]\n"
"s_add_u32 m0, s62, 32896\n"
"s_add_u32 s60, s59, 3145728\n"
"ds_read_b64_tr_b16 a[184:185], v220 offset:16712\n"
"v_mfma_f32_16x16x32_bf16 v[64:67], a[164:167], v[132:135], v[64:67]\n"
"buffer_load_dwordx4 v225, s[48:51], s60 offen lds\n"
"v_mfma_f32_16x16x32_bf16 v[80:83], a[164:167], v[140:143], v[80:83]\n"
"ds_read_b64_tr_b16 a[186:187], v220 offset:17736\n"
"v_mfma_f32_16x16x32_bf16 v[80:83], a[160:163], v[136:139], v[80:83]\n"
"v_mfma_f32_16x16x32_bf16 v[96:99], a[160:163], v[144:147], v[96:99]\n"
"s_add_u32 m0, s61, 35840\n"
"ds_read_b64_tr_b16 a[188:189], v220 offset:24936\n"
"v_mfma_f32_16x16x32_bf16 v[96:99], a[164:167], v[148:151], v[96:99]\n"
"buffer_load_dwordx4 v227, s[44:47], s58 offen lds\n"
"v_mfma_f32_16x16x32_bf16 v[112:115], a[164:167], v[156:159], v[112:115]\n"
"ds_read_b64_tr_b16 a[190:191], v220 offset:25960\n"
"v_mfma_f32_16x16x32_bf16 v[112:115], a[160:163], v[152:155], v[112:115]\n"
"s_waitcnt lgkmcnt(10)\n"
"v_mfma_f32_16x16x32_bf16 v[12:15], a[168:171], v[152:155], v[12:15]\n"
"ds_read_b64_tr_b16 a[160:161], v220 offset:16960\n"
"v_mfma_f32_16x16x32_bf16 v[12:15], a[172:175], v[156:159], v[12:15]\n"
"s_add_u32 s60, s59, 3178496\n"
"v_mfma_f32_16x16x32_bf16 v[8:11], a[172:175], v[148:151], v[8:11]\n"
"s_add_u32 m0, s62, 33920\n"
"ds_read_b64_tr_b16 a[162:163], v220 offset:17984\n"
"v_mfma_f32_16x16x32_bf16 v[8:11], a[168:171], v[144:147], v[8:11]\n"
"buffer_load_dwordx4 v225, s[48:51], s60 offen lds\n"
"v_mfma_f32_16x16x32_bf16 v[4:7], a[168:171], v[136:139], v[4:7]\n"
"ds_read_b64_tr_b16 a[164:165], v220 offset:25184\n"
"v_mfma_f32_16x16x32_bf16 v[4:7], a[172:175], v[140:143], v[4:7]\n"
"s_add_u32 s58, s87, 768\n"
"v_mfma_f32_16x16x32_bf16 v[0:3], a[172:175], v[132:135], v[0:3]\n"
"s_add_u32 m0, s61, 49152\n"
"ds_read_b64_tr_b16 a[166:167], v220 offset:26208\n"
"v_mfma_f32_16x16x32_bf16 v[0:3], a[168:171], v[128:131], v[0:3]\n"
"s_waitcnt lgkmcnt(8)\n"
"buffer_load_dwordx4 v226, s[44:47], s58 offen lds\n"
"v_mfma_f32_16x16x32_bf16 v[68:71], a[176:179], v[128:131], v[68:71]\n"
"s_waitcnt vmcnt(17)\n"
"ds_read_b64_tr_b16 a[168:169], v220 offset:16968\n"
"v_mfma_f32_16x16x32_bf16 v[68:71], a[180:183], v[132:135], v[68:71]\n"
"s_add_u32 s60, s59, 3211264\n"
"ds_read_b128 v[160:163], v228\n"
"ds_read_b128 v[164:167], v229\n"
"v_mfma_f32_16x16x32_bf16 v[84:87], a[180:183], v[140:143], v[84:87]\n"
"s_add_u32 m0, s62, 34952\n"
"ds_read_b64_tr_b16 a[170:171], v220 offset:17992\n"
"v_mfma_f32_16x16x32_bf16 v[84:87], a[176:179], v[136:139], v[84:87]\n"
"buffer_load_dwordx4 v225, s[48:51], s60 offen lds\n"
"v_mfma_f32_16x16x32_bf16 v[100:103], a[176:179], v[144:147], v[100:103]\n"
"ds_read_b64_tr_b16 a[172:173], v220 offset:25192\n"
"v_mfma_f32_16x16x32_bf16 v[100:103], a[180:183], v[148:151], v[100:103]\n"
"v_mfma_f32_16x16x32_bf16 v[116:119], a[180:183], v[156:159], v[116:119]\n"
"ds_read_b64_tr_b16 a[174:175], v220 offset:26216\n"
"v_mfma_f32_16x16x32_bf16 v[116:119], a[176:179], v[152:155], v[116:119]\n"
"s_waitcnt lgkmcnt(10)\n"
"v_mfma_f32_16x16x32_bf16 v[28:31], a[184:187], v[152:155], v[28:31]\n"
"s_add_u32 m0, s61, 50176\n"
"ds_read_b64_tr_b16 a[176:177], v220 offset:17216\n"
"v_mfma_f32_16x16x32_bf16 v[28:31], a[188:191], v[156:159], v[28:31]\n"
"buffer_load_dwordx4 v227, s[44:47], s58 offen lds\n"
"v_mfma_f32_16x16x32_bf16 v[24:27], a[188:191], v[148:151], v[24:27]\n"
"ds_read_b64_tr_b16 a[178:179], v220 offset:18240\n"
"v_mfma_f32_16x16x32_bf16 v[24:27], a[184:187], v[144:147], v[24:27]\n"
"s_add_u32 s60, s59, 3244032\n"
"v_mfma_f32_16x16x32_bf16 v[20:23], a[184:187], v[136:139], v[20:23]\n"
"s_add_u32 m0, s62, 35976\n"
"ds_read_b64_tr_b16 a[180:181], v220 offset:25440\n"
"v_mfma_f32_16x16x32_bf16 v[20:23], a[188:191], v[140:143], v[20:23]\n"
"buffer_load_dwordx4 v225, s[48:51], s60 offen lds\n"
"v_mfma_f32_16x16x32_bf16 v[16:19], a[188:191], v[132:135], v[16:19]\n"
"ds_read_b64_tr_b16 a[182:183], v220 offset:26464\n"
"v_mfma_f32_16x16x32_bf16 v[16:19], a[184:187], v[128:131], v[16:19]\n"
"s_waitcnt lgkmcnt(10)\n"
"v_mfma_f32_16x16x32_bf16 v[72:75], a[160:163], v[128:131], v[72:75]\n"
"s_waitcnt vmcnt(17)\n"
"ds_read_b64_tr_b16 a[184:185], v220 offset:17224\n"
"v_mfma_f32_16x16x32_bf16 v[72:75], a[164:167], v[132:135], v[72:75]\n"
"s_add_u32 s58, s68, 768\n"
"ds_read_b128 v[168:171], v228 offset:2048\n"
"ds_read_b128 v[172:175], v229 offset:2048\n"
"v_mfma_f32_16x16x32_bf16 v[88:91], a[164:167], v[140:143], v[88:91]\n"
"s_add_u32 m0, s61, 51200\n"
"ds_read_b64_tr_b16 a[186:187], v220 offset:18248\n"
"v_mfma_f32_16x16x32_bf16 v[88:91], a[160:163], v[136:139], v[88:91]\n"
"buffer_load_dwordx4 v226, s[44:47], s58 offen lds\n"
"v_mfma_f32_16x16x32_bf16 v[104:107], a[160:163], v[144:147], v[104:107]\n"
"ds_read_b64_tr_b16 a[188:189], v220 offset:25448\n"
"v_mfma_f32_16x16x32_bf16 v[104:107], a[164:167], v[148:151], v[104:107]\n"
"v_mfma_f32_16x16x32_bf16 v[120:123], a[164:167], v[156:159], v[120:123]\n"
"s_add_u32 m0, s61, 52224\n"
"ds_read_b64_tr_b16 a[190:191], v220 offset:26472\n"
"v_mfma_f32_16x16x32_bf16 v[120:123], a[160:163], v[152:155], v[120:123]\n"
"s_waitcnt lgkmcnt(10)\n"
"buffer_load_dwordx4 v227, s[44:47], s58 offen lds\n"
"v_mfma_f32_16x16x32_bf16 v[44:47], a[168:171], v[152:155], v[44:47]\n"
"s_waitcnt vmcnt(15)\n"
"v_mfma_f32_16x16x32_bf16 v[44:47], a[172:175], v[156:159], v[44:47]\n"
"s_add_u32 m0, s61, 0\n"
"ds_read_b128 v[176:179], v228 offset:16384\n"
"ds_read_b128 v[180:183], v229 offset:16384\n"
"v_mfma_f32_16x16x32_bf16 v[40:43], a[172:175], v[148:151], v[40:43]\n"
"s_waitcnt lgkmcnt(15)\n"
"s_add_u32 s58, s84, 896\n"
"v_mfma_f32_16x16x32_bf16 v[40:43], a[168:171], v[144:147], v[40:43]\n"
"s_waitcnt vmcnt(14) lgkmcnt(2)\n"
"buffer_load_dwordx4 v226, s[44:47], s58 offen lds\n"
"v_mfma_f32_16x16x32_bf16 v[36:39], a[168:171], v[136:139], v[36:39]\n"
"s_barrier\n"
"v_mfma_f32_16x16x32_bf16 v[36:39], a[172:175], v[140:143], v[36:39]\n"
"ds_read_b64_tr_b16 a[160:161], v221 offset:33024\n"
"ds_read_b64_tr_b16 a[162:163], v221 offset:34048\n"
"v_mfma_f32_16x16x32_bf16 v[32:35], a[172:175], v[132:135], v[32:35]\n"
"ds_read_b64_tr_b16 a[164:165], v221 offset:41248\n"
"v_mfma_f32_16x16x32_bf16 v[32:35], a[168:171], v[128:131], v[32:35]\n"
"s_add_u32 m0, s61, 1024\n"
"v_mfma_f32_16x16x32_bf16 v[76:79], a[176:179], v[128:131], v[76:79]\n"
"s_waitcnt lgkmcnt(15)\n"
"ds_read_b64_tr_b16 a[166:167], v221 offset:42272\n"
"ds_read_b64_tr_b16 a[168:169], v221 offset:33032\n"
"v_mfma_f32_16x16x32_bf16 v[76:79], a[180:183], v[132:135], v[76:79]\n"
"buffer_load_dwordx4 v227, s[44:47], s58 offen lds\n"
"v_mfma_f32_16x16x32_bf16 v[92:95], a[180:183], v[140:143], v[92:95]\n"
"s_add_u32 s58, s85, 896\n"
"ds_read_b64_tr_b16 a[170:171], v221 offset:34056\n"
"v_mfma_f32_16x16x32_bf16 v[92:95], a[176:179], v[136:139], v[92:95]\n"
"s_add_u32 m0, s61, 2048\n"
"v_mfma_f32_16x16x32_bf16 v[108:111], a[176:179], v[144:147], v[108:111]\n"
"s_waitcnt lgkmcnt(11)\n"
"ds_read_b64_tr_b16 a[172:173], v221 offset:41256\n"
"v_mfma_f32_16x16x32_bf16 v[108:111], a[180:183], v[148:151], v[108:111]\n"
"buffer_load_dwordx4 v226, s[44:47], s58 offen lds\n"
"v_mfma_f32_16x16x32_bf16 v[124:127], a[180:183], v[156:159], v[124:127]\n"
"ds_read_b64_tr_b16 a[174:175], v221 offset:42280\n"
"v_mfma_f32_16x16x32_bf16 v[124:127], a[176:179], v[152:155], v[124:127]\n"
"v_mfma_f32_16x16x32_bf16 v[60:63], a[184:187], v[152:155], v[60:63]\n"
"s_waitcnt vmcnt(15)\n"
"ds_read_b64_tr_b16 a[176:177], v221 offset:33280\n"
"v_mfma_f32_16x16x32_bf16 v[60:63], a[188:191], v[156:159], v[60:63]\n"
"s_add_u32 s60, s59, 3670016\n"
"ds_read_b128 v[184:187], v228 offset:18432\n"
"ds_read_b128 v[188:191], v229 offset:18432\n"
"v_mfma_f32_16x16x32_bf16 v[56:59], a[188:191], v[148:151], v[56:59]\n"
"s_add_u32 m0, s62, 49344\n"
"ds_read_b64_tr_b16 a[178:179], v221 offset:34304\n"
"v_mfma_f32_16x16x32_bf16 v[56:59], a[184:187], v[144:147], v[56:59]\n"
"buffer_load_dwordx4 v225, s[48:51], s60 offen lds\n"
"v_mfma_f32_16x16x32_bf16 v[52:55], a[184:187], v[136:139], v[52:55]\n"
"ds_read_b64_tr_b16 a[180:181], v221 offset:41504\n"
"v_mfma_f32_16x16x32_bf16 v[52:55], a[188:191], v[140:143], v[52:55]\n"
"s_add_u32 m0, s61, 3072\n"
"v_mfma_f32_16x16x32_bf16 v[48:51], a[188:191], v[132:135], v[48:51]\n"
"s_waitcnt lgkmcnt(15)\n"
"ds_read_b64_tr_b16 a[182:183], v221 offset:42528\n"
"v_mfma_f32_16x16x32_bf16 v[48:51], a[184:187], v[128:131], v[48:51]\n"
"s_waitcnt lgkmcnt(3)\n"
"buffer_load_dwordx4 v227, s[44:47], s58 offen lds\n"
"v_mfma_f32_16x16x32_bf16 v[64:67], a[160:163], v[160:163], v[64:67]\n"
"s_waitcnt vmcnt(15)\n"
"ds_read_b64_tr_b16 a[184:185], v221 offset:33288\n"
"v_mfma_f32_16x16x32_bf16 v[64:67], a[164:167], v[164:167], v[64:67]\n"
"s_add_u32 s60, s59, 3702784\n"
"ds_read_b128 v[128:131], v228 offset:32768\n"
"ds_read_b128 v[132:135], v229 offset:32768\n"
"v_mfma_f32_16x16x32_bf16 v[80:83], a[164:167], v[172:175], v[80:83]\n"
"s_add_u32 m0, s62, 50368\n"
"ds_read_b64_tr_b16 a[186:187], v221 offset:34312\n"
"v_mfma_f32_16x16x32_bf16 v[80:83], a[160:163], v[168:171], v[80:83]\n"
"buffer_load_dwordx4 v225, s[48:51], s60 offen lds\n"
"v_mfma_f32_16x16x32_bf16 v[96:99], a[160:163], v[176:179], v[96:99]\n"
"ds_read_b64_tr_b16 a[188:189], v221 offset:41512\n"
"v_mfma_f32_16x16x32_bf16 v[96:99], a[164:167], v[180:183], v[96:99]\n"
"s_add_u32 s58, s87, 896\n"
"v_mfma_f32_16x16x32_bf16 v[112:115], a[164:167], v[188:191], v[112:115]\n"
"s_add_u32 m0, s61, 16384\n"
"ds_read_b64_tr_b16 a[190:191], v221 offset:42536\n"
"v_mfma_f32_16x16x32_bf16 v[112:115], a[160:163], v[184:187], v[112:115]\n"
"s_waitcnt lgkmcnt(12)\n"
"buffer_load_dwordx4 v226, s[44:47], s58 offen lds\n"
"v_mfma_f32_16x16x32_bf16 v[12:15], a[168:171], v[184:187], v[12:15]\n"
"ds_read_b64_tr_b16 a[160:161], v221 offset:33536\n"
"v_mfma_f32_16x16x32_bf16 v[12:15], a[172:175], v[188:191], v[12:15]\n"
"v_mfma_f32_16x16x32_bf16 v[8:11], a[172:175], v[180:183], v[8:11]\n"
"ds_read_b64_tr_b16 a[162:163], v221 offset:34560\n"
"v_mfma_f32_16x16x32_bf16 v[8:11], a[168:171], v[176:179], v[8:11]\n"
"s_add_u32 s60, s59, 3735552\n"
"v_mfma_f32_16x16x32_bf16 v[4:7], a[168:171], v[168:171], v[4:7]\n"
"s_add_u32 m0, s62, 51400\n"
"ds_read_b64_tr_b16 a[164:165], v221 offset:41760\n"
"v_mfma_f32_16x16x32_bf16 v[4:7], a[172:175], v[172:175], v[4:7]\n"
"buffer_load_dwordx4 v225, s[48:51], s60 offen lds\n"
"v_mfma_f32_16x16x32_bf16 v[0:3], a[172:175], v[164:167], v[0:3]\n"
"ds_read_b64_tr_b16 a[166:167], v221 offset:42784\n"
"v_mfma_f32_16x16x32_bf16 v[0:3], a[168:171], v[160:163], v[0:3]\n"
"s_waitcnt lgkmcnt(10)\n"
"v_mfma_f32_16x16x32_bf16 v[68:71], a[176:179], v[160:163], v[68:71]\n"
"s_waitcnt vmcnt(15)\n"
"ds_read_b64_tr_b16 a[168:169], v221 offset:33544\n"
"v_mfma_f32_16x16x32_bf16 v[68:71], a[180:183], v[164:167], v[68:71]\n"
"ds_read_b128 v[136:139], v228 offset:34816\n"
"ds_read_b128 v[140:143], v229 offset:34816\n"
"v_mfma_f32_16x16x32_bf16 v[84:87], a[180:183], v[172:175], v[84:87]\n"
"s_add_u32 m0, s61, 17408\n"
"ds_read_b64_tr_b16 a[170:171], v221 offset:34568\n"
"v_mfma_f32_16x16x32_bf16 v[84:87], a[176:179], v[168:171], v[84:87]\n"
"buffer_load_dwordx4 v227, s[44:47], s58 offen lds\n"
"v_mfma_f32_16x16x32_bf16 v[100:103], a[176:179], v[176:179], v[100:103]\n"
"s_add_u32 m0, s62, 52424\n"
"s_add_u32 s60, s59, 3768320\n"
"ds_read_b64_tr_b16 a[172:173], v221 offset:41768\n"
"v_mfma_f32_16x16x32_bf16 v[100:103], a[180:183], v[180:183], v[100:103]\n"
"buffer_load_dwordx4 v225, s[48:51], s60 offen lds\n"
"v_mfma_f32_16x16x32_bf16 v[116:119], a[180:183], v[188:191], v[116:119]\n"
"ds_read_b64_tr_b16 a[174:175], v221 offset:42792\n"
"v_mfma_f32_16x16x32_bf16 v[116:119], a[176:179], v[184:187], v[116:119]\n"
"s_waitcnt lgkmcnt(10)\n"
"v_mfma_f32_16x16x32_bf16 v[28:31], a[184:187], v[184:187], v[28:31]\n"
"ds_read_b64_tr_b16 a[176:177], v221 offset:33792\n"
"v_mfma_f32_16x16x32_bf16 v[28:31], a[188:191], v[188:191], v[28:31]\n"
"s_add_u32 s58, s68, 896\n"
"v_mfma_f32_16x16x32_bf16 v[24:27], a[188:191], v[180:183], v[24:27]\n"
"s_add_u32 m0, s61, 18432\n"
"ds_read_b64_tr_b16 a[178:179], v221 offset:34816\n"
"v_mfma_f32_16x16x32_bf16 v[24:27], a[184:187], v[176:179], v[24:27]\n"
"buffer_load_dwordx4 v226, s[44:47], s58 offen lds\n"
"v_mfma_f32_16x16x32_bf16 v[20:23], a[184:187], v[168:171], v[20:23]\n"
"ds_read_b64_tr_b16 a[180:181], v221 offset:42016\n"
"v_mfma_f32_16x16x32_bf16 v[20:23], a[188:191], v[172:175], v[20:23]\n"
"v_mfma_f32_16x16x32_bf16 v[16:19], a[188:191], v[164:167], v[16:19]\n"
"s_add_u32 m0, s61, 19456\n"
"ds_read_b64_tr_b16 a[182:183], v221 offset:43040\n"
"v_mfma_f32_16x16x32_bf16 v[16:19], a[184:187], v[160:163], v[16:19]\n"
"s_waitcnt lgkmcnt(10)\n"
"buffer_load_dwordx4 v227, s[44:47], s58 offen lds\n"
"v_mfma_f32_16x16x32_bf16 v[72:75], a[160:163], v[160:163], v[72:75]\n"
"s_waitcnt vmcnt(15)\n"
"ds_read_b64_tr_b16 a[184:185], v221 offset:33800\n"
"v_mfma_f32_16x16x32_bf16 v[72:75], a[164:167], v[164:167], v[72:75]\n"
"ds_read_b128 v[144:147], v228 offset:49152\n"
"ds_read_b128 v[148:151], v229 offset:49152\n"
"v_mfma_f32_16x16x32_bf16 v[88:91], a[164:167], v[172:175], v[88:91]\n"
"s_add_u32 s58, s84, 1024\n"
"ds_read_b64_tr_b16 a[186:187], v221 offset:34824\n"
"v_mfma_f32_16x16x32_bf16 v[88:91], a[160:163], v[168:171], v[88:91]\n"
"s_add_u32 m0, s61, 32768\n"
"v_mfma_f32_16x16x32_bf16 v[104:107], a[160:163], v[176:179], v[104:107]\n"
"s_waitcnt lgkmcnt(15)\n"
"ds_read_b64_tr_b16 a[188:189], v221 offset:42024\n"
"v_mfma_f32_16x16x32_bf16 v[104:107], a[164:167], v[180:183], v[104:107]\n"
"buffer_load_dwordx4 v226, s[44:47], s58 offen lds\n"
"v_mfma_f32_16x16x32_bf16 v[120:123], a[164:167], v[188:191], v[120:123]\n"
"s_add_u32 m0, s61, 33792\n"
"ds_read_b64_tr_b16 a[190:191], v221 offset:43048\n"
"v_mfma_f32_16x16x32_bf16 v[120:123], a[160:163], v[184:187], v[120:123]\n"
"s_waitcnt lgkmcnt(10)\n"
"v_mfma_f32_16x16x32_bf16 v[44:47], a[168:171], v[184:187], v[44:47]\n"
"s_waitcnt lgkmcnt(15)\n"
"v_mfma_f32_16x16x32_bf16 v[44:47], a[172:175], v[188:191], v[44:47]\n"
"buffer_load_dwordx4 v227, s[44:47], s58 offen lds\n"
"v_mfma_f32_16x16x32_bf16 v[40:43], a[172:175], v[180:183], v[40:43]\n"
"s_add_u32 s58, s85, 1024\n"
"v_mfma_f32_16x16x32_bf16 v[40:43], a[168:171], v[176:179], v[40:43]\n"
"s_waitcnt vmcnt(16) lgkmcnt(0)\n"
"v_mfma_f32_16x16x32_bf16 v[36:39], a[168:171], v[168:171], v[36:39]\n"
"s_barrier\n"
"v_mfma_f32_16x16x32_bf16 v[36:39], a[172:175], v[172:175], v[36:39]\n"
"s_add_u32 m0, s61, 34816\n"
"ds_read_b64_tr_b16 a[160:161], v220 offset:32896\n"
"ds_read_b64_tr_b16 a[162:163], v220 offset:33920\n"
"v_mfma_f32_16x16x32_bf16 v[32:35], a[172:175], v[164:167], v[32:35]\n"
"s_waitcnt lgkmcnt(15)\n"
"ds_read_b64_tr_b16 a[164:165], v220 offset:41120\n"
"v_mfma_f32_16x16x32_bf16 v[32:35], a[168:171], v[160:163], v[32:35]\n"
"buffer_load_dwordx4 v226, s[44:47], s58 offen lds\n"
"v_mfma_f32_16x16x32_bf16 v[76:79], a[176:179], v[160:163], v[76:79]\n"
"s_waitcnt vmcnt(15)\n"
"ds_read_b64_tr_b16 a[166:167], v220 offset:42144\n"
"ds_read_b64_tr_b16 a[168:169], v220 offset:32904\n"
"v_mfma_f32_16x16x32_bf16 v[76:79], a[180:183], v[164:167], v[76:79]\n"
"s_add_u32 s60, s59, 4194304\n"
"ds_read_b128 v[152:155], v228 offset:51200\n"
"ds_read_b128 v[156:159], v229 offset:51200\n"
"v_mfma_f32_16x16x32_bf16 v[92:95], a[180:183], v[172:175], v[92:95]\n"
"s_add_u32 m0, s62, 65792\n"
"ds_read_b64_tr_b16 a[170:171], v220 offset:33928\n"
"v_mfma_f32_16x16x32_bf16 v[92:95], a[176:179], v[168:171], v[92:95]\n"
"buffer_load_dwordx4 v225, s[48:51], s60 offen lds\n"
"v_mfma_f32_16x16x32_bf16 v[108:111], a[176:179], v[176:179], v[108:111]\n"
"ds_read_b64_tr_b16 a[172:173], v220 offset:41128\n"
"v_mfma_f32_16x16x32_bf16 v[108:111], a[180:183], v[180:183], v[108:111]\n"
"s_add_u32 m0, s61, 35840\n"
"v_mfma_f32_16x16x32_bf16 v[124:127], a[180:183], v[188:191], v[124:127]\n"
"s_waitcnt lgkmcnt(15)\n"
"ds_read_b64_tr_b16 a[174:175], v220 offset:42152\n"
"v_mfma_f32_16x16x32_bf16 v[124:127], a[176:179], v[184:187], v[124:127]\n"
"buffer_load_dwordx4 v227, s[44:47], s58 offen lds\n"
"v_mfma_f32_16x16x32_bf16 v[60:63], a[184:187], v[184:187], v[60:63]\n"
"ds_read_b64_tr_b16 a[176:177], v220 offset:33152\n"
"v_mfma_f32_16x16x32_bf16 v[60:63], a[188:191], v[188:191], v[60:63]\n"
"s_add_u32 s60, s59, 4227072\n"
"v_mfma_f32_16x16x32_bf16 v[56:59], a[188:191], v[180:183], v[56:59]\n"
"s_add_u32 m0, s62, 66816\n"
"ds_read_b64_tr_b16 a[178:179], v220 offset:34176\n"
"v_mfma_f32_16x16x32_bf16 v[56:59], a[184:187], v[176:179], v[56:59]\n"
"buffer_load_dwordx4 v225, s[48:51], s60 offen lds\n"
"v_mfma_f32_16x16x32_bf16 v[52:55], a[184:187], v[168:171], v[52:55]\n"
"ds_read_b64_tr_b16 a[180:181], v220 offset:41376\n"
"v_mfma_f32_16x16x32_bf16 v[52:55], a[188:191], v[172:175], v[52:55]\n"
"v_mfma_f32_16x16x32_bf16 v[48:51], a[188:191], v[164:167], v[48:51]\n"
"ds_read_b64_tr_b16 a[182:183], v220 offset:42400\n"
"v_mfma_f32_16x16x32_bf16 v[48:51], a[184:187], v[160:163], v[48:51]\n"
"s_waitcnt lgkmcnt(7)\n"
"v_mfma_f32_16x16x32_bf16 v[64:67], a[160:163], v[128:131], v[64:67]\n"
"s_waitcnt vmcnt(16)\n"
"ds_read_b64_tr_b16 a[184:185], v220 offset:33160\n"
"v_mfma_f32_16x16x32_bf16 v[64:67], a[164:167], v[132:135], v[64:67]\n"
"s_add_u32 s58, s87, 1024\n"
"ds_read_b128 v[160:163], v228\n"
"ds_read_b128 v[164:167], v229\n"
"v_mfma_f32_16x16x32_bf16 v[80:83], a[164:167], v[140:143], v[80:83]\n"
"s_add_u32 m0, s61, 49152\n"
"ds_read_b64_tr_b16 a[186:187], v220 offset:34184\n"
"v_mfma_f32_16x16x32_bf16 v[80:83], a[160:163], v[136:139], v[80:83]\n"
"buffer_load_dwordx4 v226, s[44:47], s58 offen lds\n"
"v_mfma_f32_16x16x32_bf16 v[96:99], a[160:163], v[144:147], v[96:99]\n"
"s_add_u32 m0, s62, 67848\n"
"s_add_u32 s60, s59, 4259840\n"
"ds_read_b64_tr_b16 a[188:189], v220 offset:41384\n"
"v_mfma_f32_16x16x32_bf16 v[96:99], a[164:167], v[148:151], v[96:99]\n"
"buffer_load_dwordx4 v225, s[48:51], s60 offen lds\n"
"v_mfma_f32_16x16x32_bf16 v[112:115], a[164:167], v[156:159], v[112:115]\n"
"ds_read_b64_tr_b16 a[190:191], v220 offset:42408\n"
"v_mfma_f32_16x16x32_bf16 v[112:115], a[160:163], v[152:155], v[112:115]\n"
"s_waitcnt lgkmcnt(10)\n"
"v_mfma_f32_16x16x32_bf16 v[12:15], a[168:171], v[152:155], v[12:15]\n"
"ds_read_b64_tr_b16 a[160:161], v220 offset:33408\n"
"v_mfma_f32_16x16x32_bf16 v[12:15], a[172:175], v[156:159], v[12:15]\n"
"v_mfma_f32_16x16x32_bf16 v[8:11], a[172:175], v[148:151], v[8:11]\n"
"s_add_u32 m0, s61, 50176\n"
"ds_read_b64_tr_b16 a[162:163], v220 offset:34432\n"
"v_mfma_f32_16x16x32_bf16 v[8:11], a[168:171], v[144:147], v[8:11]\n"
"buffer_load_dwordx4 v227, s[44:47], s58 offen lds\n"
"v_mfma_f32_16x16x32_bf16 v[4:7], a[168:171], v[136:139], v[4:7]\n"
"ds_read_b64_tr_b16 a[164:165], v220 offset:41632\n"
"v_mfma_f32_16x16x32_bf16 v[4:7], a[172:175], v[140:143], v[4:7]\n"
"s_add_u32 s60, s59, 4292608\n"
"v_mfma_f32_16x16x32_bf16 v[0:3], a[172:175], v[132:135], v[0:3]\n"
"s_add_u32 m0, s62, 68872\n"
"ds_read_b64_tr_b16 a[166:167], v220 offset:42656\n"
"v_mfma_f32_16x16x32_bf16 v[0:3], a[168:171], v[128:131], v[0:3]\n"
"s_waitcnt lgkmcnt(10)\n"
"buffer_load_dwordx4 v225, s[48:51], s60 offen lds\n"
"v_mfma_f32_16x16x32_bf16 v[68:71], a[176:179], v[128:131], v[68:71]\n"
"s_waitcnt vmcnt(17)\n"
"ds_read_b64_tr_b16 a[168:169], v220 offset:33416\n"
"v_mfma_f32_16x16x32_bf16 v[68:71], a[180:183], v[132:135], v[68:71]\n"
"s_add_u32 s58, s68, 1024\n"
"ds_read_b128 v[168:171], v228 offset:2048\n"
"ds_read_b128 v[172:175], v229 offset:2048\n"
"v_mfma_f32_16x16x32_bf16 v[84:87], a[180:183], v[140:143], v[84:87]\n"
"s_add_u32 m0, s61, 51200\n"
"ds_read_b64_tr_b16 a[170:171], v220 offset:34440\n"
"v_mfma_f32_16x16x32_bf16 v[84:87], a[176:179], v[136:139], v[84:87]\n"
"buffer_load_dwordx4 v226, s[44:47], s58 offen lds\n"
"v_mfma_f32_16x16x32_bf16 v[100:103], a[176:179], v[144:147], v[100:103]\n"
"ds_read_b64_tr_b16 a[172:173], v220 offset:41640\n"
"v_mfma_f32_16x16x32_bf16 v[100:103], a[180:183], v[148:151], v[100:103]\n"
"v_mfma_f32_16x16x32_bf16 v[116:119], a[180:183], v[156:159], v[116:119]\n"
"ds_read_b64_tr_b16 a[174:175], v220 offset:42664\n"
"v_mfma_f32_16x16x32_bf16 v[116:119], a[176:179], v[152:155], v[116:119]\n"
"s_waitcnt lgkmcnt(10)\n"
"v_mfma_f32_16x16x32_bf16 v[28:31], a[184:187], v[152:155], v[28:31]\n"
"s_add_u32 m0, s61, 52224\n"
"ds_read_b64_tr_b16 a[176:177], v220 offset:33664\n"
"v_mfma_f32_16x16x32_bf16 v[28:31], a[188:191], v[156:159], v[28:31]\n"
"buffer_load_dwordx4 v227, s[44:47], s58 offen lds\n"
"v_mfma_f32_16x16x32_bf16 v[24:27], a[188:191], v[148:151], v[24:27]\n"
"ds_read_b64_tr_b16 a[178:179], v220 offset:34688\n"
"v_mfma_f32_16x16x32_bf16 v[24:27], a[184:187], v[144:147], v[24:27]\n"
"v_mfma_f32_16x16x32_bf16 v[20:23], a[184:187], v[136:139], v[20:23]\n"
"ds_read_b64_tr_b16 a[180:181], v220 offset:41888\n"
"v_mfma_f32_16x16x32_bf16 v[20:23], a[188:191], v[140:143], v[20:23]\n"
"v_mfma_f32_16x16x32_bf16 v[16:19], a[188:191], v[132:135], v[16:19]\n"
"s_add_u32 m0, s62, 0\n"
"ds_read_b64_tr_b16 a[182:183], v220 offset:42912\n"
"v_mfma_f32_16x16x32_bf16 v[16:19], a[184:187], v[128:131], v[16:19]\n"
"s_waitcnt lgkmcnt(10)\n"
"buffer_load_dwordx4 v224, s[40:43], s57 offen nt lds\n"
"v_mfma_f32_16x16x32_bf16 v[72:75], a[160:163], v[128:131], v[72:75]\n"
"s_waitcnt vmcnt(16)\n"
"ds_read_b64_tr_b16 a[184:185], v220 offset:33672\n"
"v_mfma_f32_16x16x32_bf16 v[72:75], a[164:167], v[132:135], v[72:75]\n"
"ds_read_b128 v[176:179], v228 offset:16384\n"
"ds_read_b128 v[180:183], v229 offset:16384\n"
"v_mfma_f32_16x16x32_bf16 v[88:91], a[164:167], v[140:143], v[88:91]\n"
"ds_read_b64_tr_b16 a[186:187], v220 offset:34696\n"
"v_mfma_f32_16x16x32_bf16 v[88:91], a[160:163], v[136:139], v[88:91]\n"
"buffer_load_dwordx4 v224, s[40:43], s57 offen offset:1024 nt lds\n"
"v_mfma_f32_16x16x32_bf16 v[104:107], a[160:163], v[144:147], v[104:107]\n"
"ds_read_b64_tr_b16 a[188:189], v220 offset:41896\n"
"v_mfma_f32_16x16x32_bf16 v[104:107], a[164:167], v[148:151], v[104:107]\n"
"v_mfma_f32_16x16x32_bf16 v[120:123], a[164:167], v[156:159], v[120:123]\n"
"s_add_u32 m0, s62, 8\n"
"ds_read_b64_tr_b16 a[190:191], v220 offset:42920\n"
"v_mfma_f32_16x16x32_bf16 v[120:123], a[160:163], v[152:155], v[120:123]\n"
"s_waitcnt lgkmcnt(10)\n"
"buffer_load_dwordx4 v224, s[40:43], s57 offen offset:2048 nt lds\n"
"v_mfma_f32_16x16x32_bf16 v[44:47], a[168:171], v[152:155], v[44:47]\n"
"v_mfma_f32_16x16x32_bf16 v[44:47], a[172:175], v[156:159], v[44:47]\n"
"v_mfma_f32_16x16x32_bf16 v[40:43], a[172:175], v[148:151], v[40:43]\n"
"v_mfma_f32_16x16x32_bf16 v[40:43], a[168:171], v[144:147], v[40:43]\n"
"s_waitcnt vmcnt(17) lgkmcnt(0)\n"
"buffer_load_dwordx4 v224, s[40:43], s57 offen offset:3072 nt lds\n"
"v_mfma_f32_16x16x32_bf16 v[36:39], a[168:171], v[136:139], v[36:39]\n"
"s_barrier\n"
"v_mfma_f32_16x16x32_bf16 v[36:39], a[172:175], v[140:143], v[36:39]\n"
"s_sub_u32 s57, s57, 159744\n"
"ds_read_b64_tr_b16 a[160:161], v220 offset:49344\n"
"ds_read_b64_tr_b16 a[162:163], v220 offset:50368\n"
"v_mfma_f32_16x16x32_bf16 v[32:35], a[172:175], v[132:135], v[32:35]\n"
"s_and_b32 s57, s57, 159744\n"
"s_add_u32 m0, s61, 0\n"
"ds_read_b64_tr_b16 a[164:165], v220 offset:57568\n"
"v_mfma_f32_16x16x32_bf16 v[32:35], a[168:171], v[128:131], v[32:35]\n"
"buffer_load_dwordx4 v222, s[36:39], s56 offen nt lds\n"
"v_mfma_f32_16x16x32_bf16 v[76:79], a[176:179], v[128:131], v[76:79]\n"
"s_waitcnt vmcnt(17)\n"
"ds_read_b64_tr_b16 a[166:167], v220 offset:58592\n"
"ds_read_b64_tr_b16 a[168:169], v220 offset:49352\n"
"v_mfma_f32_16x16x32_bf16 v[76:79], a[180:183], v[132:135], v[76:79]\n"
"ds_read_b128 v[184:187], v228 offset:18432\n"
"ds_read_b128 v[188:191], v229 offset:18432\n"
"v_mfma_f32_16x16x32_bf16 v[92:95], a[180:183], v[140:143], v[92:95]\n"
"s_mov_b32 s59, 256\n"
"ds_read_b64_tr_b16 a[170:171], v220 offset:50376\n"
"v_mfma_f32_16x16x32_bf16 v[92:95], a[176:179], v[136:139], v[92:95]\n"
"v_mfma_f32_16x16x32_bf16 v[108:111], a[176:179], v[144:147], v[108:111]\n"
"ds_read_b64_tr_b16 a[172:173], v220 offset:57576\n"
"v_mfma_f32_16x16x32_bf16 v[108:111], a[180:183], v[148:151], v[108:111]\n"
"v_mfma_f32_16x16x32_bf16 v[124:127], a[180:183], v[156:159], v[124:127]\n"
"ds_read_b64_tr_b16 a[174:175], v220 offset:58600\n"
"v_mfma_f32_16x16x32_bf16 v[124:127], a[176:179], v[152:155], v[124:127]\n"
"v_mfma_f32_16x16x32_bf16 v[60:63], a[184:187], v[152:155], v[60:63]\n"
"ds_read_b64_tr_b16 a[176:177], v220 offset:49600\n"
"v_mfma_f32_16x16x32_bf16 v[60:63], a[188:191], v[156:159], v[60:63]\n"
"v_mfma_f32_16x16x32_bf16 v[56:59], a[188:191], v[148:151], v[56:59]\n"
"ds_read_b64_tr_b16 a[178:179], v220 offset:50624\n"
"v_mfma_f32_16x16x32_bf16 v[56:59], a[184:187], v[144:147], v[56:59]\n"
"buffer_load_dwordx4 v222, s[36:39], s56 offen offset:1024 nt lds\n"
"v_mfma_f32_16x16x32_bf16 v[52:55], a[184:187], v[136:139], v[52:55]\n"
"ds_read_b64_tr_b16 a[180:181], v220 offset:57824\n"
"v_mfma_f32_16x16x32_bf16 v[52:55], a[188:191], v[140:143], v[52:55]\n"
"v_mfma_f32_16x16x32_bf16 v[48:51], a[188:191], v[132:135], v[48:51]\n"
"ds_read_b64_tr_b16 a[182:183], v220 offset:58848\n"
"v_mfma_f32_16x16x32_bf16 v[48:51], a[184:187], v[128:131], v[48:51]\n"
"s_waitcnt lgkmcnt(7)\n"
"s_branch L_q7%=\n"
"L_head%=:\n"
"v_mfma_f32_16x16x32_bf16 v[64:67], a[160:163], v[128:131], 0\n"
"s_waitcnt vmcnt(36)\n"
"ds_read_b64_tr_b16 a[184:185], v221 offset:33288\n"
"v_accvgpr_write_b32 a228, 0\n"
"v_mfma_f32_16x16x32_bf16 v[64:67], a[164:167], v[132:135], v[64:67]\n"
"s_add_u32 s60, s59, 1572864\n"
"ds_read_b128 v[160:163], v228\n"
"ds_read_b128 v[164:167], v229\n"
"v_mfma_f32_16x16x32_bf16 v[80:83], a[164:167], v[140:143], 0\n"
"s_add_u32 m0, s62, 0\n"
"ds_read_b64_tr_b16 a[186:187], v221 offset:34312\n"
"v_accvgpr_write_b32 a229, 0\n"
"v_mfma_f32_16x16x32_bf16 v[80:83], a[160:163], v[136:139], v[80:83]\n"
"buffer_load_dwordx4 v225, s[48:51], s60 offen lds\n"
"v_mfma_f32_16x16x32_bf16 v[96:99], a[160:163], v[144:147], 0\n"
"ds_read_b64_tr_b16 a[188:189], v221 offset:41512\n"
"v_accvgpr_write_b32 a230, 0\n"
"v_mfma_f32_16x16x32_bf16 v[96:99], a[164:167], v[148:151], v[96:99]\n"
"s_lshl2_add_u32 s72, s23, s69\n"
"s_lshl_b32 s72, s72, 11\n"
"s_add_u32 s72, s72, 8192\n"
"v_accvgpr_write_b32 a231, 0\n"
"v_accvgpr_read_b32 v236, a0\n"
"v_mfma_f32_16x16x32_bf16 v[112:115], a[164:167], v[156:159], 0\n"
"ds_read_b64_tr_b16 a[190:191], v221 offset:42536\n"
"v_accvgpr_read_b32 v237, a1\n"
"v_mfma_f32_16x16x32_bf16 v[112:115], a[160:163], v[152:155], v[112:115]\n"
"s_waitcnt lgkmcnt(10)\n"
"buffer_store_dwordx4 a[228:231], v232, s[96:99], s72 offen sc1\n"
"v_mfma_f32_16x16x32_bf16 v[12:15], a[168:171], v[152:155], 0\n"
"s_add_u32 s58, s84, 384\n"
"ds_read_b64_tr_b16 a[160:161], v221 offset:33536\n"
"v_accvgpr_read_b32 v238, a2\n"
"v_mfma_f32_16x16x32_bf16 v[12:15], a[172:175], v[156:159], v[12:15]\n"
"s_add_u32 m0, s61, 0\n"
"v_accvgpr_read_b32 v239, a3\n"
"v_accvgpr_read_b32 v240, a16\n"
"v_mfma_f32_16x16x32_bf16 v[8:11], a[172:175], v[148:151], 0\n"
"s_waitcnt lgkmcnt(4)\n"
"ds_read_b64_tr_b16 a[162:163], v221 offset:34560\n"
"v_accvgpr_read_b32 v241, a17\n"
"v_mfma_f32_16x16x32_bf16 v[8:11], a[168:171], v[144:147], v[8:11]\n"
"buffer_load_dwordx4 v226, s[44:47], s58 offen lds\n"
"v_mfma_f32_16x16x32_bf16 v[4:7], a[168:171], v[136:139], 0\n"
"ds_read_b64_tr_b16 a[164:165], v221 offset:41760\n"
"v_accvgpr_read_b32 v242, a18\n"
"v_mfma_f32_16x16x32_bf16 v[4:7], a[172:175], v[140:143], v[4:7]\n"
"v_accvgpr_read_b32 v243, a19\n"
"v_mul_f32 v236, v212, v236\n"
"v_mfma_f32_16x16x32_bf16 v[0:3], a[172:175], v[132:135], 0\n"
"s_add_u32 m0, s61, 1024\n"
"ds_read_b64_tr_b16 a[166:167], v221 offset:42784\n"
"v_mul_f32 v237, v212, v237\n"
"v_mfma_f32_16x16x32_bf16 v[0:3], a[168:171], v[128:131], v[0:3]\n"
"s_waitcnt lgkmcnt(10)\n"
"buffer_load_dwordx4 v227, s[44:47], s58 offen lds\n"
"v_mfma_f32_16x16x32_bf16 v[68:71], a[176:179], v[128:131], 0\n"
"s_waitcnt vmcnt(38)\n"
"ds_read_b64_tr_b16 a[168:169], v221 offset:33544\n"
"v_mul_f32 v238, v212, v238\n"
"v_mfma_f32_16x16x32_bf16 v[68:71], a[180:183], v[132:135], v[68:71]\n"
"s_add_u32 s60, s59, 1605632\n"
"ds_read_b128 v[168:171], v228 offset:2048\n"
"ds_read_b128 v[172:175], v229 offset:2048\n"
"v_mfma_f32_16x16x32_bf16 v[84:87], a[180:183], v[140:143], 0\n"
"s_add_u32 m0, s62, 1024\n"
"ds_read_b64_tr_b16 a[170:171], v221 offset:34568\n"
"v_mul_f32 v239, v212, v239\n"
"v_mfma_f32_16x16x32_bf16 v[84:87], a[176:179], v[136:139], v[84:87]\n"
"buffer_load_dwordx4 v225, s[48:51], s60 offen lds\n"
"v_mfma_f32_16x16x32_bf16 v[100:103], a[176:179], v[144:147], 0\n"
"ds_read_b64_tr_b16 a[172:173], v221 offset:41768\n"
"v_mul_f32 v240, v212, v240\n"
"v_mfma_f32_16x16x32_bf16 v[100:103], a[180:183], v[148:151], v[100:103]\n"
"v_mul_f32 v241, v212, v241\n"
"v_mul_f32 v242, v212, v242\n"
"v_mfma_f32_16x16x32_bf16 v[116:119], a[180:183], v[156:159], 0\n"
"ds_read_b64_tr_b16 a[174:175], v221 offset:42792\n"
"v_mul_f32 v243, v212, v243\n"
"v_mfma_f32_16x16x32_bf16 v[116:119], a[176:179], v[152:155], v[116:119]\n"
"s_waitcnt lgkmcnt(10)\n"
"buffer_store_dwordx4 a[228:231], v232, s[96:99], s72 offen offset:1024 sc1\n"
"v_mfma_f32_16x16x32_bf16 v[28:31], a[184:187], v[152:155], 0\n"
"s_add_u32 s58, s85, 384\n"
"ds_read_b64_tr_b16 a[176:177], v221 offset:33792\n"
"v_cvt_pk_bf16_f32 v236, v236, v237\n"
"v_mfma_f32_16x16x32_bf16 v[28:31], a[188:191], v[156:159], v[28:31]\n"
"s_add_u32 m0, s61, 2048\n"
"v_cvt_pk_bf16_f32 v237, v238, v239\n"
"v_cvt_pk_bf16_f32 v238, v240, v241\n"
"v_mfma_f32_16x16x32_bf16 v[24:27], a[188:191], v[148:151], 0\n"
"s_waitcnt lgkmcnt(4)\n"
"ds_read_b64_tr_b16 a[178:179], v221 offset:34816\n"
"v_cvt_pk_bf16_f32 v239, v242, v243\n"
"v_mfma_f32_16x16x32_bf16 v[24:27], a[184:187], v[144:147], v[24:27]\n"
"buffer_load_dwordx4 v226, s[44:47], s58 offen lds\n"
"v_mfma_f32_16x16x32_bf16 v[20:23], a[184:187], v[136:139], 0\n"
"ds_read_b64_tr_b16 a[180:181], v221 offset:42016\n"
"v_accvgpr_read_b32 v244, a32\n"
"v_mfma_f32_16x16x32_bf16 v[20:23], a[188:191], v[140:143], v[20:23]\n"
"v_accvgpr_read_b32 v245, a33\n"
"v_accvgpr_read_b32 v246, a34\n"
"v_mfma_f32_16x16x32_bf16 v[16:19], a[188:191], v[132:135], 0\n"
"s_add_u32 m0, s61, 3072\n"
"ds_read_b64_tr_b16 a[182:183], v221 offset:43040\n"
"v_accvgpr_read_b32 v247, a35\n"
"v_mfma_f32_16x16x32_bf16 v[16:19], a[184:187], v[128:131], v[16:19]\n"
"s_waitcnt lgkmcnt(10)\n"
"buffer_load_dwordx4 v227, s[44:47], s58 offen lds\n"
"v_mfma_f32_16x16x32_bf16 v[72:75], a[160:163], v[128:131], 0\n"
"s_waitcnt vmcnt(40)\n"
"ds_read_b64_tr_b16 a[184:185], v221 offset:33800\n"
"v_accvgpr_read_b32 v248, a48\n"
"v_mfma_f32_16x16x32_bf16 v[72:75], a[164:167], v[132:135], v[72:75]\n"
"s_add_u32 s60, s59, 1638400\n"
"ds_read_b128 v[176:179], v228 offset:16384\n"
"ds_read_b128 v[180:183], v229 offset:16384\n"
"v_mfma_f32_16x16x32_bf16 v[88:91], a[164:167], v[140:143], 0\n"
"s_add_u32 m0, s62, 2056\n"
"ds_read_b64_tr_b16 a[186:187], v221 offset:34824\n"
"v_accvgpr_read_b32 v249, a49\n"
"v_mfma_f32_16x16x32_bf16 v[88:91], a[160:163], v[136:139], v[88:91]\n"
"buffer_load_dwordx4 v225, s[48:51], s60 offen lds\n"
"v_mfma_f32_16x16x32_bf16 v[104:107], a[160:163], v[144:147], 0\n"
"ds_read_b64_tr_b16 a[188:189], v221 offset:42024\n"
"v_accvgpr_read_b32 v250, a50\n"
"v_mfma_f32_16x16x32_bf16 v[104:107], a[164:167], v[148:151], v[104:107]\n"
"v_accvgpr_read_b32 v251, a51\n"
"v_mul_f32 v244, v212, v244\n"
"v_mfma_f32_16x16x32_bf16 v[120:123], a[164:167], v[156:159], 0\n"
"s_add_u32 s66, s65, 0\n"
"ds_read_b64_tr_b16 a[190:191], v221 offset:43048\n"
"v_mul_f32 v245, v212, v245\n"
"v_mfma_f32_16x16x32_bf16 v[120:123], a[160:163], v[152:155], v[120:123]\n"
"s_waitcnt lgkmcnt(10)\n"
"buffer_store_dwordx4 v[236:239], v232, s[52:55], s66 offen sc1\n"
"v_mfma_f32_16x16x32_bf16 v[44:47], a[168:171], v[152:155], 0\n"
"s_add_u32 s58, s87, 384\n"
"v_mul_f32 v246, v212, v246\n"
"v_mul_f32 v247, v212, v247\n"
"v_mfma_f32_16x16x32_bf16 v[44:47], a[172:175], v[156:159], v[44:47]\n"
"s_add_u32 m0, s61, 16384\n"
"v_mul_f32 v248, v212, v248\n"
"v_mul_f32 v249, v212, v249\n"
"v_mfma_f32_16x16x32_bf16 v[40:43], a[172:175], v[148:151], 0\n"
"s_waitcnt lgkmcnt(3)\n"
"v_mul_f32 v250, v212, v250\n"
"v_mul_f32 v251, v212, v251\n"
"v_mfma_f32_16x16x32_bf16 v[40:43], a[168:171], v[144:147], v[40:43]\n"
"s_waitcnt vmcnt(18) lgkmcnt(0)\n"
"buffer_load_dwordx4 v226, s[44:47], s58 offen lds\n"
"v_mfma_f32_16x16x32_bf16 v[36:39], a[168:171], v[136:139], 0\n"
"s_barrier\n"
"v_mfma_f32_16x16x32_bf16 v[36:39], a[172:175], v[140:143], v[36:39]\n"
"ds_read_b64_tr_b16 a[160:161], v220 offset:32896\n"
"ds_read_b64_tr_b16 a[162:163], v220 offset:33920\n"
"v_mfma_f32_16x16x32_bf16 v[32:35], a[172:175], v[132:135], 0\n"
"s_add_u32 m0, s61, 17408\n"
"ds_read_b64_tr_b16 a[164:165], v220 offset:41120\n"
"v_cvt_pk_bf16_f32 v244, v244, v245\n"
"v_mfma_f32_16x16x32_bf16 v[32:35], a[168:171], v[128:131], v[32:35]\n"
"buffer_load_dwordx4 v227, s[44:47], s58 offen lds\n"
"v_mfma_f32_16x16x32_bf16 v[76:79], a[176:179], v[128:131], 0\n"
"s_waitcnt vmcnt(42)\n"
"ds_read_b64_tr_b16 a[166:167], v220 offset:42144\n"
"ds_read_b64_tr_b16 a[168:169], v220 offset:32904\n"
"v_mfma_f32_16x16x32_bf16 v[76:79], a[180:183], v[132:135], v[76:79]\n"
"s_add_u32 s60, s59, 1671168\n"
"ds_read_b128 v[184:187], v228 offset:18432\n"
"ds_read_b128 v[188:191], v229 offset:18432\n"
"v_mfma_f32_16x16x32_bf16 v[92:95], a[180:183], v[140:143], 0\n"
"s_add_u32 m0, s62, 3080\n"
"ds_read_b64_tr_b16 a[170:171], v220 offset:33928\n"
"v_cvt_pk_bf16_f32 v245, v246, v247\n"
"v_mfma_f32_16x16x32_bf16 v[92:95], a[176:179], v[136:139], v[92:95]\n"
"buffer_load_dwordx4 v225, s[48:51], s60 offen lds\n"
"v_mfma_f32_16x16x32_bf16 v[108:111], a[176:179], v[144:147], 0\n"
"ds_read_b64_tr_b16 a[172:173], v220 offset:41128\n"
"v_cvt_pk_bf16_f32 v246, v248, v249\n"
"v_mfma_f32_16x16x32_bf16 v[108:111], a[180:183], v[148:151], v[108:111]\n"
"v_cvt_pk_bf16_f32 v247, v250, v251\n"
"v_accvgpr_read_b32 v236, a64\n"
"v_mfma_f32_16x16x32_bf16 v[124:127], a[180:183], v[156:159], 0\n"
"ds_read_b64_tr_b16 a[174:175], v220 offset:42152\n"
"v_accvgpr_read_b32 v237, a65\n"
"v_mfma_f32_16x16x32_bf16 v[124:127], a[176:179], v[152:155], v[124:127]\n"
"buffer_store_dwordx4 v[244:247], v232, s[52:55], s66 offen offset:1024 sc1\n"
"v_mfma_f32_16x16x32_bf16 v[60:63], a[184:187], v[152:155], 0\n"
"s_add_u32 s58, s68, 384\n"
"ds_read_b64_tr_b16 a[176:177], v220 offset:33152\n"
"v_accvgpr_read_b32 v238, a66\n"
"v_mfma_f32_16x16x32_bf16 v[60:63], a[188:191], v[156:159], v[60:63]\n"
"s_add_u32 m0, s61, 18432\n"
"v_accvgpr_read_b32 v239, a67\n"
"v_accvgpr_read_b32 v240, a80\n"
"v_mfma_f32_16x16x32_bf16 v[56:59], a[188:191], v[148:151], 0\n"
"s_waitcnt lgkmcnt(4)\n"
"ds_read_b64_tr_b16 a[178:179], v220 offset:34176\n"
"v_accvgpr_read_b32 v241, a81\n"
"v_mfma_f32_16x16x32_bf16 v[56:59], a[184:187], v[144:147], v[56:59]\n"
"buffer_load_dwordx4 v226, s[44:47], s58 offen lds\n"
"v_mfma_f32_16x16x32_bf16 v[52:55], a[184:187], v[136:139], 0\n"
"ds_read_b64_tr_b16 a[180:181], v220 offset:41376\n"
"v_accvgpr_read_b32 v242, a82\n"
"v_mfma_f32_16x16x32_bf16 v[52:55], a[188:191], v[140:143], v[52:55]\n"
"v_accvgpr_read_b32 v243, a83\n"
"v_mul_f32 v236, v212, v236\n"
"v_mfma_f32_16x16x32_bf16 v[48:51], a[188:191], v[132:135], 0\n"
"s_add_u32 m0, s61, 19456\n"
"ds_read_b64_tr_b16 a[182:183], v220 offset:42400\n"
"v_mul_f32 v237, v212, v237\n"
"v_mfma_f32_16x16x32_bf16 v[48:51], a[184:187], v[128:131], v[48:51]\n"
"s_waitcnt lgkmcnt(7)\n"
"buffer_load_dwordx4 v227, s[44:47], s58 offen lds\n"
"v_mfma_f32_16x16x32_bf16 v[64:67], a[160:163], v[160:163], v[64:67]\n"
"s_waitcnt vmcnt(31)\n"
"ds_read_b64_tr_b16 a[184:185], v220 offset:33160\n"
"v_mul_f32 v238, v212, v238\n"
"v_mfma_f32_16x16x32_bf16 v[64:67], a[164:167], v[164:167], v[64:67]\n"
"s_add_u32 s60, s59, 2097152\n"
"ds_read_b128 v[128:131], v228 offset:32768\n"
"ds_read_b128 v[132:135], v229 offset:32768\n"
"v_mfma_f32_16x16x32_bf16 v[80:83], a[164:167], v[172:175], v[80:83]\n"
"s_add_u32 m0, s62, 16448\n"
"ds_read_b64_tr_b16 a[186:187], v220 offset:34184\n"
"v_mul_f32 v239, v212, v239\n"
"v_mfma_f32_16x16x32_bf16 v[80:83], a[160:163], v[168:171], v[80:83]\n"
"buffer_load_dwordx4 v225, s[48:51], s60 offen lds\n"
"v_mfma_f32_16x16x32_bf16 v[96:99], a[160:163], v[176:179], v[96:99]\n"
"ds_read_b64_tr_b16 a[188:189], v220 offset:41384\n"
"v_mul_f32 v240, v212, v240\n"
"v_mfma_f32_16x16x32_bf16 v[96:99], a[164:167], v[180:183], v[96:99]\n"
"v_mul_f32 v241, v212, v241\n"
"v_mul_f32 v242, v212, v242\n"
"v_mfma_f32_16x16x32_bf16 v[112:115], a[164:167], v[188:191], v[112:115]\n"
"ds_read_b64_tr_b16 a[190:191], v220 offset:42408\n"
"v_mul_f32 v243, v212, v243\n"
"v_mfma_f32_16x16x32_bf16 v[112:115], a[160:163], v[184:187], v[112:115]\n"
"s_waitcnt lgkmcnt(10)\n"
"v_cvt_pk_bf16_f32 v236, v236, v237\n"
"v_cvt_pk_bf16_f32 v237, v238, v239\n"
"v_mfma_f32_16x16x32_bf16 v[12:15], a[168:171], v[184:187], v[12:15]\n"
"s_add_u32 s58, s84, 512\n"
"ds_read_b64_tr_b16 a[160:161], v220 offset:33408\n"
"v_cvt_pk_bf16_f32 v238, v240, v241\n"
"v_mfma_f32_16x16x32_bf16 v[12:15], a[172:175], v[188:191], v[12:15]\n"
"s_add_u32 m0, s61, 32768\n"
"v_cvt_pk_bf16_f32 v239, v242, v243\n"
"v_accvgpr_read_b32 v244, a96\n"
"v_mfma_f32_16x16x32_bf16 v[8:11], a[172:175], v[180:183], v[8:11]\n"
"s_waitcnt lgkmcnt(4)\n"
"ds_read_b64_tr_b16 a[162:163], v220 offset:34432\n"
"v_accvgpr_read_b32 v245, a97\n"
"v_mfma_f32_16x16x32_bf16 v[8:11], a[168:171], v[176:179], v[8:11]\n"
"buffer_load_dwordx4 v226, s[44:47], s58 offen lds\n"
"v_mfma_f32_16x16x32_bf16 v[4:7], a[168:171], v[168:171], v[4:7]\n"
"ds_read_b64_tr_b16 a[164:165], v220 offset:41632\n"
"v_accvgpr_read_b32 v246, a98\n"
"v_mfma_f32_16x16x32_bf16 v[4:7], a[172:175], v[172:175], v[4:7]\n"
"v_accvgpr_read_b32 v247, a99\n"
"v_accvgpr_read_b32 v248, a112\n"
"v_mfma_f32_16x16x32_bf16 v[0:3], a[172:175], v[164:167], v[0:3]\n"
"s_add_u32 m0, s61, 33792\n"
"ds_read_b64_tr_b16 a[166:167], v220 offset:42656\n"
"v_accvgpr_read_b32 v249, a113\n"
"v_mfma_f32_16x16x32_bf16 v[0:3], a[168:171], v[160:163], v[0:3]\n"
"s_waitcnt lgkmcnt(10)\n"
"buffer_load_dwordx4 v227, s[44:47], s58 offen lds\n"
"v_mfma_f32_16x16x32_bf16 v[68:71], a[176:179], v[160:163], v[68:71]\n"
"s_waitcnt vmcnt(31)\n"
"ds_read_b64_tr_b16 a[168:169], v220 offset:33416\n"
"v_accvgpr_read_b32 v250, a114\n"
"v_mfma_f32_16x16x32_bf16 v[68:71], a[180:183], v[164:167], v[68:71]\n"
"s_add_u32 s60, s59, 2129920\n"
"ds_read_b128 v[136:139], v228 offset:34816\n"
"ds_read_b128 v[140:143], v229 offset:34816\n"
"v_mfma_f32_16x16x32_bf16 v[84:87], a[180:183], v[172:175], v[84:87]\n"
"s_add_u32 m0, s62, 17472\n"
"ds_read_b64_tr_b16 a[170:171], v220 offset:34440\n"
"v_accvgpr_read_b32 v251, a115\n"
"v_mfma_f32_16x16x32_bf16 v[84:87], a[176:179], v[168:171], v[84:87]\n"
"buffer_load_dwordx4 v225, s[48:51], s60 offen lds\n"
"v_mfma_f32_16x16x32_bf16 v[100:103], a[176:179], v[176:179], v[100:103]\n"
"ds_read_b64_tr_b16 a[172:173], v220 offset:41640\n"
"v_mul_f32 v244, v212, v244\n"
"v_mfma_f32_16x16x32_bf16 v[100:103], a[180:183], v[180:183], v[100:103]\n"
"v_mul_f32 v245, v212, v245\n"
"v_mul_f32 v246, v212, v246\n"
"v_mfma_f32_16x16x32_bf16 v[116:119], a[180:183], v[188:191], v[116:119]\n"
"ds_read_b64_tr_b16 a[174:175], v220 offset:42664\n"
"v_mul_f32 v247, v212, v247\n"
"v_mfma_f32_16x16x32_bf16 v[116:119], a[176:179], v[184:187], v[116:119]\n"
"s_waitcnt lgkmcnt(10)\n"
"buffer_store_dwordx4 v[236:239], v232, s[52:55], s66 offen offset:2048 sc1\n"
"v_mfma_f32_16x16x32_bf16 v[28:31], a[184:187], v[184:187], v[28:31]\n"
"s_add_u32 s58, s85, 512\n"
"ds_read_b64_tr_b16 a[176:177], v220 offset:33664\n"
"v_mul_f32 v248, v212, v248\n"
"v_mfma_f32_16x16x32_bf16 v[28:31], a[188:191], v[188:191], v[28:31]\n"
"s_add_u32 m0, s61, 34816\n"
"v_mul_f32 v249, v212, v249\n"
"v_mul_f32 v250, v212, v250\n"
"v_mfma_f32_16x16x32_bf16 v[24:27], a[188:191], v[180:183], v[24:27]\n"
"s_waitcnt lgkmcnt(4)\n"
"ds_read_b64_tr_b16 a[178:179], v220 offset:34688\n"
"v_mul_f32 v251, v212, v251\n"
"v_mfma_f32_16x16x32_bf16 v[24:27], a[184:187], v[176:179], v[24:27]\n"
"buffer_load_dwordx4 v226, s[44:47], s58 offen lds\n"
"v_mfma_f32_16x16x32_bf16 v[20:23], a[184:187], v[168:171], v[20:23]\n"
"ds_read_b64_tr_b16 a[180:181], v220 offset:41888\n"
"v_cvt_pk_bf16_f32 v244, v244, v245\n"
"v_mfma_f32_16x16x32_bf16 v[20:23], a[188:191], v[172:175], v[20:23]\n"
"v_cvt_pk_bf16_f32 v245, v246, v247\n"
"v_cvt_pk_bf16_f32 v246, v248, v249\n"
"v_mfma_f32_16x16x32_bf16 v[16:19], a[188:191], v[164:167], v[16:19]\n"
"s_add_u32 m0, s61, 35840\n"
"ds_read_b64_tr_b16 a[182:183], v220 offset:42912\n"
"v_cvt_pk_bf16_f32 v247, v250, v251\n"
"v_mfma_f32_16x16x32_bf16 v[16:19], a[184:187], v[160:163], v[16:19]\n"
"s_waitcnt lgkmcnt(10)\n"
"buffer_load_dwordx4 v227, s[44:47], s58 offen lds\n"
"v_mfma_f32_16x16x32_bf16 v[72:75], a[160:163], v[160:163], v[72:75]\n"
"s_waitcnt vmcnt(29)\n"
"ds_read_b64_tr_b16 a[184:185], v220 offset:33672\n"
"v_accvgpr_read_b32 v236, a4\n"
"v_mfma_f32_16x16x32_bf16 v[72:75], a[164:167], v[164:167], v[72:75]\n"
"s_add_u32 s60, s59, 2162688\n"
"ds_read_b128 v[144:147], v228 offset:49152\n"
"ds_read_b128 v[148:151], v229 offset:49152\n"
"v_mfma_f32_16x16x32_bf16 v[88:91], a[164:167], v[172:175], v[88:91]\n"
"s_add_u32 m0, s62, 18504\n"
"ds_read_b64_tr_b16 a[186:187], v220 offset:34696\n"
"v_accvgpr_read_b32 v237, a5\n"
"v_mfma_f32_16x16x32_bf16 v[88:91], a[160:163], v[168:171], v[88:91]\n"
"buffer_load_dwordx4 v225, s[48:51], s60 offen lds\n"
"v_mfma_f32_16x16x32_bf16 v[104:107], a[160:163], v[176:179], v[104:107]\n"
"ds_read_b64_tr_b16 a[188:189], v220 offset:41896\n"
"v_accvgpr_read_b32 v238, a6\n"
"v_mfma_f32_16x16x32_bf16 v[104:107], a[164:167], v[180:183], v[104:107]\n"
"v_accvgpr_read_b32 v239, a7\n"
"v_accvgpr_read_b32 v240, a20\n"
"v_mfma_f32_16x16x32_bf16 v[120:123], a[164:167], v[188:191], v[120:123]\n"
"ds_read_b64_tr_b16 a[190:191], v220 offset:42920\n"
"v_accvgpr_read_b32 v241, a21\n"
"v_mfma_f32_16x16x32_bf16 v[120:123], a[160:163], v[184:187], v[120:123]\n"
"s_waitcnt lgkmcnt(10)\n"
"buffer_store_dwordx4 v[244:247], v232, s[52:55], s66 offen offset:3072 sc1\n"
"v_mfma_f32_16x16x32_bf16 v[44:47], a[168:171], v[184:187], v[44:47]\n"
"s_add_u32 s58, s87, 512\n"
"v_accvgpr_read_b32 v242, a22\n"
"v_accvgpr_read_b32 v243, a23\n"
"v_mfma_f32_16x16x32_bf16 v[44:47], a[172:175], v[188:191], v[44:47]\n"
"s_add_u32 m0, s61, 49152\n"
"v_mul_f32 v236, v213, v236\n"
"v_mul_f32 v237, v213, v237\n"
"v_mfma_f32_16x16x32_bf16 v[40:43], a[172:175], v[180:183], v[40:43]\n"
"s_waitcnt lgkmcnt(3)\n"
"v_mul_f32 v238, v213, v238\n"
"v_mul_f32 v239, v213, v239\n"
"v_mfma_f32_16x16x32_bf16 v[40:43], a[168:171], v[176:179], v[40:43]\n"
"s_waitcnt vmcnt(25) lgkmcnt(0)\n"
"buffer_load_dwordx4 v226, s[44:47], s58 offen lds\n"
"v_mfma_f32_16x16x32_bf16 v[36:39], a[168:171], v[168:171], v[36:39]\n"
"s_barrier\n"
"v_mfma_f32_16x16x32_bf16 v[36:39], a[172:175], v[172:175], v[36:39]\n"
"ds_read_b64_tr_b16 a[160:161], v220 offset:49344\n"
"ds_read_b64_tr_b16 a[162:163], v220 offset:50368\n"
"v_mfma_f32_16x16x32_bf16 v[32:35], a[172:175], v[164:167], v[32:35]\n"
"s_add_u32 m0, s61, 50176\n"
"ds_read_b64_tr_b16 a[164:165], v220 offset:57568\n"
"v_mul_f32 v240, v213, v240\n"
"v_mfma_f32_16x16x32_bf16 v[32:35], a[168:171], v[160:163], v[32:35]\n"
"buffer_load_dwordx4 v227, s[44:47], s58 offen lds\n"
"v_mfma_f32_16x16x32_bf16 v[76:79], a[176:179], v[160:163], v[76:79]\n"
"s_waitcnt vmcnt(31)\n"
"ds_read_b64_tr_b16 a[166:167], v220 offset:58592\n"
"ds_read_b64_tr_b16 a[168:169], v220 offset:49352\n"
"v_mfma_f32_16x16x32_bf16 v[76:79], a[180:183], v[164:167], v[76:79]\n"
"s_add_u32 s60, s59, 2195456\n"
"ds_read_b128 v[152:155], v228 offset:51200\n"
"ds_read_b128 v[156:159], v229 offset:51200\n"
"v_mfma_f32_16x16x32_bf16 v[92:95], a[180:183], v[172:175], v[92:95]\n"
"s_add_u32 m0, s62, 19528\n"
"ds_read_b64_tr_b16 a[170:171], v220 offset:50376\n"
"v_mul_f32 v241, v213, v241\n"
"v_mfma_f32_16x16x32_bf16 v[92:95], a[176:179], v[168:171], v[92:95]\n"
"buffer_load_dwordx4 v225, s[48:51], s60 offen lds\n"
"v_mfma_f32_16x16x32_bf16 v[108:111], a[176:179], v[176:179], v[108:111]\n"
"ds_read_b64_tr_b16 a[172:173], v220 offset:57576\n"
"v_mul_f32 v242, v213, v242\n"
"v_mfma_f32_16x16x32_bf16 v[108:111], a[180:183], v[180:183], v[108:111]\n"
"v_mul_f32 v243, v213, v243\n"
"v_cvt_pk_bf16_f32 v236, v236, v237\n"
"v_mfma_f32_16x16x32_bf16 v[124:127], a[180:183], v[188:191], v[124:127]\n"
"ds_read_b64_tr_b16 a[174:175], v220 offset:58600\n"
"v_cvt_pk_bf16_f32 v237, v238, v239\n"
"v_mfma_f32_16x16x32_bf16 v[124:127], a[176:179], v[184:187], v[124:127]\n"
"v_cvt_pk_bf16_f32 v238, v240, v241\n"
"v_cvt_pk_bf16_f32 v239, v242, v243\n"
"v_mfma_f32_16x16x32_bf16 v[60:63], a[184:187], v[184:187], v[60:63]\n"
"s_add_u32 s58, s68, 512\n"
"ds_read_b64_tr_b16 a[176:177], v220 offset:49600\n"
"v_accvgpr_read_b32 v244, a36\n"
"v_mfma_f32_16x16x32_bf16 v[60:63], a[188:191], v[188:191], v[60:63]\n"
"s_add_u32 m0, s61, 51200\n"
"v_accvgpr_read_b32 v245, a37\n"
"v_accvgpr_read_b32 v246, a38\n"
"v_mfma_f32_16x16x32_bf16 v[56:59], a[188:191], v[180:183], v[56:59]\n"
"s_waitcnt lgkmcnt(4)\n"
"ds_read_b64_tr_b16 a[178:179], v220 offset:50624\n"
"v_accvgpr_read_b32 v247, a39\n"
"v_mfma_f32_16x16x32_bf16 v[56:59], a[184:187], v[176:179], v[56:59]\n"
"buffer_load_dwordx4 v226, s[44:47], s58 offen lds\n"
"v_mfma_f32_16x16x32_bf16 v[52:55], a[184:187], v[168:171], v[52:55]\n"
"ds_read_b64_tr_b16 a[180:181], v220 offset:57824\n"
"v_accvgpr_read_b32 v248, a52\n"
"v_mfma_f32_16x16x32_bf16 v[52:55], a[188:191], v[172:175], v[52:55]\n"
"v_accvgpr_read_b32 v249, a53\n"
"v_accvgpr_read_b32 v250, a54\n"
"v_mfma_f32_16x16x32_bf16 v[48:51], a[188:191], v[164:167], v[48:51]\n"
"s_add_u32 m0, s61, 52224\n"
"ds_read_b64_tr_b16 a[182:183], v220 offset:58848\n"
"v_accvgpr_read_b32 v251, a55\n"
"v_mfma_f32_16x16x32_bf16 v[48:51], a[184:187], v[160:163], v[48:51]\n"
"s_waitcnt lgkmcnt(7)\n"
"buffer_load_dwordx4 v227, s[44:47], s58 offen lds\n"
"v_mfma_f32_16x16x32_bf16 v[64:67], a[160:163], v[128:131], v[64:67]\n"
"s_waitcnt vmcnt(26)\n"
"ds_read_b64_tr_b16 a[184:185], v220 offset:49608\n"
"v_mul_f32 v244, v213, v244\n"
"v_mfma_f32_16x16x32_bf16 v[64:67], a[164:167], v[132:135], v[64:67]\n"
"s_add_u32 s60, s59, 2621440\n"
"ds_read_b128 v[160:163], v228\n"
"ds_read_b128 v[164:167], v229\n"
"v_mfma_f32_16x16x32_bf16 v[80:83], a[164:167], v[140:143], v[80:83]\n"
"s_add_u32 m0, s62, 65792\n"
"ds_read_b64_tr_b16 a[186:187], v220 offset:50632\n"
"v_mul_f32 v245, v213, v245\n"
"v_mfma_f32_16x16x32_bf16 v[80:83], a[160:163], v[136:139], v[80:83]\n"
"buffer_load_dwordx4 v225, s[48:51], s60 offen lds\n"
"v_mfma_f32_16x16x32_bf16 v[96:99], a[160:163], v[144:147], v[96:99]\n"
"ds_read_b64_tr_b16 a[188:189], v220 offset:57832\n"
"v_mul_f32 v246, v213, v246\n"
"v_mfma_f32_16x16x32_bf16 v[96:99], a[164:167], v[148:151], v[96:99]\n"
"v_mul_f32 v247, v213, v247\n"
"v_mul_f32 v248, v213, v248\n"
"v_mfma_f32_16x16x32_bf16 v[112:115], a[164:167], v[156:159], v[112:115]\n"
"s_add_u32 s66, s65, 4096\n"
"ds_read_b64_tr_b16 a[190:191], v220 offset:58856\n"
"v_mul_f32 v249, v213, v249\n"
"v_mfma_f32_16x16x32_bf16 v[112:115], a[160:163], v[152:155], v[112:115]\n"
"s_waitcnt lgkmcnt(10)\n"
"buffer_store_dwordx4 v[236:239], v232, s[52:55], s66 offen sc1\n"
"v_mfma_f32_16x16x32_bf16 v[12:15], a[168:171], v[152:155], v[12:15]\n"
"s_add_u32 s58, s84, 640\n"
"ds_read_b64_tr_b16 a[160:161], v220 offset:49856\n"
"v_mul_f32 v250, v213, v250\n"
"v_mfma_f32_16x16x32_bf16 v[12:15], a[172:175], v[156:159], v[12:15]\n"
"s_add_u32 m0, s61, 0\n"
"v_mul_f32 v251, v213, v251\n"
"v_cvt_pk_bf16_f32 v244, v244, v245\n"
"v_mfma_f32_16x16x32_bf16 v[8:11], a[172:175], v[148:151], v[8:11]\n"
"s_waitcnt lgkmcnt(4)\n"
"ds_read_b64_tr_b16 a[162:163], v220 offset:50880\n"
"v_cvt_pk_bf16_f32 v245, v246, v247\n"
"v_mfma_f32_16x16x32_bf16 v[8:11], a[168:171], v[144:147], v[8:11]\n"
"buffer_load_dwordx4 v226, s[44:47], s58 offen lds\n"
"v_mfma_f32_16x16x32_bf16 v[4:7], a[168:171], v[136:139], v[4:7]\n"
"ds_read_b64_tr_b16 a[164:165], v220 offset:58080\n"
"v_cvt_pk_bf16_f32 v246, v248, v249\n"
"v_mfma_f32_16x16x32_bf16 v[4:7], a[172:175], v[140:143], v[4:7]\n"
"v_cvt_pk_bf16_f32 v247, v250, v251\n"
"v_accvgpr_read_b32 v236, a68\n"
"v_mfma_f32_16x16x32_bf16 v[0:3], a[172:175], v[132:135], v[0:3]\n"
"s_add_u32 m0, s61, 1024\n"
"ds_read_b64_tr_b16 a[166:167], v220 offset:59104\n"
"v_accvgpr_read_b32 v237, a69\n"
"v_mfma_f32_16x16x32_bf16 v[0:3], a[168:171], v[128:131], v[0:3]\n"
"s_waitcnt lgkmcnt(10)\n"
"buffer_load_dwordx4 v227, s[44:47], s58 offen lds\n"
"v_mfma_f32_16x16x32_bf16 v[68:71], a[176:179], v[128:131], v[68:71]\n"
"s_waitcnt vmcnt(26)\n"
"ds_read_b64_tr_b16 a[168:169], v220 offset:49864\n"
"v_accvgpr_read_b32 v238, a70\n"
"v_mfma_f32_16x16x32_bf16 v[68:71], a[180:183], v[132:135], v[68:71]\n"
"s_add_u32 s60, s59, 2654208\n"
"ds_read_b128 v[168:171], v228 offset:2048\n"
"ds_read_b128 v[172:175], v229 offset:2048\n"
"v_mfma_f32_16x16x32_bf16 v[84:87], a[180:183], v[140:143], v[84:87]\n"
"s_add_u32 m0, s62, 66816\n"
"ds_read_b64_tr_b16 a[170:171], v220 offset:50888\n"
"v_accvgpr_read_b32 v239, a71\n"
"v_mfma_f32_16x16x32_bf16 v[84:87], a[176:179], v[136:139], v[84:87]\n"
"buffer_load_dwordx4 v225, s[48:51], s60 offen lds\n"
"v_mfma_f32_16x16x32_bf16 v[100:103], a[176:179], v[144:147], v[100:103]\n"
"ds_read_b64_tr_b16 a[172:173], v220 offset:58088\n"
"v_accvgpr_read_b32 v240, a84\n"
"v_mfma_f32_16x16x32_bf16 v[100:103], a[180:183], v[148:151], v[100:103]\n"
"v_accvgpr_read_b32 v241, a85\n"
"v_accvgpr_read_b32 v242, a86\n"
"v_mfma_f32_16x16x32_bf16 v[116:119], a[180:183], v[156:159], v[116:119]\n"
"ds_read_b64_tr_b16 a[174:175], v220 offset:59112\n"
"v_accvgpr_read_b32 v243, a87\n"
"v_mfma_f32_16x16x32_bf16 v[116:119], a[176:179], v[152:155], v[116:119]\n"
"s_waitcnt lgkmcnt(10)\n"
"buffer_store_dwordx4 v[244:247], v232, s[52:55], s66 offen offset:1024 sc1\n"
"v_mfma_f32_16x16x32_bf16 v[28:31], a[184:187], v[152:155], v[28:31]\n"
"s_add_u32 s58, s85, 640\n"
"ds_read_b64_tr_b16 a[176:177], v220 offset:50112\n"
"v_mul_f32 v236, v213, v236\n"
"v_mfma_f32_16x16x32_bf16 v[28:31], a[188:191], v[156:159], v[28:31]\n"
"s_add_u32 m0, s61, 2048\n"
"v_mul_f32 v237, v213, v237\n"
"v_mul_f32 v238, v213, v238\n"
"v_mfma_f32_16x16x32_bf16 v[24:27], a[188:191], v[148:151], v[24:27]\n"
"s_waitcnt lgkmcnt(4)\n"
"ds_read_b64_tr_b16 a[178:179], v220 offset:51136\n"
"v_mul_f32 v239, v213, v239\n"
"v_mfma_f32_16x16x32_bf16 v[24:27], a[184:187], v[144:147], v[24:27]\n"
"buffer_load_dwordx4 v226, s[44:47], s58 offen lds\n"
"v_mfma_f32_16x16x32_bf16 v[20:23], a[184:187], v[136:139], v[20:23]\n"
"ds_read_b64_tr_b16 a[180:181], v220 offset:58336\n"
"v_mul_f32 v240, v213, v240\n"
"v_mfma_f32_16x16x32_bf16 v[20:23], a[188:191], v[140:143], v[20:23]\n"
"v_mul_f32 v241, v213, v241\n"
"v_mul_f32 v242, v213, v242\n"
"v_mfma_f32_16x16x32_bf16 v[16:19], a[188:191], v[132:135], v[16:19]\n"
"s_add_u32 m0, s61, 3072\n"
"ds_read_b64_tr_b16 a[182:183], v220 offset:59360\n"
"v_mul_f32 v243, v213, v243\n"
"v_mfma_f32_16x16x32_bf16 v[16:19], a[184:187], v[128:131], v[16:19]\n"
"s_waitcnt lgkmcnt(10)\n"
"buffer_load_dwordx4 v227, s[44:47], s58 offen lds\n"
"v_mfma_f32_16x16x32_bf16 v[72:75], a[160:163], v[128:131], v[72:75]\n"
"s_waitcnt vmcnt(26)\n"
"ds_read_b64_tr_b16 a[184:185], v220 offset:50120\n"
"v_cvt_pk_bf16_f32 v236, v236, v237\n"
"v_mfma_f32_16x16x32_bf16 v[72:75], a[164:167], v[132:135], v[72:75]\n"
"s_add_u32 s60, s59, 2686976\n"
"ds_read_b128 v[176:179], v228 offset:16384\n"
"ds_read_b128 v[180:183], v229 offset:16384\n"
"v_mfma_f32_16x16x32_bf16 v[88:91], a[164:167], v[140:143], v[88:91]\n"
"s_add_u32 m0, s62, 67848\n"
"ds_read_b64_tr_b16 a[186:187], v220 offset:51144\n"
"v_cvt_pk_bf16_f32 v237, v238, v239\n"
"v_mfma_f32_16x16x32_bf16 v[88:91], a[160:163], v[136:139], v[88:91]\n"
"buffer_load_dwordx4 v225, s[48:51], s60 offen lds\n"
"v_mfma_f32_16x16x32_bf16 v[104:107], a[160:163], v[144:147], v[104:107]\n"
"ds_read_b64_tr_b16 a[188:189], v220 offset:58344\n"
"v_cvt_pk_bf16_f32 v238, v240, v241\n"
"v_mfma_f32_16x16x32_bf16 v[104:107], a[164:167], v[148:151], v[104:107]\n"
"v_cvt_pk_bf16_f32 v239, v242, v243\n"
"v_accvgpr_read_b32 v244, a100\n"
"v_mfma_f32_16x16x32_bf16 v[120:123], a[164:167], v[156:159], v[120:123]\n"
"ds_read_b64_tr_b16 a[190:191], v220 offset:59368\n"
"v_accvgpr_read_b32 v245, a101\n"
"v_mfma_f32_16x16x32_bf16 v[120:123], a[160:163], v[152:155], v[120:123]\n"
"s_waitcnt lgkmcnt(10)\n"
"buffer_store_dwordx4 v[236:239], v232, s[52:55], s66 offen offset:2048 sc1\n"
"v_mfma_f32_16x16x32_bf16 v[44:47], a[168:171], v[152:155], v[44:47]\n"
"s_add_u32 s58, s87, 640\n"
"v_accvgpr_read_b32 v246, a102\n"
"v_accvgpr_read_b32 v247, a103\n"
"v_mfma_f32_16x16x32_bf16 v[44:47], a[172:175], v[156:159], v[44:47]\n"
"s_add_u32 m0, s61, 16384\n"
"v_accvgpr_read_b32 v248, a116\n"
"v_accvgpr_read_b32 v249, a117\n"
"v_mfma_f32_16x16x32_bf16 v[40:43], a[172:175], v[148:151], v[40:43]\n"
"s_waitcnt lgkmcnt(3)\n"
"v_accvgpr_read_b32 v250, a118\n"
"v_accvgpr_read_b32 v251, a119\n"
"v_mfma_f32_16x16x32_bf16 v[40:43], a[168:171], v[144:147], v[40:43]\n"
"s_waitcnt vmcnt(27) lgkmcnt(0)\n"
"buffer_load_dwordx4 v226, s[44:47], s58 offen lds\n"
"v_mfma_f32_16x16x32_bf16 v[36:39], a[168:171], v[136:139], v[36:39]\n"
"s_barrier\n"
"v_mfma_f32_16x16x32_bf16 v[36:39], a[172:175], v[140:143], v[36:39]\n"
"ds_read_b64_tr_b16 a[160:161], v220\n"
"ds_read_b64_tr_b16 a[162:163], v220 offset:1024\n"
"v_mfma_f32_16x16x32_bf16 v[32:35], a[172:175], v[132:135], v[32:35]\n"
"s_add_u32 m0, s61, 17408\n"
"ds_read_b64_tr_b16 a[164:165], v220 offset:8224\n"
"v_mul_f32 v244, v213, v244\n"
"v_mfma_f32_16x16x32_bf16 v[32:35], a[168:171], v[128:131], v[32:35]\n"
"buffer_load_dwordx4 v227, s[44:47], s58 offen lds\n"
"v_mfma_f32_16x16x32_bf16 v[76:79], a[176:179], v[128:131], v[76:79]\n"
"s_waitcnt vmcnt(26)\n"
"ds_read_b64_tr_b16 a[166:167], v220 offset:9248\n"
"ds_read_b64_tr_b16 a[168:169], v220 offset:8\n"
"v_mfma_f32_16x16x32_bf16 v[76:79], a[180:183], v[132:135], v[76:79]\n"
"s_add_u32 s60, s59, 2719744\n"
"ds_read_b128 v[184:187], v228 offset:18432\n"
"ds_read_b128 v[188:191], v229 offset:18432\n"
"v_mfma_f32_16x16x32_bf16 v[92:95], a[180:183], v[140:143], v[92:95]\n"
"s_add_u32 m0, s62, 68872\n"
"ds_read_b64_tr_b16 a[170:171], v220 offset:1032\n"
"v_mul_f32 v245, v213, v245\n"
"v_mfma_f32_16x16x32_bf16 v[92:95], a[176:179], v[136:139], v[92:95]\n"
"buffer_load_dwordx4 v225, s[48:51], s60 offen lds\n"
"v_mfma_f32_16x16x32_bf16 v[108:111], a[176:179], v[144:147], v[108:111]\n"
"ds_read_b64_tr_b16 a[172:173], v220 offset:8232\n"
"v_mul_f32 v246, v213, v246\n"
"v_mfma_f32_16x16x32_bf16 v[108:111], a[180:183], v[148:151], v[108:111]\n"
"v_mul_f32 v247, v213, v247\n"
"v_mul_f32 v248, v213, v248\n"
"v_mfma_f32_16x16x32_bf16 v[124:127], a[180:183], v[156:159], v[124:127]\n"
"ds_read_b64_tr_b16 a[174:175], v220 offset:9256\n"
"v_mul_f32 v249, v213, v249\n"
"v_mfma_f32_16x16x32_bf16 v[124:127], a[176:179], v[152:155], v[124:127]\n"
"v_mul_f32 v250, v213, v250\n"
"v_mul_f32 v251, v213, v251\n"
"v_mfma_f32_16x16x32_bf16 v[60:63], a[184:187], v[152:155], v[60:63]\n"
"s_add_u32 s58, s68, 640\n"
"ds_read_b64_tr_b16 a[176:177], v220 offset:256\n"
"v_cvt_pk_bf16_f32 v244, v244, v245\n"
"v_mfma_f32_16x16x32_bf16 v[60:63], a[188:191], v[156:159], v[60:63]\n"
"s_add_u32 m0, s61, 18432\n"
"v_cvt_pk_bf16_f32 v245, v246, v247\n"
"v_cvt_pk_bf16_f32 v246, v248, v249\n"
"v_mfma_f32_16x16x32_bf16 v[56:59], a[188:191], v[148:151], v[56:59]\n"
"s_waitcnt lgkmcnt(4)\n"
"ds_read_b64_tr_b16 a[178:179], v220 offset:1280\n"
"v_cvt_pk_bf16_f32 v247, v250, v251\n"
"v_mfma_f32_16x16x32_bf16 v[56:59], a[184:187], v[144:147], v[56:59]\n"
"buffer_load_dwordx4 v226, s[44:47], s58 offen lds\n"
"v_mfma_f32_16x16x32_bf16 v[52:55], a[184:187], v[136:139], v[52:55]\n"
"ds_read_b64_tr_b16 a[180:181], v220 offset:8480\n"
"v_accvgpr_read_b32 v236, a8\n"
"v_mfma_f32_16x16x32_bf16 v[52:55], a[188:191], v[140:143], v[52:55]\n"
"v_accvgpr_read_b32 v237, a9\n"
"v_accvgpr_read_b32 v238, a10\n"
"v_mfma_f32_16x16x32_bf16 v[48:51], a[188:191], v[132:135], v[48:51]\n"
"s_add_u32 m0, s61, 19456\n"
"ds_read_b64_tr_b16 a[182:183], v220 offset:9504\n"
"v_accvgpr_read_b32 v239, a11\n"
"v_mfma_f32_16x16x32_bf16 v[48:51], a[184:187], v[128:131], v[48:51]\n"
"s_waitcnt lgkmcnt(7)\n"
"buffer_load_dwordx4 v227, s[44:47], s58 offen lds\n"
"v_mfma_f32_16x16x32_bf16 v[64:67], a[160:163], v[160:163], v[64:67]\n"
"s_waitcnt vmcnt(26)\n"
"ds_read_b64_tr_b16 a[184:185], v220 offset:264\n"
"v_accvgpr_read_b32 v240, a24\n"
"v_mfma_f32_16x16x32_bf16 v[64:67], a[164:167], v[164:167], v[64:67]\n"
"s_add_u32 s60, s59, 3145728\n"
"ds_read_b128 v[128:131], v228 offset:32768\n"
"ds_read_b128 v[132:135], v229 offset:32768\n"
"v_mfma_f32_16x16x32_bf16 v[80:83], a[164:167], v[172:175], v[80:83]\n"
"s_add_u32 m0, s62, 32896\n"
"ds_read_b64_tr_b16 a[186:187], v220 offset:1288\n"
"v_accvgpr_read_b32 v241, a25\n"
"v_mfma_f32_16x16x32_bf16 v[80:83], a[160:163], v[168:171], v[80:83]\n"
"buffer_load_dwordx4 v225, s[48:51], s60 offen lds\n"
"v_mfma_f32_16x16x32_bf16 v[96:99], a[160:163], v[176:179], v[96:99]\n"
"ds_read_b64_tr_b16 a[188:189], v220 offset:8488\n"
"v_accvgpr_read_b32 v242, a26\n"
"v_mfma_f32_16x16x32_bf16 v[96:99], a[164:167], v[180:183], v[96:99]\n"
"v_accvgpr_read_b32 v243, a27\n"
"v_mul_f32 v236, v214, v236\n"
"v_mfma_f32_16x16x32_bf16 v[112:115], a[164:167], v[188:191], v[112:115]\n"
"ds_read_b64_tr_b16 a[190:191], v220 offset:9512\n"
"v_mul_f32 v237, v214, v237\n"
"v_mfma_f32_16x16x32_bf16 v[112:115], a[160:163], v[184:187], v[112:115]\n"
"s_waitcnt lgkmcnt(10)\n"
"buffer_store_dwordx4 v[244:247], v232, s[52:55], s66 offen offset:3072 sc1\n"
"v_mfma_f32_16x16x32_bf16 v[12:15], a[168:171], v[184:187], v[12:15]\n"
"s_add_u32 s58, s84, 768\n"
"ds_read_b64_tr_b16 a[160:161], v220 offset:512\n"
"v_mul_f32 v238, v214, v238\n"
"v_mfma_f32_16x16x32_bf16 v[12:15], a[172:175], v[188:191], v[12:15]\n"
"s_add_u32 m0, s61, 32768\n"
"v_mul_f32 v239, v214, v239\n"
"v_mul_f32 v240, v214, v240\n"
"v_mfma_f32_16x16x32_bf16 v[8:11], a[172:175], v[180:183], v[8:11]\n"
"s_waitcnt lgkmcnt(4)\n"
"ds_read_b64_tr_b16 a[162:163], v220 offset:1536\n"
"v_mul_f32 v241, v214, v241\n"
"v_mfma_f32_16x16x32_bf16 v[8:11], a[168:171], v[176:179], v[8:11]\n"
"buffer_load_dwordx4 v226, s[44:47], s58 offen lds\n"
"v_mfma_f32_16x16x32_bf16 v[4:7], a[168:171], v[168:171], v[4:7]\n"
"ds_read_b64_tr_b16 a[164:165], v220 offset:8736\n"
"v_mul_f32 v242, v214, v242\n"
"v_mfma_f32_16x16x32_bf16 v[4:7], a[172:175], v[172:175], v[4:7]\n"
"v_mul_f32 v243, v214, v243\n"
"v_cvt_pk_bf16_f32 v236, v236, v237\n"
"v_mfma_f32_16x16x32_bf16 v[0:3], a[172:175], v[164:167], v[0:3]\n"
"s_add_u32 m0, s61, 33792\n"
"ds_read_b64_tr_b16 a[166:167], v220 offset:9760\n"
"v_cvt_pk_bf16_f32 v237, v238, v239\n"
"v_mfma_f32_16x16x32_bf16 v[0:3], a[168:171], v[160:163], v[0:3]\n"
"s_waitcnt lgkmcnt(10)\n"
"buffer_load_dwordx4 v227, s[44:47], s58 offen lds\n"
"v_mfma_f32_16x16x32_bf16 v[68:71], a[176:179], v[160:163], v[68:71]\n"
"s_waitcnt vmcnt(26)\n"
"ds_read_b64_tr_b16 a[168:169], v220 offset:520\n"
"v_cvt_pk_bf16_f32 v238, v240, v241\n"
"v_mfma_f32_16x16x32_bf16 v[68:71], a[180:183], v[164:167], v[68:71]\n"
"s_add_u32 s60, s59, 3178496\n"
"ds_read_b128 v[136:139], v228 offset:34816\n"
"ds_read_b128 v[140:143], v229 offset:34816\n"
"v_mfma_f32_16x16x32_bf16 v[84:87], a[180:183], v[172:175], v[84:87]\n"
"s_add_u32 m0, s62, 33920\n"
"ds_read_b64_tr_b16 a[170:171], v220 offset:1544\n"
"v_cvt_pk_bf16_f32 v239, v242, v243\n"
"v_mfma_f32_16x16x32_bf16 v[84:87], a[176:179], v[168:171], v[84:87]\n"
"buffer_load_dwordx4 v225, s[48:51], s60 offen lds\n"
"v_mfma_f32_16x16x32_bf16 v[100:103], a[176:179], v[176:179], v[100:103]\n"
"ds_read_b64_tr_b16 a[172:173], v220 offset:8744\n"
"v_accvgpr_read_b32 v244, a40\n"
"v_mfma_f32_16x16x32_bf16 v[100:103], a[180:183], v[180:183], v[100:103]\n"
"v_accvgpr_read_b32 v245, a41\n"
"v_accvgpr_read_b32 v246, a42\n"
"v_mfma_f32_16x16x32_bf16 v[116:119], a[180:183], v[188:191], v[116:119]\n"
"s_add_u32 s66, s65, 8192\n"
"ds_read_b64_tr_b16 a[174:175], v220 offset:9768\n"
"v_accvgpr_read_b32 v247, a43\n"
"v_mfma_f32_16x16x32_bf16 v[116:119], a[176:179], v[184:187], v[116:119]\n"
"s_waitcnt lgkmcnt(10)\n"
"buffer_store_dwordx4 v[236:239], v232, s[52:55], s66 offen sc1\n"
"v_mfma_f32_16x16x32_bf16 v[28:31], a[184:187], v[184:187], v[28:31]\n"
"s_add_u32 s58, s85, 768\n"
"ds_read_b64_tr_b16 a[176:177], v220 offset:768\n"
"v_accvgpr_read_b32 v248, a56\n"
"v_mfma_f32_16x16x32_bf16 v[28:31], a[188:191], v[188:191], v[28:31]\n"
"s_add_u32 m0, s61, 34816\n"
"v_accvgpr_read_b32 v249, a57\n"
"v_accvgpr_read_b32 v250, a58\n"
"v_mfma_f32_16x16x32_bf16 v[24:27], a[188:191], v[180:183], v[24:27]\n"
"s_waitcnt lgkmcnt(4)\n"
"ds_read_b64_tr_b16 a[178:179], v220 offset:1792\n"
"v_accvgpr_read_b32 v251, a59\n"
"v_mfma_f32_16x16x32_bf16 v[24:27], a[184:187], v[176:179], v[24:27]\n"
"buffer_load_dwordx4 v226, s[44:47], s58 offen lds\n"
"v_mfma_f32_16x16x32_bf16 v[20:23], a[184:187], v[168:171], v[20:23]\n"
"ds_read_b64_tr_b16 a[180:181], v220 offset:8992\n"
"v_mul_f32 v244, v214, v244\n"
"v_mfma_f32_16x16x32_bf16 v[20:23], a[188:191], v[172:175], v[20:23]\n"
"v_mul_f32 v245, v214, v245\n"
"v_mul_f32 v246, v214, v246\n"
"v_mfma_f32_16x16x32_bf16 v[16:19], a[188:191], v[164:167], v[16:19]\n"
"s_add_u32 m0, s61, 35840\n"
"ds_read_b64_tr_b16 a[182:183], v220 offset:10016\n"
"v_mul_f32 v247, v214, v247\n"
"v_mfma_f32_16x16x32_bf16 v[16:19], a[184:187], v[160:163], v[16:19]\n"
"s_waitcnt lgkmcnt(10)\n"
"buffer_load_dwordx4 v227, s[44:47], s58 offen lds\n"
"v_mfma_f32_16x16x32_bf16 v[72:75], a[160:163], v[160:163], v[72:75]\n"
"s_waitcnt vmcnt(26)\n"
"ds_read_b64_tr_b16 a[184:185], v220 offset:776\n"
"v_mul_f32 v248, v214, v248\n"
"v_mfma_f32_16x16x32_bf16 v[72:75], a[164:167], v[164:167], v[72:75]\n"
"s_add_u32 s60, s59, 3211264\n"
"ds_read_b128 v[144:147], v228 offset:49152\n"
"ds_read_b128 v[148:151], v229 offset:49152\n"
"v_mfma_f32_16x16x32_bf16 v[88:91], a[164:167], v[172:175], v[88:91]\n"
"s_add_u32 m0, s62, 34952\n"
"ds_read_b64_tr_b16 a[186:187], v220 offset:1800\n"
"v_mul_f32 v249, v214, v249\n"
"v_mfma_f32_16x16x32_bf16 v[88:91], a[160:163], v[168:171], v[88:91]\n"
"buffer_load_dwordx4 v225, s[48:51], s60 offen lds\n"
"v_mfma_f32_16x16x32_bf16 v[104:107], a[160:163], v[176:179], v[104:107]\n"
"ds_read_b64_tr_b16 a[188:189], v220 offset:9000\n"
"v_mul_f32 v250, v214, v250\n"
"v_mfma_f32_16x16x32_bf16 v[104:107], a[164:167], v[180:183], v[104:107]\n"
"v_mul_f32 v251, v214, v251\n"
"v_cvt_pk_bf16_f32 v244, v244, v245\n"
"v_mfma_f32_16x16x32_bf16 v[120:123], a[164:167], v[188:191], v[120:123]\n"
"ds_read_b64_tr_b16 a[190:191], v220 offset:10024\n"
"v_cvt_pk_bf16_f32 v245, v246, v247\n"
"v_mfma_f32_16x16x32_bf16 v[120:123], a[160:163], v[184:187], v[120:123]\n"
"s_waitcnt lgkmcnt(10)\n"
"v_cvt_pk_bf16_f32 v246, v248, v249\n"
"v_cvt_pk_bf16_f32 v247, v250, v251\n"
"v_mfma_f32_16x16x32_bf16 v[44:47], a[168:171], v[184:187], v[44:47]\n"
"s_add_u32 s58, s87, 768\n"
"v_accvgpr_read_b32 v236, a72\n"
"v_accvgpr_read_b32 v237, a73\n"
"v_mfma_f32_16x16x32_bf16 v[44:47], a[172:175], v[188:191], v[44:47]\n"
"s_add_u32 m0, s61, 49152\n"
"v_accvgpr_read_b32 v238, a74\n"
"v_accvgpr_read_b32 v239, a75\n"
"v_mfma_f32_16x16x32_bf16 v[40:43], a[172:175], v[180:183], v[40:43]\n"
"s_waitcnt lgkmcnt(3)\n"
"v_accvgpr_read_b32 v240, a88\n"
"v_accvgpr_read_b32 v241, a89\n"
"v_mfma_f32_16x16x32_bf16 v[40:43], a[168:171], v[176:179], v[40:43]\n"
"s_waitcnt vmcnt(26) lgkmcnt(0)\n"
"buffer_load_dwordx4 v226, s[44:47], s58 offen lds\n"
"v_mfma_f32_16x16x32_bf16 v[36:39], a[168:171], v[168:171], v[36:39]\n"
"s_barrier\n"
"v_mfma_f32_16x16x32_bf16 v[36:39], a[172:175], v[172:175], v[36:39]\n"
"ds_read_b64_tr_b16 a[160:161], v220 offset:16448\n"
"ds_read_b64_tr_b16 a[162:163], v220 offset:17472\n"
"v_mfma_f32_16x16x32_bf16 v[32:35], a[172:175], v[164:167], v[32:35]\n"
"s_add_u32 m0, s61, 50176\n"
"ds_read_b64_tr_b16 a[164:165], v220 offset:24672\n"
"v_accvgpr_read_b32 v242, a90\n"
"v_mfma_f32_16x16x32_bf16 v[32:35], a[168:171], v[160:163], v[32:35]\n"
"buffer_load_dwordx4 v227, s[44:47], s58 offen lds\n"
"v_mfma_f32_16x16x32_bf16 v[76:79], a[176:179], v[160:163], v[76:79]\n"
"s_waitcnt vmcnt(26)\n"
"ds_read_b64_tr_b16 a[166:167], v220 offset:25696\n"
"ds_read_b64_tr_b16 a[168:169], v220 offset:16456\n"
"v_mfma_f32_16x16x32_bf16 v[76:79], a[180:183], v[164:167], v[76:79]\n"
"s_add_u32 s60, s59, 3244032\n"
"ds_read_b128 v[152:155], v228 offset:51200\n"
"ds_read_b128 v[156:159], v229 offset:51200\n"
"v_mfma_f32_16x16x32_bf16 v[92:95], a[180:183], v[172:175], v[92:95]\n"
"s_add_u32 m0, s62, 35976\n"
"ds_read_b64_tr_b16 a[170:171], v220 offset:17480\n"
"v_accvgpr_read_b32 v243, a91\n"
"v_mfma_f32_16x16x32_bf16 v[92:95], a[176:179], v[168:171], v[92:95]\n"
"buffer_load_dwordx4 v225, s[48:51], s60 offen lds\n"
"v_mfma_f32_16x16x32_bf16 v[108:111], a[176:179], v[176:179], v[108:111]\n"
"ds_read_b64_tr_b16 a[172:173], v220 offset:24680\n"
"v_mul_f32 v236, v214, v236\n"
"v_mfma_f32_16x16x32_bf16 v[108:111], a[180:183], v[180:183], v[108:111]\n"
"v_mul_f32 v237, v214, v237\n"
"v_mul_f32 v238, v214, v238\n"
"v_mfma_f32_16x16x32_bf16 v[124:127], a[180:183], v[188:191], v[124:127]\n"
"ds_read_b64_tr_b16 a[174:175], v220 offset:25704\n"
"v_mul_f32 v239, v214, v239\n"
"v_mfma_f32_16x16x32_bf16 v[124:127], a[176:179], v[184:187], v[124:127]\n"
"buffer_store_dwordx4 v[244:247], v232, s[52:55], s66 offen offset:1024 sc1\n"
"v_mfma_f32_16x16x32_bf16 v[60:63], a[184:187], v[184:187], v[60:63]\n"
"s_add_u32 s58, s68, 768\n"
"ds_read_b64_tr_b16 a[176:177], v220 offset:16704\n"
"v_mul_f32 v240, v214, v240\n"
"v_mfma_f32_16x16x32_bf16 v[60:63], a[188:191], v[188:191], v[60:63]\n"
"s_add_u32 m0, s61, 51200\n"
"v_mul_f32 v241, v214, v241\n"
"v_mul_f32 v242, v214, v242\n"
"v_mfma_f32_16x16x32_bf16 v[56:59], a[188:191], v[180:183], v[56:59]\n"
"s_waitcnt lgkmcnt(4)\n"
"ds_read_b64_tr_b16 a[178:179], v220 offset:17728\n"
"v_mul_f32 v243, v214, v243\n"
"v_mfma_f32_16x16x32_bf16 v[56:59], a[184:187], v[176:179], v[56:59]\n"
"buffer_load_dwordx4 v226, s[44:47], s58 offen lds\n"
"v_mfma_f32_16x16x32_bf16 v[52:55], a[184:187], v[168:171], v[52:55]\n"
"ds_read_b64_tr_b16 a[180:181], v220 offset:24928\n"
"v_cvt_pk_bf16_f32 v236, v236, v237\n"
"v_mfma_f32_16x16x32_bf16 v[52:55], a[188:191], v[172:175], v[52:55]\n"
"v_cvt_pk_bf16_f32 v237, v238, v239\n"
"v_cvt_pk_bf16_f32 v238, v240, v241\n"
"v_mfma_f32_16x16x32_bf16 v[48:51], a[188:191], v[164:167], v[48:51]\n"
"s_add_u32 m0, s61, 52224\n"
"ds_read_b64_tr_b16 a[182:183], v220 offset:25952\n"
"v_cvt_pk_bf16_f32 v239, v242, v243\n"
"v_mfma_f32_16x16x32_bf16 v[48:51], a[184:187], v[160:163], v[48:51]\n"
"s_waitcnt lgkmcnt(7)\n"
"buffer_load_dwordx4 v227, s[44:47], s58 offen lds\n"
"v_mfma_f32_16x16x32_bf16 v[64:67], a[160:163], v[128:131], v[64:67]\n"
"s_waitcnt vmcnt(26)\n"
"ds_read_b64_tr_b16 a[184:185], v220 offset:16712\n"
"v_accvgpr_read_b32 v244, a104\n"
"v_mfma_f32_16x16x32_bf16 v[64:67], a[164:167], v[132:135], v[64:67]\n"
"s_add_u32 s60, s59, 3670016\n"
"ds_read_b128 v[160:163], v228\n"
"ds_read_b128 v[164:167], v229\n"
"v_mfma_f32_16x16x32_bf16 v[80:83], a[164:167], v[140:143], v[80:83]\n"
"s_add_u32 m0, s62, 49344\n"
"ds_read_b64_tr_b16 a[186:187], v220 offset:17736\n"
"v_accvgpr_read_b32 v245, a105\n"
"v_mfma_f32_16x16x32_bf16 v[80:83], a[160:163], v[136:139], v[80:83]\n"
"buffer_load_dwordx4 v225, s[48:51], s60 offen lds\n"
"v_mfma_f32_16x16x32_bf16 v[96:99], a[160:163], v[144:147], v[96:99]\n"
"ds_read_b64_tr_b16 a[188:189], v220 offset:24936\n"
"v_accvgpr_read_b32 v246, a106\n"
"v_mfma_f32_16x16x32_bf16 v[96:99], a[164:167], v[148:151], v[96:99]\n"
"v_accvgpr_read_b32 v247, a107\n"
"v_accvgpr_read_b32 v248, a120\n"
"v_mfma_f32_16x16x32_bf16 v[112:115], a[164:167], v[156:159], v[112:115]\n"
"ds_read_b64_tr_b16 a[190:191], v220 offset:25960\n"
"v_accvgpr_read_b32 v249, a121\n"
"v_mfma_f32_16x16x32_bf16 v[112:115], a[160:163], v[152:155], v[112:115]\n"
"s_waitcnt lgkmcnt(10)\n"
"buffer_store_dwordx4 v[236:239], v232, s[52:55], s66 offen offset:2048 sc1\n"
"v_mfma_f32_16x16x32_bf16 v[12:15], a[168:171], v[152:155], v[12:15]\n"
"s_add_u32 s58, s84, 896\n"
"ds_read_b64_tr_b16 a[160:161], v220 offset:16960\n"
"v_accvgpr_read_b32 v250, a122\n"
"v_mfma_f32_16x16x32_bf16 v[12:15], a[172:175], v[156:159], v[12:15]\n"
"s_add_u32 m0, s61, 0\n"
"v_accvgpr_read_b32 v251, a123\n"
"v_mul_f32 v244, v214, v244\n"
"v_mfma_f32_16x16x32_bf16 v[8:11], a[172:175], v[148:151], v[8:11]\n"
"s_waitcnt lgkmcnt(4)\n"
"ds_read_b64_tr_b16 a[162:163], v220 offset:17984\n"
"v_mul_f32 v245, v214, v245\n"
"v_mfma_f32_16x16x32_bf16 v[8:11], a[168:171], v[144:147], v[8:11]\n"
"buffer_load_dwordx4 v226, s[44:47], s58 offen lds\n"
"v_mfma_f32_16x16x32_bf16 v[4:7], a[168:171], v[136:139], v[4:7]\n"
"ds_read_b64_tr_b16 a[164:165], v220 offset:25184\n"
"v_mul_f32 v246, v214, v246\n"
"v_mfma_f32_16x16x32_bf16 v[4:7], a[172:175], v[140:143], v[4:7]\n"
"v_mul_f32 v247, v214, v247\n"
"v_mul_f32 v248, v214, v248\n"
"v_mfma_f32_16x16x32_bf16 v[0:3], a[172:175], v[132:135], v[0:3]\n"
"s_add_u32 m0, s61, 1024\n"
"ds_read_b64_tr_b16 a[166:167], v220 offset:26208\n"
"v_mul_f32 v249, v214, v249\n"
"v_mfma_f32_16x16x32_bf16 v[0:3], a[168:171], v[128:131], v[0:3]\n"
"s_waitcnt lgkmcnt(10)\n"
"buffer_load_dwordx4 v227, s[44:47], s58 offen lds\n"
"v_mfma_f32_16x16x32_bf16 v[68:71], a[176:179], v[128:131], v[68:71]\n"
"s_waitcnt vmcnt(26)\n"
"ds_read_b64_tr_b16 a[168:169], v220 offset:16968\n"
"v_mul_f32 v250, v214, v250\n"
"v_mfma_f32_16x16x32_bf16 v[68:71], a[180:183], v[132:135], v[68:71]\n"
"s_add_u32 s60, s59, 3702784\n"
"ds_read_b128 v[168:171], v228 offset:2048\n"
"ds_read_b128 v[172:175], v229 offset:2048\n"
"v_mfma_f32_16x16x32_bf16 v[84:87], a[180:183], v[140:143], v[84:87]\n"
"s_add_u32 m0, s62, 50368\n"
"ds_read_b64_tr_b16 a[170:171], v220 offset:17992\n"
"v_mul_f32 v251, v214, v251\n"
"v_mfma_f32_16x16x32_bf16 v[84:87], a[176:179], v[136:139], v[84:87]\n"
"buffer_load_dwordx4 v225, s[48:51], s60 offen lds\n"
"v_mfma_f32_16x16x32_bf16 v[100:103], a[176:179], v[144:147], v[100:103]\n"
"ds_read_b64_tr_b16 a[172:173], v220 offset:25192\n"
"v_cvt_pk_bf16_f32 v244, v244, v245\n"
"v_mfma_f32_16x16x32_bf16 v[100:103], a[180:183], v[148:151], v[100:103]\n"
"v_cvt_pk_bf16_f32 v245, v246, v247\n"
"v_cvt_pk_bf16_f32 v246, v248, v249\n"
"v_mfma_f32_16x16x32_bf16 v[116:119], a[180:183], v[156:159], v[116:119]\n"
"ds_read_b64_tr_b16 a[174:175], v220 offset:26216\n"
"v_cvt_pk_bf16_f32 v247, v250, v251\n"
"v_mfma_f32_16x16x32_bf16 v[116:119], a[176:179], v[152:155], v[116:119]\n"
"s_waitcnt lgkmcnt(10)\n"
"buffer_store_dwordx4 v[244:247], v232, s[52:55], s66 offen offset:3072 sc1\n"
"v_mfma_f32_16x16x32_bf16 v[28:31], a[184:187], v[152:155], v[28:31]\n"
"s_add_u32 s58, s85, 896\n"
"ds_read_b64_tr_b16 a[176:177], v220 offset:17216\n"
"v_accvgpr_read_b32 v236, a12\n"
"v_mfma_f32_16x16x32_bf16 v[28:31], a[188:191], v[156:159], v[28:31]\n"
"s_add_u32 m0, s61, 2048\n"
"v_accvgpr_read_b32 v237, a13\n"
"v_accvgpr_read_b32 v238, a14\n"
"v_mfma_f32_16x16x32_bf16 v[24:27], a[188:191], v[148:151], v[24:27]\n"
"s_waitcnt lgkmcnt(4)\n"
"ds_read_b64_tr_b16 a[178:179], v220 offset:18240\n"
"v_accvgpr_read_b32 v239, a15\n"
"v_mfma_f32_16x16x32_bf16 v[24:27], a[184:187], v[144:147], v[24:27]\n"
"buffer_load_dwordx4 v226, s[44:47], s58 offen lds\n"
"v_mfma_f32_16x16x32_bf16 v[20:23], a[184:187], v[136:139], v[20:23]\n"
"ds_read_b64_tr_b16 a[180:181], v220 offset:25440\n"
"v_accvgpr_read_b32 v240, a28\n"
"v_mfma_f32_16x16x32_bf16 v[20:23], a[188:191], v[140:143], v[20:23]\n"
"v_accvgpr_read_b32 v241, a29\n"
"v_accvgpr_read_b32 v242, a30\n"
"v_mfma_f32_16x16x32_bf16 v[16:19], a[188:191], v[132:135], v[16:19]\n"
"s_add_u32 m0, s61, 3072\n"
"ds_read_b64_tr_b16 a[182:183], v220 offset:26464\n"
"v_accvgpr_read_b32 v243, a31\n"
"v_mfma_f32_16x16x32_bf16 v[16:19], a[184:187], v[128:131], v[16:19]\n"
"s_waitcnt lgkmcnt(10)\n"
"buffer_load_dwordx4 v227, s[44:47], s58 offen lds\n"
"v_mfma_f32_16x16x32_bf16 v[72:75], a[160:163], v[128:131], v[72:75]\n"
"s_waitcnt vmcnt(26)\n"
"ds_read_b64_tr_b16 a[184:185], v220 offset:17224\n"
"v_mul_f32 v236, v215, v236\n"
"v_mfma_f32_16x16x32_bf16 v[72:75], a[164:167], v[132:135], v[72:75]\n"
"s_add_u32 s60, s59, 3735552\n"
"ds_read_b128 v[176:179], v228 offset:16384\n"
"ds_read_b128 v[180:183], v229 offset:16384\n"
"v_mfma_f32_16x16x32_bf16 v[88:91], a[164:167], v[140:143], v[88:91]\n"
"s_add_u32 m0, s62, 51400\n"
"ds_read_b64_tr_b16 a[186:187], v220 offset:18248\n"
"v_mul_f32 v237, v215, v237\n"
"v_mfma_f32_16x16x32_bf16 v[88:91], a[160:163], v[136:139], v[88:91]\n"
"buffer_load_dwordx4 v225, s[48:51], s60 offen lds\n"
"v_mfma_f32_16x16x32_bf16 v[104:107], a[160:163], v[144:147], v[104:107]\n"
"ds_read_b64_tr_b16 a[188:189], v220 offset:25448\n"
"v_mul_f32 v238, v215, v238\n"
"v_mfma_f32_16x16x32_bf16 v[104:107], a[164:167], v[148:151], v[104:107]\n"
"v_mul_f32 v239, v215, v239\n"
"v_mul_f32 v240, v215, v240\n"
"v_mfma_f32_16x16x32_bf16 v[120:123], a[164:167], v[156:159], v[120:123]\n"
"ds_read_b64_tr_b16 a[190:191], v220 offset:26472\n"
"v_mul_f32 v241, v215, v241\n"
"v_mfma_f32_16x16x32_bf16 v[120:123], a[160:163], v[152:155], v[120:123]\n"
"s_waitcnt lgkmcnt(10)\n"
"v_mul_f32 v242, v215, v242\n"
"v_mul_f32 v243, v215, v243\n"
"v_mfma_f32_16x16x32_bf16 v[44:47], a[168:171], v[152:155], v[44:47]\n"
"s_add_u32 s58, s87, 896\n"
"v_cvt_pk_bf16_f32 v236, v236, v237\n"
"v_cvt_pk_bf16_f32 v237, v238, v239\n"
"v_mfma_f32_16x16x32_bf16 v[44:47], a[172:175], v[156:159], v[44:47]\n"
"s_add_u32 m0, s61, 16384\n"
"v_cvt_pk_bf16_f32 v238, v240, v241\n"
"v_cvt_pk_bf16_f32 v239, v242, v243\n"
"v_mfma_f32_16x16x32_bf16 v[40:43], a[172:175], v[148:151], v[40:43]\n"
"s_waitcnt lgkmcnt(3)\n"
"v_accvgpr_read_b32 v244, a44\n"
"v_accvgpr_read_b32 v245, a45\n"
"v_mfma_f32_16x16x32_bf16 v[40:43], a[168:171], v[144:147], v[40:43]\n"
"s_waitcnt vmcnt(26) lgkmcnt(0)\n"
"buffer_load_dwordx4 v226, s[44:47], s58 offen lds\n"
"v_mfma_f32_16x16x32_bf16 v[36:39], a[168:171], v[136:139], v[36:39]\n"
"s_barrier\n"
"v_mfma_f32_16x16x32_bf16 v[36:39], a[172:175], v[140:143], v[36:39]\n"
"ds_read_b64_tr_b16 a[160:161], v221 offset:33024\n"
"ds_read_b64_tr_b16 a[162:163], v221 offset:34048\n"
"v_mfma_f32_16x16x32_bf16 v[32:35], a[172:175], v[132:135], v[32:35]\n"
"s_add_u32 m0, s61, 17408\n"
"ds_read_b64_tr_b16 a[164:165], v221 offset:41248\n"
"v_accvgpr_read_b32 v246, a46\n"
"v_mfma_f32_16x16x32_bf16 v[32:35], a[168:171], v[128:131], v[32:35]\n"
"buffer_load_dwordx4 v227, s[44:47], s58 offen lds\n"
"v_mfma_f32_16x16x32_bf16 v[76:79], a[176:179], v[128:131], v[76:79]\n"
"s_waitcnt vmcnt(26)\n"
"ds_read_b64_tr_b16 a[166:167], v221 offset:42272\n"
"ds_read_b64_tr_b16 a[168:169], v221 offset:33032\n"
"v_mfma_f32_16x16x32_bf16 v[76:79], a[180:183], v[132:135], v[76:79]\n"
"s_add_u32 s60, s59, 3768320\n"
"ds_read_b128 v[184:187], v228 offset:18432\n"
"ds_read_b128 v[188:191], v229 offset:18432\n"
"v_mfma_f32_16x16x32_bf16 v[92:95], a[180:183], v[140:143], v[92:95]\n"
"s_add_u32 m0, s62, 52424\n"
"ds_read_b64_tr_b16 a[170:171], v221 offset:34056\n"
"v_accvgpr_read_b32 v247, a47\n"
"v_mfma_f32_16x16x32_bf16 v[92:95], a[176:179], v[136:139], v[92:95]\n"
"buffer_load_dwordx4 v225, s[48:51], s60 offen lds\n"
"v_mfma_f32_16x16x32_bf16 v[108:111], a[176:179], v[144:147], v[108:111]\n"
"ds_read_b64_tr_b16 a[172:173], v221 offset:41256\n"
"v_accvgpr_read_b32 v248, a60\n"
"v_mfma_f32_16x16x32_bf16 v[108:111], a[180:183], v[148:151], v[108:111]\n"
"v_accvgpr_read_b32 v249, a61\n"
"v_accvgpr_read_b32 v250, a62\n"
"v_mfma_f32_16x16x32_bf16 v[124:127], a[180:183], v[156:159], v[124:127]\n"
"s_add_u32 s66, s65, 12288\n"
"ds_read_b64_tr_b16 a[174:175], v221 offset:42280\n"
"v_accvgpr_read_b32 v251, a63\n"
"v_mfma_f32_16x16x32_bf16 v[124:127], a[176:179], v[152:155], v[124:127]\n"
"buffer_store_dwordx4 v[236:239], v232, s[52:55], s66 offen sc1\n"
"v_mfma_f32_16x16x32_bf16 v[60:63], a[184:187], v[152:155], v[60:63]\n"
"s_add_u32 s58, s68, 896\n"
"ds_read_b64_tr_b16 a[176:177], v221 offset:33280\n"
"v_mul_f32 v244, v215, v244\n"
"v_mfma_f32_16x16x32_bf16 v[60:63], a[188:191], v[156:159], v[60:63]\n"
"s_add_u32 m0, s61, 18432\n"
"v_mul_f32 v245, v215, v245\n"
"v_mul_f32 v246, v215, v246\n"
"v_mfma_f32_16x16x32_bf16 v[56:59], a[188:191], v[148:151], v[56:59]\n"
"s_waitcnt lgkmcnt(4)\n"
"ds_read_b64_tr_b16 a[178:179], v221 offset:34304\n"
"v_mul_f32 v247, v215, v247\n"
"v_mfma_f32_16x16x32_bf16 v[56:59], a[184:187], v[144:147], v[56:59]\n"
"buffer_load_dwordx4 v226, s[44:47], s58 offen lds\n"
"v_mfma_f32_16x16x32_bf16 v[52:55], a[184:187], v[136:139], v[52:55]\n"
"ds_read_b64_tr_b16 a[180:181], v221 offset:41504\n"
"v_mul_f32 v248, v215, v248\n"
"v_mfma_f32_16x16x32_bf16 v[52:55], a[188:191], v[140:143], v[52:55]\n"
"v_mul_f32 v249, v215, v249\n"
"v_mul_f32 v250, v215, v250\n"
"v_mfma_f32_16x16x32_bf16 v[48:51], a[188:191], v[132:135], v[48:51]\n"
"s_add_u32 m0, s61, 19456\n"
"ds_read_b64_tr_b16 a[182:183], v221 offset:42528\n"
"v_mul_f32 v251, v215, v251\n"
"v_mfma_f32_16x16x32_bf16 v[48:51], a[184:187], v[128:131], v[48:51]\n"
"s_waitcnt lgkmcnt(7)\n"
"buffer_load_dwordx4 v227, s[44:47], s58 offen lds\n"
"v_mfma_f32_16x16x32_bf16 v[64:67], a[160:163], v[160:163], v[64:67]\n"
"s_waitcnt vmcnt(26)\n"
"ds_read_b64_tr_b16 a[184:185], v221 offset:33288\n"
"v_cvt_pk_bf16_f32 v244, v244, v245\n"
"v_mfma_f32_16x16x32_bf16 v[64:67], a[164:167], v[164:167], v[64:67]\n"
"ds_read_b128 v[128:131], v228 offset:32768\n"
"ds_read_b128 v[132:135], v229 offset:32768\n"
"v_mfma_f32_16x16x32_bf16 v[80:83], a[164:167], v[172:175], v[80:83]\n"
"ds_read_b64_tr_b16 a[186:187], v221 offset:34312\n"
"v_cvt_pk_bf16_f32 v245, v246, v247\n"
"v_mfma_f32_16x16x32_bf16 v[80:83], a[160:163], v[168:171], v[80:83]\n"
"v_cvt_pk_bf16_f32 v246, v248, v249\n"
"v_cvt_pk_bf16_f32 v247, v250, v251\n"
"v_mfma_f32_16x16x32_bf16 v[96:99], a[160:163], v[176:179], v[96:99]\n"
"ds_read_b64_tr_b16 a[188:189], v221 offset:41512\n"
"v_accvgpr_read_b32 v236, a76\n"
"v_mfma_f32_16x16x32_bf16 v[96:99], a[164:167], v[180:183], v[96:99]\n"
"v_accvgpr_read_b32 v237, a77\n"
"v_accvgpr_read_b32 v238, a78\n"
"v_mfma_f32_16x16x32_bf16 v[112:115], a[164:167], v[188:191], v[112:115]\n"
"ds_read_b64_tr_b16 a[190:191], v221 offset:42536\n"
"v_accvgpr_read_b32 v239, a79\n"
"v_mfma_f32_16x16x32_bf16 v[112:115], a[160:163], v[184:187], v[112:115]\n"
"s_waitcnt lgkmcnt(10)\n"
"buffer_store_dwordx4 v[244:247], v232, s[52:55], s66 offen offset:1024 sc1\n"
"v_mfma_f32_16x16x32_bf16 v[12:15], a[168:171], v[184:187], v[12:15]\n"
"s_add_u32 s58, s84, 1024\n"
"ds_read_b64_tr_b16 a[160:161], v221 offset:33536\n"
"v_accvgpr_read_b32 v240, a92\n"
"v_mfma_f32_16x16x32_bf16 v[12:15], a[172:175], v[188:191], v[12:15]\n"
"s_add_u32 m0, s61, 32768\n"
"v_accvgpr_read_b32 v241, a93\n"
"v_accvgpr_read_b32 v242, a94\n"
"v_mfma_f32_16x16x32_bf16 v[8:11], a[172:175], v[180:183], v[8:11]\n"
"s_waitcnt lgkmcnt(4)\n"
"ds_read_b64_tr_b16 a[162:163], v221 offset:34560\n"
"v_accvgpr_read_b32 v243, a95\n"
"v_mfma_f32_16x16x32_bf16 v[8:11], a[168:171], v[176:179], v[8:11]\n"
"buffer_load_dwordx4 v226, s[44:47], s58 offen lds\n"
"v_mfma_f32_16x16x32_bf16 v[4:7], a[168:171], v[168:171], v[4:7]\n"
"ds_read_b64_tr_b16 a[164:165], v221 offset:41760\n"
"v_mul_f32 v236, v215, v236\n"
"v_mfma_f32_16x16x32_bf16 v[4:7], a[172:175], v[172:175], v[4:7]\n"
"v_mul_f32 v237, v215, v237\n"
"v_mul_f32 v238, v215, v238\n"
"v_mfma_f32_16x16x32_bf16 v[0:3], a[172:175], v[164:167], v[0:3]\n"
"s_add_u32 m0, s61, 33792\n"
"ds_read_b64_tr_b16 a[166:167], v221 offset:42784\n"
"v_mul_f32 v239, v215, v239\n"
"v_mfma_f32_16x16x32_bf16 v[0:3], a[168:171], v[160:163], v[0:3]\n"
"s_waitcnt lgkmcnt(10)\n"
"buffer_load_dwordx4 v227, s[44:47], s58 offen lds\n"
"v_mfma_f32_16x16x32_bf16 v[68:71], a[176:179], v[160:163], v[68:71]\n"
"s_waitcnt vmcnt(25)\n"
"ds_read_b64_tr_b16 a[168:169], v221 offset:33544\n"
"v_mul_f32 v240, v215, v240\n"
"v_mfma_f32_16x16x32_bf16 v[68:71], a[180:183], v[164:167], v[68:71]\n"
"ds_read_b128 v[136:139], v228 offset:34816\n"
"ds_read_b128 v[140:143], v229 offset:34816\n"
"v_mfma_f32_16x16x32_bf16 v[84:87], a[180:183], v[172:175], v[84:87]\n"
"ds_read_b64_tr_b16 a[170:171], v221 offset:34568\n"
"v_mul_f32 v241, v215, v241\n"
"v_mfma_f32_16x16x32_bf16 v[84:87], a[176:179], v[168:171], v[84:87]\n"
"v_mul_f32 v242, v215, v242\n"
"v_mul_f32 v243, v215, v243\n"
"v_mfma_f32_16x16x32_bf16 v[100:103], a[176:179], v[176:179], v[100:103]\n"
"ds_read_b64_tr_b16 a[172:173], v221 offset:41768\n"
"v_cvt_pk_bf16_f32 v236, v236, v237\n"
"v_mfma_f32_16x16x32_bf16 v[100:103], a[180:183], v[180:183], v[100:103]\n"
"v_cvt_pk_bf16_f32 v237, v238, v239\n"
"v_cvt_pk_bf16_f32 v238, v240, v241\n"
"v_mfma_f32_16x16x32_bf16 v[116:119], a[180:183], v[188:191], v[116:119]\n"
"ds_read_b64_tr_b16 a[174:175], v221 offset:42792\n"
"v_cvt_pk_bf16_f32 v239, v242, v243\n"
"v_mfma_f32_16x16x32_bf16 v[116:119], a[176:179], v[184:187], v[116:119]\n"
"s_waitcnt lgkmcnt(10)\n"
"buffer_store_dwordx4 v[236:239], v232, s[52:55], s66 offen offset:2048 sc1\n"
"v_mfma_f32_16x16x32_bf16 v[28:31], a[184:187], v[184:187], v[28:31]\n"
"s_add_u32 s58, s85, 1024\n"
"ds_read_b64_tr_b16 a[176:177], v221 offset:33792\n"
"v_accvgpr_read_b32 v244, a108\n"
"v_mfma_f32_16x16x32_bf16 v[28:31], a[188:191], v[188:191], v[28:31]\n"
"s_add_u32 m0, s61, 34816\n"
"v_accvgpr_read_b32 v245, a109\n"
"v_accvgpr_read_b32 v246, a110\n"
"v_mfma_f32_16x16x32_bf16 v[24:27], a[188:191], v[180:183], v[24:27]\n"
"s_waitcnt lgkmcnt(4)\n"
"ds_read_b64_tr_b16 a[178:179], v221 offset:34816\n"
"v_accvgpr_read_b32 v247, a111\n"
"v_mfma_f32_16x16x32_bf16 v[24:27], a[184:187], v[176:179], v[24:27]\n"
"buffer_load_dwordx4 v226, s[44:47], s58 offen lds\n"
"v_mfma_f32_16x16x32_bf16 v[20:23], a[184:187], v[168:171], v[20:23]\n"
"ds_read_b64_tr_b16 a[180:181], v221 offset:42016\n"
"v_accvgpr_read_b32 v248, a124\n"
"v_mfma_f32_16x16x32_bf16 v[20:23], a[188:191], v[172:175], v[20:23]\n"
"v_accvgpr_read_b32 v249, a125\n"
"v_accvgpr_read_b32 v250, a126\n"
"v_mfma_f32_16x16x32_bf16 v[16:19], a[188:191], v[164:167], v[16:19]\n"
"s_add_u32 m0, s61, 35840\n"
"ds_read_b64_tr_b16 a[182:183], v221 offset:43040\n"
"v_accvgpr_read_b32 v251, a127\n"
"v_mfma_f32_16x16x32_bf16 v[16:19], a[184:187], v[160:163], v[16:19]\n"
"s_waitcnt lgkmcnt(10)\n"
"buffer_load_dwordx4 v227, s[44:47], s58 offen lds\n"
"v_mfma_f32_16x16x32_bf16 v[72:75], a[160:163], v[160:163], v[72:75]\n"
"s_waitcnt vmcnt(25)\n"
"ds_read_b64_tr_b16 a[184:185], v221 offset:33800\n"
"v_mul_f32 v244, v215, v244\n"
"v_mfma_f32_16x16x32_bf16 v[72:75], a[164:167], v[164:167], v[72:75]\n"
"ds_read_b128 v[144:147], v228 offset:49152\n"
"ds_read_b128 v[148:151], v229 offset:49152\n"
"v_mfma_f32_16x16x32_bf16 v[88:91], a[164:167], v[172:175], v[88:91]\n"
"ds_read_b64_tr_b16 a[186:187], v221 offset:34824\n"
"v_mul_f32 v245, v215, v245\n"
"v_mfma_f32_16x16x32_bf16 v[88:91], a[160:163], v[168:171], v[88:91]\n"
"v_mul_f32 v246, v215, v246\n"
"v_mul_f32 v247, v215, v247\n"
"v_mfma_f32_16x16x32_bf16 v[104:107], a[160:163], v[176:179], v[104:107]\n"
"ds_read_b64_tr_b16 a[188:189], v221 offset:42024\n"
"v_mul_f32 v248, v215, v248\n"
"v_mfma_f32_16x16x32_bf16 v[104:107], a[164:167], v[180:183], v[104:107]\n"
"v_mul_f32 v249, v215, v249\n"
"v_mul_f32 v250, v215, v250\n"
"v_mfma_f32_16x16x32_bf16 v[120:123], a[164:167], v[188:191], v[120:123]\n"
"ds_read_b64_tr_b16 a[190:191], v221 offset:43048\n"
"v_mul_f32 v251, v215, v251\n"
"v_mfma_f32_16x16x32_bf16 v[120:123], a[160:163], v[184:187], v[120:123]\n"
"s_waitcnt lgkmcnt(10)\n"
"v_cvt_pk_bf16_f32 v244, v244, v245\n"
"v_cvt_pk_bf16_f32 v245, v246, v247\n"
"v_mfma_f32_16x16x32_bf16 v[44:47], a[168:171], v[184:187], v[44:47]\n"
"s_add_u32 s58, s87, 1024\n"
"v_cvt_pk_bf16_f32 v246, v248, v249\n"
"v_cvt_pk_bf16_f32 v247, v250, v251\n"
"v_mfma_f32_16x16x32_bf16 v[44:47], a[172:175], v[188:191], v[44:47]\n"
"s_add_u32 m0, s61, 49152\n"
"v_mfma_f32_16x16x32_bf16 v[40:43], a[172:175], v[180:183], v[40:43]\n"
"s_waitcnt lgkmcnt(3)\n"
"v_mfma_f32_16x16x32_bf16 v[40:43], a[168:171], v[176:179], v[40:43]\n"
"s_waitcnt vmcnt(24) lgkmcnt(0)\n"
"buffer_load_dwordx4 v226, s[44:47], s58 offen lds\n"
"v_mfma_f32_16x16x32_bf16 v[36:39], a[168:171], v[168:171], v[36:39]\n"
"s_barrier\n"
"v_mfma_f32_16x16x32_bf16 v[36:39], a[172:175], v[172:175], v[36:39]\n"
"ds_read_b64_tr_b16 a[160:161], v220 offset:32896\n"
"ds_read_b64_tr_b16 a[162:163], v220 offset:33920\n"
"v_mfma_f32_16x16x32_bf16 v[32:35], a[172:175], v[164:167], v[32:35]\n"
"s_add_u32 m0, s61, 50176\n"
"ds_read_b64_tr_b16 a[164:165], v220 offset:41120\n"
"v_mfma_f32_16x16x32_bf16 v[32:35], a[168:171], v[160:163], v[32:35]\n"
"buffer_load_dwordx4 v227, s[44:47], s58 offen lds\n"
"v_mfma_f32_16x16x32_bf16 v[76:79], a[176:179], v[160:163], v[76:79]\n"
"s_waitcnt vmcnt(23)\n"
"ds_read_b64_tr_b16 a[166:167], v220 offset:42144\n"
"ds_read_b64_tr_b16 a[168:169], v220 offset:32904\n"
"v_mfma_f32_16x16x32_bf16 v[76:79], a[180:183], v[164:167], v[76:79]\n"
"s_add_u32 s60, s59, 4194304\n"
"ds_read_b128 v[152:155], v228 offset:51200\n"
"ds_read_b128 v[156:159], v229 offset:51200\n"
"v_mfma_f32_16x16x32_bf16 v[92:95], a[180:183], v[172:175], v[92:95]\n"
"s_add_u32 m0, s62, 65792\n"
"ds_read_b64_tr_b16 a[170:171], v220 offset:33928\n"
"v_mfma_f32_16x16x32_bf16 v[92:95], a[176:179], v[168:171], v[92:95]\n"
"buffer_load_dwordx4 v225, s[48:51], s60 offen lds\n"
"v_mfma_f32_16x16x32_bf16 v[108:111], a[176:179], v[176:179], v[108:111]\n"
"ds_read_b64_tr_b16 a[172:173], v220 offset:41128\n"
"v_mfma_f32_16x16x32_bf16 v[108:111], a[180:183], v[180:183], v[108:111]\n"
"s_add_u32 s60, s59, 4227072\n"
"v_mfma_f32_16x16x32_bf16 v[124:127], a[180:183], v[188:191], v[124:127]\n"
"s_add_u32 m0, s62, 66816\n"
"ds_read_b64_tr_b16 a[174:175], v220 offset:42152\n"
"v_mfma_f32_16x16x32_bf16 v[124:127], a[176:179], v[184:187], v[124:127]\n"
"buffer_load_dwordx4 v225, s[48:51], s60 offen lds\n"
"v_mfma_f32_16x16x32_bf16 v[60:63], a[184:187], v[184:187], v[60:63]\n"
"s_add_u32 s58, s68, 1024\n"
"ds_read_b64_tr_b16 a[176:177], v220 offset:33152\n"
"v_mfma_f32_16x16x32_bf16 v[60:63], a[188:191], v[188:191], v[60:63]\n"
"s_add_u32 m0, s61, 51200\n"
"v_mfma_f32_16x16x32_bf16 v[56:59], a[188:191], v[180:183], v[56:59]\n"
"s_waitcnt lgkmcnt(4)\n"
"ds_read_b64_tr_b16 a[178:179], v220 offset:34176\n"
"v_mfma_f32_16x16x32_bf16 v[56:59], a[184:187], v[176:179], v[56:59]\n"
"buffer_load_dwordx4 v226, s[44:47], s58 offen lds\n"
"v_mfma_f32_16x16x32_bf16 v[52:55], a[184:187], v[168:171], v[52:55]\n"
"ds_read_b64_tr_b16 a[180:181], v220 offset:41376\n"
"v_mfma_f32_16x16x32_bf16 v[52:55], a[188:191], v[172:175], v[52:55]\n"
"v_mfma_f32_16x16x32_bf16 v[48:51], a[188:191], v[164:167], v[48:51]\n"
"s_add_u32 m0, s61, 52224\n"
"ds_read_b64_tr_b16 a[182:183], v220 offset:42400\n"
"v_mfma_f32_16x16x32_bf16 v[48:51], a[184:187], v[160:163], v[48:51]\n"
"s_waitcnt lgkmcnt(7)\n"
"buffer_load_dwordx4 v227, s[44:47], s58 offen lds\n"
"v_mfma_f32_16x16x32_bf16 v[64:67], a[160:163], v[128:131], v[64:67]\n"
"s_waitcnt vmcnt(23)\n"
"ds_read_b64_tr_b16 a[184:185], v220 offset:33160\n"
"v_mfma_f32_16x16x32_bf16 v[64:67], a[164:167], v[132:135], v[64:67]\n"
"s_add_u32 s60, s59, 4259840\n"
"ds_read_b128 v[160:163], v228\n"
"ds_read_b128 v[164:167], v229\n"
"v_mfma_f32_16x16x32_bf16 v[80:83], a[164:167], v[140:143], v[80:83]\n"
"s_add_u32 m0, s62, 67848\n"
"ds_read_b64_tr_b16 a[186:187], v220 offset:34184\n"
"v_mfma_f32_16x16x32_bf16 v[80:83], a[160:163], v[136:139], v[80:83]\n"
"buffer_load_dwordx4 v225, s[48:51], s60 offen lds\n"
"v_mfma_f32_16x16x32_bf16 v[96:99], a[160:163], v[144:147], v[96:99]\n"
"ds_read_b64_tr_b16 a[188:189], v220 offset:41384\n"
"v_mfma_f32_16x16x32_bf16 v[96:99], a[164:167], v[148:151], v[96:99]\n"
"s_add_u32 s36, s36, 262144\n"
"s_addc_u32 s37, s37, 0\n"
"v_mfma_f32_16x16x32_bf16 v[112:115], a[164:167], v[156:159], v[112:115]\n"
"ds_read_b64_tr_b16 a[190:191], v220 offset:42408\n"
"v_mfma_f32_16x16x32_bf16 v[112:115], a[160:163], v[152:155], v[112:115]\n"
"s_waitcnt lgkmcnt(10)\n"
"buffer_store_dwordx4 v[244:247], v232, s[52:55], s66 offen offset:3072 sc1\n"
"v_mfma_f32_16x16x32_bf16 v[12:15], a[168:171], v[152:155], v[12:15]\n"
"s_add_u32 s60, s59, 4292608\n"
"ds_read_b64_tr_b16 a[160:161], v220 offset:33408\n"
"v_mfma_f32_16x16x32_bf16 v[12:15], a[172:175], v[156:159], v[12:15]\n"
"s_add_u32 s40, s40, 262144\n"
"s_addc_u32 s41, s41, 0\n"
"v_mfma_f32_16x16x32_bf16 v[8:11], a[172:175], v[148:151], v[8:11]\n"
"s_add_u32 m0, s62, 68872\n"
"ds_read_b64_tr_b16 a[162:163], v220 offset:34432\n"
"v_mfma_f32_16x16x32_bf16 v[8:11], a[168:171], v[144:147], v[8:11]\n"
"buffer_load_dwordx4 v225, s[48:51], s60 offen lds\n"
"v_mfma_f32_16x16x32_bf16 v[4:7], a[168:171], v[136:139], v[4:7]\n"
"ds_read_b64_tr_b16 a[164:165], v220 offset:41632\n"
"v_mfma_f32_16x16x32_bf16 v[4:7], a[172:175], v[140:143], v[4:7]\n"
"v_mfma_f32_16x16x32_bf16 v[0:3], a[172:175], v[132:135], v[0:3]\n"
"s_add_u32 m0, s62, 0\n"
"ds_read_b64_tr_b16 a[166:167], v220 offset:42656\n"
"v_mfma_f32_16x16x32_bf16 v[0:3], a[168:171], v[128:131], v[0:3]\n"
"s_waitcnt lgkmcnt(10)\n"
"buffer_load_dwordx4 v224, s[40:43], s57 offen nt lds\n"
"v_mfma_f32_16x16x32_bf16 v[68:71], a[176:179], v[128:131], v[68:71]\n"
"s_waitcnt vmcnt(23)\n"
"ds_read_b64_tr_b16 a[168:169], v220 offset:33416\n"
"v_mfma_f32_16x16x32_bf16 v[68:71], a[180:183], v[132:135], v[68:71]\n"
"ds_read_b128 v[168:171], v228 offset:2048\n"
"ds_read_b128 v[172:175], v229 offset:2048\n"
"v_mfma_f32_16x16x32_bf16 v[84:87], a[180:183], v[140:143], v[84:87]\n"
"ds_read_b64_tr_b16 a[170:171], v220 offset:34440\n"
"v_mfma_f32_16x16x32_bf16 v[84:87], a[176:179], v[136:139], v[84:87]\n"
"v_mfma_f32_16x16x32_bf16 v[100:103], a[176:179], v[144:147], v[100:103]\n"
"ds_read_b64_tr_b16 a[172:173], v220 offset:41640\n"
"v_mfma_f32_16x16x32_bf16 v[100:103], a[180:183], v[148:151], v[100:103]\n"
"v_mfma_f32_16x16x32_bf16 v[116:119], a[180:183], v[156:159], v[116:119]\n"
"ds_read_b64_tr_b16 a[174:175], v220 offset:42664\n"
"v_mfma_f32_16x16x32_bf16 v[116:119], a[176:179], v[152:155], v[116:119]\n"
"s_waitcnt lgkmcnt(10)\n"
"buffer_load_dwordx4 v224, s[40:43], s57 offen offset:1024 nt lds\n"
"v_mfma_f32_16x16x32_bf16 v[28:31], a[184:187], v[152:155], v[28:31]\n"
"ds_read_b64_tr_b16 a[176:177], v220 offset:33664\n"
"v_mfma_f32_16x16x32_bf16 v[28:31], a[188:191], v[156:159], v[28:31]\n"
"v_lshrrev_b32 v253, 2, v232\n"
"v_mfma_f32_16x16x32_bf16 v[24:27], a[188:191], v[148:151], v[24:27]\n"
"ds_read_b64_tr_b16 a[178:179], v220 offset:34688\n"
"v_mfma_f32_16x16x32_bf16 v[24:27], a[184:187], v[144:147], v[24:27]\n"
"buffer_load_dword a222, v253, s[96:99], 0 offen sc1\n"
"v_mfma_f32_16x16x32_bf16 v[20:23], a[184:187], v[136:139], v[20:23]\n"
"ds_read_b64_tr_b16 a[180:181], v220 offset:41888\n"
"v_mfma_f32_16x16x32_bf16 v[20:23], a[188:191], v[140:143], v[20:23]\n"
"v_mfma_f32_16x16x32_bf16 v[16:19], a[188:191], v[132:135], v[16:19]\n"
"s_add_u32 m0, s62, 8\n"
"ds_read_b64_tr_b16 a[182:183], v220 offset:42912\n"
"v_mfma_f32_16x16x32_bf16 v[16:19], a[184:187], v[128:131], v[16:19]\n"
"s_waitcnt lgkmcnt(10)\n"
"buffer_load_dwordx4 v224, s[40:43], s57 offen offset:2048 nt lds\n"
"v_mfma_f32_16x16x32_bf16 v[72:75], a[160:163], v[128:131], v[72:75]\n"
"s_waitcnt vmcnt(23)\n"
"ds_read_b64_tr_b16 a[184:185], v220 offset:33672\n"
"v_mfma_f32_16x16x32_bf16 v[72:75], a[164:167], v[132:135], v[72:75]\n"
"ds_read_b128 v[176:179], v228 offset:16384\n"
"ds_read_b128 v[180:183], v229 offset:16384\n"
"v_mfma_f32_16x16x32_bf16 v[88:91], a[164:167], v[140:143], v[88:91]\n"
"ds_read_b64_tr_b16 a[186:187], v220 offset:34696\n"
"v_mfma_f32_16x16x32_bf16 v[88:91], a[160:163], v[136:139], v[88:91]\n"
"v_mfma_f32_16x16x32_bf16 v[104:107], a[160:163], v[144:147], v[104:107]\n"
"ds_read_b64_tr_b16 a[188:189], v220 offset:41896\n"
"v_mfma_f32_16x16x32_bf16 v[104:107], a[164:167], v[148:151], v[104:107]\n"
"v_mfma_f32_16x16x32_bf16 v[120:123], a[164:167], v[156:159], v[120:123]\n"
"ds_read_b64_tr_b16 a[190:191], v220 offset:42920\n"
"v_mfma_f32_16x16x32_bf16 v[120:123], a[160:163], v[152:155], v[120:123]\n"
"s_waitcnt lgkmcnt(10)\n"
"v_mfma_f32_16x16x32_bf16 v[44:47], a[168:171], v[152:155], v[44:47]\n"
"v_mfma_f32_16x16x32_bf16 v[44:47], a[172:175], v[156:159], v[44:47]\n"
"v_mfma_f32_16x16x32_bf16 v[40:43], a[172:175], v[148:151], v[40:43]\n"
"v_mfma_f32_16x16x32_bf16 v[40:43], a[168:171], v[144:147], v[40:43]\n"
"s_waitcnt vmcnt(22) lgkmcnt(0)\n"
"buffer_load_dwordx4 v224, s[40:43], s57 offen offset:3072 nt lds\n"
"v_mfma_f32_16x16x32_bf16 v[36:39], a[168:171], v[136:139], v[36:39]\n"
"s_barrier\n"
"v_mfma_f32_16x16x32_bf16 v[36:39], a[172:175], v[140:143], v[36:39]\n"
"s_sub_u32 s57, s57, 159744\n"
"ds_read_b64_tr_b16 a[160:161], v220 offset:49344\n"
"ds_read_b64_tr_b16 a[162:163], v220 offset:50368\n"
"v_mfma_f32_16x16x32_bf16 v[32:35], a[172:175], v[132:135], v[32:35]\n"
"s_and_b32 s57, s57, 159744\n"
"s_add_u32 m0, s61, 0\n"
"ds_read_b64_tr_b16 a[164:165], v220 offset:57568\n"
"v_mfma_f32_16x16x32_bf16 v[32:35], a[168:171], v[128:131], v[32:35]\n"
"buffer_load_dwordx4 v222, s[36:39], s56 offen nt lds\n"
"v_mfma_f32_16x16x32_bf16 v[76:79], a[176:179], v[128:131], v[76:79]\n"
"s_waitcnt vmcnt(21)\n"
"ds_read_b64_tr_b16 a[166:167], v220 offset:58592\n"
"ds_read_b64_tr_b16 a[168:169], v220 offset:49352\n"
"v_mfma_f32_16x16x32_bf16 v[76:79], a[180:183], v[132:135], v[76:79]\n"
"ds_read_b128 v[184:187], v228 offset:18432\n"
"ds_read_b128 v[188:191], v229 offset:18432\n"
"v_mfma_f32_16x16x32_bf16 v[92:95], a[180:183], v[140:143], v[92:95]\n"
"ds_read_b64_tr_b16 a[170:171], v220 offset:50376\n"
"v_mfma_f32_16x16x32_bf16 v[92:95], a[176:179], v[136:139], v[92:95]\n"
"v_mfma_f32_16x16x32_bf16 v[108:111], a[176:179], v[144:147], v[108:111]\n"
"ds_read_b64_tr_b16 a[172:173], v220 offset:57576\n"
"v_mfma_f32_16x16x32_bf16 v[108:111], a[180:183], v[148:151], v[108:111]\n"
"v_mfma_f32_16x16x32_bf16 v[124:127], a[180:183], v[156:159], v[124:127]\n"
"ds_read_b64_tr_b16 a[174:175], v220 offset:58600\n"
"v_mfma_f32_16x16x32_bf16 v[124:127], a[176:179], v[152:155], v[124:127]\n"
"v_mfma_f32_16x16x32_bf16 v[60:63], a[184:187], v[152:155], v[60:63]\n"
"ds_read_b64_tr_b16 a[176:177], v220 offset:49600\n"
"v_mfma_f32_16x16x32_bf16 v[60:63], a[188:191], v[156:159], v[60:63]\n"
"v_mfma_f32_16x16x32_bf16 v[56:59], a[188:191], v[148:151], v[56:59]\n"
"ds_read_b64_tr_b16 a[178:179], v220 offset:50624\n"
"v_mfma_f32_16x16x32_bf16 v[56:59], a[184:187], v[144:147], v[56:59]\n"
"buffer_load_dwordx4 v222, s[36:39], s56 offen offset:1024 nt lds\n"
"v_mfma_f32_16x16x32_bf16 v[52:55], a[184:187], v[136:139], v[52:55]\n"
"ds_read_b64_tr_b16 a[180:181], v220 offset:57824\n"
"v_mfma_f32_16x16x32_bf16 v[52:55], a[188:191], v[140:143], v[52:55]\n"
"v_mfma_f32_16x16x32_bf16 v[48:51], a[188:191], v[132:135], v[48:51]\n"
"ds_read_b64_tr_b16 a[182:183], v220 offset:58848\n"
"v_mfma_f32_16x16x32_bf16 v[48:51], a[184:187], v[128:131], v[48:51]\n"
"s_waitcnt lgkmcnt(7)\n"
"L_q7%=:\n"
"v_mfma_f32_16x16x32_bf16 v[64:67], a[160:163], v[160:163], v[64:67]\n"
"s_waitcnt vmcnt(16)\n"
"ds_read_b64_tr_b16 a[184:185], v220 offset:49608\n"
"v_accvgpr_write_b32 a228, 0\n"
"v_mfma_f32_16x16x32_bf16 v[64:67], a[164:167], v[164:167], v[64:67]\n"
"ds_read_b128 v[128:131], v228 offset:32768\n"
"ds_read_b128 v[132:135], v229 offset:32768\n"
"v_mfma_f32_16x16x32_bf16 v[80:83], a[164:167], v[172:175], v[80:83]\n"
"ds_read_b64_tr_b16 a[186:187], v220 offset:50632\n"
"v_accvgpr_write_b32 a229, 0\n"
"v_mfma_f32_16x16x32_bf16 v[80:83], a[160:163], v[168:171], v[80:83]\n"
"v_accvgpr_write_b32 a230, 0\n"
"v_accvgpr_write_b32 a231, 0\n"
"v_mfma_f32_16x16x32_bf16 v[96:99], a[160:163], v[176:179], v[96:99]\n"
"ds_read_b64_tr_b16 a[188:189], v220 offset:57832\n"
"v_accvgpr_write_b32 a232, 0\n"
"v_mfma_f32_16x16x32_bf16 v[96:99], a[164:167], v[180:183], v[96:99]\n"
"v_accvgpr_write_b32 a233, 0\n"
"v_accvgpr_write_b32 a234, 0\n"
"v_mfma_f32_16x16x32_bf16 v[112:115], a[164:167], v[188:191], v[112:115]\n"
"ds_read_b64_tr_b16 a[190:191], v220 offset:58856\n"
"v_accvgpr_write_b32 a235, 0\n"
"v_mfma_f32_16x16x32_bf16 v[112:115], a[160:163], v[184:187], v[112:115]\n"
"s_waitcnt lgkmcnt(10)\n"
"v_accvgpr_write_b32 a236, 0\n"
"v_accvgpr_write_b32 a237, 0\n"
"v_mfma_f32_16x16x32_bf16 v[12:15], a[168:171], v[184:187], v[12:15]\n"
"ds_read_b64_tr_b16 a[160:161], v220 offset:49856\n"
"v_accvgpr_write_b32 a238, 0\n"
"v_mfma_f32_16x16x32_bf16 v[12:15], a[172:175], v[188:191], v[12:15]\n"
"v_accvgpr_write_b32 a239, 0\n"
"v_accvgpr_write_b32 a240, 0\n"
"v_mfma_f32_16x16x32_bf16 v[8:11], a[172:175], v[180:183], v[8:11]\n"
"ds_read_b64_tr_b16 a[162:163], v220 offset:50880\n"
"v_accvgpr_write_b32 a241, 0\n"
"v_mfma_f32_16x16x32_bf16 v[8:11], a[168:171], v[176:179], v[8:11]\n"
"buffer_load_dwordx4 v223, s[36:39], s56 offen offset:2048 nt lds\n"
"v_mfma_f32_16x16x32_bf16 v[4:7], a[168:171], v[168:171], v[4:7]\n"
"ds_read_b64_tr_b16 a[164:165], v220 offset:58080\n"
"v_accvgpr_write_b32 a242, 0\n"
"v_mfma_f32_16x16x32_bf16 v[4:7], a[172:175], v[172:175], v[4:7]\n"
"v_accvgpr_write_b32 a243, 0\n"
"v_mfma_f32_16x16x32_bf16 v[0:3], a[172:175], v[164:167], v[0:3]\n"
"ds_read_b64_tr_b16 a[166:167], v220 offset:59104\n"
"v_mfma_f32_16x16x32_bf16 v[0:3], a[168:171], v[160:163], v[0:3]\n"
"s_waitcnt lgkmcnt(10)\n"
"v_mfma_f32_16x16x32_bf16 v[68:71], a[176:179], v[160:163], v[68:71]\n"
"s_waitcnt vmcnt(14)\n"
"ds_read_b64_tr_b16 a[168:169], v220 offset:49864\n"
"v_mfma_f32_16x16x32_bf16 v[68:71], a[180:183], v[164:167], v[68:71]\n"
"ds_read_b128 v[136:139], v228 offset:34816\n"
"ds_read_b128 v[140:143], v229 offset:34816\n"
"v_mfma_f32_16x16x32_bf16 v[84:87], a[180:183], v[172:175], v[84:87]\n"
"ds_read_b64_tr_b16 a[170:171], v220 offset:50888\n"
"v_mfma_f32_16x16x32_bf16 v[84:87], a[176:179], v[168:171], v[84:87]\n"
"v_mfma_f32_16x16x32_bf16 v[100:103], a[176:179], v[176:179], v[100:103]\n"
"ds_read_b64_tr_b16 a[172:173], v220 offset:58088\n"
"v_mfma_f32_16x16x32_bf16 v[100:103], a[180:183], v[180:183], v[100:103]\n"
"v_mfma_f32_16x16x32_bf16 v[116:119], a[180:183], v[188:191], v[116:119]\n"
"ds_read_b64_tr_b16 a[174:175], v220 offset:59112\n"
"v_mfma_f32_16x16x32_bf16 v[116:119], a[176:179], v[184:187], v[116:119]\n"
"s_waitcnt lgkmcnt(10)\n"
"v_mfma_f32_16x16x32_bf16 v[28:31], a[184:187], v[184:187], v[28:31]\n"
"ds_read_b64_tr_b16 a[176:177], v220 offset:50112\n"
"v_mfma_f32_16x16x32_bf16 v[28:31], a[188:191], v[188:191], v[28:31]\n"
"v_mfma_f32_16x16x32_bf16 v[24:27], a[188:191], v[180:183], v[24:27]\n"
"ds_read_b64_tr_b16 a[178:179], v220 offset:51136\n"
"v_mfma_f32_16x16x32_bf16 v[24:27], a[184:187], v[176:179], v[24:27]\n"
"buffer_load_dwordx4 v223, s[36:39], s56 offen offset:3072 nt lds\n"
"v_mfma_f32_16x16x32_bf16 v[20:23], a[184:187], v[168:171], v[20:23]\n"
"s_sub_u32 s56, s56, 159744\n"
"ds_read_b64_tr_b16 a[180:181], v220 offset:58336\n"
"v_mfma_f32_16x16x32_bf16 v[20:23], a[188:191], v[172:175], v[20:23]\n"
"s_and_b32 s56, s56, 159744\n"
"v_mfma_f32_16x16x32_bf16 v[16:19], a[188:191], v[164:167], v[16:19]\n"
"ds_read_b64_tr_b16 a[182:183], v220 offset:59360\n"
"v_mfma_f32_16x16x32_bf16 v[16:19], a[184:187], v[160:163], v[16:19]\n"
"s_waitcnt lgkmcnt(10)\n"
"v_mfma_f32_16x16x32_bf16 v[72:75], a[160:163], v[160:163], v[72:75]\n"
"s_waitcnt vmcnt(11)\n"
"ds_read_b64_tr_b16 a[184:185], v220 offset:50120\n"
"v_mfma_f32_16x16x32_bf16 v[72:75], a[164:167], v[164:167], v[72:75]\n"
"ds_read_b128 v[144:147], v228 offset:49152\n"
"ds_read_b128 v[148:151], v229 offset:49152\n"
"v_mfma_f32_16x16x32_bf16 v[88:91], a[164:167], v[172:175], v[88:91]\n"
"ds_read_b64_tr_b16 a[186:187], v220 offset:51144\n"
"v_mfma_f32_16x16x32_bf16 v[88:91], a[160:163], v[168:171], v[88:91]\n"
"v_mfma_f32_16x16x32_bf16 v[104:107], a[160:163], v[176:179], v[104:107]\n"
"ds_read_b64_tr_b16 a[188:189], v220 offset:58344\n"
"v_mfma_f32_16x16x32_bf16 v[104:107], a[164:167], v[180:183], v[104:107]\n"
"v_mfma_f32_16x16x32_bf16 v[120:123], a[164:167], v[188:191], v[120:123]\n"
"ds_read_b64_tr_b16 a[190:191], v220 offset:59368\n"
"v_mfma_f32_16x16x32_bf16 v[120:123], a[160:163], v[184:187], v[120:123]\n"
"s_waitcnt lgkmcnt(10)\n"
"v_mfma_f32_16x16x32_bf16 v[44:47], a[168:171], v[184:187], v[44:47]\n"
"v_mfma_f32_16x16x32_bf16 v[44:47], a[172:175], v[188:191], v[44:47]\n"
"s_memtime s[76:77]\n"
"v_mfma_f32_16x16x32_bf16 v[40:43], a[172:175], v[180:183], v[40:43]\n"
"v_mfma_f32_16x16x32_bf16 v[40:43], a[168:171], v[176:179], v[40:43]\n"
"s_waitcnt vmcnt(9) lgkmcnt(0)\n"
"v_mfma_f32_16x16x32_bf16 v[36:39], a[168:171], v[168:171], v[36:39]\n"
"s_barrier\n"
"v_mfma_f32_16x16x32_bf16 v[36:39], a[172:175], v[172:175], v[36:39]\n"
"ds_read_b64_tr_b16 a[160:161], v221 offset:33024\n"
"ds_read_b64_tr_b16 a[162:163], v221 offset:34048\n"
"v_mfma_f32_16x16x32_bf16 v[32:35], a[172:175], v[164:167], v[32:35]\n"
"s_add_u32 m0, s61, 16384\n"
"ds_read_b64_tr_b16 a[164:165], v221 offset:41248\n"
"v_mfma_f32_16x16x32_bf16 v[32:35], a[168:171], v[160:163], v[32:35]\n"
"buffer_load_dwordx4 v222, s[36:39], s56 offen nt lds\n"
"v_mfma_f32_16x16x32_bf16 v[76:79], a[176:179], v[160:163], v[76:79]\n"
"s_waitcnt vmcnt(9)\n"
"ds_read_b64_tr_b16 a[166:167], v221 offset:42272\n"
"ds_read_b64_tr_b16 a[168:169], v221 offset:33032\n"
"v_mfma_f32_16x16x32_bf16 v[76:79], a[180:183], v[164:167], v[76:79]\n"
"ds_read_b128 v[152:155], v228 offset:51200\n"
"ds_read_b128 v[156:159], v229 offset:51200\n"
"v_mfma_f32_16x16x32_bf16 v[92:95], a[180:183], v[172:175], v[92:95]\n"
"ds_read_b64_tr_b16 a[170:171], v221 offset:34056\n"
"v_mfma_f32_16x16x32_bf16 v[92:95], a[176:179], v[168:171], v[92:95]\n"
"v_mfma_f32_16x16x32_bf16 v[108:111], a[176:179], v[176:179], v[108:111]\n"
"ds_read_b64_tr_b16 a[172:173], v221 offset:41256\n"
"v_mfma_f32_16x16x32_bf16 v[108:111], a[180:183], v[180:183], v[108:111]\n"
"v_mfma_f32_16x16x32_bf16 v[124:127], a[180:183], v[188:191], v[124:127]\n"
"s_add_u32 s66, s76, 2200\n"
"ds_read_b64_tr_b16 a[174:175], v221 offset:42280\n"
"v_mfma_f32_16x16x32_bf16 v[124:127], a[176:179], v[184:187], v[124:127]\n"
"v_mfma_f32_16x16x32_bf16 v[60:63], a[184:187], v[184:187], v[60:63]\n"
"ds_read_b64_tr_b16 a[176:177], v221 offset:33280\n"
"v_mfma_f32_16x16x32_bf16 v[60:63], a[188:191], v[188:191], v[60:63]\n"
"v_mfma_f32_16x16x32_bf16 v[56:59], a[188:191], v[180:183], v[56:59]\n"
"ds_read_b64_tr_b16 a[178:179], v221 offset:34304\n"
"v_mfma_f32_16x16x32_bf16 v[56:59], a[184:187], v[176:179], v[56:59]\n"
"buffer_load_dwordx4 v222, s[36:39], s56 offen offset:1024 nt lds\n"
"v_mfma_f32_16x16x32_bf16 v[52:55], a[184:187], v[168:171], v[52:55]\n"
"ds_read_b64_tr_b16 a[180:181], v221 offset:41504\n"
"v_mfma_f32_16x16x32_bf16 v[52:55], a[188:191], v[172:175], v[52:55]\n"
"v_mfma_f32_16x16x32_bf16 v[48:51], a[188:191], v[164:167], v[48:51]\n"
"ds_read_b64_tr_b16 a[182:183], v221 offset:42528\n"
"v_mfma_f32_16x16x32_bf16 v[48:51], a[184:187], v[160:163], v[48:51]\n"
"s_waitcnt lgkmcnt(7)\n"
"v_mfma_f32_16x16x32_bf16 v[64:67], a[160:163], v[128:131], v[64:67]\n"
"ds_read_b64_tr_b16 a[184:185], v221 offset:33288\n"
"v_mfma_f32_16x16x32_bf16 v[64:67], a[164:167], v[132:135], v[64:67]\n"
"v_mfma_f32_16x16x32_bf16 v[80:83], a[164:167], v[140:143], v[80:83]\n"
"ds_read_b64_tr_b16 a[186:187], v221 offset:34312\n"
"v_mfma_f32_16x16x32_bf16 v[80:83], a[160:163], v[136:139], v[80:83]\n"
"v_mfma_f32_16x16x32_bf16 v[96:99], a[160:163], v[144:147], v[96:99]\n"
"ds_read_b64_tr_b16 a[188:189], v221 offset:41512\n"
"v_mfma_f32_16x16x32_bf16 v[96:99], a[164:167], v[148:151], v[96:99]\n"
"s_nop 1\n"
"v_mul_f32 v64, s70, v64\n"
"v_mul_f32 v65, s70, v65\n"
"v_mfma_f32_16x16x32_bf16 v[112:115], a[164:167], v[156:159], v[112:115]\n"
"ds_read_b64_tr_b16 a[190:191], v221 offset:42536\n"
"v_mul_f32 v66, s70, v66\n"
"v_mfma_f32_16x16x32_bf16 v[112:115], a[160:163], v[152:155], v[112:115]\n"
"s_waitcnt lgkmcnt(8)\n"
"v_mul_f32 v67, s70, v67\n"
"v_cvt_pk_bf16_f32 v64, v64, v65\n"
"v_mfma_f32_16x16x32_bf16 v[0:3], a[168:171], v[128:131], v[0:3]\n"
"ds_read_b64_tr_b16 a[160:161], v221 offset:33536\n"
"v_cvt_pk_bf16_f32 v65, v66, v67\n"
"v_mfma_f32_16x16x32_bf16 v[0:3], a[172:175], v[132:135], v[0:3]\n"
"v_mul_f32 v80, s70, v80\n"
"v_mul_f32 v81, s70, v81\n"
"v_mfma_f32_16x16x32_bf16 v[4:7], a[172:175], v[140:143], v[4:7]\n"
"ds_read_b64_tr_b16 a[162:163], v221 offset:34560\n"
"v_mul_f32 v82, s70, v82\n"
"v_mfma_f32_16x16x32_bf16 v[4:7], a[168:171], v[136:139], v[4:7]\n"
"buffer_load_dwordx4 v223, s[36:39], s56 offen offset:2048 nt lds\n"
"v_mfma_f32_16x16x32_bf16 v[8:11], a[168:171], v[144:147], v[8:11]\n"
"ds_read_b64_tr_b16 a[164:165], v221 offset:41760\n"
"v_mul_f32 v83, s70, v83\n"
"v_mfma_f32_16x16x32_bf16 v[8:11], a[172:175], v[148:151], v[8:11]\n"
"v_cvt_pk_bf16_f32 v80, v80, v81\n"
"v_cvt_pk_bf16_f32 v81, v82, v83\n"
"v_mfma_f32_16x16x32_bf16 v[12:15], a[172:175], v[156:159], v[12:15]\n"
"ds_read_b64_tr_b16 a[166:167], v221 offset:42784\n"
"v_mul_f32 v96, s70, v96\n"
"v_mfma_f32_16x16x32_bf16 v[12:15], a[168:171], v[152:155], v[12:15]\n"
"s_waitcnt lgkmcnt(8)\n"
"v_mul_f32 v97, s70, v97\n"
"v_mul_f32 v98, s70, v98\n"
"v_mfma_f32_16x16x32_bf16 v[68:71], a[176:179], v[128:131], v[68:71]\n"
"ds_read_b64_tr_b16 a[168:169], v221 offset:33544\n"
"v_mul_f32 v99, s70, v99\n"
"v_mfma_f32_16x16x32_bf16 v[68:71], a[180:183], v[132:135], v[68:71]\n"
"v_cvt_pk_bf16_f32 v96, v96, v97\n"
"v_cvt_pk_bf16_f32 v97, v98, v99\n"
"v_mfma_f32_16x16x32_bf16 v[84:87], a[180:183], v[140:143], v[84:87]\n"
"ds_read_b64_tr_b16 a[170:171], v221 offset:34568\n"
"v_mul_f32 v112, s70, v112\n"
"v_mfma_f32_16x16x32_bf16 v[84:87], a[176:179], v[136:139], v[84:87]\n"
"v_mul_f32 v113, s70, v113\n"
"v_mul_f32 v114, s70, v114\n"
"v_mfma_f32_16x16x32_bf16 v[100:103], a[176:179], v[144:147], v[100:103]\n"
"ds_read_b64_tr_b16 a[172:173], v221 offset:41768\n"
"v_mul_f32 v115, s70, v115\n"
"v_mfma_f32_16x16x32_bf16 v[100:103], a[180:183], v[148:151], v[100:103]\n"
"v_cvt_pk_bf16_f32 v112, v112, v113\n"
"v_cvt_pk_bf16_f32 v113, v114, v115\n"
"v_mfma_f32_16x16x32_bf16 v[116:119], a[180:183], v[156:159], v[116:119]\n"
"ds_read_b64_tr_b16 a[174:175], v221 offset:42792\n"
"v_mul_f32 v0, s70, v0\n"
"v_mfma_f32_16x16x32_bf16 v[116:119], a[176:179], v[152:155], v[116:119]\n"
"s_waitcnt lgkmcnt(8)\n"
"buffer_load_dwordx4 v223, s[36:39], s56 offen offset:3072 nt lds\n"
"v_mfma_f32_16x16x32_bf16 v[16:19], a[184:187], v[128:131], v[16:19]\n"
"s_sub_u32 s56, s56, 159744\n"
"ds_read_b64_tr_b16 a[176:177], v221 offset:33792\n"
"v_mul_f32 v1, s70, v1\n"
"v_mfma_f32_16x16x32_bf16 v[16:19], a[188:191], v[132:135], v[16:19]\n"
"s_and_b32 s56, s56, 159744\n"
"v_mul_f32 v2, s70, v2\n"
"v_mul_f32 v3, s70, v3\n"
"v_mfma_f32_16x16x32_bf16 v[20:23], a[188:191], v[140:143], v[20:23]\n"
"ds_read_b64_tr_b16 a[178:179], v221 offset:34816\n"
"v_cvt_pk_bf16_f32 v66, v0, v1\n"
"v_mfma_f32_16x16x32_bf16 v[20:23], a[184:187], v[136:139], v[20:23]\n"
"v_cvt_pk_bf16_f32 v67, v2, v3\n"
"v_mul_f32 v4, s70, v4\n"
"v_mfma_f32_16x16x32_bf16 v[24:27], a[184:187], v[144:147], v[24:27]\n"
"ds_read_b64_tr_b16 a[180:181], v221 offset:42016\n"
"v_mul_f32 v5, s70, v5\n"
"v_mfma_f32_16x16x32_bf16 v[24:27], a[188:191], v[148:151], v[24:27]\n"
"v_mul_f32 v6, s70, v6\n"
"v_mul_f32 v7, s70, v7\n"
"v_mfma_f32_16x16x32_bf16 v[28:31], a[188:191], v[156:159], v[28:31]\n"
"ds_read_b64_tr_b16 a[182:183], v221 offset:43040\n"
"v_cvt_pk_bf16_f32 v82, v4, v5\n"
"v_mfma_f32_16x16x32_bf16 v[28:31], a[184:187], v[152:155], v[28:31]\n"
"s_waitcnt lgkmcnt(8)\n"
"v_cvt_pk_bf16_f32 v83, v6, v7\n"
"v_mul_f32 v8, s70, v8\n"
"v_mfma_f32_16x16x32_bf16 v[72:75], a[160:163], v[128:131], v[72:75]\n"
"ds_read_b64_tr_b16 a[184:185], v221 offset:33800\n"
"v_mul_f32 v9, s70, v9\n"
"v_mfma_f32_16x16x32_bf16 v[72:75], a[164:167], v[132:135], v[72:75]\n"
"v_mul_f32 v10, s70, v10\n"
"v_mul_f32 v11, s70, v11\n"
"v_mfma_f32_16x16x32_bf16 v[88:91], a[164:167], v[140:143], v[88:91]\n"
"s_add_u32 m0, s62, 16448\n"
"ds_read_b64_tr_b16 a[186:187], v221 offset:34824\n"
"v_cvt_pk_bf16_f32 v98, v8, v9\n"
"v_mfma_f32_16x16x32_bf16 v[88:91], a[160:163], v[136:139], v[88:91]\n"
"buffer_load_dwordx4 v224, s[40:43], s57 offen nt lds\n"
"v_mfma_f32_16x16x32_bf16 v[104:107], a[160:163], v[144:147], v[104:107]\n"
"ds_read_b64_tr_b16 a[188:189], v221 offset:42024\n"
"v_cvt_pk_bf16_f32 v99, v10, v11\n"
"v_mfma_f32_16x16x32_bf16 v[104:107], a[164:167], v[148:151], v[104:107]\n"
"v_mul_f32 v12, s70, v12\n"
"v_mul_f32 v13, s70, v13\n"
"v_mfma_f32_16x16x32_bf16 v[120:123], a[164:167], v[156:159], v[120:123]\n"
"ds_read_b64_tr_b16 a[190:191], v221 offset:43048\n"
"v_mul_f32 v14, s70, v14\n"
"v_mfma_f32_16x16x32_bf16 v[120:123], a[160:163], v[152:155], v[120:123]\n"
"s_waitcnt lgkmcnt(8)\n"
"v_mul_f32 v15, s70, v15\n"
"v_cvt_pk_bf16_f32 v114, v12, v13\n"
"v_mfma_f32_16x16x32_bf16 v[32:35], a[168:171], v[128:131], v[32:35]\n"
"v_cvt_pk_bf16_f32 v115, v14, v15\n"
"v_mul_f32 v68, s70, v68\n"
"v_mfma_f32_16x16x32_bf16 v[32:35], a[172:175], v[132:135], v[32:35]\n"
"v_mul_f32 v69, s70, v69\n"
"v_mul_f32 v70, s70, v70\n"
"v_mfma_f32_16x16x32_bf16 v[36:39], a[172:175], v[140:143], v[36:39]\n"
"v_mul_f32 v71, s70, v71\n"
"v_cvt_pk_bf16_f32 v68, v68, v69\n"
"v_mfma_f32_16x16x32_bf16 v[36:39], a[168:171], v[136:139], v[36:39]\n"
"buffer_load_dwordx4 v224, s[40:43], s57 offen offset:1024 nt lds\n"
"v_mfma_f32_16x16x32_bf16 v[40:43], a[168:171], v[144:147], v[40:43]\n"
"v_cvt_pk_bf16_f32 v69, v70, v71\n"
"v_mul_f32 v84, s70, v84\n"
"v_mfma_f32_16x16x32_bf16 v[40:43], a[172:175], v[148:151], v[40:43]\n"
"v_mul_f32 v85, s70, v85\n"
"v_mul_f32 v86, s70, v86\n"
"v_mfma_f32_16x16x32_bf16 v[44:47], a[172:175], v[156:159], v[44:47]\n"
"v_mul_f32 v87, s70, v87\n"
"v_cvt_pk_bf16_f32 v84, v84, v85\n"
"v_mfma_f32_16x16x32_bf16 v[44:47], a[168:171], v[152:155], v[44:47]\n"
"s_waitcnt lgkmcnt(4)\n"
"v_cvt_pk_bf16_f32 v85, v86, v87\n"
"v_mul_f32 v100, s70, v100\n"
"v_mfma_f32_16x16x32_bf16 v[76:79], a[176:179], v[128:131], v[76:79]\n"
"v_mul_f32 v101, s70, v101\n"
"v_mul_f32 v102, s70, v102\n"
"v_mfma_f32_16x16x32_bf16 v[76:79], a[180:183], v[132:135], v[76:79]\n"
"v_mul_f32 v103, s70, v103\n"
"v_cvt_pk_bf16_f32 v100, v100, v101\n"
"v_mfma_f32_16x16x32_bf16 v[92:95], a[180:183], v[140:143], v[92:95]\n"
"s_add_u32 m0, s62, 16456\n"
"v_cvt_pk_bf16_f32 v101, v102, v103\n"
"v_mul_f32 v116, s70, v116\n"
"v_mfma_f32_16x16x32_bf16 v[92:95], a[176:179], v[136:139], v[92:95]\n"
"buffer_load_dwordx4 v224, s[40:43], s57 offen offset:2048 nt lds\n"
"v_mfma_f32_16x16x32_bf16 v[108:111], a[176:179], v[144:147], v[108:111]\n"
"v_mul_f32 v117, s70, v117\n"
"v_mul_f32 v118, s70, v118\n"
"v_mfma_f32_16x16x32_bf16 v[108:111], a[180:183], v[148:151], v[108:111]\n"
"v_mul_f32 v119, s70, v119\n"
"v_cvt_pk_bf16_f32 v116, v116, v117\n"
"v_mfma_f32_16x16x32_bf16 v[124:127], a[180:183], v[156:159], v[124:127]\n"
"v_cvt_pk_bf16_f32 v117, v118, v119\n"
"v_mul_f32 v16, s70, v16\n"
"v_mfma_f32_16x16x32_bf16 v[124:127], a[176:179], v[152:155], v[124:127]\n"
"s_waitcnt lgkmcnt(0)\n"
"v_mul_f32 v17, s70, v17\n"
"v_mul_f32 v18, s70, v18\n"
"v_mfma_f32_16x16x32_bf16 v[48:51], a[184:187], v[128:131], v[48:51]\n"
"v_mul_f32 v19, s70, v19\n"
"v_cvt_pk_bf16_f32 v70, v16, v17\n"
"v_mfma_f32_16x16x32_bf16 v[48:51], a[188:191], v[132:135], v[48:51]\n"
"v_cvt_pk_bf16_f32 v71, v18, v19\n"
"v_mul_f32 v20, s70, v20\n"
"v_mfma_f32_16x16x32_bf16 v[52:55], a[188:191], v[140:143], v[52:55]\n"
"v_mul_f32 v21, s70, v21\n"
"v_mul_f32 v22, s70, v22\n"
"v_mfma_f32_16x16x32_bf16 v[52:55], a[184:187], v[136:139], v[52:55]\n"
"buffer_load_dwordx4 v224, s[40:43], s57 offen offset:3072 nt lds\n"
"v_mfma_f32_16x16x32_bf16 v[56:59], a[184:187], v[144:147], v[56:59]\n"
"s_sub_u32 s57, s57, 159744\n"
"v_mul_f32 v23, s70, v23\n"
"v_cvt_pk_bf16_f32 v86, v20, v21\n"
"v_mfma_f32_16x16x32_bf16 v[56:59], a[188:191], v[148:151], v[56:59]\n"
"s_and_b32 s57, s57, 159744\n"
"v_cvt_pk_bf16_f32 v87, v22, v23\n"
"v_mul_f32 v24, s70, v24\n"
"v_mfma_f32_16x16x32_bf16 v[60:63], a[188:191], v[156:159], v[60:63]\n"
"v_mul_f32 v25, s70, v25\n"
"v_mul_f32 v26, s70, v26\n"
"v_mfma_f32_16x16x32_bf16 v[60:63], a[184:187], v[152:155], v[60:63]\n"
"v_mul_f32 v27, s70, v27\n"
"v_cvt_pk_bf16_f32 v102, v24, v25\n"
"v_cvt_pk_bf16_f32 v103, v26, v27\n"
"v_pk_mul_f32 v[28:29], s[70:71], v[28:29]\n"
"v_pk_mul_f32 v[30:31], s[70:71], v[30:31]\n"
"v_cvt_pk_bf16_f32 v118, v28, v29\n"
"v_cvt_pk_bf16_f32 v119, v30, v31\n"
"v_pk_mul_f32 v[72:73], s[70:71], v[72:73]\n"
"v_pk_mul_f32 v[74:75], s[70:71], v[74:75]\n"
"v_cvt_pk_bf16_f32 v72, v72, v73\n"
"v_cvt_pk_bf16_f32 v73, v74, v75\n"
"v_pk_mul_f32 v[88:89], s[70:71], v[88:89]\n"
"v_pk_mul_f32 v[90:91], s[70:71], v[90:91]\n"
"v_cvt_pk_bf16_f32 v88, v88, v89\n"
"v_cvt_pk_bf16_f32 v89, v90, v91\n"
"v_pk_mul_f32 v[104:105], s[70:71], v[104:105]\n"
"v_pk_mul_f32 v[106:107], s[70:71], v[106:107]\n"
"v_cvt_pk_bf16_f32 v104, v104, v105\n"
"v_cvt_pk_bf16_f32 v105, v106, v107\n"
"v_pk_mul_f32 v[120:121], s[70:71], v[120:121]\n"
"v_pk_mul_f32 v[122:123], s[70:71], v[122:123]\n"
"v_cvt_pk_bf16_f32 v120, v120, v121\n"
"v_cvt_pk_bf16_f32 v121, v122, v123\n"
"v_pk_mul_f32 v[32:33], s[70:71], v[32:33]\n"
"v_pk_mul_f32 v[34:35], s[70:71], v[34:35]\n"
"v_cvt_pk_bf16_f32 v74, v32, v33\n"
"v_cvt_pk_bf16_f32 v75, v34, v35\n"
"v_pk_mul_f32 v[36:37], s[70:71], v[36:37]\n"
"v_pk_mul_f32 v[38:39], s[70:71], v[38:39]\n"
"v_cvt_pk_bf16_f32 v90, v36, v37\n"
"v_cvt_pk_bf16_f32 v91, v38, v39\n"
"s_waitcnt vmcnt(8)\n"
"s_barrier\n"
"ds_read_b128 a[128:131], v216\n"
"ds_read_b128 a[132:135], v217\n"
"ds_read_b128 a[136:139], v218\n"
"ds_read_b128 a[140:143], v219\n"
"ds_read_b128 a[144:147], v216 offset:1024\n"
"ds_read_b128 a[148:151], v217 offset:1024\n"
"ds_read_b128 a[152:155], v218 offset:1024\n"
"ds_read_b128 a[156:159], v219 offset:1024\n"
"v_pk_mul_f32 v[40:41], s[70:71], v[40:41]\n"
"v_pk_mul_f32 v[42:43], s[70:71], v[42:43]\n"
"v_cvt_pk_bf16_f32 v106, v40, v41\n"
"v_cvt_pk_bf16_f32 v107, v42, v43\n"
"v_pk_mul_f32 v[44:45], s[70:71], v[44:45]\n"
"v_pk_mul_f32 v[46:47], s[70:71], v[46:47]\n"
"v_cvt_pk_bf16_f32 v122, v44, v45\n"
"v_cvt_pk_bf16_f32 v123, v46, v47\n"
"v_pk_mul_f32 v[76:77], s[70:71], v[76:77]\n"
"v_pk_mul_f32 v[78:79], s[70:71], v[78:79]\n"
"v_cvt_pk_bf16_f32 v76, v76, v77\n"
"v_cvt_pk_bf16_f32 v77, v78, v79\n"
"v_pk_mul_f32 v[92:93], s[70:71], v[92:93]\n"
"v_pk_mul_f32 v[94:95], s[70:71], v[94:95]\n"
"v_cvt_pk_bf16_f32 v92, v92, v93\n"
"v_cvt_pk_bf16_f32 v93, v94, v95\n"
"v_pk_mul_f32 v[108:109], s[70:71], v[108:109]\n"
"v_pk_mul_f32 v[110:111], s[70:71], v[110:111]\n"
"v_cvt_pk_bf16_f32 v108, v108, v109\n"
"v_cvt_pk_bf16_f32 v109, v110, v111\n"
"v_pk_mul_f32 v[124:125], s[70:71], v[124:125]\n"
"v_pk_mul_f32 v[126:127], s[70:71], v[126:127]\n"
"v_cvt_pk_bf16_f32 v124, v124, v125\n"
"v_cvt_pk_bf16_f32 v125, v126, v127\n"
"v_pk_mul_f32 v[48:49], s[70:71], v[48:49]\n"
"v_pk_mul_f32 v[50:51], s[70:71], v[50:51]\n"
"v_cvt_pk_bf16_f32 v78, v48, v49\n"
"v_cvt_pk_bf16_f32 v79, v50, v51\n"
"v_pk_mul_f32 v[52:53], s[70:71], v[52:53]\n"
"v_pk_mul_f32 v[54:55], s[70:71], v[54:55]\n"
"v_cvt_pk_bf16_f32 v94, v52, v53\n"
"v_cvt_pk_bf16_f32 v95, v54, v55\n"
"v_pk_mul_f32 v[56:57], s[70:71], v[56:57]\n"
"v_pk_mul_f32 v[58:59], s[70:71], v[58:59]\n"
"v_cvt_pk_bf16_f32 v110, v56, v57\n"
"v_cvt_pk_bf16_f32 v111, v58, v59\n"
"v_pk_mul_f32 v[60:61], s[70:71], v[60:61]\n"
"v_pk_mul_f32 v[62:63], s[70:71], v[62:63]\n"
"v_cvt_pk_bf16_f32 v126, v60, v61\n"
"v_cvt_pk_bf16_f32 v127, v62, v63\n"
"s_waitcnt lgkmcnt(7)\n"
"v_mfma_f32_16x16x32_bf16 v[0:3], a[128:131], v[64:67], 0\n"
"s_waitcnt lgkmcnt(6)\n"
"s_add_u32 m0, s61, 32768\n"
"v_mfma_f32_16x16x32_bf16 v[0:3], a[132:135], v[68:71], v[0:3]\n"
"s_waitcnt lgkmcnt(5)\n"
"buffer_load_dwordx4 v222, s[36:39], s56 offen nt lds\n"
"v_mfma_f32_16x16x32_bf16 v[0:3], a[136:139], v[72:75], v[0:3]\n"
"s_waitcnt lgkmcnt(4)\n"
"v_mfma_f32_16x16x32_bf16 v[0:3], a[140:143], v[76:79], v[0:3]\n"
"v_mfma_f32_16x16x32_bf16 v[4:7], a[140:143], v[92:95], 0\n"
"buffer_load_dwordx4 v222, s[36:39], s56 offen offset:1024 nt lds\n"
"v_mfma_f32_16x16x32_bf16 v[4:7], a[136:139], v[88:91], v[4:7]\n"
"v_mfma_f32_16x16x32_bf16 v[4:7], a[132:135], v[84:87], v[4:7]\n"
"v_mfma_f32_16x16x32_bf16 v[4:7], a[128:131], v[80:83], v[4:7]\n"
"buffer_load_dwordx4 v223, s[36:39], s56 offen offset:2048 nt lds\n"
"v_mfma_f32_16x16x32_bf16 v[8:11], a[128:131], v[96:99], 0\n"
"v_mfma_f32_16x16x32_bf16 v[8:11], a[132:135], v[100:103], v[8:11]\n"
"v_max3_f32 v236, v0, v1, v2\n"
"v_mfma_f32_16x16x32_bf16 v[8:11], a[136:139], v[104:107], v[8:11]\n"
"v_mfma_f32_16x16x32_bf16 v[8:11], a[140:143], v[108:111], v[8:11]\n"
"s_nop 1\n"
"v_max3_f32 v237, v4, v5, v6\n"
"v_max_f32 v236, v236, v3\n"
"v_mfma_f32_16x16x32_bf16 v[12:15], a[140:143], v[124:127], 0\n"
"v_max_f32 v237, v237, v7\n"
"v_mov_b32 v240, v236\n"
"v_mfma_f32_16x16x32_bf16 v[12:15], a[136:139], v[120:123], v[12:15]\n"
"v_mov_b32 v241, v237\n"
"v_permlane32_swap_b32 v236, v240\n"
"v_mfma_f32_16x16x32_bf16 v[12:15], a[132:135], v[116:119], v[12:15]\n"
"v_permlane32_swap_b32 v237, v241\n"
"v_max_f32 v236, v236, v240\n"
"v_mfma_f32_16x16x32_bf16 v[12:15], a[128:131], v[112:115], v[12:15]\n"
"s_waitcnt lgkmcnt(3)\n"
"ds_read_b128 a[128:131], v216 offset:8192\n"
"v_max_f32 v237, v237, v241\n"
"v_mfma_f32_16x16x32_bf16 v[28:31], a[144:147], v[112:115], 0\n"
"s_waitcnt lgkmcnt(3)\n"
"v_mov_b32 v240, v236\n"
"v_mov_b32 v241, v237\n"
"v_mfma_f32_16x16x32_bf16 v[28:31], a[148:151], v[116:119], v[28:31]\n"
"s_waitcnt lgkmcnt(2)\n"
"ds_read_b128 a[132:135], v217 offset:8192\n"
"v_permlane16_swap_b32 v236, v240\n"
"v_mfma_f32_16x16x32_bf16 v[28:31], a[152:155], v[120:123], v[28:31]\n"
"s_waitcnt lgkmcnt(2)\n"
"v_permlane16_swap_b32 v237, v241\n"
"v_max_f32 v236, v236, v240\n"
"v_mfma_f32_16x16x32_bf16 v[28:31], a[156:159], v[124:127], v[28:31]\n"
"ds_read_b128 a[136:139], v218 offset:8192\n"
"v_max_f32 v237, v237, v241\n"
"v_mfma_f32_16x16x32_bf16 v[24:27], a[156:159], v[108:111], 0\n"
"v_mul_f32 v192, -1.0, v236\n"
"v_mul_f32 v196, -1.0, v237\n"
"v_mfma_f32_16x16x32_bf16 v[24:27], a[152:155], v[104:107], v[24:27]\n"
"v_mul_f32 v193, -1.0, v236\n"
"v_mul_f32 v197, -1.0, v237\n"
"v_mfma_f32_16x16x32_bf16 v[24:27], a[148:151], v[100:103], v[24:27]\n"
"v_mul_f32 v194, -1.0, v236\n"
"v_mul_f32 v198, -1.0, v237\n"
"v_mfma_f32_16x16x32_bf16 v[24:27], a[144:147], v[96:99], v[24:27]\n"
"ds_read_b128 a[140:143], v219 offset:8192\n"
"v_mul_f32 v195, -1.0, v236\n"
"v_mfma_f32_16x16x32_bf16 v[20:23], a[144:147], v[80:83], 0\n"
"v_mul_f32 v199, -1.0, v237\n"
"v_max3_f32 v238, v8, v9, v10\n"
"v_mfma_f32_16x16x32_bf16 v[20:23], a[148:151], v[84:87], v[20:23]\n"
"v_max3_f32 v239, v12, v13, v14\n"
"v_max_f32 v238, v238, v11\n"
"v_mfma_f32_16x16x32_bf16 v[20:23], a[152:155], v[88:91], v[20:23]\n"
"v_max_f32 v239, v239, v15\n"
"v_mov_b32 v242, v238\n"
"v_mfma_f32_16x16x32_bf16 v[20:23], a[156:159], v[92:95], v[20:23]\n"
"v_mov_b32 v243, v239\n"
"v_permlane32_swap_b32 v238, v242\n"
"v_mfma_f32_16x16x32_bf16 v[16:19], a[156:159], v[76:79], 0\n"
"v_permlane32_swap_b32 v239, v243\n"
"v_max_f32 v238, v238, v242\n"
"v_mfma_f32_16x16x32_bf16 v[16:19], a[152:155], v[72:75], v[16:19]\n"
"v_max_f32 v239, v239, v243\n"
"v_mov_b32 v242, v238\n"
"v_mfma_f32_16x16x32_bf16 v[16:19], a[148:151], v[68:71], v[16:19]\n"
"v_mov_b32 v243, v239\n"
"v_permlane16_swap_b32 v238, v242\n"
"v_mfma_f32_16x16x32_bf16 v[16:19], a[144:147], v[64:67], v[16:19]\n"
"s_waitcnt lgkmcnt(2)\n"
"ds_read_b128 a[144:147], v216 offset:9216\n"
"v_permlane16_swap_b32 v239, v243\n"
"v_mfma_f32_16x16x32_bf16 v[32:35], a[128:131], v[64:67], v[192:195]\n"
"v_max_f32 v238, v238, v242\n"
"v_max_f32 v239, v239, v243\n"
"v_mfma_f32_16x16x32_bf16 v[32:35], a[132:135], v[68:71], v[32:35]\n"
"s_waitcnt lgkmcnt(1)\n"
"ds_read_b128 a[148:151], v217 offset:9216\n"
"v_mul_f32 v200, -1.0, v238\n"
"v_mfma_f32_16x16x32_bf16 v[32:35], a[136:139], v[72:75], v[32:35]\n"
"v_mul_f32 v204, -1.0, v239\n"
"v_mul_f32 v201, -1.0, v238\n"
"v_mfma_f32_16x16x32_bf16 v[32:35], a[140:143], v[76:79], v[32:35]\n"
"ds_read_b128 a[152:155], v218 offset:9216\n"
"v_mul_f32 v205, -1.0, v239\n"
"v_mfma_f32_16x16x32_bf16 v[36:39], a[140:143], v[92:95], v[196:199]\n"
"v_mul_f32 v202, -1.0, v238\n"
"v_mul_f32 v206, -1.0, v239\n"
"v_mfma_f32_16x16x32_bf16 v[36:39], a[136:139], v[88:91], v[36:39]\n"
"v_mul_f32 v203, -1.0, v238\n"
"v_mul_f32 v207, -1.0, v239\n"
"v_mfma_f32_16x16x32_bf16 v[36:39], a[132:135], v[84:87], v[36:39]\n"
"v_add_f32 v0, v0, v192\n"
"v_add_f32 v1, v1, v192\n"
"v_mfma_f32_16x16x32_bf16 v[36:39], a[128:131], v[80:83], v[36:39]\n"
"ds_read_b128 a[156:159], v219 offset:9216\n"
"v_add_f32 v2, v2, v192\n"
"v_mfma_f32_16x16x32_bf16 v[40:43], a[128:131], v[96:99], v[200:203]\n"
"v_add_f32 v3, v3, v192\n"
"v_add_f32 v4, v4, v196\n"
"v_mfma_f32_16x16x32_bf16 v[40:43], a[132:135], v[100:103], v[40:43]\n"
"v_add_f32 v5, v5, v196\n"
"v_add_f32 v6, v6, v196\n"
"v_mfma_f32_16x16x32_bf16 v[40:43], a[136:139], v[104:107], v[40:43]\n"
"v_add_f32 v7, v7, v196\n"
"v_add_f32 v8, v8, v200\n"
"v_mfma_f32_16x16x32_bf16 v[40:43], a[140:143], v[108:111], v[40:43]\n"
"v_add_f32 v9, v9, v200\n"
"v_add_f32 v10, v10, v200\n"
"v_mfma_f32_16x16x32_bf16 v[44:47], a[140:143], v[124:127], v[204:207]\n"
"v_add_f32 v11, v11, v200\n"
"v_add_f32 v12, v12, v204\n"
"v_mfma_f32_16x16x32_bf16 v[44:47], a[136:139], v[120:123], v[44:47]\n"
"v_add_f32 v13, v13, v204\n"
"v_add_f32 v14, v14, v204\n"
"v_mfma_f32_16x16x32_bf16 v[44:47], a[132:135], v[116:119], v[44:47]\n"
"v_add_f32 v15, v15, v204\n"
"v_add_f32 v16, v16, v192\n"
"v_mfma_f32_16x16x32_bf16 v[44:47], a[128:131], v[112:115], v[44:47]\n"
"s_waitcnt lgkmcnt(2)\n"
"v_add_f32 v17, v17, v192\n"
"v_add_f32 v18, v18, v192\n"
"v_mfma_f32_16x16x32_bf16 v[60:63], a[144:147], v[112:115], v[204:207]\n"
"v_add_f32 v19, v19, v192\n"
"v_add_f32 v20, v20, v196\n"
"v_mfma_f32_16x16x32_bf16 v[60:63], a[148:151], v[116:119], v[60:63]\n"
"s_waitcnt lgkmcnt(0)\n"
"v_add_f32 v21, v21, v196\n"
"v_add_f32 v22, v22, v196\n"
"v_mfma_f32_16x16x32_bf16 v[60:63], a[152:155], v[120:123], v[60:63]\n"
"v_add_f32 v23, v23, v196\n"
"v_add_f32 v24, v24, v200\n"
"v_mfma_f32_16x16x32_bf16 v[60:63], a[156:159], v[124:127], v[60:63]\n"
"buffer_load_dwordx4 v223, s[36:39], s56 offen offset:3072 nt lds\n"
"v_mfma_f32_16x16x32_bf16 v[56:59], a[156:159], v[108:111], v[200:203]\n"
"s_sub_u32 s56, s56, 159744\n"
"v_add_f32 v25, v25, v200\n"
"v_add_f32 v26, v26, v200\n"
"v_mfma_f32_16x16x32_bf16 v[56:59], a[152:155], v[104:107], v[56:59]\n"
"s_and_b32 s56, s56, 159744\n"
"v_add_f32 v27, v27, v200\n"
"v_add_f32 v28, v28, v204\n"
"v_mfma_f32_16x16x32_bf16 v[56:59], a[148:151], v[100:103], v[56:59]\n"
"v_add_f32 v29, v29, v204\n"
"v_add_f32 v30, v30, v204\n"
"v_mfma_f32_16x16x32_bf16 v[56:59], a[144:147], v[96:99], v[56:59]\n"
"v_add_f32 v31, v31, v204\n"
"v_mfma_f32_16x16x32_bf16 v[52:55], a[144:147], v[80:83], v[196:199]\n"
"v_exp_f32 v0, v0\n"
"v_mfma_f32_16x16x32_bf16 v[52:55], a[148:151], v[84:87], v[52:55]\n"
"v_exp_f32 v1, v1\n"
"v_mfma_f32_16x16x32_bf16 v[52:55], a[152:155], v[88:91], v[52:55]\n"
"v_exp_f32 v2, v2\n"
"v_mfma_f32_16x16x32_bf16 v[52:55], a[156:159], v[92:95], v[52:55]\n"
"v_exp_f32 v3, v3\n"
"v_mfma_f32_16x16x32_bf16 v[48:51], a[156:159], v[76:79], v[192:195]\n"
"v_cvt_pk_bf16_f32 v128, v0, v1\n"
"v_cvt_pk_bf16_f32 v129, v2, v3\n"
"v_mfma_f32_16x16x32_bf16 v[48:51], a[152:155], v[72:75], v[48:51]\n"
"v_exp_f32 v4, v4\n"
"v_mfma_f32_16x16x32_bf16 v[48:51], a[148:151], v[68:71], v[48:51]\n"
"v_exp_f32 v5, v5\n"
"v_mfma_f32_16x16x32_bf16 v[48:51], a[144:147], v[64:67], v[48:51]\n"
"v_exp_f32 v6, v6\n"
"v_exp_f32 v7, v7\n"
"v_cvt_pk_bf16_f32 v136, v4, v5\n"
"v_exp_f32 v8, v8\n"
"v_cvt_pk_bf16_f32 v137, v6, v7\n"
"v_exp_f32 v9, v9\n"
"v_exp_f32 v10, v10\n"
"v_cvt_pk_bf16_f32 v144, v8, v9\n"
"v_exp_f32 v11, v11\n"
"v_exp_f32 v12, v12\n"
"v_cvt_pk_bf16_f32 v145, v10, v11\n"
"v_exp_f32 v13, v13\n"
"v_exp_f32 v14, v14\n"
"v_cvt_pk_bf16_f32 v152, v12, v13\n"
"v_exp_f32 v15, v15\n"
"v_exp_f32 v28, v28\n"
"v_cvt_pk_bf16_f32 v153, v14, v15\n"
"v_exp_f32 v29, v29\n"
"v_exp_f32 v30, v30\n"
"v_cvt_pk_bf16_f32 v154, v28, v29\n"
"v_exp_f32 v31, v31\n"
"v_exp_f32 v24, v24\n"
"v_cvt_pk_bf16_f32 v155, v30, v31\n"
"v_exp_f32 v25, v25\n"
"v_exp_f32 v26, v26\n"
"v_cvt_pk_bf16_f32 v146, v24, v25\n"
"v_exp_f32 v27, v27\n"
"v_exp_f32 v20, v20\n"
"v_cvt_pk_bf16_f32 v147, v26, v27\n"
"v_exp_f32 v21, v21\n"
"v_exp_f32 v22, v22\n"
"v_cvt_pk_bf16_f32 v138, v20, v21\n"
"s_waitcnt vmcnt(8) lgkmcnt(0)\n"
"s_barrier\n"
"ds_read_b128 a[128:131], v216 offset:16384\n"
"ds_read_b128 a[132:135], v217 offset:16384\n"
"ds_read_b128 a[136:139], v218 offset:16384\n"
"ds_read_b128 a[140:143], v219 offset:16384\n"
"ds_read_b128 a[144:147], v216 offset:17408\n"
"ds_read_b128 a[148:151], v217 offset:17408\n"
"ds_read_b128 a[152:155], v218 offset:17408\n"
"ds_read_b128 a[156:159], v219 offset:17408\n"
"v_exp_f32 v23, v23\n"
"v_exp_f32 v16, v16\n"
"v_cvt_pk_bf16_f32 v139, v22, v23\n"
"v_exp_f32 v17, v17\n"
"v_exp_f32 v18, v18\n"
"v_cvt_pk_bf16_f32 v130, v16, v17\n"
"v_exp_f32 v19, v19\n"
"v_exp_f32 v32, v32\n"
"v_cvt_pk_bf16_f32 v131, v18, v19\n"
"v_exp_f32 v33, v33\n"
"v_exp_f32 v34, v34\n"
"v_cvt_pk_bf16_f32 v132, v32, v33\n"
"v_exp_f32 v35, v35\n"
"v_exp_f32 v36, v36\n"
"v_cvt_pk_bf16_f32 v133, v34, v35\n"
"v_exp_f32 v37, v37\n"
"v_exp_f32 v38, v38\n"
"v_cvt_pk_bf16_f32 v140, v36, v37\n"
"v_exp_f32 v39, v39\n"
"s_nop 0\n"
"v_cvt_pk_bf16_f32 v141, v38, v39\n"
"s_waitcnt lgkmcnt(4)\n"
"v_mfma_f32_16x16x32_bf16 v[0:3], a[128:131], v[64:67], v[192:195]\n"
"v_exp_f32 v40, v40\n"
"v_mfma_f32_16x16x32_bf16 v[0:3], a[132:135], v[68:71], v[0:3]\n"
"s_waitcnt lgkmcnt(4)\n"
"v_exp_f32 v41, v41\n"
"v_mfma_f32_16x16x32_bf16 v[0:3], a[136:139], v[72:75], v[0:3]\n"
"v_exp_f32 v42, v42\n"
"v_mfma_f32_16x16x32_bf16 v[0:3], a[140:143], v[76:79], v[0:3]\n"
"v_exp_f32 v43, v43\n"
"v_mfma_f32_16x16x32_bf16 v[4:7], a[140:143], v[92:95], v[196:199]\n"
"s_add_u32 m0, s61, 49152\n"
"v_cvt_pk_bf16_f32 v148, v40, v41\n"
"v_cvt_pk_bf16_f32 v149, v42, v43\n"
"v_mfma_f32_16x16x32_bf16 v[4:7], a[136:139], v[88:91], v[4:7]\n"
"buffer_load_dwordx4 v222, s[36:39], s56 offen nt lds\n"
"v_mfma_f32_16x16x32_bf16 v[4:7], a[132:135], v[84:87], v[4:7]\n"
"v_exp_f32 v44, v44\n"
"v_mfma_f32_16x16x32_bf16 v[4:7], a[128:131], v[80:83], v[4:7]\n"
"v_exp_f32 v45, v45\n"
"v_mfma_f32_16x16x32_bf16 v[8:11], a[128:131], v[96:99], v[200:203]\n"
"s_memtime s[76:77]\n"
"v_exp_f32 v46, v46\n"
"v_mfma_f32_16x16x32_bf16 v[8:11], a[132:135], v[100:103], v[8:11]\n"
"s_memrealtime s[34:35]\n"
"v_exp_f32 v47, v47\n"
"v_mfma_f32_16x16x32_bf16 v[8:11], a[136:139], v[104:107], v[8:11]\n"
"v_exp_f32 v60, v60\n"
"v_mfma_f32_16x16x32_bf16 v[8:11], a[140:143], v[108:111], v[8:11]\n"
"v_exp_f32 v61, v61\n"
"v_mfma_f32_16x16x32_bf16 v[12:15], a[140:143], v[124:127], v[204:207]\n"
"v_exp_f32 v62, v62\n"
"v_mfma_f32_16x16x32_bf16 v[12:15], a[136:139], v[120:123], v[12:15]\n"
"v_exp_f32 v63, v63\n"
"v_mfma_f32_16x16x32_bf16 v[12:15], a[132:135], v[116:119], v[12:15]\n"
"v_exp_f32 v56, v56\n"
"v_mfma_f32_16x16x32_bf16 v[12:15], a[128:131], v[112:115], v[12:15]\n"
"s_waitcnt lgkmcnt(0)\n"
"ds_read_b128 a[128:131], v216 offset:24576\n"
"v_cvt_pk_bf16_f32 v156, v44, v45\n"
"v_mfma_f32_16x16x32_bf16 v[28:31], a[144:147], v[112:115], v[204:207]\n"
"v_exp_f32 v57, v57\n"
"v_mfma_f32_16x16x32_bf16 v[28:31], a[148:151], v[116:119], v[28:31]\n"
"ds_read_b128 a[132:135], v217 offset:24576\n"
"v_cvt_pk_bf16_f32 v157, v46, v47\n"
"v_mfma_f32_16x16x32_bf16 v[28:31], a[152:155], v[120:123], v[28:31]\n"
"v_exp_f32 v58, v58\n"
"v_mfma_f32_16x16x32_bf16 v[28:31], a[156:159], v[124:127], v[28:31]\n"
"s_sub_u32 s66, s76, s66\n"
"ds_read_b128 a[136:139], v218 offset:24576\n"
"v_cvt_pk_bf16_f32 v158, v60, v61\n"
"v_mfma_f32_16x16x32_bf16 v[24:27], a[156:159], v[108:111], v[200:203]\n"
"s_mul_i32 s75, s63, 0\n"
"v_exp_f32 v59, v59\n"
"v_mfma_f32_16x16x32_bf16 v[24:27], a[152:155], v[104:107], v[24:27]\n"
"s_add_u32 s75, s75, 0\n"
"buffer_load_dwordx4 v222, s[36:39], s56 offen offset:1024 nt lds\n"
"v_mfma_f32_16x16x32_bf16 v[24:27], a[148:151], v[100:103], v[24:27]\n"
"s_mul_i32 s58, s63, -1450\n"
"v_cvt_pk_bf16_f32 v159, v62, v63\n"
"v_cvt_pk_bf16_f32 v150, v56, v57\n"
"v_mfma_f32_16x16x32_bf16 v[24:27], a[144:147], v[96:99], v[24:27]\n"
"s_add_u32 s58, s58, 4200\n"
"ds_read_b128 a[140:143], v219 offset:24576\n"
"v_cvt_pk_bf16_f32 v151, v58, v59\n"
"v_mfma_f32_16x16x32_bf16 v[20:23], a[144:147], v[80:83], v[196:199]\n"
"s_cmp_gt_i32 s66, s58\n"
"v_exp_f32 v52, v52\n"
"v_mfma_f32_16x16x32_bf16 v[20:23], a[148:151], v[84:87], v[20:23]\n"
"s_cselect_b32 s75, 148, s75\n"
"v_exp_f32 v53, v53\n"
"v_mfma_f32_16x16x32_bf16 v[20:23], a[152:155], v[88:91], v[20:23]\n"
"s_add_u32 s73, s34, s75\n"
"v_exp_f32 v54, v54\n"
"v_mfma_f32_16x16x32_bf16 v[20:23], a[156:159], v[92:95], v[20:23]\n"
"s_mov_b32 s66, s76\n"
"v_exp_f32 v55, v55\n"
"v_mfma_f32_16x16x32_bf16 v[16:19], a[156:159], v[76:79], v[192:195]\n"
"v_exp_f32 v48, v48\n"
"v_mfma_f32_16x16x32_bf16 v[16:19], a[152:155], v[72:75], v[16:19]\n"
"v_exp_f32 v49, v49\n"
"v_mfma_f32_16x16x32_bf16 v[16:19], a[148:151], v[68:71], v[16:19]\n"
"v_exp_f32 v50, v50\n"
"v_mfma_f32_16x16x32_bf16 v[16:19], a[144:147], v[64:67], v[16:19]\n"
"s_waitcnt lgkmcnt(2)\n"
"ds_read_b128 a[144:147], v216 offset:25600\n"
"v_cvt_pk_bf16_f32 v142, v52, v53\n"
"v_mfma_f32_16x16x32_bf16 v[32:35], a[128:131], v[64:67], v[192:195]\n"
"v_exp_f32 v51, v51\n"
"v_mfma_f32_16x16x32_bf16 v[32:35], a[132:135], v[68:71], v[32:35]\n"
"s_waitcnt lgkmcnt(1)\n"
"ds_read_b128 a[148:151], v217 offset:25600\n"
"v_cvt_pk_bf16_f32 v143, v54, v55\n"
"v_mfma_f32_16x16x32_bf16 v[32:35], a[136:139], v[72:75], v[32:35]\n"
"v_exp_f32 v0, v0\n"
"v_mfma_f32_16x16x32_bf16 v[32:35], a[140:143], v[76:79], v[32:35]\n"
"ds_read_b128 a[152:155], v218 offset:25600\n"
"v_cvt_pk_bf16_f32 v134, v48, v49\n"
"v_mfma_f32_16x16x32_bf16 v[36:39], a[140:143], v[92:95], v[196:199]\n"
"v_exp_f32 v1, v1\n"
"v_mfma_f32_16x16x32_bf16 v[36:39], a[136:139], v[88:91], v[36:39]\n"
"buffer_load_dwordx4 v223, s[36:39], s56 offen offset:2048 nt lds\n"
"v_mfma_f32_16x16x32_bf16 v[36:39], a[132:135], v[84:87], v[36:39]\n"
"v_exp_f32 v2, v2\n"
"v_mfma_f32_16x16x32_bf16 v[36:39], a[128:131], v[80:83], v[36:39]\n"
"ds_read_b128 a[156:159], v219 offset:25600\n"
"v_cvt_pk_bf16_f32 v135, v50, v51\n"
"v_mfma_f32_16x16x32_bf16 v[40:43], a[128:131], v[96:99], v[200:203]\n"
"v_exp_f32 v3, v3\n"
"v_mfma_f32_16x16x32_bf16 v[40:43], a[132:135], v[100:103], v[40:43]\n"
"ds_read_b64_tr_b16 a[160:161], v220\n"
"v_cvt_pk_bf16_f32 v160, v0, v1\n"
"v_mfma_f32_16x16x32_bf16 v[40:43], a[136:139], v[104:107], v[40:43]\n"
"v_exp_f32 v4, v4\n"
"v_mfma_f32_16x16x32_bf16 v[40:43], a[140:143], v[108:111], v[40:43]\n"
"ds_read_b64_tr_b16 a[162:163], v220 offset:1024\n"
"v_cvt_pk_bf16_f32 v161, v2, v3\n"
"v_mfma_f32_16x16x32_bf16 v[44:47], a[140:143], v[124:127], v[204:207]\n"
"v_exp_f32 v5, v5\n"
"v_mfma_f32_16x16x32_bf16 v[44:47], a[136:139], v[120:123], v[44:47]\n"
"ds_read_b64_tr_b16 a[164:165], v220 offset:8224\n"
"ds_read_b64_tr_b16 a[166:167], v220 offset:9248\n"
"v_mfma_f32_16x16x32_bf16 v[44:47], a[132:135], v[116:119], v[44:47]\n"
"v_exp_f32 v6, v6\n"
"v_mfma_f32_16x16x32_bf16 v[44:47], a[128:131], v[112:115], v[44:47]\n"
"s_waitcnt lgkmcnt(6)\n"
"v_exp_f32 v7, v7\n"
"v_mfma_f32_16x16x32_bf16 v[60:63], a[144:147], v[112:115], v[204:207]\n"
"v_exp_f32 v8, v8\n"
"v_mfma_f32_16x16x32_bf16 v[60:63], a[148:151], v[116:119], v[60:63]\n"
"s_waitcnt lgkmcnt(4)\n"
"ds_read_b64_tr_b16 a[168:169], v220 offset:8\n"
"v_cvt_pk_bf16_f32 v168, v4, v5\n"
"v_mfma_f32_16x16x32_bf16 v[60:63], a[152:155], v[120:123], v[60:63]\n"
"v_exp_f32 v9, v9\n"
"v_mfma_f32_16x16x32_bf16 v[60:63], a[156:159], v[124:127], v[60:63]\n"
"ds_read_b64_tr_b16 a[170:171], v220 offset:1032\n"
"v_cvt_pk_bf16_f32 v169, v6, v7\n"
"v_mfma_f32_16x16x32_bf16 v[56:59], a[156:159], v[108:111], v[200:203]\n"
"v_exp_f32 v10, v10\n"
"v_mfma_f32_16x16x32_bf16 v[56:59], a[152:155], v[104:107], v[56:59]\n"
"ds_read_b64_tr_b16 a[172:173], v220 offset:8232\n"
"ds_read_b64_tr_b16 a[174:175], v220 offset:9256\n"
"v_mfma_f32_16x16x32_bf16 v[56:59], a[148:151], v[100:103], v[56:59]\n"
"v_exp_f32 v11, v11\n"
"v_mfma_f32_16x16x32_bf16 v[56:59], a[144:147], v[96:99], v[56:59]\n"
"buffer_load_dwordx4 v223, s[36:39], s56 offen offset:3072 nt lds\n"
"v_mfma_f32_16x16x32_bf16 v[52:55], a[144:147], v[80:83], v[196:199]\n"
"s_sub_u32 s56, s56, 159744\n"
"v_exp_f32 v12, v12\n"
"v_mfma_f32_16x16x32_bf16 v[52:55], a[148:151], v[84:87], v[52:55]\n"
"s_and_b32 s56, s56, 159744\n"
"v_exp_f32 v13, v13\n"
"v_mfma_f32_16x16x32_bf16 v[52:55], a[152:155], v[88:91], v[52:55]\n"
"v_exp_f32 v14, v14\n"
"v_mfma_f32_16x16x32_bf16 v[52:55], a[156:159], v[92:95], v[52:55]\n"
"ds_read_b64_tr_b16 a[176:177], v220 offset:256\n"
"v_cvt_pk_bf16_f32 v176, v8, v9\n"
"v_mfma_f32_16x16x32_bf16 v[48:51], a[156:159], v[76:79], v[192:195]\n"
"v_exp_f32 v15, v15\n"
"v_mfma_f32_16x16x32_bf16 v[48:51], a[152:155], v[72:75], v[48:51]\n"
"ds_read_b64_tr_b16 a[178:179], v220 offset:1280\n"
"v_cvt_pk_bf16_f32 v177, v10, v11\n"
"v_mfma_f32_16x16x32_bf16 v[48:51], a[148:151], v[68:71], v[48:51]\n"
"v_exp_f32 v28, v28\n"
"v_mfma_f32_16x16x32_bf16 v[48:51], a[144:147], v[64:67], v[48:51]\n"
"ds_read_b64_tr_b16 a[180:181], v220 offset:8480\n"
"ds_read_b64_tr_b16 a[182:183], v220 offset:9504\n"
"v_smfmac_f32_16x16x64_bf16 a[228:231], a[200:203], v[128:135], v208\n"
"v_cvt_pk_bf16_f32 v184, v12, v13\n"
"v_smfmac_f32_16x16x64_bf16 a[232:235], a[200:203], v[136:143], v208\n"
"v_cvt_pk_bf16_f32 v185, v14, v15\n"
"v_smfmac_f32_16x16x64_bf16 a[236:239], a[200:203], v[144:151], v208\n"
"v_smfmac_f32_16x16x64_bf16 a[240:243], a[200:203], v[152:159], v208\n"
"s_waitcnt lgkmcnt(8)\n"
"ds_read_b64_tr_b16 a[184:185], v220 offset:264\n"
"v_mfma_f32_16x16x32_bf16 a[0:3], a[160:163], v[128:131], 0\n"
"v_exp_f32 v29, v29\n"
"v_mfma_f32_16x16x32_bf16 a[0:3], a[164:167], v[132:135], a[0:3]\n"
"ds_read_b64_tr_b16 a[186:187], v220 offset:1288\n"
"v_cvt_pk_bf16_f32 v186, v28, v29\n"
"v_mfma_f32_16x16x32_bf16 a[4:7], a[164:167], v[140:143], 0\n"
"s_add_u32 m0, s62, 32896\n"
"v_exp_f32 v30, v30\n"
"v_mfma_f32_16x16x32_bf16 a[4:7], a[160:163], v[136:139], a[4:7]\n"
"buffer_load_dwordx4 v224, s[40:43], s57 offen nt lds\n"
"v_mfma_f32_16x16x32_bf16 a[8:11], a[160:163], v[144:147], 0\n"
"v_exp_f32 v31, v31\n"
"v_mfma_f32_16x16x32_bf16 a[8:11], a[164:167], v[148:151], a[8:11]\n"
"ds_read_b64_tr_b16 a[188:189], v220 offset:8488\n"
"ds_read_b64_tr_b16 a[190:191], v220 offset:9512\n"
"v_mfma_f32_16x16x32_bf16 a[12:15], a[164:167], v[156:159], 0\n"
"v_exp_f32 v24, v24\n"
"v_mfma_f32_16x16x32_bf16 a[12:15], a[160:163], v[152:155], a[12:15]\n"
"s_waitcnt lgkmcnt(8)\n"
"v_exp_f32 v25, v25\n"
"v_mfma_f32_16x16x32_bf16 a[28:31], a[168:171], v[152:155], 0\n"
"v_exp_f32 v26, v26\n"
"v_mfma_f32_16x16x32_bf16 a[28:31], a[172:175], v[156:159], a[28:31]\n"
"ds_read_b64_tr_b16 a[160:161], v220 offset:512\n"
"v_cvt_pk_bf16_f32 v187, v30, v31\n"
"v_mfma_f32_16x16x32_bf16 a[24:27], a[172:175], v[148:151], 0\n"
"v_exp_f32 v27, v27\n"
"v_mfma_f32_16x16x32_bf16 a[24:27], a[168:171], v[144:147], a[24:27]\n"
"ds_read_b64_tr_b16 a[162:163], v220 offset:1536\n"
"v_cvt_pk_bf16_f32 v178, v24, v25\n"
"v_mfma_f32_16x16x32_bf16 a[20:23], a[168:171], v[136:139], 0\n"
"v_exp_f32 v20, v20\n"
"v_mfma_f32_16x16x32_bf16 a[20:23], a[172:175], v[140:143], a[20:23]\n"
"ds_read_b64_tr_b16 a[164:165], v220 offset:8736\n"
"ds_read_b64_tr_b16 a[166:167], v220 offset:9760\n"
"v_mfma_f32_16x16x32_bf16 a[16:19], a[172:175], v[132:135], 0\n"
"v_exp_f32 v21, v21\n"
"v_mfma_f32_16x16x32_bf16 a[16:19], a[168:171], v[128:131], a[16:19]\n"
"s_waitcnt lgkmcnt(8)\n"
"v_exp_f32 v22, v22\n"
"v_mfma_f32_16x16x32_bf16 a[32:35], a[176:179], v[128:131], 0\n"
"v_exp_f32 v23, v23\n"
"v_mfma_f32_16x16x32_bf16 a[32:35], a[180:183], v[132:135], a[32:35]\n"
"ds_read_b64_tr_b16 a[168:169], v220 offset:520\n"
"v_cvt_pk_bf16_f32 v179, v26, v27\n"
"v_mfma_f32_16x16x32_bf16 a[36:39], a[180:183], v[140:143], 0\n"
"v_exp_f32 v16, v16\n"
"v_mfma_f32_16x16x32_bf16 a[36:39], a[176:179], v[136:139], a[36:39]\n"
"buffer_load_dwordx4 v224, s[40:43], s57 offen offset:1024 nt lds\n"
"v_mfma_f32_16x16x32_bf16 a[40:43], a[176:179], v[144:147], 0\n"
"v_exp_f32 v17, v17\n"
"v_mfma_f32_16x16x32_bf16 a[40:43], a[180:183], v[148:151], a[40:43]\n"
"ds_read_b64_tr_b16 a[170:171], v220 offset:1544\n"
"v_cvt_pk_bf16_f32 v170, v20, v21\n"
"v_mfma_f32_16x16x32_bf16 a[44:47], a[180:183], v[156:159], 0\n"
"s_mov_b32 s54, 3145728\n"
"v_exp_f32 v18, v18\n"
"v_mfma_f32_16x16x32_bf16 a[44:47], a[176:179], v[152:155], a[44:47]\n"
"s_waitcnt lgkmcnt(6)\n"
"ds_read_b64_tr_b16 a[172:173], v220 offset:8744\n"
"ds_read_b64_tr_b16 a[174:175], v220 offset:9768\n"
"v_mfma_f32_16x16x32_bf16 a[60:63], a[184:187], v[152:155], 0\n"
"s_mov_b32 s65, s67\n"
"v_exp_f32 v19, v19\n"
"v_mfma_f32_16x16x32_bf16 a[60:63], a[188:191], v[156:159], a[60:63]\n"
"s_add_u32 s67, s67, 65536\n"
"v_exp_f32 v32, v32\n"
"v_mfma_f32_16x16x32_bf16 a[56:59], a[188:191], v[148:151], 0\n"
"v_exp_f32 v33, v33\n"
"v_mfma_f32_16x16x32_bf16 a[56:59], a[184:187], v[144:147], a[56:59]\n"
"ds_read_b64_tr_b16 a[176:177], v220 offset:768\n"
"v_cvt_pk_bf16_f32 v171, v22, v23\n"
"v_mfma_f32_16x16x32_bf16 a[52:55], a[184:187], v[136:139], 0\n"
"v_exp_f32 v34, v34\n"
"v_mfma_f32_16x16x32_bf16 a[52:55], a[188:191], v[140:143], a[52:55]\n"
"ds_read_b64_tr_b16 a[178:179], v220 offset:1792\n"
"v_cvt_pk_bf16_f32 v162, v16, v17\n"
"v_mfma_f32_16x16x32_bf16 a[48:51], a[188:191], v[132:135], 0\n"
"v_exp_f32 v35, v35\n"
"v_mfma_f32_16x16x32_bf16 a[48:51], a[184:187], v[128:131], a[48:51]\n"
"s_waitcnt lgkmcnt(6)\n"
"ds_read_b64_tr_b16 a[180:181], v220 offset:8992\n"
"ds_read_b64_tr_b16 a[182:183], v220 offset:10016\n"
"v_mfma_f32_16x16x32_bf16 a[64:67], a[160:163], v[128:131], 0\n"
"s_cmp_eq_u32 s63, 0\n"
"s_cselect_b32 s98, 0, s79\n"
"v_accvgpr_read_b32 v253, a220\n"
"v_cvt_pk_bf16_f32 v163, v18, v19\n"
"v_mfma_f32_16x16x32_bf16 a[64:67], a[164:167], v[132:135], a[64:67]\n"
"v_exp_f32 v36, v36\n"
"v_mfma_f32_16x16x32_bf16 a[68:71], a[164:167], v[140:143], 0\n"
"s_add_u32 m0, s62, 32904\n"
"v_exp_f32 v37, v37\n"
"v_mfma_f32_16x16x32_bf16 a[68:71], a[160:163], v[136:139], a[68:71]\n"
"buffer_load_dwordx4 v224, s[40:43], s57 offen offset:2048 nt lds\n"
"v_mfma_f32_16x16x32_bf16 a[72:75], a[160:163], v[144:147], 0\n"
"v_exp_f32 v38, v38\n"
"v_mfma_f32_16x16x32_bf16 a[72:75], a[164:167], v[148:151], a[72:75]\n"
"ds_read_b64_tr_b16 a[184:185], v220 offset:776\n"
"v_cvt_pk_bf16_f32 v164, v32, v33\n"
"v_mfma_f32_16x16x32_bf16 a[76:79], a[164:167], v[156:159], 0\n"
"v_exp_f32 v39, v39\n"
"v_mfma_f32_16x16x32_bf16 a[76:79], a[160:163], v[152:155], a[76:79]\n"
"s_waitcnt lgkmcnt(5)\n"
"ds_read_b64_tr_b16 a[186:187], v220 offset:1800\n"
"v_cvt_pk_bf16_f32 v165, v34, v35\n"
"v_mfma_f32_16x16x32_bf16 a[92:95], a[168:171], v[152:155], 0\n"
"v_cvt_pk_bf16_f32 v172, v36, v37\n"
"v_cvt_pk_bf16_f32 v173, v38, v39\n"
"v_mfma_f32_16x16x32_bf16 a[92:95], a[172:175], v[156:159], a[92:95]\n"
"ds_read_b64_tr_b16 a[188:189], v220 offset:9000\n"
"ds_read_b64_tr_b16 a[190:191], v220 offset:10024\n"
"v_mfma_f32_16x16x32_bf16 a[88:91], a[172:175], v[148:151], 0\n"
"v_exp_f32 v40, v40\n"
"v_mfma_f32_16x16x32_bf16 a[88:91], a[168:171], v[144:147], a[88:91]\n"
"v_exp_f32 v41, v41\n"
"v_mfma_f32_16x16x32_bf16 a[84:87], a[168:171], v[136:139], 0\n"
"v_exp_f32 v42, v42\n"
"v_mfma_f32_16x16x32_bf16 a[84:87], a[172:175], v[140:143], a[84:87]\n"
"v_exp_f32 v43, v43\n"
"v_mfma_f32_16x16x32_bf16 a[80:83], a[172:175], v[132:135], 0\n"
"v_exp_f32 v44, v44\n"
"v_mfma_f32_16x16x32_bf16 a[80:83], a[168:171], v[128:131], a[80:83]\n"
"s_waitcnt lgkmcnt(4)\n"
"v_exp_f32 v45, v45\n"
"v_mfma_f32_16x16x32_bf16 a[96:99], a[176:179], v[128:131], 0\n"
"s_waitcnt vmcnt(7) lgkmcnt(0)\n"
"v_exp_f32 v46, v46\n"
"v_mfma_f32_16x16x32_bf16 a[96:99], a[180:183], v[132:135], a[96:99]\n"
"s_barrier\n"
"v_mfma_f32_16x16x32_bf16 a[100:103], a[180:183], v[140:143], 0\n"
"ds_read_b128 a[128:131], v216 offset:32768\n"
"v_cvt_pk_bf16_f32 v180, v40, v41\n"
"v_mfma_f32_16x16x32_bf16 a[100:103], a[176:179], v[136:139], a[100:103]\n"
"ds_read_b128 a[132:135], v217 offset:32768\n"
"v_cvt_pk_bf16_f32 v181, v42, v43\n"
"v_mfma_f32_16x16x32_bf16 a[104:107], a[176:179], v[144:147], 0\n"
"v_exp_f32 v47, v47\n"
"v_mfma_f32_16x16x32_bf16 a[104:107], a[180:183], v[148:151], a[104:107]\n"
"buffer_load_dwordx4 v224, s[40:43], s57 offen offset:3072 nt lds\n"
"v_mfma_f32_16x16x32_bf16 a[108:111], a[180:183], v[156:159], 0\n"
"s_sub_u32 s57, s57, 159744\n"
"v_exp_f32 v60, v60\n"
"v_mfma_f32_16x16x32_bf16 a[108:111], a[176:179], v[152:155], a[108:111]\n"
"s_and_b32 s57, s57, 159744\n"
"ds_read_b128 a[136:139], v218 offset:32768\n"
"v_cvt_pk_bf16_f32 v188, v44, v45\n"
"v_mfma_f32_16x16x32_bf16 a[124:127], a[184:187], v[152:155], 0\n"
"buffer_store_dword a222, v253, s[96:99], 0 offen sc1\n"
"v_mfma_f32_16x16x32_bf16 a[124:127], a[188:191], v[156:159], a[124:127]\n"
"s_mov_b32 s98, 139264\n"
"ds_read_b128 a[140:143], v219 offset:32768\n"
"v_cvt_pk_bf16_f32 v189, v46, v47\n"
"v_mfma_f32_16x16x32_bf16 a[120:123], a[188:191], v[148:151], 0\n"
"v_exp_f32 v61, v61\n"
"v_mfma_f32_16x16x32_bf16 a[120:123], a[184:187], v[144:147], a[120:123]\n"
"ds_read_b128 a[144:147], v216 offset:33792\n"
"v_cvt_pk_bf16_f32 v190, v60, v61\n"
"v_mfma_f32_16x16x32_bf16 a[116:119], a[184:187], v[136:139], 0\n"
"v_mfma_f32_16x16x32_bf16 a[116:119], a[188:191], v[140:143], a[116:119]\n"
"ds_read_b128 a[148:151], v217 offset:33792\n"
"v_mfma_f32_16x16x32_bf16 a[112:115], a[188:191], v[132:135], 0\n"
"s_mov_b32 s64, 2\n"
"v_mfma_f32_16x16x32_bf16 a[112:115], a[184:187], v[128:131], a[112:115]\n"
"s_waitcnt lgkmcnt(4)\n"
"ds_read_b128 a[152:155], v218 offset:33792\n"
"ds_read_b128 a[156:159], v219 offset:33792\n"
"L_loop%=:\n"
"v_mfma_f32_16x16x32_bf16 v[0:3], a[128:131], v[64:67], v[192:195]\n"
"v_exp_f32 v62, v62\n"
"v_mfma_f32_16x16x32_bf16 v[0:3], a[132:135], v[68:71], v[0:3]\n"
"s_waitcnt lgkmcnt(4)\n"
"v_exp_f32 v63, v63\n"
"v_mfma_f32_16x16x32_bf16 v[0:3], a[136:139], v[72:75], v[0:3]\n"
"v_exp_f32 v56, v56\n"
"v_mfma_f32_16x16x32_bf16 v[0:3], a[140:143], v[76:79], v[0:3]\n"
"v_exp_f32 v57, v57\n"
"v_mfma_f32_16x16x32_bf16 v[4:7], a[140:143], v[92:95], v[196:199]\n"
"s_add_u32 m0, s61, 0\n"
"v_cvt_pk_bf16_f32 v191, v62, v63\n"
"v_cvt_pk_bf16_f32 v182, v56, v57\n"
"v_mfma_f32_16x16x32_bf16 v[4:7], a[136:139], v[88:91], v[4:7]\n"
"buffer_load_dwordx4 v222, s[36:39], s56 offen nt lds\n"
"v_mfma_f32_16x16x32_bf16 v[4:7], a[132:135], v[84:87], v[4:7]\n"
"v_exp_f32 v58, v58\n"
"v_mfma_f32_16x16x32_bf16 v[4:7], a[128:131], v[80:83], v[4:7]\n"
"v_exp_f32 v59, v59\n"
"v_mfma_f32_16x16x32_bf16 v[8:11], a[128:131], v[96:99], v[200:203]\n"
"s_memtime s[76:77]\n"
"v_exp_f32 v52, v52\n"
"v_mfma_f32_16x16x32_bf16 v[8:11], a[132:135], v[100:103], v[8:11]\n"
"s_memrealtime s[34:35]\n"
"v_exp_f32 v53, v53\n"
"v_mfma_f32_16x16x32_bf16 v[8:11], a[136:139], v[104:107], v[8:11]\n"
"v_exp_f32 v54, v54\n"
"v_mfma_f32_16x16x32_bf16 v[8:11], a[140:143], v[108:111], v[8:11]\n"
"v_exp_f32 v55, v55\n"
"v_mfma_f32_16x16x32_bf16 v[12:15], a[140:143], v[124:127], v[204:207]\n"
"v_exp_f32 v48, v48\n"
"v_mfma_f32_16x16x32_bf16 v[12:15], a[136:139], v[120:123], v[12:15]\n"
"v_exp_f32 v49, v49\n"
"v_mfma_f32_16x16x32_bf16 v[12:15], a[132:135], v[116:119], v[12:15]\n"
"v_exp_f32 v50, v50\n"
"v_mfma_f32_16x16x32_bf16 v[12:15], a[128:131], v[112:115], v[12:15]\n"
"s_waitcnt lgkmcnt(0)\n"
"ds_read_b128 a[128:131], v216 offset:40960\n"
"v_cvt_pk_bf16_f32 v183, v58, v59\n"
"v_mfma_f32_16x16x32_bf16 v[28:31], a[144:147], v[112:115], v[204:207]\n"
"s_sub_u32 s35, s34, s73\n"
"v_exp_f32 v51, v51\n"
"v_mfma_f32_16x16x32_bf16 v[28:31], a[148:151], v[116:119], v[28:31]\n"
"s_cmp_lt_i32 s35, -2\n"
"ds_read_b128 a[132:135], v217 offset:40960\n"
"v_cvt_pk_bf16_f32 v174, v52, v53\n"
"v_mfma_f32_16x16x32_bf16 v[28:31], a[152:155], v[120:123], v[28:31]\n"
"s_cbranch_scc1 L_wL0%=\n"
"L_wbL0%=:\n"
"v_mfma_f32_16x16x32_bf16 v[28:31], a[156:159], v[124:127], v[28:31]\n"
"s_sub_u32 s66, s76, s66\n"
"ds_read_b128 a[136:139], v218 offset:40960\n"
"v_cvt_pk_bf16_f32 v175, v54, v55\n"
"v_mfma_f32_16x16x32_bf16 v[24:27], a[156:159], v[108:111], v[200:203]\n"
"s_sub_u32 s75, s75, 5\n"
"v_exp_f32 v0, v0\n"
"v_mfma_f32_16x16x32_bf16 v[24:27], a[152:155], v[104:107], v[24:27]\n"
"s_max_i32 s75, s75, 0\n"
"buffer_load_dwordx4 v222, s[36:39], s56 offen offset:1024 nt lds\n"
"v_mfma_f32_16x16x32_bf16 v[24:27], a[148:151], v[100:103], v[24:27]\n"
"s_cmp_gt_i32 s66, s58\n"
"v_exp_f32 v1, v1\n"
"v_mfma_f32_16x16x32_bf16 v[24:27], a[144:147], v[96:99], v[24:27]\n"
"s_cselect_b32 s75, 148, s75\n"
"ds_read_b128 a[140:143], v219 offset:40960\n"
"v_cvt_pk_bf16_f32 v166, v48, v49\n"
"v_mfma_f32_16x16x32_bf16 v[20:23], a[144:147], v[80:83], v[196:199]\n"
"s_add_u32 s73, s34, s75\n"
"v_cvt_pk_bf16_f32 v167, v50, v51\n"
"v_cvt_pk_bf16_f32 v128, v0, v1\n"
"v_mfma_f32_16x16x32_bf16 v[20:23], a[148:151], v[84:87], v[20:23]\n"
"s_mov_b32 s66, s76\n"
"v_exp_f32 v2, v2\n"
"v_mfma_f32_16x16x32_bf16 v[20:23], a[152:155], v[88:91], v[20:23]\n"
"v_exp_f32 v3, v3\n"
"v_mfma_f32_16x16x32_bf16 v[20:23], a[156:159], v[92:95], v[20:23]\n"
"v_exp_f32 v4, v4\n"
"v_mfma_f32_16x16x32_bf16 v[16:19], a[156:159], v[76:79], v[192:195]\n"
"v_exp_f32 v5, v5\n"
"v_mfma_f32_16x16x32_bf16 v[16:19], a[152:155], v[72:75], v[16:19]\n"
"v_exp_f32 v6, v6\n"
"v_mfma_f32_16x16x32_bf16 v[16:19], a[148:151], v[68:71], v[16:19]\n"
"v_exp_f32 v7, v7\n"
"v_mfma_f32_16x16x32_bf16 v[16:19], a[144:147], v[64:67], v[16:19]\n"
"s_waitcnt lgkmcnt(2)\n"
"ds_read_b128 a[144:147], v216 offset:41984\n"
"v_cvt_pk_bf16_f32 v129, v2, v3\n"
"v_mfma_f32_16x16x32_bf16 v[32:35], a[128:131], v[64:67], v[192:195]\n"
"v_exp_f32 v8, v8\n"
"v_mfma_f32_16x16x32_bf16 v[32:35], a[132:135], v[68:71], v[32:35]\n"
"s_waitcnt lgkmcnt(1)\n"
"ds_read_b128 a[148:151], v217 offset:41984\n"
"v_cvt_pk_bf16_f32 v136, v4, v5\n"
"v_mfma_f32_16x16x32_bf16 v[32:35], a[136:139], v[72:75], v[32:35]\n"
"v_exp_f32 v9, v9\n"
"v_mfma_f32_16x16x32_bf16 v[32:35], a[140:143], v[76:79], v[32:35]\n"
"ds_read_b128 a[152:155], v218 offset:41984\n"
"v_cvt_pk_bf16_f32 v137, v6, v7\n"
"v_mfma_f32_16x16x32_bf16 v[36:39], a[140:143], v[92:95], v[196:199]\n"
"v_exp_f32 v10, v10\n"
"v_mfma_f32_16x16x32_bf16 v[36:39], a[136:139], v[88:91], v[36:39]\n"
"buffer_load_dwordx4 v223, s[36:39], s56 offen offset:2048 nt lds\n"
"v_mfma_f32_16x16x32_bf16 v[36:39], a[132:135], v[84:87], v[36:39]\n"
"v_exp_f32 v11, v11\n"
"v_mfma_f32_16x16x32_bf16 v[36:39], a[128:131], v[80:83], v[36:39]\n"
"ds_read_b128 a[156:159], v219 offset:41984\n"
"v_cvt_pk_bf16_f32 v144, v8, v9\n"
"v_mfma_f32_16x16x32_bf16 v[40:43], a[128:131], v[96:99], v[200:203]\n"
"v_exp_f32 v12, v12\n"
"v_mfma_f32_16x16x32_bf16 v[40:43], a[132:135], v[100:103], v[40:43]\n"
"ds_read_b64_tr_b16 a[160:161], v220 offset:16448\n"
"v_cvt_pk_bf16_f32 v145, v10, v11\n"
"v_mfma_f32_16x16x32_bf16 v[40:43], a[136:139], v[104:107], v[40:43]\n"
"v_exp_f32 v13, v13\n"
"v_mfma_f32_16x16x32_bf16 v[40:43], a[140:143], v[108:111], v[40:43]\n"
"ds_read_b64_tr_b16 a[162:163], v220 offset:17472\n"
"v_cvt_pk_bf16_f32 v152, v12, v13\n"
"v_mfma_f32_16x16x32_bf16 v[44:47], a[140:143], v[124:127], v[204:207]\n"
"v_exp_f32 v14, v14\n"
"v_mfma_f32_16x16x32_bf16 v[44:47], a[136:139], v[120:123], v[44:47]\n"
"ds_read_b64_tr_b16 a[164:165], v220 offset:24672\n"
"v_mfma_f32_16x16x32_bf16 v[44:47], a[132:135], v[116:119], v[44:47]\n"
"v_exp_f32 v15, v15\n"
"v_mfma_f32_16x16x32_bf16 v[44:47], a[128:131], v[112:115], v[44:47]\n"
"s_waitcnt lgkmcnt(5)\n"
"ds_read_b64_tr_b16 a[166:167], v220 offset:25696\n"
"v_cvt_pk_bf16_f32 v153, v14, v15\n"
"v_mfma_f32_16x16x32_bf16 v[60:63], a[144:147], v[112:115], v[204:207]\n"
"v_exp_f32 v28, v28\n"
"v_mfma_f32_16x16x32_bf16 v[60:63], a[148:151], v[116:119], v[60:63]\n"
"s_waitcnt lgkmcnt(4)\n"
"ds_read_b64_tr_b16 a[168:169], v220 offset:16456\n"
"v_mfma_f32_16x16x32_bf16 v[60:63], a[152:155], v[120:123], v[60:63]\n"
"v_exp_f32 v29, v29\n"
"v_mfma_f32_16x16x32_bf16 v[60:63], a[156:159], v[124:127], v[60:63]\n"
"ds_read_b64_tr_b16 a[170:171], v220 offset:17480\n"
"v_cvt_pk_bf16_f32 v154, v28, v29\n"
"v_mfma_f32_16x16x32_bf16 v[56:59], a[156:159], v[108:111], v[200:203]\n"
"v_exp_f32 v30, v30\n"
"v_mfma_f32_16x16x32_bf16 v[56:59], a[152:155], v[104:107], v[56:59]\n"
"ds_read_b64_tr_b16 a[172:173], v220 offset:24680\n"
"v_mfma_f32_16x16x32_bf16 v[56:59], a[148:151], v[100:103], v[56:59]\n"
"v_exp_f32 v31, v31\n"
"v_mfma_f32_16x16x32_bf16 v[56:59], a[144:147], v[96:99], v[56:59]\n"
"buffer_load_dwordx4 v223, s[36:39], s56 offen offset:3072 nt lds\n"
"v_mfma_f32_16x16x32_bf16 v[52:55], a[144:147], v[80:83], v[196:199]\n"
"s_sub_u32 s56, s56, 159744\n"
"v_exp_f32 v24, v24\n"
"v_mfma_f32_16x16x32_bf16 v[52:55], a[148:151], v[84:87], v[52:55]\n"
"s_and_b32 s56, s56, 159744\n"
"ds_read_b64_tr_b16 a[174:175], v220 offset:25704\n"
"v_cvt_pk_bf16_f32 v155, v30, v31\n"
"v_mfma_f32_16x16x32_bf16 v[52:55], a[152:155], v[88:91], v[52:55]\n"
"v_exp_f32 v25, v25\n"
"v_mfma_f32_16x16x32_bf16 v[52:55], a[156:159], v[92:95], v[52:55]\n"
"ds_read_b64_tr_b16 a[176:177], v220 offset:16704\n"
"v_cvt_pk_bf16_f32 v146, v24, v25\n"
"v_mfma_f32_16x16x32_bf16 v[48:51], a[156:159], v[76:79], v[192:195]\n"
"v_exp_f32 v26, v26\n"
"v_mfma_f32_16x16x32_bf16 v[48:51], a[152:155], v[72:75], v[48:51]\n"
"ds_read_b64_tr_b16 a[178:179], v220 offset:17728\n"
"v_mfma_f32_16x16x32_bf16 v[48:51], a[148:151], v[68:71], v[48:51]\n"
"v_exp_f32 v27, v27\n"
"v_mfma_f32_16x16x32_bf16 v[48:51], a[144:147], v[64:67], v[48:51]\n"
"ds_read_b64_tr_b16 a[180:181], v220 offset:24928\n"
"v_cvt_pk_bf16_f32 v147, v26, v27\n"
"v_smfmac_f32_16x16x64_bf16 a[228:231], a[200:203], v[160:167], v208\n"
"v_smfmac_f32_16x16x64_bf16 a[232:235], a[200:203], v[168:175], v208\n"
"ds_read_b64_tr_b16 a[182:183], v220 offset:25952\n"
"v_smfmac_f32_16x16x64_bf16 a[236:239], a[200:203], v[176:183], v208\n"
"v_smfmac_f32_16x16x64_bf16 a[240:243], a[200:203], v[184:191], v208\n"
"s_waitcnt lgkmcnt(8)\n"
"ds_read_b64_tr_b16 a[184:185], v220 offset:16712\n"
"v_mfma_f32_16x16x32_bf16 a[0:3], a[160:163], v[160:163], a[0:3]\n"
"v_exp_f32 v20, v20\n"
"v_mfma_f32_16x16x32_bf16 a[0:3], a[164:167], v[164:167], a[0:3]\n"
"ds_read_b64_tr_b16 a[186:187], v220 offset:17736\n"
"v_mfma_f32_16x16x32_bf16 a[4:7], a[164:167], v[172:175], a[4:7]\n"
"s_add_u32 m0, s62, 49344\n"
"v_exp_f32 v21, v21\n"
"v_mfma_f32_16x16x32_bf16 a[4:7], a[160:163], v[168:171], a[4:7]\n"
"buffer_load_dwordx4 v224, s[40:43], s57 offen nt lds\n"
"v_mfma_f32_16x16x32_bf16 a[8:11], a[160:163], v[176:179], a[8:11]\n"
"v_exp_f32 v22, v22\n"
"v_mfma_f32_16x16x32_bf16 a[8:11], a[164:167], v[180:183], a[8:11]\n"
"ds_read_b64_tr_b16 a[188:189], v220 offset:24936\n"
"v_cvt_pk_bf16_f32 v138, v20, v21\n"
"v_mfma_f32_16x16x32_bf16 a[12:15], a[164:167], v[188:191], a[12:15]\n"
"v_exp_f32 v23, v23\n"
"v_mfma_f32_16x16x32_bf16 a[12:15], a[160:163], v[184:187], a[12:15]\n"
"s_waitcnt lgkmcnt(7)\n"
"ds_read_b64_tr_b16 a[190:191], v220 offset:25960\n"
"v_cvt_pk_bf16_f32 v139, v22, v23\n"
"v_mfma_f32_16x16x32_bf16 a[28:31], a[168:171], v[184:187], a[28:31]\n"
"v_exp_f32 v16, v16\n"
"v_mfma_f32_16x16x32_bf16 a[28:31], a[172:175], v[188:191], a[28:31]\n"
"ds_read_b64_tr_b16 a[160:161], v220 offset:16960\n"
"v_mfma_f32_16x16x32_bf16 a[24:27], a[172:175], v[180:183], a[24:27]\n"
"v_exp_f32 v17, v17\n"
"v_mfma_f32_16x16x32_bf16 a[24:27], a[168:171], v[176:179], a[24:27]\n"
"ds_read_b64_tr_b16 a[162:163], v220 offset:17984\n"
"v_cvt_pk_bf16_f32 v130, v16, v17\n"
"v_mfma_f32_16x16x32_bf16 a[20:23], a[168:171], v[168:171], a[20:23]\n"
"v_exp_f32 v18, v18\n"
"v_mfma_f32_16x16x32_bf16 a[20:23], a[172:175], v[172:175], a[20:23]\n"
"ds_read_b64_tr_b16 a[164:165], v220 offset:25184\n"
"v_mfma_f32_16x16x32_bf16 a[16:19], a[172:175], v[164:167], a[16:19]\n"
"v_exp_f32 v19, v19\n"
"v_mfma_f32_16x16x32_bf16 a[16:19], a[168:171], v[160:163], a[16:19]\n"
"s_waitcnt lgkmcnt(7)\n"
"ds_read_b64_tr_b16 a[166:167], v220 offset:26208\n"
"v_cvt_pk_bf16_f32 v131, v18, v19\n"
"v_mfma_f32_16x16x32_bf16 a[32:35], a[176:179], v[160:163], a[32:35]\n"
"v_exp_f32 v32, v32\n"
"v_mfma_f32_16x16x32_bf16 a[32:35], a[180:183], v[164:167], a[32:35]\n"
"ds_read_b64_tr_b16 a[168:169], v220 offset:16968\n"
"v_mfma_f32_16x16x32_bf16 a[36:39], a[180:183], v[172:175], a[36:39]\n"
"v_exp_f32 v33, v33\n"
"v_mfma_f32_16x16x32_bf16 a[36:39], a[176:179], v[168:171], a[36:39]\n"
"buffer_load_dwordx4 v224, s[40:43], s57 offen offset:1024 nt lds\n"
"v_mfma_f32_16x16x32_bf16 a[40:43], a[176:179], v[176:179], a[40:43]\n"
"v_exp_f32 v34, v34\n"
"v_mfma_f32_16x16x32_bf16 a[40:43], a[180:183], v[180:183], a[40:43]\n"
"ds_read_b64_tr_b16 a[170:171], v220 offset:17992\n"
"v_cvt_pk_bf16_f32 v132, v32, v33\n"
"v_mfma_f32_16x16x32_bf16 a[44:47], a[180:183], v[188:191], a[44:47]\n"
"v_exp_f32 v35, v35\n"
"v_mfma_f32_16x16x32_bf16 a[44:47], a[176:179], v[184:187], a[44:47]\n"
"s_waitcnt lgkmcnt(6)\n"
"ds_read_b64_tr_b16 a[172:173], v220 offset:25192\n"
"v_cvt_pk_bf16_f32 v133, v34, v35\n"
"v_mfma_f32_16x16x32_bf16 a[60:63], a[184:187], v[184:187], a[60:63]\n"
"v_exp_f32 v36, v36\n"
"v_mfma_f32_16x16x32_bf16 a[60:63], a[188:191], v[188:191], a[60:63]\n"
"ds_read_b64_tr_b16 a[174:175], v220 offset:26216\n"
"v_mfma_f32_16x16x32_bf16 a[56:59], a[188:191], v[180:183], a[56:59]\n"
"v_exp_f32 v37, v37\n"
"v_mfma_f32_16x16x32_bf16 a[56:59], a[184:187], v[176:179], a[56:59]\n"
"ds_read_b64_tr_b16 a[176:177], v220 offset:17216\n"
"v_cvt_pk_bf16_f32 v140, v36, v37\n"
"v_mfma_f32_16x16x32_bf16 a[52:55], a[184:187], v[168:171], a[52:55]\n"
"v_exp_f32 v38, v38\n"
"v_mfma_f32_16x16x32_bf16 a[52:55], a[188:191], v[172:175], a[52:55]\n"
"ds_read_b64_tr_b16 a[178:179], v220 offset:18240\n"
"v_mfma_f32_16x16x32_bf16 a[48:51], a[188:191], v[164:167], a[48:51]\n"
"v_exp_f32 v39, v39\n"
"v_mfma_f32_16x16x32_bf16 a[48:51], a[184:187], v[160:163], a[48:51]\n"
"s_waitcnt lgkmcnt(6)\n"
"ds_read_b64_tr_b16 a[180:181], v220 offset:25440\n"
"v_cvt_pk_bf16_f32 v141, v38, v39\n"
"v_mfma_f32_16x16x32_bf16 a[64:67], a[160:163], v[160:163], a[64:67]\n"
"v_exp_f32 v40, v40\n"
"v_mfma_f32_16x16x32_bf16 a[64:67], a[164:167], v[164:167], a[64:67]\n"
"ds_read_b64_tr_b16 a[182:183], v220 offset:26464\n"
"v_mfma_f32_16x16x32_bf16 a[68:71], a[164:167], v[172:175], a[68:71]\n"
"s_add_u32 m0, s62, 49352\n"
"v_exp_f32 v41, v41\n"
"v_mfma_f32_16x16x32_bf16 a[68:71], a[160:163], v[168:171], a[68:71]\n"
"buffer_load_dwordx4 v224, s[40:43], s57 offen offset:2048 nt lds\n"
"v_mfma_f32_16x16x32_bf16 a[72:75], a[160:163], v[176:179], a[72:75]\n"
"v_exp_f32 v42, v42\n"
"v_mfma_f32_16x16x32_bf16 a[72:75], a[164:167], v[180:183], a[72:75]\n"
"ds_read_b64_tr_b16 a[184:185], v220 offset:17224\n"
"v_cvt_pk_bf16_f32 v148, v40, v41\n"
"v_mfma_f32_16x16x32_bf16 a[76:79], a[164:167], v[188:191], a[76:79]\n"
"v_exp_f32 v43, v43\n"
"v_mfma_f32_16x16x32_bf16 a[76:79], a[160:163], v[184:187], a[76:79]\n"
"s_waitcnt lgkmcnt(5)\n"
"ds_read_b64_tr_b16 a[186:187], v220 offset:18248\n"
"v_cvt_pk_bf16_f32 v149, v42, v43\n"
"v_mfma_f32_16x16x32_bf16 a[92:95], a[168:171], v[184:187], a[92:95]\n"
"v_exp_f32 v44, v44\n"
"v_mfma_f32_16x16x32_bf16 a[92:95], a[172:175], v[188:191], a[92:95]\n"
"ds_read_b64_tr_b16 a[188:189], v220 offset:25448\n"
"v_mfma_f32_16x16x32_bf16 a[88:91], a[172:175], v[180:183], a[88:91]\n"
"v_exp_f32 v45, v45\n"
"v_mfma_f32_16x16x32_bf16 a[88:91], a[168:171], v[176:179], a[88:91]\n"
"ds_read_b64_tr_b16 a[190:191], v220 offset:26472\n"
"v_cvt_pk_bf16_f32 v156, v44, v45\n"
"v_mfma_f32_16x16x32_bf16 a[84:87], a[168:171], v[168:171], a[84:87]\n"
"v_exp_f32 v46, v46\n"
"v_mfma_f32_16x16x32_bf16 a[84:87], a[172:175], v[172:175], a[84:87]\n"
"v_exp_f32 v47, v47\n"
"v_mfma_f32_16x16x32_bf16 a[80:83], a[172:175], v[164:167], a[80:83]\n"
"v_exp_f32 v60, v60\n"
"v_mfma_f32_16x16x32_bf16 a[80:83], a[168:171], v[160:163], a[80:83]\n"
"s_waitcnt lgkmcnt(4)\n"
"v_exp_f32 v61, v61\n"
"v_mfma_f32_16x16x32_bf16 a[96:99], a[176:179], v[160:163], a[96:99]\n"
"s_waitcnt vmcnt(7) lgkmcnt(0)\n"
"v_cvt_pk_bf16_f32 v157, v46, v47\n"
"v_cvt_pk_bf16_f32 v158, v60, v61\n"
"v_mfma_f32_16x16x32_bf16 a[96:99], a[180:183], v[164:167], a[96:99]\n"
"s_barrier\n"
"v_mfma_f32_16x16x32_bf16 a[100:103], a[180:183], v[172:175], a[100:103]\n"
"ds_read_b128 a[128:131], v216 offset:49152\n"
"v_mfma_f32_16x16x32_bf16 a[100:103], a[176:179], v[168:171], a[100:103]\n"
"ds_read_b128 a[132:135], v217 offset:49152\n"
"v_mfma_f32_16x16x32_bf16 a[104:107], a[176:179], v[176:179], a[104:107]\n"
"v_mfma_f32_16x16x32_bf16 a[104:107], a[180:183], v[180:183], a[104:107]\n"
"buffer_load_dwordx4 v224, s[40:43], s57 offen offset:3072 nt lds\n"
"v_mfma_f32_16x16x32_bf16 a[108:111], a[180:183], v[188:191], a[108:111]\n"
"s_sub_u32 s57, s57, 159744\n"
"v_mfma_f32_16x16x32_bf16 a[108:111], a[176:179], v[184:187], a[108:111]\n"
"s_and_b32 s57, s57, 159744\n"
"ds_read_b128 a[136:139], v218 offset:49152\n"
"v_mfma_f32_16x16x32_bf16 a[124:127], a[184:187], v[184:187], a[124:127]\n"
"v_mfma_f32_16x16x32_bf16 a[124:127], a[188:191], v[188:191], a[124:127]\n"
"ds_read_b128 a[140:143], v219 offset:49152\n"
"v_mfma_f32_16x16x32_bf16 a[120:123], a[188:191], v[180:183], a[120:123]\n"
"v_mfma_f32_16x16x32_bf16 a[120:123], a[184:187], v[176:179], a[120:123]\n"
"ds_read_b128 a[144:147], v216 offset:50176\n"
"v_mfma_f32_16x16x32_bf16 a[116:119], a[184:187], v[168:171], a[116:119]\n"
"v_mfma_f32_16x16x32_bf16 a[116:119], a[188:191], v[172:175], a[116:119]\n"
"ds_read_b128 a[148:151], v217 offset:50176\n"
"v_mfma_f32_16x16x32_bf16 a[112:115], a[188:191], v[164:167], a[112:115]\n"
"v_mfma_f32_16x16x32_bf16 a[112:115], a[184:187], v[160:163], a[112:115]\n"
"s_waitcnt lgkmcnt(4)\n"
"ds_read_b128 a[152:155], v218 offset:50176\n"
"ds_read_b128 a[156:159], v219 offset:50176\n"
"v_mfma_f32_16x16x32_bf16 v[0:3], a[128:131], v[64:67], v[192:195]\n"
"v_exp_f32 v62, v62\n"
"v_mfma_f32_16x16x32_bf16 v[0:3], a[132:135], v[68:71], v[0:3]\n"
"s_waitcnt lgkmcnt(4)\n"
"v_exp_f32 v63, v63\n"
"v_mfma_f32_16x16x32_bf16 v[0:3], a[136:139], v[72:75], v[0:3]\n"
"v_exp_f32 v56, v56\n"
"v_mfma_f32_16x16x32_bf16 v[0:3], a[140:143], v[76:79], v[0:3]\n"
"v_exp_f32 v57, v57\n"
"v_mfma_f32_16x16x32_bf16 v[4:7], a[140:143], v[92:95], v[196:199]\n"
"s_add_u32 m0, s61, 16384\n"
"v_cvt_pk_bf16_f32 v159, v62, v63\n"
"v_cvt_pk_bf16_f32 v150, v56, v57\n"
"v_mfma_f32_16x16x32_bf16 v[4:7], a[136:139], v[88:91], v[4:7]\n"
"buffer_load_dwordx4 v222, s[36:39], s56 offen nt lds\n"
"v_mfma_f32_16x16x32_bf16 v[4:7], a[132:135], v[84:87], v[4:7]\n"
"v_exp_f32 v58, v58\n"
"v_mfma_f32_16x16x32_bf16 v[4:7], a[128:131], v[80:83], v[4:7]\n"
"v_exp_f32 v59, v59\n"
"v_mfma_f32_16x16x32_bf16 v[8:11], a[128:131], v[96:99], v[200:203]\n"
"s_memtime s[76:77]\n"
"v_exp_f32 v52, v52\n"
"v_mfma_f32_16x16x32_bf16 v[8:11], a[132:135], v[100:103], v[8:11]\n"
"s_memrealtime s[34:35]\n"
"v_exp_f32 v53, v53\n"
"v_mfma_f32_16x16x32_bf16 v[8:11], a[136:139], v[104:107], v[8:11]\n"
"v_exp_f32 v54, v54\n"
"v_mfma_f32_16x16x32_bf16 v[8:11], a[140:143], v[108:111], v[8:11]\n"
"v_exp_f32 v55, v55\n"
"v_mfma_f32_16x16x32_bf16 v[12:15], a[140:143], v[124:127], v[204:207]\n"
"v_exp_f32 v48, v48\n"
"v_mfma_f32_16x16x32_bf16 v[12:15], a[136:139], v[120:123], v[12:15]\n"
"v_exp_f32 v49, v49\n"
"v_mfma_f32_16x16x32_bf16 v[12:15], a[132:135], v[116:119], v[12:15]\n"
"v_exp_f32 v50, v50\n"
"v_mfma_f32_16x16x32_bf16 v[12:15], a[128:131], v[112:115], v[12:15]\n"
"s_waitcnt lgkmcnt(0)\n"
"ds_read_b128 a[128:131], v216 offset:57344\n"
"v_cvt_pk_bf16_f32 v151, v58, v59\n"
"v_mfma_f32_16x16x32_bf16 v[28:31], a[144:147], v[112:115], v[204:207]\n"
"s_sub_u32 s35, s34, s73\n"
"v_exp_f32 v51, v51\n"
"v_mfma_f32_16x16x32_bf16 v[28:31], a[148:151], v[116:119], v[28:31]\n"
"s_cmp_lt_i32 s35, -2\n"
"ds_read_b128 a[132:135], v217 offset:57344\n"
"v_cvt_pk_bf16_f32 v142, v52, v53\n"
"v_mfma_f32_16x16x32_bf16 v[28:31], a[152:155], v[120:123], v[28:31]\n"
"s_cbranch_scc1 L_wL1%=\n"
"L_wbL1%=:\n"
"v_mfma_f32_16x16x32_bf16 v[28:31], a[156:159], v[124:127], v[28:31]\n"
"s_sub_u32 s66, s76, s66\n"
"ds_read_b128 a[136:139], v218 offset:57344\n"
"v_cvt_pk_bf16_f32 v143, v54, v55\n"
"v_mfma_f32_16x16x32_bf16 v[24:27], a[156:159], v[108:111], v[200:203]\n"
"s_sub_u32 s75, s75, 5\n"
"v_exp_f32 v0, v0\n"
"v_mfma_f32_16x16x32_bf16 v[24:27], a[152:155], v[104:107], v[24:27]\n"
"s_max_i32 s75, s75, 0\n"
"buffer_load_dwordx4 v222, s[36:39], s56 offen offset:1024 nt lds\n"
"v_mfma_f32_16x16x32_bf16 v[24:27], a[148:151], v[100:103], v[24:27]\n"
"s_cmp_gt_i32 s66, s58\n"
"v_exp_f32 v1, v1\n"
"v_mfma_f32_16x16x32_bf16 v[24:27], a[144:147], v[96:99], v[24:27]\n"
"s_cselect_b32 s75, 148, s75\n"
"ds_read_b128 a[140:143], v219 offset:57344\n"
"v_cvt_pk_bf16_f32 v134, v48, v49\n"
"v_mfma_f32_16x16x32_bf16 v[20:23], a[144:147], v[80:83], v[196:199]\n"
"s_add_u32 s73, s34, s75\n"
"v_cvt_pk_bf16_f32 v135, v50, v51\n"
"v_cvt_pk_bf16_f32 v160, v0, v1\n"
"v_mfma_f32_16x16x32_bf16 v[20:23], a[148:151], v[84:87], v[20:23]\n"
"s_mov_b32 s66, s76\n"
"v_exp_f32 v2, v2\n"
"v_mfma_f32_16x16x32_bf16 v[20:23], a[152:155], v[88:91], v[20:23]\n"
"v_exp_f32 v3, v3\n"
"v_mfma_f32_16x16x32_bf16 v[20:23], a[156:159], v[92:95], v[20:23]\n"
"v_exp_f32 v4, v4\n"
"v_mfma_f32_16x16x32_bf16 v[16:19], a[156:159], v[76:79], v[192:195]\n"
"v_exp_f32 v5, v5\n"
"v_mfma_f32_16x16x32_bf16 v[16:19], a[152:155], v[72:75], v[16:19]\n"
"v_exp_f32 v6, v6\n"
"v_mfma_f32_16x16x32_bf16 v[16:19], a[148:151], v[68:71], v[16:19]\n"
"v_exp_f32 v7, v7\n"
"v_mfma_f32_16x16x32_bf16 v[16:19], a[144:147], v[64:67], v[16:19]\n"
"s_waitcnt lgkmcnt(2)\n"
"ds_read_b128 a[144:147], v216 offset:58368\n"
"v_cvt_pk_bf16_f32 v161, v2, v3\n"
"v_mfma_f32_16x16x32_bf16 v[32:35], a[128:131], v[64:67], v[192:195]\n"
"v_exp_f32 v8, v8\n"
"v_mfma_f32_16x16x32_bf16 v[32:35], a[132:135], v[68:71], v[32:35]\n"
"s_waitcnt lgkmcnt(1)\n"
"ds_read_b128 a[148:151], v217 offset:58368\n"
"v_cvt_pk_bf16_f32 v168, v4, v5\n"
"v_mfma_f32_16x16x32_bf16 v[32:35], a[136:139], v[72:75], v[32:35]\n"
"v_exp_f32 v9, v9\n"
"v_mfma_f32_16x16x32_bf16 v[32:35], a[140:143], v[76:79], v[32:35]\n"
"ds_read_b128 a[152:155], v218 offset:58368\n"
"v_cvt_pk_bf16_f32 v169, v6, v7\n"
"v_mfma_f32_16x16x32_bf16 v[36:39], a[140:143], v[92:95], v[196:199]\n"
"v_exp_f32 v10, v10\n"
"v_mfma_f32_16x16x32_bf16 v[36:39], a[136:139], v[88:91], v[36:39]\n"
"buffer_load_dwordx4 v223, s[36:39], s56 offen offset:2048 nt lds\n"
"v_mfma_f32_16x16x32_bf16 v[36:39], a[132:135], v[84:87], v[36:39]\n"
"v_exp_f32 v11, v11\n"
"v_mfma_f32_16x16x32_bf16 v[36:39], a[128:131], v[80:83], v[36:39]\n"
"ds_read_b128 a[156:159], v219 offset:58368\n"
"v_cvt_pk_bf16_f32 v176, v8, v9\n"
"v_mfma_f32_16x16x32_bf16 v[40:43], a[128:131], v[96:99], v[200:203]\n"
"v_exp_f32 v12, v12\n"
"v_mfma_f32_16x16x32_bf16 v[40:43], a[132:135], v[100:103], v[40:43]\n"
"ds_read_b64_tr_b16 a[160:161], v220 offset:32896\n"
"v_cvt_pk_bf16_f32 v177, v10, v11\n"
"v_mfma_f32_16x16x32_bf16 v[40:43], a[136:139], v[104:107], v[40:43]\n"
"v_exp_f32 v13, v13\n"
"v_mfma_f32_16x16x32_bf16 v[40:43], a[140:143], v[108:111], v[40:43]\n"
"ds_read_b64_tr_b16 a[162:163], v220 offset:33920\n"
"v_cvt_pk_bf16_f32 v184, v12, v13\n"
"v_mfma_f32_16x16x32_bf16 v[44:47], a[140:143], v[124:127], v[204:207]\n"
"v_exp_f32 v14, v14\n"
"v_mfma_f32_16x16x32_bf16 v[44:47], a[136:139], v[120:123], v[44:47]\n"
"ds_read_b64_tr_b16 a[164:165], v220 offset:41120\n"
"v_mfma_f32_16x16x32_bf16 v[44:47], a[132:135], v[116:119], v[44:47]\n"
"v_exp_f32 v15, v15\n"
"v_mfma_f32_16x16x32_bf16 v[44:47], a[128:131], v[112:115], v[44:47]\n"
"s_waitcnt lgkmcnt(5)\n"
"ds_read_b64_tr_b16 a[166:167], v220 offset:42144\n"
"v_cvt_pk_bf16_f32 v185, v14, v15\n"
"v_mfma_f32_16x16x32_bf16 v[60:63], a[144:147], v[112:115], v[204:207]\n"
"v_exp_f32 v28, v28\n"
"v_mfma_f32_16x16x32_bf16 v[60:63], a[148:151], v[116:119], v[60:63]\n"
"s_waitcnt lgkmcnt(4)\n"
"ds_read_b64_tr_b16 a[168:169], v220 offset:32904\n"
"v_mfma_f32_16x16x32_bf16 v[60:63], a[152:155], v[120:123], v[60:63]\n"
"v_exp_f32 v29, v29\n"
"v_mfma_f32_16x16x32_bf16 v[60:63], a[156:159], v[124:127], v[60:63]\n"
"ds_read_b64_tr_b16 a[170:171], v220 offset:33928\n"
"v_cvt_pk_bf16_f32 v186, v28, v29\n"
"v_mfma_f32_16x16x32_bf16 v[56:59], a[156:159], v[108:111], v[200:203]\n"
"v_exp_f32 v30, v30\n"
"v_mfma_f32_16x16x32_bf16 v[56:59], a[152:155], v[104:107], v[56:59]\n"
"ds_read_b64_tr_b16 a[172:173], v220 offset:41128\n"
"v_mfma_f32_16x16x32_bf16 v[56:59], a[148:151], v[100:103], v[56:59]\n"
"v_exp_f32 v31, v31\n"
"v_mfma_f32_16x16x32_bf16 v[56:59], a[144:147], v[96:99], v[56:59]\n"
"buffer_load_dwordx4 v223, s[36:39], s56 offen offset:3072 nt lds\n"
"v_mfma_f32_16x16x32_bf16 v[52:55], a[144:147], v[80:83], v[196:199]\n"
"s_sub_u32 s56, s56, 159744\n"
"v_exp_f32 v24, v24\n"
"v_mfma_f32_16x16x32_bf16 v[52:55], a[148:151], v[84:87], v[52:55]\n"
"s_and_b32 s56, s56, 159744\n"
"ds_read_b64_tr_b16 a[174:175], v220 offset:42152\n"
"v_cvt_pk_bf16_f32 v187, v30, v31\n"
"v_mfma_f32_16x16x32_bf16 v[52:55], a[152:155], v[88:91], v[52:55]\n"
"v_exp_f32 v25, v25\n"
"v_mfma_f32_16x16x32_bf16 v[52:55], a[156:159], v[92:95], v[52:55]\n"
"ds_read_b64_tr_b16 a[176:177], v220 offset:33152\n"
"v_cvt_pk_bf16_f32 v178, v24, v25\n"
"v_mfma_f32_16x16x32_bf16 v[48:51], a[156:159], v[76:79], v[192:195]\n"
"v_exp_f32 v26, v26\n"
"v_mfma_f32_16x16x32_bf16 v[48:51], a[152:155], v[72:75], v[48:51]\n"
"ds_read_b64_tr_b16 a[178:179], v220 offset:34176\n"
"v_mfma_f32_16x16x32_bf16 v[48:51], a[148:151], v[68:71], v[48:51]\n"
"v_exp_f32 v27, v27\n"
"v_mfma_f32_16x16x32_bf16 v[48:51], a[144:147], v[64:67], v[48:51]\n"
"ds_read_b64_tr_b16 a[180:181], v220 offset:41376\n"
"v_cvt_pk_bf16_f32 v179, v26, v27\n"
"v_smfmac_f32_16x16x64_bf16 a[228:231], a[200:203], v[128:135], v208\n"
"v_smfmac_f32_16x16x64_bf16 a[232:235], a[200:203], v[136:143], v208\n"
"ds_read_b64_tr_b16 a[182:183], v220 offset:42400\n"
"v_smfmac_f32_16x16x64_bf16 a[236:239], a[200:203], v[144:151], v208\n"
"v_smfmac_f32_16x16x64_bf16 a[240:243], a[200:203], v[152:159], v208\n"
"s_waitcnt lgkmcnt(8)\n"
"ds_read_b64_tr_b16 a[184:185], v220 offset:33160\n"
"v_mfma_f32_16x16x32_bf16 a[0:3], a[160:163], v[128:131], a[0:3]\n"
"v_exp_f32 v20, v20\n"
"v_mfma_f32_16x16x32_bf16 a[0:3], a[164:167], v[132:135], a[0:3]\n"
"ds_read_b64_tr_b16 a[186:187], v220 offset:34184\n"
"v_mfma_f32_16x16x32_bf16 a[4:7], a[164:167], v[140:143], a[4:7]\n"
"s_add_u32 m0, s62, 0\n"
"v_exp_f32 v21, v21\n"
"v_mfma_f32_16x16x32_bf16 a[4:7], a[160:163], v[136:139], a[4:7]\n"
"buffer_load_dwordx4 v224, s[40:43], s57 offen nt lds\n"
"v_mfma_f32_16x16x32_bf16 a[8:11], a[160:163], v[144:147], a[8:11]\n"
"v_exp_f32 v22, v22\n"
"v_mfma_f32_16x16x32_bf16 a[8:11], a[164:167], v[148:151], a[8:11]\n"
"ds_read_b64_tr_b16 a[188:189], v220 offset:41384\n"
"v_cvt_pk_bf16_f32 v170, v20, v21\n"
"v_mfma_f32_16x16x32_bf16 a[12:15], a[164:167], v[156:159], a[12:15]\n"
"v_exp_f32 v23, v23\n"
"v_mfma_f32_16x16x32_bf16 a[12:15], a[160:163], v[152:155], a[12:15]\n"
"s_waitcnt lgkmcnt(7)\n"
"ds_read_b64_tr_b16 a[190:191], v220 offset:42408\n"
"v_cvt_pk_bf16_f32 v171, v22, v23\n"
"v_mfma_f32_16x16x32_bf16 a[28:31], a[168:171], v[152:155], a[28:31]\n"
"v_exp_f32 v16, v16\n"
"v_mfma_f32_16x16x32_bf16 a[28:31], a[172:175], v[156:159], a[28:31]\n"
"ds_read_b64_tr_b16 a[160:161], v220 offset:33408\n"
"v_mfma_f32_16x16x32_bf16 a[24:27], a[172:175], v[148:151], a[24:27]\n"
"v_exp_f32 v17, v17\n"
"v_mfma_f32_16x16x32_bf16 a[24:27], a[168:171], v[144:147], a[24:27]\n"
"ds_read_b64_tr_b16 a[162:163], v220 offset:34432\n"
"v_cvt_pk_bf16_f32 v162, v16, v17\n"
"v_mfma_f32_16x16x32_bf16 a[20:23], a[168:171], v[136:139], a[20:23]\n"
"v_exp_f32 v18, v18\n"
"v_mfma_f32_16x16x32_bf16 a[20:23], a[172:175], v[140:143], a[20:23]\n"
"ds_read_b64_tr_b16 a[164:165], v220 offset:41632\n"
"v_mfma_f32_16x16x32_bf16 a[16:19], a[172:175], v[132:135], a[16:19]\n"
"v_exp_f32 v19, v19\n"
"v_mfma_f32_16x16x32_bf16 a[16:19], a[168:171], v[128:131], a[16:19]\n"
"s_waitcnt lgkmcnt(7)\n"
"ds_read_b64_tr_b16 a[166:167], v220 offset:42656\n"
"v_cvt_pk_bf16_f32 v163, v18, v19\n"
"v_mfma_f32_16x16x32_bf16 a[32:35], a[176:179], v[128:131], a[32:35]\n"
"v_exp_f32 v32, v32\n"
"v_mfma_f32_16x16x32_bf16 a[32:35], a[180:183], v[132:135], a[32:35]\n"
"ds_read_b64_tr_b16 a[168:169], v220 offset:33416\n"
"v_mfma_f32_16x16x32_bf16 a[36:39], a[180:183], v[140:143], a[36:39]\n"
"v_exp_f32 v33, v33\n"
"v_mfma_f32_16x16x32_bf16 a[36:39], a[176:179], v[136:139], a[36:39]\n"
"buffer_load_dwordx4 v224, s[40:43], s57 offen offset:1024 nt lds\n"
"v_mfma_f32_16x16x32_bf16 a[40:43], a[176:179], v[144:147], a[40:43]\n"
"v_exp_f32 v34, v34\n"
"v_mfma_f32_16x16x32_bf16 a[40:43], a[180:183], v[148:151], a[40:43]\n"
"ds_read_b64_tr_b16 a[170:171], v220 offset:34440\n"
"v_cvt_pk_bf16_f32 v164, v32, v33\n"
"v_mfma_f32_16x16x32_bf16 a[44:47], a[180:183], v[156:159], a[44:47]\n"
"v_exp_f32 v35, v35\n"
"v_mfma_f32_16x16x32_bf16 a[44:47], a[176:179], v[152:155], a[44:47]\n"
"s_waitcnt lgkmcnt(6)\n"
"ds_read_b64_tr_b16 a[172:173], v220 offset:41640\n"
"v_cvt_pk_bf16_f32 v165, v34, v35\n"
"v_mfma_f32_16x16x32_bf16 a[60:63], a[184:187], v[152:155], a[60:63]\n"
"v_exp_f32 v36, v36\n"
"v_mfma_f32_16x16x32_bf16 a[60:63], a[188:191], v[156:159], a[60:63]\n"
"ds_read_b64_tr_b16 a[174:175], v220 offset:42664\n"
"v_mfma_f32_16x16x32_bf16 a[56:59], a[188:191], v[148:151], a[56:59]\n"
"v_exp_f32 v37, v37\n"
"v_mfma_f32_16x16x32_bf16 a[56:59], a[184:187], v[144:147], a[56:59]\n"
"ds_read_b64_tr_b16 a[176:177], v220 offset:33664\n"
"v_cvt_pk_bf16_f32 v172, v36, v37\n"
"v_mfma_f32_16x16x32_bf16 a[52:55], a[184:187], v[136:139], a[52:55]\n"
"v_exp_f32 v38, v38\n"
"v_mfma_f32_16x16x32_bf16 a[52:55], a[188:191], v[140:143], a[52:55]\n"
"ds_read_b64_tr_b16 a[178:179], v220 offset:34688\n"
"v_mfma_f32_16x16x32_bf16 a[48:51], a[188:191], v[132:135], a[48:51]\n"
"v_exp_f32 v39, v39\n"
"v_mfma_f32_16x16x32_bf16 a[48:51], a[184:187], v[128:131], a[48:51]\n"
"s_waitcnt lgkmcnt(6)\n"
"ds_read_b64_tr_b16 a[180:181], v220 offset:41888\n"
"v_cvt_pk_bf16_f32 v173, v38, v39\n"
"v_mfma_f32_16x16x32_bf16 a[64:67], a[160:163], v[128:131], a[64:67]\n"
"v_exp_f32 v40, v40\n"
"v_mfma_f32_16x16x32_bf16 a[64:67], a[164:167], v[132:135], a[64:67]\n"
"ds_read_b64_tr_b16 a[182:183], v220 offset:42912\n"
"v_mfma_f32_16x16x32_bf16 a[68:71], a[164:167], v[140:143], a[68:71]\n"
"s_add_u32 m0, s62, 8\n"
"v_exp_f32 v41, v41\n"
"v_mfma_f32_16x16x32_bf16 a[68:71], a[160:163], v[136:139], a[68:71]\n"
"buffer_load_dwordx4 v224, s[40:43], s57 offen offset:2048 nt lds\n"
"v_mfma_f32_16x16x32_bf16 a[72:75], a[160:163], v[144:147], a[72:75]\n"
"v_exp_f32 v42, v42\n"
"v_mfma_f32_16x16x32_bf16 a[72:75], a[164:167], v[148:151], a[72:75]\n"
"ds_read_b64_tr_b16 a[184:185], v220 offset:33672\n"
"v_cvt_pk_bf16_f32 v180, v40, v41\n"
"v_mfma_f32_16x16x32_bf16 a[76:79], a[164:167], v[156:159], a[76:79]\n"
"v_exp_f32 v43, v43\n"
"v_mfma_f32_16x16x32_bf16 a[76:79], a[160:163], v[152:155], a[76:79]\n"
"s_waitcnt lgkmcnt(5)\n"
"ds_read_b64_tr_b16 a[186:187], v220 offset:34696\n"
"v_cvt_pk_bf16_f32 v181, v42, v43\n"
"v_mfma_f32_16x16x32_bf16 a[92:95], a[168:171], v[152:155], a[92:95]\n"
"v_exp_f32 v44, v44\n"
"v_mfma_f32_16x16x32_bf16 a[92:95], a[172:175], v[156:159], a[92:95]\n"
"ds_read_b64_tr_b16 a[188:189], v220 offset:41896\n"
"v_mfma_f32_16x16x32_bf16 a[88:91], a[172:175], v[148:151], a[88:91]\n"
"v_exp_f32 v45, v45\n"
"v_mfma_f32_16x16x32_bf16 a[88:91], a[168:171], v[144:147], a[88:91]\n"
"ds_read_b64_tr_b16 a[190:191], v220 offset:42920\n"
"v_cvt_pk_bf16_f32 v188, v44, v45\n"
"v_mfma_f32_16x16x32_bf16 a[84:87], a[168:171], v[136:139], a[84:87]\n"
"v_exp_f32 v46, v46\n"
"v_mfma_f32_16x16x32_bf16 a[84:87], a[172:175], v[140:143], a[84:87]\n"
"v_exp_f32 v47, v47\n"
"v_mfma_f32_16x16x32_bf16 a[80:83], a[172:175], v[132:135], a[80:83]\n"
"v_exp_f32 v60, v60\n"
"v_mfma_f32_16x16x32_bf16 a[80:83], a[168:171], v[128:131], a[80:83]\n"
"s_waitcnt lgkmcnt(4)\n"
"v_exp_f32 v61, v61\n"
"v_mfma_f32_16x16x32_bf16 a[96:99], a[176:179], v[128:131], a[96:99]\n"
"s_waitcnt vmcnt(7) lgkmcnt(0)\n"
"v_cvt_pk_bf16_f32 v189, v46, v47\n"
"v_cvt_pk_bf16_f32 v190, v60, v61\n"
"v_mfma_f32_16x16x32_bf16 a[96:99], a[180:183], v[132:135], a[96:99]\n"
"s_barrier\n"
"v_mfma_f32_16x16x32_bf16 a[100:103], a[180:183], v[140:143], a[100:103]\n"
"ds_read_b128 a[128:131], v216\n"
"v_mfma_f32_16x16x32_bf16 a[100:103], a[176:179], v[136:139], a[100:103]\n"
"ds_read_b128 a[132:135], v217\n"
"v_mfma_f32_16x16x32_bf16 a[104:107], a[176:179], v[144:147], a[104:107]\n"
"v_mfma_f32_16x16x32_bf16 a[104:107], a[180:183], v[148:151], a[104:107]\n"
"buffer_load_dwordx4 v224, s[40:43], s57 offen offset:3072 nt lds\n"
"v_mfma_f32_16x16x32_bf16 a[108:111], a[180:183], v[156:159], a[108:111]\n"
"s_sub_u32 s57, s57, 159744\n"
"v_mfma_f32_16x16x32_bf16 a[108:111], a[176:179], v[152:155], a[108:111]\n"
"s_and_b32 s57, s57, 159744\n"
"ds_read_b128 a[136:139], v218\n"
"v_mfma_f32_16x16x32_bf16 a[124:127], a[184:187], v[152:155], a[124:127]\n"
"v_mfma_f32_16x16x32_bf16 a[124:127], a[188:191], v[156:159], a[124:127]\n"
"ds_read_b128 a[140:143], v219\n"
"v_mfma_f32_16x16x32_bf16 a[120:123], a[188:191], v[148:151], a[120:123]\n"
"v_mfma_f32_16x16x32_bf16 a[120:123], a[184:187], v[144:147], a[120:123]\n"
"ds_read_b128 a[144:147], v216 offset:1024\n"
"v_mfma_f32_16x16x32_bf16 a[116:119], a[184:187], v[136:139], a[116:119]\n"
"v_mfma_f32_16x16x32_bf16 a[116:119], a[188:191], v[140:143], a[116:119]\n"
"ds_read_b128 a[148:151], v217 offset:1024\n"
"v_mfma_f32_16x16x32_bf16 a[112:115], a[188:191], v[132:135], a[112:115]\n"
"v_mfma_f32_16x16x32_bf16 a[112:115], a[184:187], v[128:131], a[112:115]\n"
"s_waitcnt lgkmcnt(4)\n"
"ds_read_b128 a[152:155], v218 offset:1024\n"
"ds_read_b128 a[156:159], v219 offset:1024\n"
"v_mfma_f32_16x16x32_bf16 v[0:3], a[128:131], v[64:67], v[192:195]\n"
"v_exp_f32 v62, v62\n"
"v_mfma_f32_16x16x32_bf16 v[0:3], a[132:135], v[68:71], v[0:3]\n"
"s_waitcnt lgkmcnt(4)\n"
"v_exp_f32 v63, v63\n"
"v_mfma_f32_16x16x32_bf16 v[0:3], a[136:139], v[72:75], v[0:3]\n"
"v_exp_f32 v56, v56\n"
"v_mfma_f32_16x16x32_bf16 v[0:3], a[140:143], v[76:79], v[0:3]\n"
"v_exp_f32 v57, v57\n"
"v_mfma_f32_16x16x32_bf16 v[4:7], a[140:143], v[92:95], v[196:199]\n"
"s_add_u32 m0, s61, 32768\n"
"v_cvt_pk_bf16_f32 v191, v62, v63\n"
"v_cvt_pk_bf16_f32 v182, v56, v57\n"
"v_mfma_f32_16x16x32_bf16 v[4:7], a[136:139], v[88:91], v[4:7]\n"
"buffer_load_dwordx4 v222, s[36:39], s56 offen nt lds\n"
"v_mfma_f32_16x16x32_bf16 v[4:7], a[132:135], v[84:87], v[4:7]\n"
"v_exp_f32 v58, v58\n"
"v_mfma_f32_16x16x32_bf16 v[4:7], a[128:131], v[80:83], v[4:7]\n"
"v_exp_f32 v59, v59\n"
"v_mfma_f32_16x16x32_bf16 v[8:11], a[128:131], v[96:99], v[200:203]\n"
"s_memtime s[76:77]\n"
"v_exp_f32 v52, v52\n"
"v_mfma_f32_16x16x32_bf16 v[8:11], a[132:135], v[100:103], v[8:11]\n"
"s_memrealtime s[34:35]\n"
"v_exp_f32 v53, v53\n"
"v_mfma_f32_16x16x32_bf16 v[8:11], a[136:139], v[104:107], v[8:11]\n"
"v_exp_f32 v54, v54\n"
"v_mfma_f32_16x16x32_bf16 v[8:11], a[140:143], v[108:111], v[8:11]\n"
"v_exp_f32 v55, v55\n"
"v_mfma_f32_16x16x32_bf16 v[12:15], a[140:143], v[124:127], v[204:207]\n"
"v_exp_f32 v48, v48\n"
"v_mfma_f32_16x16x32_bf16 v[12:15], a[136:139], v[120:123], v[12:15]\n"
"v_exp_f32 v49, v49\n"
"v_mfma_f32_16x16x32_bf16 v[12:15], a[132:135], v[116:119], v[12:15]\n"
"v_exp_f32 v50, v50\n"
"v_mfma_f32_16x16x32_bf16 v[12:15], a[128:131], v[112:115], v[12:15]\n"
"s_waitcnt lgkmcnt(0)\n"
"ds_read_b128 a[128:131], v216 offset:8192\n"
"v_cvt_pk_bf16_f32 v183, v58, v59\n"
"v_mfma_f32_16x16x32_bf16 v[28:31], a[144:147], v[112:115], v[204:207]\n"
"s_sub_u32 s35, s34, s73\n"
"v_exp_f32 v51, v51\n"
"v_mfma_f32_16x16x32_bf16 v[28:31], a[148:151], v[116:119], v[28:31]\n"
"s_cmp_lt_i32 s35, -2\n"
"ds_read_b128 a[132:135], v217 offset:8192\n"
"v_cvt_pk_bf16_f32 v174, v52, v53\n"
"v_mfma_f32_16x16x32_bf16 v[28:31], a[152:155], v[120:123], v[28:31]\n"
"s_cbranch_scc1 L_wL2%=\n"
"L_wbL2%=:\n"
"v_mfma_f32_16x16x32_bf16 v[28:31], a[156:159], v[124:127], v[28:31]\n"
"s_sub_u32 s66, s76, s66\n"
"ds_read_b128 a[136:139], v218 offset:8192\n"
"v_cvt_pk_bf16_f32 v175, v54, v55\n"
"v_mfma_f32_16x16x32_bf16 v[24:27], a[156:159], v[108:111], v[200:203]\n"
"s_sub_u32 s75, s75, 5\n"
"v_exp_f32 v0, v0\n"
"v_mfma_f32_16x16x32_bf16 v[24:27], a[152:155], v[104:107], v[24:27]\n"
"s_max_i32 s75, s75, 0\n"
"buffer_load_dwordx4 v222, s[36:39], s56 offen offset:1024 nt lds\n"
"v_mfma_f32_16x16x32_bf16 v[24:27], a[148:151], v[100:103], v[24:27]\n"
"s_cmp_gt_i32 s66, s58\n"
"v_exp_f32 v1, v1\n"
"v_mfma_f32_16x16x32_bf16 v[24:27], a[144:147], v[96:99], v[24:27]\n"
"s_cselect_b32 s75, 148, s75\n"
"ds_read_b128 a[140:143], v219 offset:8192\n"
"v_cvt_pk_bf16_f32 v166, v48, v49\n"
"v_mfma_f32_16x16x32_bf16 v[20:23], a[144:147], v[80:83], v[196:199]\n"
"s_add_u32 s73, s34, s75\n"
"v_cvt_pk_bf16_f32 v167, v50, v51\n"
"v_cvt_pk_bf16_f32 v128, v0, v1\n"
"v_mfma_f32_16x16x32_bf16 v[20:23], a[148:151], v[84:87], v[20:23]\n"
"s_mov_b32 s66, s76\n"
"v_exp_f32 v2, v2\n"
"v_mfma_f32_16x16x32_bf16 v[20:23], a[152:155], v[88:91], v[20:23]\n"
"v_exp_f32 v3, v3\n"
"v_mfma_f32_16x16x32_bf16 v[20:23], a[156:159], v[92:95], v[20:23]\n"
"v_exp_f32 v4, v4\n"
"v_mfma_f32_16x16x32_bf16 v[16:19], a[156:159], v[76:79], v[192:195]\n"
"v_exp_f32 v5, v5\n"
"v_mfma_f32_16x16x32_bf16 v[16:19], a[152:155], v[72:75], v[16:19]\n"
"v_exp_f32 v6, v6\n"
"v_mfma_f32_16x16x32_bf16 v[16:19], a[148:151], v[68:71], v[16:19]\n"
"v_exp_f32 v7, v7\n"
"v_mfma_f32_16x16x32_bf16 v[16:19], a[144:147], v[64:67], v[16:19]\n"
"s_waitcnt lgkmcnt(2)\n"
"ds_read_b128 a[144:147], v216 offset:9216\n"
"v_cvt_pk_bf16_f32 v129, v2, v3\n"
"v_mfma_f32_16x16x32_bf16 v[32:35], a[128:131], v[64:67], v[192:195]\n"
"v_exp_f32 v8, v8\n"
"v_mfma_f32_16x16x32_bf16 v[32:35], a[132:135], v[68:71], v[32:35]\n"
"s_waitcnt lgkmcnt(1)\n"
"ds_read_b128 a[148:151], v217 offset:9216\n"
"v_cvt_pk_bf16_f32 v136, v4, v5\n"
"v_mfma_f32_16x16x32_bf16 v[32:35], a[136:139], v[72:75], v[32:35]\n"
"v_exp_f32 v9, v9\n"
"v_mfma_f32_16x16x32_bf16 v[32:35], a[140:143], v[76:79], v[32:35]\n"
"ds_read_b128 a[152:155], v218 offset:9216\n"
"v_cvt_pk_bf16_f32 v137, v6, v7\n"
"v_mfma_f32_16x16x32_bf16 v[36:39], a[140:143], v[92:95], v[196:199]\n"
"v_exp_f32 v10, v10\n"
"v_mfma_f32_16x16x32_bf16 v[36:39], a[136:139], v[88:91], v[36:39]\n"
"buffer_load_dwordx4 v223, s[36:39], s56 offen offset:2048 nt lds\n"
"v_mfma_f32_16x16x32_bf16 v[36:39], a[132:135], v[84:87], v[36:39]\n"
"v_exp_f32 v11, v11\n"
"v_mfma_f32_16x16x32_bf16 v[36:39], a[128:131], v[80:83], v[36:39]\n"
"ds_read_b128 a[156:159], v219 offset:9216\n"
"v_cvt_pk_bf16_f32 v144, v8, v9\n"
"v_mfma_f32_16x16x32_bf16 v[40:43], a[128:131], v[96:99], v[200:203]\n"
"v_exp_f32 v12, v12\n"
"v_mfma_f32_16x16x32_bf16 v[40:43], a[132:135], v[100:103], v[40:43]\n"
"ds_read_b64_tr_b16 a[160:161], v220 offset:49344\n"
"v_cvt_pk_bf16_f32 v145, v10, v11\n"
"v_mfma_f32_16x16x32_bf16 v[40:43], a[136:139], v[104:107], v[40:43]\n"
"v_exp_f32 v13, v13\n"
"v_mfma_f32_16x16x32_bf16 v[40:43], a[140:143], v[108:111], v[40:43]\n"
"ds_read_b64_tr_b16 a[162:163], v220 offset:50368\n"
"v_cvt_pk_bf16_f32 v152, v12, v13\n"
"v_mfma_f32_16x16x32_bf16 v[44:47], a[140:143], v[124:127], v[204:207]\n"
"v_exp_f32 v14, v14\n"
"v_mfma_f32_16x16x32_bf16 v[44:47], a[136:139], v[120:123], v[44:47]\n"
"ds_read_b64_tr_b16 a[164:165], v220 offset:57568\n"
"v_mfma_f32_16x16x32_bf16 v[44:47], a[132:135], v[116:119], v[44:47]\n"
"v_exp_f32 v15, v15\n"
"v_mfma_f32_16x16x32_bf16 v[44:47], a[128:131], v[112:115], v[44:47]\n"
"s_waitcnt lgkmcnt(5)\n"
"ds_read_b64_tr_b16 a[166:167], v220 offset:58592\n"
"v_cvt_pk_bf16_f32 v153, v14, v15\n"
"v_mfma_f32_16x16x32_bf16 v[60:63], a[144:147], v[112:115], v[204:207]\n"
"v_exp_f32 v28, v28\n"
"v_mfma_f32_16x16x32_bf16 v[60:63], a[148:151], v[116:119], v[60:63]\n"
"s_waitcnt lgkmcnt(4)\n"
"ds_read_b64_tr_b16 a[168:169], v220 offset:49352\n"
"v_mfma_f32_16x16x32_bf16 v[60:63], a[152:155], v[120:123], v[60:63]\n"
"v_exp_f32 v29, v29\n"
"v_mfma_f32_16x16x32_bf16 v[60:63], a[156:159], v[124:127], v[60:63]\n"
"ds_read_b64_tr_b16 a[170:171], v220 offset:50376\n"
"v_cvt_pk_bf16_f32 v154, v28, v29\n"
"v_mfma_f32_16x16x32_bf16 v[56:59], a[156:159], v[108:111], v[200:203]\n"
"v_exp_f32 v30, v30\n"
"v_mfma_f32_16x16x32_bf16 v[56:59], a[152:155], v[104:107], v[56:59]\n"
"ds_read_b64_tr_b16 a[172:173], v220 offset:57576\n"
"v_mfma_f32_16x16x32_bf16 v[56:59], a[148:151], v[100:103], v[56:59]\n"
"v_exp_f32 v31, v31\n"
"v_mfma_f32_16x16x32_bf16 v[56:59], a[144:147], v[96:99], v[56:59]\n"
"buffer_load_dwordx4 v223, s[36:39], s56 offen offset:3072 nt lds\n"
"v_mfma_f32_16x16x32_bf16 v[52:55], a[144:147], v[80:83], v[196:199]\n"
"s_sub_u32 s56, s56, 159744\n"
"v_exp_f32 v24, v24\n"
"v_mfma_f32_16x16x32_bf16 v[52:55], a[148:151], v[84:87], v[52:55]\n"
"s_and_b32 s56, s56, 159744\n"
"ds_read_b64_tr_b16 a[174:175], v220 offset:58600\n"
"v_cvt_pk_bf16_f32 v155, v30, v31\n"
"v_mfma_f32_16x16x32_bf16 v[52:55], a[152:155], v[88:91], v[52:55]\n"
"v_exp_f32 v25, v25\n"
"v_mfma_f32_16x16x32_bf16 v[52:55], a[156:159], v[92:95], v[52:55]\n"
"ds_read_b64_tr_b16 a[176:177], v220 offset:49600\n"
"v_cvt_pk_bf16_f32 v146, v24, v25\n"
"v_mfma_f32_16x16x32_bf16 v[48:51], a[156:159], v[76:79], v[192:195]\n"
"v_exp_f32 v26, v26\n"
"v_mfma_f32_16x16x32_bf16 v[48:51], a[152:155], v[72:75], v[48:51]\n"
"ds_read_b64_tr_b16 a[178:179], v220 offset:50624\n"
"v_mfma_f32_16x16x32_bf16 v[48:51], a[148:151], v[68:71], v[48:51]\n"
"v_exp_f32 v27, v27\n"
"v_mfma_f32_16x16x32_bf16 v[48:51], a[144:147], v[64:67], v[48:51]\n"
"ds_read_b64_tr_b16 a[180:181], v220 offset:57824\n"
"v_cvt_pk_bf16_f32 v147, v26, v27\n"
"v_smfmac_f32_16x16x64_bf16 a[228:231], a[200:203], v[160:167], v208\n"
"v_smfmac_f32_16x16x64_bf16 a[232:235], a[200:203], v[168:175], v208\n"
"ds_read_b64_tr_b16 a[182:183], v220 offset:58848\n"
"v_smfmac_f32_16x16x64_bf16 a[236:239], a[200:203], v[176:183], v208\n"
"v_smfmac_f32_16x16x64_bf16 a[240:243], a[200:203], v[184:191], v208\n"
"s_waitcnt lgkmcnt(8)\n"
"ds_read_b64_tr_b16 a[184:185], v220 offset:49608\n"
"v_mfma_f32_16x16x32_bf16 a[0:3], a[160:163], v[160:163], a[0:3]\n"
"v_exp_f32 v20, v20\n"
"v_mfma_f32_16x16x32_bf16 a[0:3], a[164:167], v[164:167], a[0:3]\n"
"ds_read_b64_tr_b16 a[186:187], v220 offset:50632\n"
"v_mfma_f32_16x16x32_bf16 a[4:7], a[164:167], v[172:175], a[4:7]\n"
"s_add_u32 m0, s62, 16448\n"
"v_exp_f32 v21, v21\n"
"v_mfma_f32_16x16x32_bf16 a[4:7], a[160:163], v[168:171], a[4:7]\n"
"buffer_load_dwordx4 v224, s[40:43], s57 offen nt lds\n"
"v_mfma_f32_16x16x32_bf16 a[8:11], a[160:163], v[176:179], a[8:11]\n"
"v_exp_f32 v22, v22\n"
"v_mfma_f32_16x16x32_bf16 a[8:11], a[164:167], v[180:183], a[8:11]\n"
"ds_read_b64_tr_b16 a[188:189], v220 offset:57832\n"
"v_cvt_pk_bf16_f32 v138, v20, v21\n"
"v_mfma_f32_16x16x32_bf16 a[12:15], a[164:167], v[188:191], a[12:15]\n"
"v_exp_f32 v23, v23\n"
"v_mfma_f32_16x16x32_bf16 a[12:15], a[160:163], v[184:187], a[12:15]\n"
"s_waitcnt lgkmcnt(7)\n"
"ds_read_b64_tr_b16 a[190:191], v220 offset:58856\n"
"v_cvt_pk_bf16_f32 v139, v22, v23\n"
"v_mfma_f32_16x16x32_bf16 a[28:31], a[168:171], v[184:187], a[28:31]\n"
"v_exp_f32 v16, v16\n"
"v_mfma_f32_16x16x32_bf16 a[28:31], a[172:175], v[188:191], a[28:31]\n"
"ds_read_b64_tr_b16 a[160:161], v220 offset:49856\n"
"v_mfma_f32_16x16x32_bf16 a[24:27], a[172:175], v[180:183], a[24:27]\n"
"v_exp_f32 v17, v17\n"
"v_mfma_f32_16x16x32_bf16 a[24:27], a[168:171], v[176:179], a[24:27]\n"
"ds_read_b64_tr_b16 a[162:163], v220 offset:50880\n"
"v_cvt_pk_bf16_f32 v130, v16, v17\n"
"v_mfma_f32_16x16x32_bf16 a[20:23], a[168:171], v[168:171], a[20:23]\n"
"v_exp_f32 v18, v18\n"
"v_mfma_f32_16x16x32_bf16 a[20:23], a[172:175], v[172:175], a[20:23]\n"
"ds_read_b64_tr_b16 a[164:165], v220 offset:58080\n"
"v_mfma_f32_16x16x32_bf16 a[16:19], a[172:175], v[164:167], a[16:19]\n"
"v_exp_f32 v19, v19\n"
"v_mfma_f32_16x16x32_bf16 a[16:19], a[168:171], v[160:163], a[16:19]\n"
"s_waitcnt lgkmcnt(7)\n"
"ds_read_b64_tr_b16 a[166:167], v220 offset:59104\n"
"v_cvt_pk_bf16_f32 v131, v18, v19\n"
"v_mfma_f32_16x16x32_bf16 a[32:35], a[176:179], v[160:163], a[32:35]\n"
"v_exp_f32 v32, v32\n"
"v_mfma_f32_16x16x32_bf16 a[32:35], a[180:183], v[164:167], a[32:35]\n"
"ds_read_b64_tr_b16 a[168:169], v220 offset:49864\n"
"v_mfma_f32_16x16x32_bf16 a[36:39], a[180:183], v[172:175], a[36:39]\n"
"v_exp_f32 v33, v33\n"
"v_mfma_f32_16x16x32_bf16 a[36:39], a[176:179], v[168:171], a[36:39]\n"
"buffer_load_dwordx4 v224, s[40:43], s57 offen offset:1024 nt lds\n"
"v_mfma_f32_16x16x32_bf16 a[40:43], a[176:179], v[176:179], a[40:43]\n"
"v_exp_f32 v34, v34\n"
"v_mfma_f32_16x16x32_bf16 a[40:43], a[180:183], v[180:183], a[40:43]\n"
"ds_read_b64_tr_b16 a[170:171], v220 offset:50888\n"
"v_cvt_pk_bf16_f32 v132, v32, v33\n"
"v_mfma_f32_16x16x32_bf16 a[44:47], a[180:183], v[188:191], a[44:47]\n"
"v_exp_f32 v35, v35\n"
"v_mfma_f32_16x16x32_bf16 a[44:47], a[176:179], v[184:187], a[44:47]\n"
"s_waitcnt lgkmcnt(6)\n"
"ds_read_b64_tr_b16 a[172:173], v220 offset:58088\n"
"v_cvt_pk_bf16_f32 v133, v34, v35\n"
"v_mfma_f32_16x16x32_bf16 a[60:63], a[184:187], v[184:187], a[60:63]\n"
"v_exp_f32 v36, v36\n"
"v_mfma_f32_16x16x32_bf16 a[60:63], a[188:191], v[188:191], a[60:63]\n"
"ds_read_b64_tr_b16 a[174:175], v220 offset:59112\n"
"v_mfma_f32_16x16x32_bf16 a[56:59], a[188:191], v[180:183], a[56:59]\n"
"v_exp_f32 v37, v37\n"
"v_mfma_f32_16x16x32_bf16 a[56:59], a[184:187], v[176:179], a[56:59]\n"
"ds_read_b64_tr_b16 a[176:177], v220 offset:50112\n"
"v_cvt_pk_bf16_f32 v140, v36, v37\n"
"v_mfma_f32_16x16x32_bf16 a[52:55], a[184:187], v[168:171], a[52:55]\n"
"v_exp_f32 v38, v38\n"
"v_mfma_f32_16x16x32_bf16 a[52:55], a[188:191], v[172:175], a[52:55]\n"
"ds_read_b64_tr_b16 a[178:179], v220 offset:51136\n"
"v_mfma_f32_16x16x32_bf16 a[48:51], a[188:191], v[164:167], a[48:51]\n"
"v_exp_f32 v39, v39\n"
"v_mfma_f32_16x16x32_bf16 a[48:51], a[184:187], v[160:163], a[48:51]\n"
"s_waitcnt lgkmcnt(6)\n"
"ds_read_b64_tr_b16 a[180:181], v220 offset:58336\n"
"v_cvt_pk_bf16_f32 v141, v38, v39\n"
"v_mfma_f32_16x16x32_bf16 a[64:67], a[160:163], v[160:163], a[64:67]\n"
"v_exp_f32 v40, v40\n"
"v_mfma_f32_16x16x32_bf16 a[64:67], a[164:167], v[164:167], a[64:67]\n"
"ds_read_b64_tr_b16 a[182:183], v220 offset:59360\n"
"v_mfma_f32_16x16x32_bf16 a[68:71], a[164:167], v[172:175], a[68:71]\n"
"s_add_u32 m0, s62, 16456\n"
"v_exp_f32 v41, v41\n"
"v_mfma_f32_16x16x32_bf16 a[68:71], a[160:163], v[168:171], a[68:71]\n"
"buffer_load_dwordx4 v224, s[40:43], s57 offen offset:2048 nt lds\n"
"v_mfma_f32_16x16x32_bf16 a[72:75], a[160:163], v[176:179], a[72:75]\n"
"v_exp_f32 v42, v42\n"
"v_mfma_f32_16x16x32_bf16 a[72:75], a[164:167], v[180:183], a[72:75]\n"
"ds_read_b64_tr_b16 a[184:185], v220 offset:50120\n"
"v_cvt_pk_bf16_f32 v148, v40, v41\n"
"v_mfma_f32_16x16x32_bf16 a[76:79], a[164:167], v[188:191], a[76:79]\n"
"v_exp_f32 v43, v43\n"
"v_mfma_f32_16x16x32_bf16 a[76:79], a[160:163], v[184:187], a[76:79]\n"
"s_waitcnt lgkmcnt(5)\n"
"ds_read_b64_tr_b16 a[186:187], v220 offset:51144\n"
"v_cvt_pk_bf16_f32 v149, v42, v43\n"
"v_mfma_f32_16x16x32_bf16 a[92:95], a[168:171], v[184:187], a[92:95]\n"
"v_exp_f32 v44, v44\n"
"v_mfma_f32_16x16x32_bf16 a[92:95], a[172:175], v[188:191], a[92:95]\n"
"ds_read_b64_tr_b16 a[188:189], v220 offset:58344\n"
"v_mfma_f32_16x16x32_bf16 a[88:91], a[172:175], v[180:183], a[88:91]\n"
"v_exp_f32 v45, v45\n"
"v_mfma_f32_16x16x32_bf16 a[88:91], a[168:171], v[176:179], a[88:91]\n"
"ds_read_b64_tr_b16 a[190:191], v220 offset:59368\n"
"v_cvt_pk_bf16_f32 v156, v44, v45\n"
"v_mfma_f32_16x16x32_bf16 a[84:87], a[168:171], v[168:171], a[84:87]\n"
"v_exp_f32 v46, v46\n"
"v_mfma_f32_16x16x32_bf16 a[84:87], a[172:175], v[172:175], a[84:87]\n"
"v_exp_f32 v47, v47\n"
"v_mfma_f32_16x16x32_bf16 a[80:83], a[172:175], v[164:167], a[80:83]\n"
"v_exp_f32 v60, v60\n"
"v_mfma_f32_16x16x32_bf16 a[80:83], a[168:171], v[160:163], a[80:83]\n"
"s_waitcnt lgkmcnt(4)\n"
"v_exp_f32 v61, v61\n"
"v_mfma_f32_16x16x32_bf16 a[96:99], a[176:179], v[160:163], a[96:99]\n"
"s_waitcnt vmcnt(7) lgkmcnt(0)\n"
"v_cvt_pk_bf16_f32 v157, v46, v47\n"
"v_cvt_pk_bf16_f32 v158, v60, v61\n"
"v_mfma_f32_16x16x32_bf16 a[96:99], a[180:183], v[164:167], a[96:99]\n"
"s_barrier\n"
"v_mfma_f32_16x16x32_bf16 a[100:103], a[180:183], v[172:175], a[100:103]\n"
"ds_read_b128 a[128:131], v216 offset:16384\n"
"v_mfma_f32_16x16x32_bf16 a[100:103], a[176:179], v[168:171], a[100:103]\n"
"ds_read_b128 a[132:135], v217 offset:16384\n"
"v_mfma_f32_16x16x32_bf16 a[104:107], a[176:179], v[176:179], a[104:107]\n"
"v_mfma_f32_16x16x32_bf16 a[104:107], a[180:183], v[180:183], a[104:107]\n"
"buffer_load_dwordx4 v224, s[40:43], s57 offen offset:3072 nt lds\n"
"v_mfma_f32_16x16x32_bf16 a[108:111], a[180:183], v[188:191], a[108:111]\n"
"s_sub_u32 s57, s57, 159744\n"
"v_mfma_f32_16x16x32_bf16 a[108:111], a[176:179], v[184:187], a[108:111]\n"
"s_and_b32 s57, s57, 159744\n"
"ds_read_b128 a[136:139], v218 offset:16384\n"
"v_mfma_f32_16x16x32_bf16 a[124:127], a[184:187], v[184:187], a[124:127]\n"
"v_mfma_f32_16x16x32_bf16 a[124:127], a[188:191], v[188:191], a[124:127]\n"
"ds_read_b128 a[140:143], v219 offset:16384\n"
"v_mfma_f32_16x16x32_bf16 a[120:123], a[188:191], v[180:183], a[120:123]\n"
"v_mfma_f32_16x16x32_bf16 a[120:123], a[184:187], v[176:179], a[120:123]\n"
"ds_read_b128 a[144:147], v216 offset:17408\n"
"v_mfma_f32_16x16x32_bf16 a[116:119], a[184:187], v[168:171], a[116:119]\n"
"v_mfma_f32_16x16x32_bf16 a[116:119], a[188:191], v[172:175], a[116:119]\n"
"ds_read_b128 a[148:151], v217 offset:17408\n"
"v_mfma_f32_16x16x32_bf16 a[112:115], a[188:191], v[164:167], a[112:115]\n"
"v_mfma_f32_16x16x32_bf16 a[112:115], a[184:187], v[160:163], a[112:115]\n"
"s_waitcnt lgkmcnt(4)\n"
"ds_read_b128 a[152:155], v218 offset:17408\n"
"ds_read_b128 a[156:159], v219 offset:17408\n"
"v_mfma_f32_16x16x32_bf16 v[0:3], a[128:131], v[64:67], v[192:195]\n"
"v_exp_f32 v62, v62\n"
"v_mfma_f32_16x16x32_bf16 v[0:3], a[132:135], v[68:71], v[0:3]\n"
"s_waitcnt lgkmcnt(4)\n"
"v_exp_f32 v63, v63\n"
"v_mfma_f32_16x16x32_bf16 v[0:3], a[136:139], v[72:75], v[0:3]\n"
"v_exp_f32 v56, v56\n"
"v_mfma_f32_16x16x32_bf16 v[0:3], a[140:143], v[76:79], v[0:3]\n"
"v_exp_f32 v57, v57\n"
"v_mfma_f32_16x16x32_bf16 v[4:7], a[140:143], v[92:95], v[196:199]\n"
"s_add_u32 m0, s61, 49152\n"
"v_cvt_pk_bf16_f32 v159, v62, v63\n"
"v_cvt_pk_bf16_f32 v150, v56, v57\n"
"v_mfma_f32_16x16x32_bf16 v[4:7], a[136:139], v[88:91], v[4:7]\n"
"buffer_load_dwordx4 v222, s[36:39], s56 offen nt lds\n"
"v_mfma_f32_16x16x32_bf16 v[4:7], a[132:135], v[84:87], v[4:7]\n"
"v_exp_f32 v58, v58\n"
"v_mfma_f32_16x16x32_bf16 v[4:7], a[128:131], v[80:83], v[4:7]\n"
"v_exp_f32 v59, v59\n"
"v_mfma_f32_16x16x32_bf16 v[8:11], a[128:131], v[96:99], v[200:203]\n"
"s_memtime s[76:77]\n"
"v_exp_f32 v52, v52\n"
"v_mfma_f32_16x16x32_bf16 v[8:11], a[132:135], v[100:103], v[8:11]\n"
"s_memrealtime s[34:35]\n"
"v_exp_f32 v53, v53\n"
"v_mfma_f32_16x16x32_bf16 v[8:11], a[136:139], v[104:107], v[8:11]\n"
"v_exp_f32 v54, v54\n"
"v_mfma_f32_16x16x32_bf16 v[8:11], a[140:143], v[108:111], v[8:11]\n"
"v_exp_f32 v55, v55\n"
"v_mfma_f32_16x16x32_bf16 v[12:15], a[140:143], v[124:127], v[204:207]\n"
"v_exp_f32 v48, v48\n"
"v_mfma_f32_16x16x32_bf16 v[12:15], a[136:139], v[120:123], v[12:15]\n"
"v_exp_f32 v49, v49\n"
"v_mfma_f32_16x16x32_bf16 v[12:15], a[132:135], v[116:119], v[12:15]\n"
"v_exp_f32 v50, v50\n"
"v_mfma_f32_16x16x32_bf16 v[12:15], a[128:131], v[112:115], v[12:15]\n"
"s_waitcnt lgkmcnt(0)\n"
"ds_read_b128 a[128:131], v216 offset:24576\n"
"v_cvt_pk_bf16_f32 v151, v58, v59\n"
"v_mfma_f32_16x16x32_bf16 v[28:31], a[144:147], v[112:115], v[204:207]\n"
"s_sub_u32 s35, s34, s73\n"
"v_exp_f32 v51, v51\n"
"v_mfma_f32_16x16x32_bf16 v[28:31], a[148:151], v[116:119], v[28:31]\n"
"s_cmp_lt_i32 s35, -2\n"
"ds_read_b128 a[132:135], v217 offset:24576\n"
"v_cvt_pk_bf16_f32 v142, v52, v53\n"
"v_mfma_f32_16x16x32_bf16 v[28:31], a[152:155], v[120:123], v[28:31]\n"
"s_cbranch_scc1 L_wL3%=\n"
"L_wbL3%=:\n"
"v_mfma_f32_16x16x32_bf16 v[28:31], a[156:159], v[124:127], v[28:31]\n"
"s_sub_u32 s66, s76, s66\n"
"ds_read_b128 a[136:139], v218 offset:24576\n"
"v_cvt_pk_bf16_f32 v143, v54, v55\n"
"v_mfma_f32_16x16x32_bf16 v[24:27], a[156:159], v[108:111], v[200:203]\n"
"s_sub_u32 s75, s75, 5\n"
"v_exp_f32 v0, v0\n"
"v_mfma_f32_16x16x32_bf16 v[24:27], a[152:155], v[104:107], v[24:27]\n"
"s_max_i32 s75, s75, 0\n"
"buffer_load_dwordx4 v222, s[36:39], s56 offen offset:1024 nt lds\n"
"v_mfma_f32_16x16x32_bf16 v[24:27], a[148:151], v[100:103], v[24:27]\n"
"s_cmp_gt_i32 s66, s58\n"
"v_exp_f32 v1, v1\n"
"v_mfma_f32_16x16x32_bf16 v[24:27], a[144:147], v[96:99], v[24:27]\n"
"s_cselect_b32 s75, 148, s75\n"
"ds_read_b128 a[140:143], v219 offset:24576\n"
"v_cvt_pk_bf16_f32 v134, v48, v49\n"
"v_mfma_f32_16x16x32_bf16 v[20:23], a[144:147], v[80:83], v[196:199]\n"
"s_add_u32 s73, s34, s75\n"
"v_cvt_pk_bf16_f32 v135, v50, v51\n"
"v_cvt_pk_bf16_f32 v160, v0, v1\n"
"v_mfma_f32_16x16x32_bf16 v[20:23], a[148:151], v[84:87], v[20:23]\n"
"s_mov_b32 s66, s76\n"
"v_exp_f32 v2, v2\n"
"v_mfma_f32_16x16x32_bf16 v[20:23], a[152:155], v[88:91], v[20:23]\n"
"v_exp_f32 v3, v3\n"
"v_mfma_f32_16x16x32_bf16 v[20:23], a[156:159], v[92:95], v[20:23]\n"
"v_exp_f32 v4, v4\n"
"v_mfma_f32_16x16x32_bf16 v[16:19], a[156:159], v[76:79], v[192:195]\n"
"v_exp_f32 v5, v5\n"
"v_mfma_f32_16x16x32_bf16 v[16:19], a[152:155], v[72:75], v[16:19]\n"
"v_exp_f32 v6, v6\n"
"v_mfma_f32_16x16x32_bf16 v[16:19], a[148:151], v[68:71], v[16:19]\n"
"v_exp_f32 v7, v7\n"
"v_mfma_f32_16x16x32_bf16 v[16:19], a[144:147], v[64:67], v[16:19]\n"
"s_waitcnt lgkmcnt(2)\n"
"ds_read_b128 a[144:147], v216 offset:25600\n"
"v_cvt_pk_bf16_f32 v161, v2, v3\n"
"v_mfma_f32_16x16x32_bf16 v[32:35], a[128:131], v[64:67], v[192:195]\n"
"v_exp_f32 v8, v8\n"
"v_mfma_f32_16x16x32_bf16 v[32:35], a[132:135], v[68:71], v[32:35]\n"
"s_waitcnt lgkmcnt(1)\n"
"ds_read_b128 a[148:151], v217 offset:25600\n"
"v_cvt_pk_bf16_f32 v168, v4, v5\n"
"v_mfma_f32_16x16x32_bf16 v[32:35], a[136:139], v[72:75], v[32:35]\n"
"v_exp_f32 v9, v9\n"
"v_mfma_f32_16x16x32_bf16 v[32:35], a[140:143], v[76:79], v[32:35]\n"
"ds_read_b128 a[152:155], v218 offset:25600\n"
"v_cvt_pk_bf16_f32 v169, v6, v7\n"
"v_mfma_f32_16x16x32_bf16 v[36:39], a[140:143], v[92:95], v[196:199]\n"
"v_exp_f32 v10, v10\n"
"v_mfma_f32_16x16x32_bf16 v[36:39], a[136:139], v[88:91], v[36:39]\n"
"buffer_load_dwordx4 v223, s[36:39], s56 offen offset:2048 nt lds\n"
"v_mfma_f32_16x16x32_bf16 v[36:39], a[132:135], v[84:87], v[36:39]\n"
"v_exp_f32 v11, v11\n"
"v_mfma_f32_16x16x32_bf16 v[36:39], a[128:131], v[80:83], v[36:39]\n"
"ds_read_b128 a[156:159], v219 offset:25600\n"
"v_cvt_pk_bf16_f32 v176, v8, v9\n"
"v_mfma_f32_16x16x32_bf16 v[40:43], a[128:131], v[96:99], v[200:203]\n"
"v_exp_f32 v12, v12\n"
"v_mfma_f32_16x16x32_bf16 v[40:43], a[132:135], v[100:103], v[40:43]\n"
"ds_read_b64_tr_b16 a[160:161], v220\n"
"v_cvt_pk_bf16_f32 v177, v10, v11\n"
"v_mfma_f32_16x16x32_bf16 v[40:43], a[136:139], v[104:107], v[40:43]\n"
"v_exp_f32 v13, v13\n"
"v_mfma_f32_16x16x32_bf16 v[40:43], a[140:143], v[108:111], v[40:43]\n"
"ds_read_b64_tr_b16 a[162:163], v220 offset:1024\n"
"v_cvt_pk_bf16_f32 v184, v12, v13\n"
"v_mfma_f32_16x16x32_bf16 v[44:47], a[140:143], v[124:127], v[204:207]\n"
"v_exp_f32 v14, v14\n"
"v_mfma_f32_16x16x32_bf16 v[44:47], a[136:139], v[120:123], v[44:47]\n"
"ds_read_b64_tr_b16 a[164:165], v220 offset:8224\n"
"v_mfma_f32_16x16x32_bf16 v[44:47], a[132:135], v[116:119], v[44:47]\n"
"v_exp_f32 v15, v15\n"
"v_mfma_f32_16x16x32_bf16 v[44:47], a[128:131], v[112:115], v[44:47]\n"
"s_waitcnt lgkmcnt(5)\n"
"ds_read_b64_tr_b16 a[166:167], v220 offset:9248\n"
"v_cvt_pk_bf16_f32 v185, v14, v15\n"
"v_mfma_f32_16x16x32_bf16 v[60:63], a[144:147], v[112:115], v[204:207]\n"
"v_exp_f32 v28, v28\n"
"v_mfma_f32_16x16x32_bf16 v[60:63], a[148:151], v[116:119], v[60:63]\n"
"s_waitcnt lgkmcnt(4)\n"
"ds_read_b64_tr_b16 a[168:169], v220 offset:8\n"
"v_mfma_f32_16x16x32_bf16 v[60:63], a[152:155], v[120:123], v[60:63]\n"
"v_exp_f32 v29, v29\n"
"v_mfma_f32_16x16x32_bf16 v[60:63], a[156:159], v[124:127], v[60:63]\n"
"ds_read_b64_tr_b16 a[170:171], v220 offset:1032\n"
"v_cvt_pk_bf16_f32 v186, v28, v29\n"
"v_mfma_f32_16x16x32_bf16 v[56:59], a[156:159], v[108:111], v[200:203]\n"
"v_exp_f32 v30, v30\n"
"v_mfma_f32_16x16x32_bf16 v[56:59], a[152:155], v[104:107], v[56:59]\n"
"ds_read_b64_tr_b16 a[172:173], v220 offset:8232\n"
"v_mfma_f32_16x16x32_bf16 v[56:59], a[148:151], v[100:103], v[56:59]\n"
"v_exp_f32 v31, v31\n"
"v_mfma_f32_16x16x32_bf16 v[56:59], a[144:147], v[96:99], v[56:59]\n"
"buffer_load_dwordx4 v223, s[36:39], s56 offen offset:3072 nt lds\n"
"v_mfma_f32_16x16x32_bf16 v[52:55], a[144:147], v[80:83], v[196:199]\n"
"s_sub_u32 s56, s56, 159744\n"
"v_exp_f32 v24, v24\n"
"v_mfma_f32_16x16x32_bf16 v[52:55], a[148:151], v[84:87], v[52:55]\n"
"s_and_b32 s56, s56, 159744\n"
"ds_read_b64_tr_b16 a[174:175], v220 offset:9256\n"
"v_cvt_pk_bf16_f32 v187, v30, v31\n"
"v_mfma_f32_16x16x32_bf16 v[52:55], a[152:155], v[88:91], v[52:55]\n"
"v_exp_f32 v25, v25\n"
"v_mfma_f32_16x16x32_bf16 v[52:55], a[156:159], v[92:95], v[52:55]\n"
"ds_read_b64_tr_b16 a[176:177], v220 offset:256\n"
"v_cvt_pk_bf16_f32 v178, v24, v25\n"
"v_mfma_f32_16x16x32_bf16 v[48:51], a[156:159], v[76:79], v[192:195]\n"
"v_exp_f32 v26, v26\n"
"v_mfma_f32_16x16x32_bf16 v[48:51], a[152:155], v[72:75], v[48:51]\n"
"ds_read_b64_tr_b16 a[178:179], v220 offset:1280\n"
"v_mfma_f32_16x16x32_bf16 v[48:51], a[148:151], v[68:71], v[48:51]\n"
"v_exp_f32 v27, v27\n"
"v_mfma_f32_16x16x32_bf16 v[48:51], a[144:147], v[64:67], v[48:51]\n"
"ds_read_b64_tr_b16 a[180:181], v220 offset:8480\n"
"v_cvt_pk_bf16_f32 v179, v26, v27\n"
"v_smfmac_f32_16x16x64_bf16 a[228:231], a[200:203], v[128:135], v208\n"
"v_smfmac_f32_16x16x64_bf16 a[232:235], a[200:203], v[136:143], v208\n"
"ds_read_b64_tr_b16 a[182:183], v220 offset:9504\n"
"v_smfmac_f32_16x16x64_bf16 a[236:239], a[200:203], v[144:151], v208\n"
"v_smfmac_f32_16x16x64_bf16 a[240:243], a[200:203], v[152:159], v208\n"
"s_waitcnt lgkmcnt(8)\n"
"ds_read_b64_tr_b16 a[184:185], v220 offset:264\n"
"v_mfma_f32_16x16x32_bf16 a[0:3], a[160:163], v[128:131], a[0:3]\n"
"v_exp_f32 v20, v20\n"
"v_mfma_f32_16x16x32_bf16 a[0:3], a[164:167], v[132:135], a[0:3]\n"
"ds_read_b64_tr_b16 a[186:187], v220 offset:1288\n"
"v_mfma_f32_16x16x32_bf16 a[4:7], a[164:167], v[140:143], a[4:7]\n"
"s_add_u32 m0, s62, 32896\n"
"v_exp_f32 v21, v21\n"
"v_mfma_f32_16x16x32_bf16 a[4:7], a[160:163], v[136:139], a[4:7]\n"
"buffer_load_dwordx4 v224, s[40:43], s57 offen nt lds\n"
"v_mfma_f32_16x16x32_bf16 a[8:11], a[160:163], v[144:147], a[8:11]\n"
"v_exp_f32 v22, v22\n"
"v_mfma_f32_16x16x32_bf16 a[8:11], a[164:167], v[148:151], a[8:11]\n"
"ds_read_b64_tr_b16 a[188:189], v220 offset:8488\n"
"v_cvt_pk_bf16_f32 v170, v20, v21\n"
"v_mfma_f32_16x16x32_bf16 a[12:15], a[164:167], v[156:159], a[12:15]\n"
"v_exp_f32 v23, v23\n"
"v_mfma_f32_16x16x32_bf16 a[12:15], a[160:163], v[152:155], a[12:15]\n"
"s_waitcnt lgkmcnt(7)\n"
"ds_read_b64_tr_b16 a[190:191], v220 offset:9512\n"
"v_cvt_pk_bf16_f32 v171, v22, v23\n"
"v_mfma_f32_16x16x32_bf16 a[28:31], a[168:171], v[152:155], a[28:31]\n"
"v_exp_f32 v16, v16\n"
"v_mfma_f32_16x16x32_bf16 a[28:31], a[172:175], v[156:159], a[28:31]\n"
"ds_read_b64_tr_b16 a[160:161], v220 offset:512\n"
"v_mfma_f32_16x16x32_bf16 a[24:27], a[172:175], v[148:151], a[24:27]\n"
"v_exp_f32 v17, v17\n"
"v_mfma_f32_16x16x32_bf16 a[24:27], a[168:171], v[144:147], a[24:27]\n"
"ds_read_b64_tr_b16 a[162:163], v220 offset:1536\n"
"v_cvt_pk_bf16_f32 v162, v16, v17\n"
"v_mfma_f32_16x16x32_bf16 a[20:23], a[168:171], v[136:139], a[20:23]\n"
"v_exp_f32 v18, v18\n"
"v_mfma_f32_16x16x32_bf16 a[20:23], a[172:175], v[140:143], a[20:23]\n"
"ds_read_b64_tr_b16 a[164:165], v220 offset:8736\n"
"v_mfma_f32_16x16x32_bf16 a[16:19], a[172:175], v[132:135], a[16:19]\n"
"v_exp_f32 v19, v19\n"
"v_mfma_f32_16x16x32_bf16 a[16:19], a[168:171], v[128:131], a[16:19]\n"
"s_waitcnt lgkmcnt(7)\n"
"ds_read_b64_tr_b16 a[166:167], v220 offset:9760\n"
"v_cvt_pk_bf16_f32 v163, v18, v19\n"
"v_mfma_f32_16x16x32_bf16 a[32:35], a[176:179], v[128:131], a[32:35]\n"
"v_exp_f32 v32, v32\n"
"v_mfma_f32_16x16x32_bf16 a[32:35], a[180:183], v[132:135], a[32:35]\n"
"ds_read_b64_tr_b16 a[168:169], v220 offset:520\n"
"v_mfma_f32_16x16x32_bf16 a[36:39], a[180:183], v[140:143], a[36:39]\n"
"v_exp_f32 v33, v33\n"
"v_mfma_f32_16x16x32_bf16 a[36:39], a[176:179], v[136:139], a[36:39]\n"
"buffer_load_dwordx4 v224, s[40:43], s57 offen offset:1024 nt lds\n"
"v_mfma_f32_16x16x32_bf16 a[40:43], a[176:179], v[144:147], a[40:43]\n"
"v_exp_f32 v34, v34\n"
"v_mfma_f32_16x16x32_bf16 a[40:43], a[180:183], v[148:151], a[40:43]\n"
"ds_read_b64_tr_b16 a[170:171], v220 offset:1544\n"
"v_cvt_pk_bf16_f32 v164, v32, v33\n"
"v_mfma_f32_16x16x32_bf16 a[44:47], a[180:183], v[156:159], a[44:47]\n"
"v_exp_f32 v35, v35\n"
"v_mfma_f32_16x16x32_bf16 a[44:47], a[176:179], v[152:155], a[44:47]\n"
"s_waitcnt lgkmcnt(6)\n"
"ds_read_b64_tr_b16 a[172:173], v220 offset:8744\n"
"v_cvt_pk_bf16_f32 v165, v34, v35\n"
"v_mfma_f32_16x16x32_bf16 a[60:63], a[184:187], v[152:155], a[60:63]\n"
"v_exp_f32 v36, v36\n"
"v_mfma_f32_16x16x32_bf16 a[60:63], a[188:191], v[156:159], a[60:63]\n"
"ds_read_b64_tr_b16 a[174:175], v220 offset:9768\n"
"v_mfma_f32_16x16x32_bf16 a[56:59], a[188:191], v[148:151], a[56:59]\n"
"v_exp_f32 v37, v37\n"
"v_mfma_f32_16x16x32_bf16 a[56:59], a[184:187], v[144:147], a[56:59]\n"
"ds_read_b64_tr_b16 a[176:177], v220 offset:768\n"
"v_cvt_pk_bf16_f32 v172, v36, v37\n"
"v_mfma_f32_16x16x32_bf16 a[52:55], a[184:187], v[136:139], a[52:55]\n"
"v_exp_f32 v38, v38\n"
"v_mfma_f32_16x16x32_bf16 a[52:55], a[188:191], v[140:143], a[52:55]\n"
"ds_read_b64_tr_b16 a[178:179], v220 offset:1792\n"
"v_mfma_f32_16x16x32_bf16 a[48:51], a[188:191], v[132:135], a[48:51]\n"
"v_exp_f32 v39, v39\n"
"v_mfma_f32_16x16x32_bf16 a[48:51], a[184:187], v[128:131], a[48:51]\n"
"s_waitcnt lgkmcnt(6)\n"
"ds_read_b64_tr_b16 a[180:181], v220 offset:8992\n"
"v_cvt_pk_bf16_f32 v173, v38, v39\n"
"v_mfma_f32_16x16x32_bf16 a[64:67], a[160:163], v[128:131], a[64:67]\n"
"v_exp_f32 v40, v40\n"
"v_mfma_f32_16x16x32_bf16 a[64:67], a[164:167], v[132:135], a[64:67]\n"
"ds_read_b64_tr_b16 a[182:183], v220 offset:10016\n"
"v_mfma_f32_16x16x32_bf16 a[68:71], a[164:167], v[140:143], a[68:71]\n"
"s_add_u32 m0, s62, 32904\n"
"v_exp_f32 v41, v41\n"
"v_mfma_f32_16x16x32_bf16 a[68:71], a[160:163], v[136:139], a[68:71]\n"
"buffer_load_dwordx4 v224, s[40:43], s57 offen offset:2048 nt lds\n"
"v_mfma_f32_16x16x32_bf16 a[72:75], a[160:163], v[144:147], a[72:75]\n"
"v_exp_f32 v42, v42\n"
"v_mfma_f32_16x16x32_bf16 a[72:75], a[164:167], v[148:151], a[72:75]\n"
"ds_read_b64_tr_b16 a[184:185], v220 offset:776\n"
"v_cvt_pk_bf16_f32 v180, v40, v41\n"
"v_mfma_f32_16x16x32_bf16 a[76:79], a[164:167], v[156:159], a[76:79]\n"
"v_exp_f32 v43, v43\n"
"v_mfma_f32_16x16x32_bf16 a[76:79], a[160:163], v[152:155], a[76:79]\n"
"s_waitcnt lgkmcnt(5)\n"
"ds_read_b64_tr_b16 a[186:187], v220 offset:1800\n"
"v_cvt_pk_bf16_f32 v181, v42, v43\n"
"v_mfma_f32_16x16x32_bf16 a[92:95], a[168:171], v[152:155], a[92:95]\n"
"v_exp_f32 v44, v44\n"
"v_mfma_f32_16x16x32_bf16 a[92:95], a[172:175], v[156:159], a[92:95]\n"
"ds_read_b64_tr_b16 a[188:189], v220 offset:9000\n"
"v_mfma_f32_16x16x32_bf16 a[88:91], a[172:175], v[148:151], a[88:91]\n"
"v_exp_f32 v45, v45\n"
"v_mfma_f32_16x16x32_bf16 a[88:91], a[168:171], v[144:147], a[88:91]\n"
"ds_read_b64_tr_b16 a[190:191], v220 offset:10024\n"
"v_cvt_pk_bf16_f32 v188, v44, v45\n"
"v_mfma_f32_16x16x32_bf16 a[84:87], a[168:171], v[136:139], a[84:87]\n"
"v_exp_f32 v46, v46\n"
"v_mfma_f32_16x16x32_bf16 a[84:87], a[172:175], v[140:143], a[84:87]\n"
"v_exp_f32 v47, v47\n"
"v_mfma_f32_16x16x32_bf16 a[80:83], a[172:175], v[132:135], a[80:83]\n"
"v_exp_f32 v60, v60\n"
"v_mfma_f32_16x16x32_bf16 a[80:83], a[168:171], v[128:131], a[80:83]\n"
"s_waitcnt lgkmcnt(4)\n"
"v_exp_f32 v61, v61\n"
"v_mfma_f32_16x16x32_bf16 a[96:99], a[176:179], v[128:131], a[96:99]\n"
"s_waitcnt vmcnt(7) lgkmcnt(0)\n"
"v_cvt_pk_bf16_f32 v189, v46, v47\n"
"v_cvt_pk_bf16_f32 v190, v60, v61\n"
"v_mfma_f32_16x16x32_bf16 a[96:99], a[180:183], v[132:135], a[96:99]\n"
"s_barrier\n"
"v_mfma_f32_16x16x32_bf16 a[100:103], a[180:183], v[140:143], a[100:103]\n"
"ds_read_b128 a[128:131], v216 offset:32768\n"
"v_mfma_f32_16x16x32_bf16 a[100:103], a[176:179], v[136:139], a[100:103]\n"
"ds_read_b128 a[132:135], v217 offset:32768\n"
"v_mfma_f32_16x16x32_bf16 a[104:107], a[176:179], v[144:147], a[104:107]\n"
"v_mfma_f32_16x16x32_bf16 a[104:107], a[180:183], v[148:151], a[104:107]\n"
"buffer_load_dwordx4 v224, s[40:43], s57 offen offset:3072 nt lds\n"
"v_mfma_f32_16x16x32_bf16 a[108:111], a[180:183], v[156:159], a[108:111]\n"
"s_sub_u32 s57, s57, 159744\n"
"v_mfma_f32_16x16x32_bf16 a[108:111], a[176:179], v[152:155], a[108:111]\n"
"s_and_b32 s57, s57, 159744\n"
"ds_read_b128 a[136:139], v218 offset:32768\n"
"v_mfma_f32_16x16x32_bf16 a[124:127], a[184:187], v[152:155], a[124:127]\n"
"v_mfma_f32_16x16x32_bf16 a[124:127], a[188:191], v[156:159], a[124:127]\n"
"ds_read_b128 a[140:143], v219 offset:32768\n"
"v_mfma_f32_16x16x32_bf16 a[120:123], a[188:191], v[148:151], a[120:123]\n"
"v_mfma_f32_16x16x32_bf16 a[120:123], a[184:187], v[144:147], a[120:123]\n"
"ds_read_b128 a[144:147], v216 offset:33792\n"
"v_mfma_f32_16x16x32_bf16 a[116:119], a[184:187], v[136:139], a[116:119]\n"
"v_mfma_f32_16x16x32_bf16 a[116:119], a[188:191], v[140:143], a[116:119]\n"
"ds_read_b128 a[148:151], v217 offset:33792\n"
"v_mfma_f32_16x16x32_bf16 a[112:115], a[188:191], v[132:135], a[112:115]\n"
"s_sub_u32 s64, s64, 1\n"
"v_mfma_f32_16x16x32_bf16 a[112:115], a[184:187], v[128:131], a[112:115]\n"
"s_waitcnt lgkmcnt(4)\n"
"ds_read_b128 a[152:155], v218 offset:33792\n"
"ds_read_b128 a[156:159], v219 offset:33792\n"
"s_cbranch_scc0 L_loop%=\n"
"s_cmp_eq_u32 s63, 0\n"
"s_cbranch_scc0 L_tble%=\n"
"v_mfma_f32_16x16x32_bf16 v[0:3], a[128:131], v[64:67], v[192:195]\n"
"v_exp_f32 v62, v62\n"
"v_mfma_f32_16x16x32_bf16 v[0:3], a[132:135], v[68:71], v[0:3]\n"
"s_waitcnt lgkmcnt(4)\n"
"v_exp_f32 v63, v63\n"
"v_mfma_f32_16x16x32_bf16 v[0:3], a[136:139], v[72:75], v[0:3]\n"
"v_accvgpr_read_b32 v248, a221\n"
"v_cvt_pk_bf16_f32 v191, v62, v63\n"
"v_mfma_f32_16x16x32_bf16 v[0:3], a[140:143], v[76:79], v[0:3]\n"
"buffer_load_dword v248, v248, s[96:99], 0 offen sc1\n"
"v_mfma_f32_16x16x32_bf16 v[4:7], a[140:143], v[92:95], v[196:199]\n"
"s_add_u32 m0, s62, 49344\n"
"v_exp_f32 v56, v56\n"
"v_mfma_f32_16x16x32_bf16 v[4:7], a[136:139], v[88:91], v[4:7]\n"
"buffer_load_dwordx4 v224, s[40:43], s57 offen nt lds\n"
"v_mfma_f32_16x16x32_bf16 v[4:7], a[132:135], v[84:87], v[4:7]\n"
"v_exp_f32 v57, v57\n"
"v_mfma_f32_16x16x32_bf16 v[4:7], a[128:131], v[80:83], v[4:7]\n"
"v_exp_f32 v58, v58\n"
"v_mfma_f32_16x16x32_bf16 v[8:11], a[128:131], v[96:99], v[200:203]\n"
"v_exp_f32 v59, v59\n"
"v_mfma_f32_16x16x32_bf16 v[8:11], a[132:135], v[100:103], v[8:11]\n"
"v_exp_f32 v52, v52\n"
"v_mfma_f32_16x16x32_bf16 v[8:11], a[136:139], v[104:107], v[8:11]\n"
"buffer_load_dwordx4 v224, s[40:43], s57 offen offset:1024 nt lds\n"
"v_mfma_f32_16x16x32_bf16 v[8:11], a[140:143], v[108:111], v[8:11]\n"
"v_exp_f32 v53, v53\n"
"v_mfma_f32_16x16x32_bf16 v[12:15], a[140:143], v[124:127], v[204:207]\n"
"v_exp_f32 v54, v54\n"
"v_mfma_f32_16x16x32_bf16 v[12:15], a[136:139], v[120:123], v[12:15]\n"
"s_add_u32 m0, s62, 49352\n"
"v_exp_f32 v55, v55\n"
"v_mfma_f32_16x16x32_bf16 v[12:15], a[132:135], v[116:119], v[12:15]\n"
"buffer_load_dwordx4 v224, s[40:43], s57 offen offset:2048 nt lds\n"
"v_mfma_f32_16x16x32_bf16 v[12:15], a[128:131], v[112:115], v[12:15]\n"
"s_waitcnt lgkmcnt(0)\n"
"ds_read_b128 a[128:131], v216 offset:40960\n"
"v_cvt_pk_bf16_f32 v182, v56, v57\n"
"v_mfma_f32_16x16x32_bf16 v[28:31], a[144:147], v[112:115], v[204:207]\n"
"v_exp_f32 v48, v48\n"
"v_mfma_f32_16x16x32_bf16 v[28:31], a[148:151], v[116:119], v[28:31]\n"
"ds_read_b128 a[132:135], v217 offset:40960\n"
"v_cvt_pk_bf16_f32 v183, v58, v59\n"
"v_mfma_f32_16x16x32_bf16 v[28:31], a[152:155], v[120:123], v[28:31]\n"
"v_exp_f32 v49, v49\n"
"v_mfma_f32_16x16x32_bf16 v[28:31], a[156:159], v[124:127], v[28:31]\n"
"ds_read_b128 a[136:139], v218 offset:40960\n"
"v_cvt_pk_bf16_f32 v174, v52, v53\n"
"v_mfma_f32_16x16x32_bf16 v[24:27], a[156:159], v[108:111], v[200:203]\n"
"v_exp_f32 v50, v50\n"
"v_mfma_f32_16x16x32_bf16 v[24:27], a[152:155], v[104:107], v[24:27]\n"
"buffer_load_dwordx4 v224, s[40:43], s57 offen offset:3072 nt lds\n"
"v_mfma_f32_16x16x32_bf16 v[24:27], a[148:151], v[100:103], v[24:27]\n"
"s_sub_u32 s57, s57, 159744\n"
"v_exp_f32 v51, v51\n"
"v_mfma_f32_16x16x32_bf16 v[24:27], a[144:147], v[96:99], v[24:27]\n"
"s_and_b32 s57, s57, 159744\n"
"ds_read_b128 a[140:143], v219 offset:40960\n"
"v_cvt_pk_bf16_f32 v175, v54, v55\n"
"v_mfma_f32_16x16x32_bf16 v[20:23], a[144:147], v[80:83], v[196:199]\n"
"v_cvt_pk_bf16_f32 v166, v48, v49\n"
"v_cvt_pk_bf16_f32 v167, v50, v51\n"
"v_mfma_f32_16x16x32_bf16 v[20:23], a[148:151], v[84:87], v[20:23]\n"
"v_exp_f32 v0, v0\n"
"v_mfma_f32_16x16x32_bf16 v[20:23], a[152:155], v[88:91], v[20:23]\n"
"s_add_u32 s58, s84, 128\n"
"v_exp_f32 v1, v1\n"
"v_mfma_f32_16x16x32_bf16 v[20:23], a[156:159], v[92:95], v[20:23]\n"
"s_add_u32 m0, s61, 0\n"
"v_exp_f32 v2, v2\n"
"v_mfma_f32_16x16x32_bf16 v[16:19], a[156:159], v[76:79], v[192:195]\n"
"buffer_load_dwordx4 v226, s[44:47], s58 offen lds\n"
"v_mfma_f32_16x16x32_bf16 v[16:19], a[152:155], v[72:75], v[16:19]\n"
"v_exp_f32 v3, v3\n"
"v_mfma_f32_16x16x32_bf16 v[16:19], a[148:151], v[68:71], v[16:19]\n"
"v_exp_f32 v4, v4\n"
"v_mfma_f32_16x16x32_bf16 v[16:19], a[144:147], v[64:67], v[16:19]\n"
"s_waitcnt lgkmcnt(2)\n"
"ds_read_b128 a[144:147], v216 offset:41984\n"
"v_cvt_pk_bf16_f32 v128, v0, v1\n"
"v_mfma_f32_16x16x32_bf16 v[32:35], a[128:131], v[64:67], v[192:195]\n"
"v_exp_f32 v5, v5\n"
"v_mfma_f32_16x16x32_bf16 v[32:35], a[132:135], v[68:71], v[32:35]\n"
"s_waitcnt lgkmcnt(1)\n"
"ds_read_b128 a[148:151], v217 offset:41984\n"
"v_cvt_pk_bf16_f32 v129, v2, v3\n"
"v_mfma_f32_16x16x32_bf16 v[32:35], a[136:139], v[72:75], v[32:35]\n"
"v_exp_f32 v6, v6\n"
"v_mfma_f32_16x16x32_bf16 v[32:35], a[140:143], v[76:79], v[32:35]\n"
"ds_read_b128 a[152:155], v218 offset:41984\n"
"v_cvt_pk_bf16_f32 v136, v4, v5\n"
"v_mfma_f32_16x16x32_bf16 v[36:39], a[140:143], v[92:95], v[196:199]\n"
"s_add_u32 m0, s61, 1024\n"
"v_exp_f32 v7, v7\n"
"v_mfma_f32_16x16x32_bf16 v[36:39], a[136:139], v[88:91], v[36:39]\n"
"buffer_load_dwordx4 v227, s[44:47], s58 offen lds\n"
"v_mfma_f32_16x16x32_bf16 v[36:39], a[132:135], v[84:87], v[36:39]\n"
"v_exp_f32 v8, v8\n"
"v_mfma_f32_16x16x32_bf16 v[36:39], a[128:131], v[80:83], v[36:39]\n"
"ds_read_b128 a[156:159], v219 offset:41984\n"
"v_cvt_pk_bf16_f32 v137, v6, v7\n"
"v_mfma_f32_16x16x32_bf16 v[40:43], a[128:131], v[96:99], v[200:203]\n"
"v_exp_f32 v9, v9\n"
"v_mfma_f32_16x16x32_bf16 v[40:43], a[132:135], v[100:103], v[40:43]\n"
"ds_read_b64_tr_b16 a[160:161], v220 offset:16448\n"
"v_cvt_pk_bf16_f32 v144, v8, v9\n"
"v_mfma_f32_16x16x32_bf16 v[40:43], a[136:139], v[104:107], v[40:43]\n"
"v_exp_f32 v10, v10\n"
"v_mfma_f32_16x16x32_bf16 v[40:43], a[140:143], v[108:111], v[40:43]\n"
"ds_read_b64_tr_b16 a[162:163], v220 offset:17472\n"
"v_mfma_f32_16x16x32_bf16 v[44:47], a[140:143], v[124:127], v[204:207]\n"
"s_add_u32 s58, s85, 128\n"
"v_exp_f32 v11, v11\n"
"v_mfma_f32_16x16x32_bf16 v[44:47], a[136:139], v[120:123], v[44:47]\n"
"s_add_u32 m0, s61, 2048\n"
"ds_read_b64_tr_b16 a[164:165], v220 offset:24672\n"
"ds_read_b64_tr_b16 a[166:167], v220 offset:25696\n"
"v_mfma_f32_16x16x32_bf16 v[44:47], a[132:135], v[116:119], v[44:47]\n"
"buffer_load_dwordx4 v226, s[44:47], s58 offen lds\n"
"v_mfma_f32_16x16x32_bf16 v[44:47], a[128:131], v[112:115], v[44:47]\n"
"s_waitcnt lgkmcnt(6)\n"
"v_exp_f32 v12, v12\n"
"v_mfma_f32_16x16x32_bf16 v[60:63], a[144:147], v[112:115], v[204:207]\n"
"v_exp_f32 v13, v13\n"
"v_mfma_f32_16x16x32_bf16 v[60:63], a[148:151], v[116:119], v[60:63]\n"
"s_waitcnt lgkmcnt(4)\n"
"ds_read_b64_tr_b16 a[168:169], v220 offset:16456\n"
"v_cvt_pk_bf16_f32 v145, v10, v11\n"
"v_mfma_f32_16x16x32_bf16 v[60:63], a[152:155], v[120:123], v[60:63]\n"
"v_exp_f32 v14, v14\n"
"v_mfma_f32_16x16x32_bf16 v[60:63], a[156:159], v[124:127], v[60:63]\n"
"ds_read_b64_tr_b16 a[170:171], v220 offset:17480\n"
"v_cvt_pk_bf16_f32 v152, v12, v13\n"
"v_mfma_f32_16x16x32_bf16 v[56:59], a[156:159], v[108:111], v[200:203]\n"
"v_exp_f32 v15, v15\n"
"v_mfma_f32_16x16x32_bf16 v[56:59], a[152:155], v[104:107], v[56:59]\n"
"ds_read_b64_tr_b16 a[172:173], v220 offset:24680\n"
"ds_read_b64_tr_b16 a[174:175], v220 offset:25704\n"
"v_mfma_f32_16x16x32_bf16 v[56:59], a[148:151], v[100:103], v[56:59]\n"
"s_add_u32 m0, s61, 3072\n"
"v_exp_f32 v28, v28\n"
"v_mfma_f32_16x16x32_bf16 v[56:59], a[144:147], v[96:99], v[56:59]\n"
"buffer_load_dwordx4 v227, s[44:47], s58 offen lds\n"
"v_mfma_f32_16x16x32_bf16 v[52:55], a[144:147], v[80:83], v[196:199]\n"
"v_exp_f32 v29, v29\n"
"v_mfma_f32_16x16x32_bf16 v[52:55], a[148:151], v[84:87], v[52:55]\n"
"v_exp_f32 v30, v30\n"
"v_mfma_f32_16x16x32_bf16 v[52:55], a[152:155], v[88:91], v[52:55]\n"
"v_exp_f32 v31, v31\n"
"v_mfma_f32_16x16x32_bf16 v[52:55], a[156:159], v[92:95], v[52:55]\n"
"ds_read_b64_tr_b16 a[176:177], v220 offset:16704\n"
"v_cvt_pk_bf16_f32 v153, v14, v15\n"
"v_mfma_f32_16x16x32_bf16 v[48:51], a[156:159], v[76:79], v[192:195]\n"
"s_add_u32 s58, s87, 128\n"
"v_exp_f32 v24, v24\n"
"v_mfma_f32_16x16x32_bf16 v[48:51], a[152:155], v[72:75], v[48:51]\n"
"s_add_u32 m0, s61, 16384\n"
"ds_read_b64_tr_b16 a[178:179], v220 offset:17728\n"
"v_cvt_pk_bf16_f32 v154, v28, v29\n"
"v_mfma_f32_16x16x32_bf16 v[48:51], a[148:151], v[68:71], v[48:51]\n"
"buffer_load_dwordx4 v226, s[44:47], s58 offen lds\n"
"v_mfma_f32_16x16x32_bf16 v[48:51], a[144:147], v[64:67], v[48:51]\n"
"ds_read_b64_tr_b16 a[180:181], v220 offset:24928\n"
"ds_read_b64_tr_b16 a[182:183], v220 offset:25952\n"
"v_smfmac_f32_16x16x64_bf16 a[228:231], a[200:203], v[160:167], v208\n"
"v_cvt_pk_bf16_f32 v155, v30, v31\n"
"v_smfmac_f32_16x16x64_bf16 a[232:235], a[200:203], v[168:175], v208\n"
"v_smfmac_f32_16x16x64_bf16 a[236:239], a[200:203], v[176:183], v208\n"
"v_smfmac_f32_16x16x64_bf16 a[240:243], a[200:203], v[184:191], v208\n"
"s_waitcnt lgkmcnt(8)\n"
"ds_read_b64_tr_b16 a[184:185], v220 offset:16712\n"
"v_mfma_f32_16x16x32_bf16 a[0:3], a[160:163], v[160:163], a[0:3]\n"
"v_exp_f32 v25, v25\n"
"v_mfma_f32_16x16x32_bf16 a[0:3], a[164:167], v[164:167], a[0:3]\n"
"ds_read_b64_tr_b16 a[186:187], v220 offset:17736\n"
"v_cvt_pk_bf16_f32 v146, v24, v25\n"
"v_mfma_f32_16x16x32_bf16 a[4:7], a[164:167], v[172:175], a[4:7]\n"
"s_add_u32 m0, s61, 17408\n"
"v_exp_f32 v26, v26\n"
"v_mfma_f32_16x16x32_bf16 a[4:7], a[160:163], v[168:171], a[4:7]\n"
"buffer_load_dwordx4 v227, s[44:47], s58 offen lds\n"
"v_mfma_f32_16x16x32_bf16 a[8:11], a[160:163], v[176:179], a[8:11]\n"
"v_exp_f32 v27, v27\n"
"v_mfma_f32_16x16x32_bf16 a[8:11], a[164:167], v[180:183], a[8:11]\n"
"ds_read_b64_tr_b16 a[188:189], v220 offset:24936\n"
"ds_read_b64_tr_b16 a[190:191], v220 offset:25960\n"
"v_mfma_f32_16x16x32_bf16 a[12:15], a[164:167], v[188:191], a[12:15]\n"
"v_exp_f32 v20, v20\n"
"v_mfma_f32_16x16x32_bf16 a[12:15], a[160:163], v[184:187], a[12:15]\n"
"s_waitcnt lgkmcnt(8)\n"
"v_exp_f32 v21, v21\n"
"v_mfma_f32_16x16x32_bf16 a[28:31], a[168:171], v[184:187], a[28:31]\n"
"v_exp_f32 v22, v22\n"
"v_mfma_f32_16x16x32_bf16 a[28:31], a[172:175], v[188:191], a[28:31]\n"
"ds_read_b64_tr_b16 a[160:161], v220 offset:16960\n"
"v_cvt_pk_bf16_f32 v147, v26, v27\n"
"v_mfma_f32_16x16x32_bf16 a[24:27], a[172:175], v[180:183], a[24:27]\n"
"s_add_u32 s58, s68, 128\n"
"v_exp_f32 v23, v23\n"
"v_mfma_f32_16x16x32_bf16 a[24:27], a[168:171], v[176:179], a[24:27]\n"
"s_add_u32 m0, s61, 18432\n"
"ds_read_b64_tr_b16 a[162:163], v220 offset:17984\n"
"v_cvt_pk_bf16_f32 v138, v20, v21\n"
"v_mfma_f32_16x16x32_bf16 a[20:23], a[168:171], v[168:171], a[20:23]\n"
"buffer_load_dwordx4 v226, s[44:47], s58 offen lds\n"
"v_mfma_f32_16x16x32_bf16 a[20:23], a[172:175], v[172:175], a[20:23]\n"
"ds_read_b64_tr_b16 a[164:165], v220 offset:25184\n"
"ds_read_b64_tr_b16 a[166:167], v220 offset:26208\n"
"v_mfma_f32_16x16x32_bf16 a[16:19], a[172:175], v[164:167], a[16:19]\n"
"v_exp_f32 v16, v16\n"
"v_mfma_f32_16x16x32_bf16 a[16:19], a[168:171], v[160:163], a[16:19]\n"
"s_waitcnt lgkmcnt(8)\n"
"v_exp_f32 v17, v17\n"
"v_mfma_f32_16x16x32_bf16 a[32:35], a[176:179], v[160:163], a[32:35]\n"
"v_exp_f32 v18, v18\n"
"v_mfma_f32_16x16x32_bf16 a[32:35], a[180:183], v[164:167], a[32:35]\n"
"ds_read_b64_tr_b16 a[168:169], v220 offset:16968\n"
"v_cvt_pk_bf16_f32 v139, v22, v23\n"
"v_mfma_f32_16x16x32_bf16 a[36:39], a[180:183], v[172:175], a[36:39]\n"
"s_add_u32 m0, s61, 19456\n"
"v_exp_f32 v19, v19\n"
"v_mfma_f32_16x16x32_bf16 a[36:39], a[176:179], v[168:171], a[36:39]\n"
"buffer_load_dwordx4 v227, s[44:47], s58 offen lds\n"
"v_mfma_f32_16x16x32_bf16 a[40:43], a[176:179], v[176:179], a[40:43]\n"
"v_exp_f32 v32, v32\n"
"v_mfma_f32_16x16x32_bf16 a[40:43], a[180:183], v[180:183], a[40:43]\n"
"ds_read_b64_tr_b16 a[170:171], v220 offset:17992\n"
"v_cvt_pk_bf16_f32 v130, v16, v17\n"
"v_mfma_f32_16x16x32_bf16 a[44:47], a[180:183], v[188:191], a[44:47]\n"
"v_exp_f32 v33, v33\n"
"v_mfma_f32_16x16x32_bf16 a[44:47], a[176:179], v[184:187], a[44:47]\n"
"s_waitcnt lgkmcnt(6)\n"
"ds_read_b64_tr_b16 a[172:173], v220 offset:25192\n"
"ds_read_b64_tr_b16 a[174:175], v220 offset:26216\n"
"v_mfma_f32_16x16x32_bf16 a[60:63], a[184:187], v[184:187], a[60:63]\n"
"v_exp_f32 v34, v34\n"
"v_mfma_f32_16x16x32_bf16 a[60:63], a[188:191], v[188:191], a[60:63]\n"
"v_exp_f32 v35, v35\n"
"v_mfma_f32_16x16x32_bf16 a[56:59], a[188:191], v[180:183], a[56:59]\n"
"s_add_u32 s60, s59, 0\n"
"v_exp_f32 v36, v36\n"
"v_mfma_f32_16x16x32_bf16 a[56:59], a[184:187], v[176:179], a[56:59]\n"
"s_add_u32 m0, s62, 65792\n"
"ds_read_b64_tr_b16 a[176:177], v220 offset:17216\n"
"v_cvt_pk_bf16_f32 v131, v18, v19\n"
"v_mfma_f32_16x16x32_bf16 a[52:55], a[184:187], v[168:171], a[52:55]\n"
"buffer_load_dwordx4 v225, s[48:51], s60 offen lds\n"
"v_mfma_f32_16x16x32_bf16 a[52:55], a[188:191], v[172:175], a[52:55]\n"
"ds_read_b64_tr_b16 a[178:179], v220 offset:18240\n"
"v_cvt_pk_bf16_f32 v132, v32, v33\n"
"v_mfma_f32_16x16x32_bf16 a[48:51], a[188:191], v[164:167], a[48:51]\n"
"v_exp_f32 v37, v37\n"
"v_mfma_f32_16x16x32_bf16 a[48:51], a[184:187], v[160:163], a[48:51]\n"
"s_waitcnt lgkmcnt(6)\n"
"ds_read_b64_tr_b16 a[180:181], v220 offset:25440\n"
"ds_read_b64_tr_b16 a[182:183], v220 offset:26464\n"
"v_mfma_f32_16x16x32_bf16 a[64:67], a[160:163], v[160:163], a[64:67]\n"
"v_exp_f32 v38, v38\n"
"v_mfma_f32_16x16x32_bf16 a[64:67], a[164:167], v[164:167], a[64:67]\n"
"s_add_u32 s60, s59, 32768\n"
"v_exp_f32 v39, v39\n"
"v_mfma_f32_16x16x32_bf16 a[68:71], a[164:167], v[172:175], a[68:71]\n"
"s_add_u32 m0, s62, 66816\n"
"v_exp_f32 v40, v40\n"
"v_mfma_f32_16x16x32_bf16 a[68:71], a[160:163], v[168:171], a[68:71]\n"
"buffer_load_dwordx4 v225, s[48:51], s60 offen lds\n"
"v_mfma_f32_16x16x32_bf16 a[72:75], a[160:163], v[176:179], a[72:75]\n"
"v_exp_f32 v41, v41\n"
"v_mfma_f32_16x16x32_bf16 a[72:75], a[164:167], v[180:183], a[72:75]\n"
"ds_read_b64_tr_b16 a[184:185], v220 offset:17224\n"
"v_cvt_pk_bf16_f32 v133, v34, v35\n"
"v_mfma_f32_16x16x32_bf16 a[76:79], a[164:167], v[188:191], a[76:79]\n"
"v_cvt_pk_bf16_f32 v140, v36, v37\n"
"v_cvt_pk_bf16_f32 v141, v38, v39\n"
"v_mfma_f32_16x16x32_bf16 a[76:79], a[160:163], v[184:187], a[76:79]\n"
"s_waitcnt lgkmcnt(5)\n"
"ds_read_b64_tr_b16 a[186:187], v220 offset:18248\n"
"v_cvt_pk_bf16_f32 v148, v40, v41\n"
"v_mfma_f32_16x16x32_bf16 a[92:95], a[168:171], v[184:187], a[92:95]\n"
"v_exp_f32 v42, v42\n"
"v_mfma_f32_16x16x32_bf16 a[92:95], a[172:175], v[188:191], a[92:95]\n"
"ds_read_b64_tr_b16 a[188:189], v220 offset:25448\n"
"ds_read_b64_tr_b16 a[190:191], v220 offset:26472\n"
"v_mfma_f32_16x16x32_bf16 a[88:91], a[172:175], v[180:183], a[88:91]\n"
"s_add_u32 s60, s59, 65536\n"
"v_exp_f32 v43, v43\n"
"v_mfma_f32_16x16x32_bf16 a[88:91], a[168:171], v[176:179], a[88:91]\n"
"s_add_u32 m0, s62, 67848\n"
"v_exp_f32 v44, v44\n"
"v_mfma_f32_16x16x32_bf16 a[84:87], a[168:171], v[168:171], a[84:87]\n"
"buffer_load_dwordx4 v225, s[48:51], s60 offen lds\n"
"v_mfma_f32_16x16x32_bf16 a[84:87], a[172:175], v[172:175], a[84:87]\n"
"v_exp_f32 v45, v45\n"
"v_mfma_f32_16x16x32_bf16 a[80:83], a[172:175], v[164:167], a[80:83]\n"
"v_exp_f32 v46, v46\n"
"v_mfma_f32_16x16x32_bf16 a[80:83], a[168:171], v[160:163], a[80:83]\n"
"s_waitcnt lgkmcnt(4)\n"
"v_exp_f32 v47, v47\n"
"v_mfma_f32_16x16x32_bf16 a[96:99], a[176:179], v[160:163], a[96:99]\n"
"s_waitcnt vmcnt(16) lgkmcnt(0)\n"
"v_exp_f32 v60, v60\n"
"v_mfma_f32_16x16x32_bf16 a[96:99], a[180:183], v[164:167], a[96:99]\n"
"s_barrier\n"
"v_mfma_f32_16x16x32_bf16 a[100:103], a[180:183], v[172:175], a[100:103]\n"
"ds_read_b128 a[128:131], v216 offset:49152\n"
"v_cvt_pk_bf16_f32 v149, v42, v43\n"
"v_mfma_f32_16x16x32_bf16 a[100:103], a[176:179], v[168:171], a[100:103]\n"
"s_add_u32 s60, s59, 98304\n"
"ds_read_b128 a[132:135], v217 offset:49152\n"
"v_cvt_pk_bf16_f32 v156, v44, v45\n"
"v_mfma_f32_16x16x32_bf16 a[104:107], a[176:179], v[176:179], a[104:107]\n"
"s_add_u32 m0, s62, 68872\n"
"v_exp_f32 v61, v61\n"
"v_mfma_f32_16x16x32_bf16 a[104:107], a[180:183], v[180:183], a[104:107]\n"
"buffer_load_dwordx4 v225, s[48:51], s60 offen lds\n"
"v_mfma_f32_16x16x32_bf16 a[108:111], a[180:183], v[188:191], a[108:111]\n"
"v_exp_f32 v62, v62\n"
"v_mfma_f32_16x16x32_bf16 a[108:111], a[176:179], v[184:187], a[108:111]\n"
"ds_read_b128 a[136:139], v218 offset:49152\n"
"v_accvgpr_read_b32 v253, a204\n"
"v_mfma_f32_16x16x32_bf16 a[124:127], a[184:187], v[184:187], a[124:127]\n"
"buffer_load_dword a206, v253, s[48:51], 0 offen offset:256\n"
"v_mfma_f32_16x16x32_bf16 a[124:127], a[188:191], v[188:191], a[124:127]\n"
"ds_read_b128 a[140:143], v219 offset:49152\n"
"v_cvt_pk_bf16_f32 v157, v46, v47\n"
"v_mfma_f32_16x16x32_bf16 a[120:123], a[188:191], v[180:183], a[120:123]\n"
"v_exp_f32 v63, v63\n"
"v_mfma_f32_16x16x32_bf16 a[120:123], a[184:187], v[176:179], a[120:123]\n"
"ds_read_b128 a[144:147], v216 offset:50176\n"
"v_cvt_pk_bf16_f32 v158, v60, v61\n"
"v_mfma_f32_16x16x32_bf16 a[116:119], a[184:187], v[168:171], a[116:119]\n"
"v_exp_f32 v56, v56\n"
"v_mfma_f32_16x16x32_bf16 a[116:119], a[188:191], v[172:175], a[116:119]\n"
"ds_read_b128 a[148:151], v217 offset:50176\n"
"v_cvt_pk_bf16_f32 v159, v62, v63\n"
"v_mfma_f32_16x16x32_bf16 a[112:115], a[188:191], v[164:167], a[112:115]\n"
"v_exp_f32 v57, v57\n"
"v_mfma_f32_16x16x32_bf16 a[112:115], a[184:187], v[160:163], a[112:115]\n"
"s_waitcnt lgkmcnt(4)\n"
"ds_read_b128 a[152:155], v218 offset:50176\n"
"ds_read_b128 a[156:159], v219 offset:50176\n"
"v_mfma_f32_16x16x32_bf16 v[0:3], a[128:131], v[64:67], v[192:195]\n"
"v_exp_f32 v58, v58\n"
"v_mfma_f32_16x16x32_bf16 v[0:3], a[132:135], v[68:71], v[0:3]\n"
"s_waitcnt lgkmcnt(4)\n"
"v_exp_f32 v59, v59\n"
"v_mfma_f32_16x16x32_bf16 v[0:3], a[136:139], v[72:75], v[0:3]\n"
"v_cvt_pk_bf16_f32 v150, v56, v57\n"
"v_cvt_pk_bf16_f32 v151, v58, v59\n"
"v_mfma_f32_16x16x32_bf16 v[0:3], a[140:143], v[76:79], v[0:3]\n"
"s_add_u32 s58, s84, 0\n"
"v_exp_f32 v52, v52\n"
"v_mfma_f32_16x16x32_bf16 v[4:7], a[140:143], v[92:95], v[196:199]\n"
"s_add_u32 m0, s61, 65536\n"
"v_exp_f32 v53, v53\n"
"v_mfma_f32_16x16x32_bf16 v[4:7], a[136:139], v[88:91], v[4:7]\n"
"buffer_load_dwordx4 v226, s[44:47], s58 offen lds\n"
"v_mfma_f32_16x16x32_bf16 v[4:7], a[132:135], v[84:87], v[4:7]\n"
"v_exp_f32 v54, v54\n"
"v_mfma_f32_16x16x32_bf16 v[4:7], a[128:131], v[80:83], v[4:7]\n"
"v_exp_f32 v55, v55\n"
"v_mfma_f32_16x16x32_bf16 v[8:11], a[128:131], v[96:99], v[200:203]\n"
"v_cvt_pk_bf16_f32 v142, v52, v53\n"
"v_cvt_pk_bf16_f32 v143, v54, v55\n"
"v_mfma_f32_16x16x32_bf16 v[8:11], a[132:135], v[100:103], v[8:11]\n"
"v_exp_f32 v48, v48\n"
"v_mfma_f32_16x16x32_bf16 v[8:11], a[136:139], v[104:107], v[8:11]\n"
"v_exp_f32 v49, v49\n"
"v_mfma_f32_16x16x32_bf16 v[8:11], a[140:143], v[108:111], v[8:11]\n"
"s_add_u32 m0, s61, 66560\n"
"v_exp_f32 v50, v50\n"
"v_mfma_f32_16x16x32_bf16 v[12:15], a[140:143], v[124:127], v[204:207]\n"
"buffer_load_dwordx4 v227, s[44:47], s58 offen lds\n"
"v_mfma_f32_16x16x32_bf16 v[12:15], a[136:139], v[120:123], v[12:15]\n"
"v_exp_f32 v51, v51\n"
"v_mfma_f32_16x16x32_bf16 v[12:15], a[132:135], v[116:119], v[12:15]\n"
"v_exp_f32 v0, v0\n"
"v_mfma_f32_16x16x32_bf16 v[12:15], a[128:131], v[112:115], v[12:15]\n"
"s_waitcnt lgkmcnt(0)\n"
"ds_read_b128 a[128:131], v216 offset:57344\n"
"v_cvt_pk_bf16_f32 v134, v48, v49\n"
"v_mfma_f32_16x16x32_bf16 v[28:31], a[144:147], v[112:115], v[204:207]\n"
"v_exp_f32 v1, v1\n"
"v_mfma_f32_16x16x32_bf16 v[28:31], a[148:151], v[116:119], v[28:31]\n"
"ds_read_b128 a[132:135], v217 offset:57344\n"
"v_cvt_pk_bf16_f32 v135, v50, v51\n"
"v_mfma_f32_16x16x32_bf16 v[28:31], a[152:155], v[120:123], v[28:31]\n"
"v_exp_f32 v2, v2\n"
"v_mfma_f32_16x16x32_bf16 v[28:31], a[156:159], v[124:127], v[28:31]\n"
"s_add_u32 s58, s85, 0\n"
"ds_read_b128 a[136:139], v218 offset:57344\n"
"v_cvt_pk_bf16_f32 v160, v0, v1\n"
"v_mfma_f32_16x16x32_bf16 v[24:27], a[156:159], v[108:111], v[200:203]\n"
"s_add_u32 m0, s61, 67584\n"
"v_exp_f32 v3, v3\n"
"v_mfma_f32_16x16x32_bf16 v[24:27], a[152:155], v[104:107], v[24:27]\n"
"buffer_load_dwordx4 v226, s[44:47], s58 offen lds\n"
"v_mfma_f32_16x16x32_bf16 v[24:27], a[148:151], v[100:103], v[24:27]\n"
"v_exp_f32 v4, v4\n"
"v_mfma_f32_16x16x32_bf16 v[24:27], a[144:147], v[96:99], v[24:27]\n"
"ds_read_b128 a[140:143], v219 offset:57344\n"
"v_cvt_pk_bf16_f32 v161, v2, v3\n"
"v_mfma_f32_16x16x32_bf16 v[20:23], a[144:147], v[80:83], v[196:199]\n"
"v_exp_f32 v5, v5\n"
"v_mfma_f32_16x16x32_bf16 v[20:23], a[148:151], v[84:87], v[20:23]\n"
"v_exp_f32 v6, v6\n"
"v_mfma_f32_16x16x32_bf16 v[20:23], a[152:155], v[88:91], v[20:23]\n"
"v_exp_f32 v7, v7\n"
"v_mfma_f32_16x16x32_bf16 v[20:23], a[156:159], v[92:95], v[20:23]\n"
"s_add_u32 m0, s61, 68608\n"
"v_exp_f32 v8, v8\n"
"v_mfma_f32_16x16x32_bf16 v[16:19], a[156:159], v[76:79], v[192:195]\n"
"buffer_load_dwordx4 v227, s[44:47], s58 offen lds\n"
"v_mfma_f32_16x16x32_bf16 v[16:19], a[152:155], v[72:75], v[16:19]\n"
"v_exp_f32 v9, v9\n"
"v_mfma_f32_16x16x32_bf16 v[16:19], a[148:151], v[68:71], v[16:19]\n"
"v_exp_f32 v10, v10\n"
"v_mfma_f32_16x16x32_bf16 v[16:19], a[144:147], v[64:67], v[16:19]\n"
"s_waitcnt lgkmcnt(2)\n"
"ds_read_b128 a[144:147], v216 offset:58368\n"
"v_cvt_pk_bf16_f32 v168, v4, v5\n"
"v_mfma_f32_16x16x32_bf16 v[32:35], a[128:131], v[64:67], v[192:195]\n"
"v_exp_f32 v11, v11\n"
"v_mfma_f32_16x16x32_bf16 v[32:35], a[132:135], v[68:71], v[32:35]\n"
"s_waitcnt lgkmcnt(1)\n"
"ds_read_b128 a[148:151], v217 offset:58368\n"
"v_cvt_pk_bf16_f32 v169, v6, v7\n"
"v_mfma_f32_16x16x32_bf16 v[32:35], a[136:139], v[72:75], v[32:35]\n"
"v_exp_f32 v12, v12\n"
"v_mfma_f32_16x16x32_bf16 v[32:35], a[140:143], v[76:79], v[32:35]\n"
"s_add_u32 s58, s87, 0\n"
"ds_read_b128 a[152:155], v218 offset:58368\n"
"v_cvt_pk_bf16_f32 v176, v8, v9\n"
"v_mfma_f32_16x16x32_bf16 v[36:39], a[140:143], v[92:95], v[196:199]\n"
"s_add_u32 m0, s61, 81984\n"
"v_exp_f32 v13, v13\n"
"v_mfma_f32_16x16x32_bf16 v[36:39], a[136:139], v[88:91], v[36:39]\n"
"buffer_load_dwordx4 v226, s[44:47], s58 offen lds\n"
"v_mfma_f32_16x16x32_bf16 v[36:39], a[132:135], v[84:87], v[36:39]\n"
"v_exp_f32 v14, v14\n"
"v_mfma_f32_16x16x32_bf16 v[36:39], a[128:131], v[80:83], v[36:39]\n"
"ds_read_b128 a[156:159], v219 offset:58368\n"
"v_cvt_pk_bf16_f32 v177, v10, v11\n"
"v_mfma_f32_16x16x32_bf16 v[40:43], a[128:131], v[96:99], v[200:203]\n"
"v_exp_f32 v15, v15\n"
"v_mfma_f32_16x16x32_bf16 v[40:43], a[132:135], v[100:103], v[40:43]\n"
"ds_read_b64_tr_b16 a[160:161], v220 offset:32896\n"
"v_cvt_pk_bf16_f32 v184, v12, v13\n"
"v_mfma_f32_16x16x32_bf16 v[40:43], a[136:139], v[104:107], v[40:43]\n"
"v_exp_f32 v28, v28\n"
"v_mfma_f32_16x16x32_bf16 v[40:43], a[140:143], v[108:111], v[40:43]\n"
"ds_read_b64_tr_b16 a[162:163], v220 offset:33920\n"
"v_cvt_pk_bf16_f32 v185, v14, v15\n"
"v_mfma_f32_16x16x32_bf16 v[44:47], a[140:143], v[124:127], v[204:207]\n"
"v_exp_f32 v29, v29\n"
"v_mfma_f32_16x16x32_bf16 v[44:47], a[136:139], v[120:123], v[44:47]\n"
"s_add_u32 m0, s61, 83008\n"
"ds_read_b64_tr_b16 a[164:165], v220 offset:41120\n"
"ds_read_b64_tr_b16 a[166:167], v220 offset:42144\n"
"v_mfma_f32_16x16x32_bf16 v[44:47], a[132:135], v[116:119], v[44:47]\n"
"buffer_load_dwordx4 v227, s[44:47], s58 offen lds\n"
"v_mfma_f32_16x16x32_bf16 v[44:47], a[128:131], v[112:115], v[44:47]\n"
"s_waitcnt lgkmcnt(6)\n"
"v_exp_f32 v30, v30\n"
"v_mfma_f32_16x16x32_bf16 v[60:63], a[144:147], v[112:115], v[204:207]\n"
"v_exp_f32 v31, v31\n"
"v_mfma_f32_16x16x32_bf16 v[60:63], a[148:151], v[116:119], v[60:63]\n"
"s_waitcnt lgkmcnt(4)\n"
"ds_read_b64_tr_b16 a[168:169], v220 offset:32904\n"
"v_cvt_pk_bf16_f32 v186, v28, v29\n"
"v_mfma_f32_16x16x32_bf16 v[60:63], a[152:155], v[120:123], v[60:63]\n"
"v_exp_f32 v24, v24\n"
"v_mfma_f32_16x16x32_bf16 v[60:63], a[156:159], v[124:127], v[60:63]\n"
"ds_read_b64_tr_b16 a[170:171], v220 offset:33928\n"
"v_cvt_pk_bf16_f32 v187, v30, v31\n"
"v_mfma_f32_16x16x32_bf16 v[56:59], a[156:159], v[108:111], v[200:203]\n"
"v_exp_f32 v25, v25\n"
"v_mfma_f32_16x16x32_bf16 v[56:59], a[152:155], v[104:107], v[56:59]\n"
"s_add_u32 s58, s68, 0\n"
"ds_read_b64_tr_b16 a[172:173], v220 offset:41128\n"
"ds_read_b64_tr_b16 a[174:175], v220 offset:42152\n"
"v_mfma_f32_16x16x32_bf16 v[56:59], a[148:151], v[100:103], v[56:59]\n"
"s_add_u32 m0, s61, 84032\n"
"v_exp_f32 v26, v26\n"
"v_mfma_f32_16x16x32_bf16 v[56:59], a[144:147], v[96:99], v[56:59]\n"
"buffer_load_dwordx4 v226, s[44:47], s58 offen lds\n"
"v_mfma_f32_16x16x32_bf16 v[52:55], a[144:147], v[80:83], v[196:199]\n"
"v_exp_f32 v27, v27\n"
"v_mfma_f32_16x16x32_bf16 v[52:55], a[148:151], v[84:87], v[52:55]\n"
"v_exp_f32 v20, v20\n"
"v_mfma_f32_16x16x32_bf16 v[52:55], a[152:155], v[88:91], v[52:55]\n"
"v_exp_f32 v21, v21\n"
"v_mfma_f32_16x16x32_bf16 v[52:55], a[156:159], v[92:95], v[52:55]\n"
"ds_read_b64_tr_b16 a[176:177], v220 offset:33152\n"
"v_cvt_pk_bf16_f32 v178, v24, v25\n"
"v_mfma_f32_16x16x32_bf16 v[48:51], a[156:159], v[76:79], v[192:195]\n"
"v_exp_f32 v22, v22\n"
"v_mfma_f32_16x16x32_bf16 v[48:51], a[152:155], v[72:75], v[48:51]\n"
"s_add_u32 m0, s61, 85056\n"
"ds_read_b64_tr_b16 a[178:179], v220 offset:34176\n"
"v_cvt_pk_bf16_f32 v179, v26, v27\n"
"v_mfma_f32_16x16x32_bf16 v[48:51], a[148:151], v[68:71], v[48:51]\n"
"buffer_load_dwordx4 v227, s[44:47], s58 offen lds\n"
"v_mfma_f32_16x16x32_bf16 v[48:51], a[144:147], v[64:67], v[48:51]\n"
"ds_read_b64_tr_b16 a[180:181], v220 offset:41376\n"
"ds_read_b64_tr_b16 a[182:183], v220 offset:42400\n"
"v_smfmac_f32_16x16x64_bf16 a[228:231], a[200:203], v[128:135], v208\n"
"v_cvt_pk_bf16_f32 v170, v20, v21\n"
"v_smfmac_f32_16x16x64_bf16 a[232:235], a[200:203], v[136:143], v208\n"
"v_smfmac_f32_16x16x64_bf16 a[236:239], a[200:203], v[144:151], v208\n"
"v_smfmac_f32_16x16x64_bf16 a[240:243], a[200:203], v[152:159], v208\n"
"s_waitcnt lgkmcnt(8)\n"
"ds_read_b64_tr_b16 a[184:185], v220 offset:33160\n"
"v_mfma_f32_16x16x32_bf16 a[0:3], a[160:163], v[128:131], a[0:3]\n"
"v_exp_f32 v23, v23\n"
"v_mfma_f32_16x16x32_bf16 a[0:3], a[164:167], v[132:135], a[0:3]\n"
"s_add_u32 s58, s84, 256\n"
"ds_read_b64_tr_b16 a[186:187], v220 offset:34184\n"
"v_lshrrev_b32 v247, 4, v232\n"
"v_mfma_f32_16x16x32_bf16 a[4:7], a[164:167], v[140:143], a[4:7]\n"
"s_add_u32 m0, s61, 32768\n"
"v_exp_f32 v16, v16\n"
"v_mfma_f32_16x16x32_bf16 a[4:7], a[160:163], v[136:139], a[4:7]\n"
"s_lshl_b32 s72, s23, 2\n"
"buffer_load_dwordx4 v226, s[44:47], s58 offen lds\n"
"v_mfma_f32_16x16x32_bf16 a[8:11], a[160:163], v[144:147], a[8:11]\n"
"v_exp_f32 v17, v17\n"
"v_mfma_f32_16x16x32_bf16 a[8:11], a[164:167], v[148:151], a[8:11]\n"
"ds_read_b64_tr_b16 a[188:189], v220 offset:41384\n"
"ds_read_b64_tr_b16 a[190:191], v220 offset:42408\n"
"v_mfma_f32_16x16x32_bf16 a[12:15], a[164:167], v[156:159], a[12:15]\n"
"s_waitcnt vmcnt(26)\n"
"v_lshrrev_b32 v248, v247, v248\n"
"v_cvt_pk_bf16_f32 v171, v22, v23\n"
"v_mfma_f32_16x16x32_bf16 a[12:15], a[160:163], v[152:155], a[12:15]\n"
"s_waitcnt lgkmcnt(8)\n"
"v_exp_f32 v18, v18\n"
"v_mfma_f32_16x16x32_bf16 a[28:31], a[168:171], v[152:155], a[28:31]\n"
"v_exp_f32 v19, v19\n"
"v_mfma_f32_16x16x32_bf16 a[28:31], a[172:175], v[156:159], a[28:31]\n"
"ds_read_b64_tr_b16 a[160:161], v220 offset:33408\n"
"v_and_b32 v248, 1, v248\n"
"v_mfma_f32_16x16x32_bf16 a[24:27], a[172:175], v[148:151], a[24:27]\n"
"v_exp_f32 v32, v32\n"
"v_mfma_f32_16x16x32_bf16 a[24:27], a[168:171], v[144:147], a[24:27]\n"
"ds_read_b64_tr_b16 a[162:163], v220 offset:34432\n"
"v_cmp_eq_u32 vcc, 0, v248\n"
"v_mfma_f32_16x16x32_bf16 a[20:23], a[168:171], v[136:139], a[20:23]\n"
"v_exp_f32 v33, v33\n"
"v_mfma_f32_16x16x32_bf16 a[20:23], a[172:175], v[140:143], a[20:23]\n"
"ds_read_b64_tr_b16 a[164:165], v220 offset:41632\n"
"ds_read_b64_tr_b16 a[166:167], v220 offset:42656\n"
"v_mfma_f32_16x16x32_bf16 a[16:19], a[172:175], v[132:135], a[16:19]\n"
"v_mov_b32 v249, s72\n"
"v_cvt_pk_bf16_f32 v162, v16, v17\n"
"v_mfma_f32_16x16x32_bf16 a[16:19], a[168:171], v[128:131], a[16:19]\n"
"s_waitcnt lgkmcnt(8)\n"
"v_exp_f32 v34, v34\n"
"v_mfma_f32_16x16x32_bf16 a[32:35], a[176:179], v[128:131], a[32:35]\n"
"s_mov_b32 s98, s79\n"
"v_exp_f32 v35, v35\n"
"v_mfma_f32_16x16x32_bf16 a[32:35], a[180:183], v[132:135], a[32:35]\n"
"ds_read_b64_tr_b16 a[168:169], v220 offset:33416\n"
"v_cvt_pk_bf16_f32 v163, v18, v19\n"
"v_mfma_f32_16x16x32_bf16 a[36:39], a[180:183], v[140:143], a[36:39]\n"
"s_add_u32 m0, s61, 33792\n"
"v_exp_f32 v36, v36\n"
"v_mfma_f32_16x16x32_bf16 a[36:39], a[176:179], v[136:139], a[36:39]\n"
"buffer_load_dwordx4 v227, s[44:47], s58 offen lds\n"
"v_mfma_f32_16x16x32_bf16 a[40:43], a[176:179], v[144:147], a[40:43]\n"
"v_exp_f32 v37, v37\n"
"v_mfma_f32_16x16x32_bf16 a[40:43], a[180:183], v[148:151], a[40:43]\n"
"ds_read_b64_tr_b16 a[170:171], v220 offset:34440\n"
"v_mov_b32 v248, vcc_lo\n"
"v_mfma_f32_16x16x32_bf16 a[44:47], a[180:183], v[156:159], a[44:47]\n"
"v_cvt_pk_bf16_f32 v164, v32, v33\n"
"v_cvt_pk_bf16_f32 v165, v34, v35\n"
"v_mfma_f32_16x16x32_bf16 a[44:47], a[176:179], v[152:155], a[44:47]\n"
"s_waitcnt lgkmcnt(6)\n"
"ds_read_b64_tr_b16 a[172:173], v220 offset:41640\n"
"ds_read_b64_tr_b16 a[174:175], v220 offset:42664\n"
"v_mfma_f32_16x16x32_bf16 a[60:63], a[184:187], v[152:155], a[60:63]\n"
"v_or_b32 v248, 0x80000000, v248\n"
"v_cvt_pk_bf16_f32 v172, v36, v37\n"
"v_mfma_f32_16x16x32_bf16 a[60:63], a[188:191], v[156:159], a[60:63]\n"
"v_exp_f32 v38, v38\n"
"v_mfma_f32_16x16x32_bf16 a[56:59], a[188:191], v[148:151], a[56:59]\n"
"v_exp_f32 v39, v39\n"
"v_mfma_f32_16x16x32_bf16 a[56:59], a[184:187], v[144:147], a[56:59]\n"
"ds_read_b64_tr_b16 a[176:177], v220 offset:33664\n"
"ds_write_b32 v233, v248 offset:16\n"
"v_mfma_f32_16x16x32_bf16 a[52:55], a[184:187], v[136:139], a[52:55]\n"
"v_exp_f32 v40, v40\n"
"v_mfma_f32_16x16x32_bf16 a[52:55], a[188:191], v[140:143], a[52:55]\n"
"ds_read_b64_tr_b16 a[178:179], v220 offset:34688\n"
"v_cvt_pk_bf16_f32 v173, v38, v39\n"
"v_mfma_f32_16x16x32_bf16 a[48:51], a[188:191], v[132:135], a[48:51]\n"
"buffer_store_dword v248, v249, s[96:99], 0 offen sc1\n"
"v_mfma_f32_16x16x32_bf16 a[48:51], a[184:187], v[128:131], a[48:51]\n"
"s_waitcnt lgkmcnt(7)\n"
"ds_read_b64_tr_b16 a[180:181], v220 offset:41888\n"
"ds_read_b64_tr_b16 a[182:183], v220 offset:42912\n"
"v_mfma_f32_16x16x32_bf16 a[64:67], a[160:163], v[128:131], a[64:67]\n"
"s_mov_b32 s98, 139264\n"
"v_exp_f32 v41, v41\n"
"v_mfma_f32_16x16x32_bf16 a[64:67], a[164:167], v[132:135], a[64:67]\n"
"s_add_u32 s58, s85, 256\n"
"v_exp_f32 v42, v42\n"
"v_mfma_f32_16x16x32_bf16 a[68:71], a[164:167], v[140:143], a[68:71]\n"
"s_add_u32 m0, s61, 34816\n"
"v_exp_f32 v43, v43\n"
"v_mfma_f32_16x16x32_bf16 a[68:71], a[160:163], v[136:139], a[68:71]\n"
"buffer_load_dwordx4 v226, s[44:47], s58 offen lds\n"
"v_mfma_f32_16x16x32_bf16 a[72:75], a[160:163], v[144:147], a[72:75]\n"
"v_exp_f32 v44, v44\n"
"v_mfma_f32_16x16x32_bf16 a[72:75], a[164:167], v[148:151], a[72:75]\n"
"ds_read_b64_tr_b16 a[184:185], v220 offset:33672\n"
"v_cvt_pk_bf16_f32 v180, v40, v41\n"
"v_mfma_f32_16x16x32_bf16 a[76:79], a[164:167], v[156:159], a[76:79]\n"
"v_exp_f32 v45, v45\n"
"v_mfma_f32_16x16x32_bf16 a[76:79], a[160:163], v[152:155], a[76:79]\n"
"s_waitcnt lgkmcnt(6)\n"
"ds_read_b64_tr_b16 a[186:187], v220 offset:34696\n"
"v_cvt_pk_bf16_f32 v181, v42, v43\n"
"v_mfma_f32_16x16x32_bf16 a[92:95], a[168:171], v[152:155], a[92:95]\n"
"v_exp_f32 v46, v46\n"
"v_mfma_f32_16x16x32_bf16 a[92:95], a[172:175], v[156:159], a[92:95]\n"
"ds_read_b64_tr_b16 a[188:189], v220 offset:41896\n"
"ds_read_b64_tr_b16 a[190:191], v220 offset:42920\n"
"v_mfma_f32_16x16x32_bf16 a[88:91], a[172:175], v[148:151], a[88:91]\n"
"v_exp_f32 v47, v47\n"
"v_mfma_f32_16x16x32_bf16 a[88:91], a[168:171], v[144:147], a[88:91]\n"
"v_cvt_pk_bf16_f32 v188, v44, v45\n"
"v_cvt_pk_bf16_f32 v189, v46, v47\n"
"v_mfma_f32_16x16x32_bf16 a[84:87], a[168:171], v[136:139], a[84:87]\n"
"v_exp_f32 v60, v60\n"
"v_mfma_f32_16x16x32_bf16 a[84:87], a[172:175], v[140:143], a[84:87]\n"
"v_exp_f32 v61, v61\n"
"v_mfma_f32_16x16x32_bf16 a[80:83], a[172:175], v[132:135], a[80:83]\n"
"v_exp_f32 v62, v62\n"
"v_mfma_f32_16x16x32_bf16 a[80:83], a[168:171], v[128:131], a[80:83]\n"
"s_waitcnt lgkmcnt(4)\n"
"v_exp_f32 v63, v63\n"
"v_mfma_f32_16x16x32_bf16 a[96:99], a[176:179], v[128:131], a[96:99]\n"
"s_waitcnt vmcnt(25) lgkmcnt(0)\n"
"v_exp_f32 v56, v56\n"
"v_mfma_f32_16x16x32_bf16 a[96:99], a[180:183], v[132:135], a[96:99]\n"
"s_barrier\n"
"v_mfma_f32_16x16x32_bf16 a[100:103], a[180:183], v[140:143], a[100:103]\n"
"ds_read_b64_tr_b16 a[160:161], v220 offset:49344\n"
"v_cvt_pk_bf16_f32 v190, v60, v61\n"
"v_mfma_f32_16x16x32_bf16 a[100:103], a[176:179], v[136:139], a[100:103]\n"
"ds_read_b64_tr_b16 a[162:163], v220 offset:50368\n"
"v_cvt_pk_bf16_f32 v191, v62, v63\n"
"v_mfma_f32_16x16x32_bf16 a[104:107], a[176:179], v[144:147], a[104:107]\n"
"s_add_u32 m0, s61, 35840\n"
"v_exp_f32 v57, v57\n"
"v_mfma_f32_16x16x32_bf16 a[104:107], a[180:183], v[148:151], a[104:107]\n"
"buffer_load_dwordx4 v227, s[44:47], s58 offen lds\n"
"v_mfma_f32_16x16x32_bf16 a[108:111], a[180:183], v[156:159], a[108:111]\n"
"v_exp_f32 v58, v58\n"
"v_mfma_f32_16x16x32_bf16 a[108:111], a[176:179], v[152:155], a[108:111]\n"
"ds_read_b64_tr_b16 a[164:165], v220 offset:57568\n"
"v_cvt_pk_bf16_f32 v182, v56, v57\n"
"v_mfma_f32_16x16x32_bf16 a[124:127], a[184:187], v[152:155], a[124:127]\n"
"v_exp_f32 v59, v59\n"
"v_mfma_f32_16x16x32_bf16 a[124:127], a[188:191], v[156:159], a[124:127]\n"
"ds_read_b64_tr_b16 a[166:167], v220 offset:58592\n"
"v_cvt_pk_bf16_f32 v183, v58, v59\n"
"v_mfma_f32_16x16x32_bf16 a[120:123], a[188:191], v[148:151], a[120:123]\n"
"v_exp_f32 v52, v52\n"
"v_mfma_f32_16x16x32_bf16 a[120:123], a[184:187], v[144:147], a[120:123]\n"
"ds_read_b64_tr_b16 a[168:169], v220 offset:49352\n"
"v_mfma_f32_16x16x32_bf16 a[116:119], a[184:187], v[136:139], a[116:119]\n"
"v_exp_f32 v53, v53\n"
"v_mfma_f32_16x16x32_bf16 a[116:119], a[188:191], v[140:143], a[116:119]\n"
"ds_read_b64_tr_b16 a[170:171], v220 offset:50376\n"
"v_cvt_pk_bf16_f32 v174, v52, v53\n"
"v_mfma_f32_16x16x32_bf16 a[112:115], a[188:191], v[132:135], a[112:115]\n"
"v_exp_f32 v54, v54\n"
"v_mfma_f32_16x16x32_bf16 a[112:115], a[184:187], v[128:131], a[112:115]\n"
"ds_read_b64_tr_b16 a[172:173], v220 offset:57576\n"
"ds_read_b64_tr_b16 a[174:175], v220 offset:58600\n"
"v_exp_f32 v55, v55\n"
"v_exp_f32 v48, v48\n"
"v_cvt_pk_bf16_f32 v175, v54, v55\n"
"v_exp_f32 v49, v49\n"
"v_exp_f32 v50, v50\n"
"v_cvt_pk_bf16_f32 v166, v48, v49\n"
"v_exp_f32 v51, v51\n"
"s_nop 0\n"
"v_cvt_pk_bf16_f32 v167, v50, v51\n"
"s_nop 1\n"
"v_smfmac_f32_16x16x64_bf16 a[228:231], a[200:203], v[160:167], v208\n"
"ds_read_b64_tr_b16 a[176:177], v220 offset:49600\n"
"v_smfmac_f32_16x16x64_bf16 a[232:235], a[200:203], v[168:175], v208\n"
"ds_read_b64_tr_b16 a[178:179], v220 offset:50624\n"
"v_smfmac_f32_16x16x64_bf16 a[236:239], a[200:203], v[176:183], v208\n"
"s_add_u32 s60, s59, 524288\n"
"ds_read_b64_tr_b16 a[180:181], v220 offset:57824\n"
"v_smfmac_f32_16x16x64_bf16 a[240:243], a[200:203], v[184:191], v208\n"
"s_waitcnt lgkmcnt(7)\n"
"ds_read_b64_tr_b16 a[182:183], v220 offset:58848\n"
"v_mfma_f32_16x16x32_bf16 a[0:3], a[160:163], v[160:163], a[0:3]\n"
"s_add_u32 m0, s62, 32896\n"
"ds_read_b64_tr_b16 a[184:185], v220 offset:49608\n"
"v_mfma_f32_16x16x32_bf16 a[0:3], a[164:167], v[164:167], a[0:3]\n"
"buffer_load_dwordx4 v225, s[48:51], s60 offen lds\n"
"v_mfma_f32_16x16x32_bf16 a[4:7], a[164:167], v[172:175], a[4:7]\n"
"ds_read_b64_tr_b16 a[186:187], v220 offset:50632\n"
"v_mfma_f32_16x16x32_bf16 a[4:7], a[160:163], v[168:171], a[4:7]\n"
"v_mfma_f32_16x16x32_bf16 a[8:11], a[160:163], v[176:179], a[8:11]\n"
"ds_read_b64_tr_b16 a[188:189], v220 offset:57832\n"
"v_accvgpr_read_b32 v212, a228\n"
"v_mfma_f32_16x16x32_bf16 a[8:11], a[164:167], v[180:183], a[8:11]\n"
"s_add_u32 s60, s59, 557056\n"
"v_accvgpr_read_b32 v213, a232\n"
"v_accvgpr_read_b32 v214, a236\n"
"v_mfma_f32_16x16x32_bf16 a[12:15], a[164:167], v[188:191], a[12:15]\n"
"s_add_u32 m0, s62, 33920\n"
"ds_read_b64_tr_b16 a[190:191], v220 offset:58856\n"
"v_accvgpr_read_b32 v215, a240\n"
"v_mfma_f32_16x16x32_bf16 a[12:15], a[160:163], v[184:187], a[12:15]\n"
"s_waitcnt lgkmcnt(8)\n"
"buffer_load_dwordx4 v225, s[48:51], s60 offen lds\n"
"v_mfma_f32_16x16x32_bf16 a[28:31], a[168:171], v[184:187], a[28:31]\n"
"s_waitcnt vmcnt(7)\n"
"ds_read_b64_tr_b16 a[160:161], v220 offset:49856\n"
"v_accvgpr_read_b32 v240, a229\n"
"v_mfma_f32_16x16x32_bf16 a[28:31], a[172:175], v[188:191], a[28:31]\n"
"ds_read_b128 v[128:131], v230\n"
"v_accvgpr_read_b32 v241, a233\n"
"v_mfma_f32_16x16x32_bf16 a[24:27], a[172:175], v[180:183], a[24:27]\n"
"ds_read_b64_tr_b16 a[162:163], v220 offset:50880\n"
"v_accvgpr_read_b32 v242, a237\n"
"v_mfma_f32_16x16x32_bf16 a[24:27], a[168:171], v[176:179], a[24:27]\n"
"s_add_u32 s60, s59, 589824\n"
"ds_read_b128 v[132:135], v231\n"
"v_accvgpr_read_b32 v243, a241\n"
"v_mfma_f32_16x16x32_bf16 a[20:23], a[168:171], v[168:171], a[20:23]\n"
"s_add_u32 m0, s62, 34952\n"
"ds_read_b64_tr_b16 a[164:165], v220 offset:58080\n"
"v_add_f32 v212, v212, v240\n"
"v_mfma_f32_16x16x32_bf16 a[20:23], a[172:175], v[172:175], a[20:23]\n"
"buffer_load_dwordx4 v225, s[48:51], s60 offen lds\n"
"v_mfma_f32_16x16x32_bf16 a[16:19], a[172:175], v[164:167], a[16:19]\n"
"ds_read_b64_tr_b16 a[166:167], v220 offset:59104\n"
"v_add_f32 v213, v213, v241\n"
"v_mfma_f32_16x16x32_bf16 a[16:19], a[168:171], v[160:163], a[16:19]\n"
"s_waitcnt lgkmcnt(10)\n"
"ds_read_b128 v[136:139], v230 offset:2048\n"
"v_add_f32 v214, v214, v242\n"
"v_mfma_f32_16x16x32_bf16 a[32:35], a[176:179], v[160:163], a[32:35]\n"
"ds_read_b64_tr_b16 a[168:169], v220 offset:49864\n"
"v_add_f32 v215, v215, v243\n"
"v_mfma_f32_16x16x32_bf16 a[32:35], a[180:183], v[164:167], a[32:35]\n"
"s_add_u32 s60, s59, 622592\n"
"ds_read_b128 v[140:143], v231 offset:2048\n"
"v_mov_b32 v240, v212\n"
"v_mfma_f32_16x16x32_bf16 a[36:39], a[180:183], v[172:175], a[36:39]\n"
"s_add_u32 m0, s62, 35976\n"
"ds_read_b64_tr_b16 a[170:171], v220 offset:50888\n"
"v_mov_b32 v241, v213\n"
"v_mfma_f32_16x16x32_bf16 a[36:39], a[176:179], v[168:171], a[36:39]\n"
"buffer_load_dwordx4 v225, s[48:51], s60 offen lds\n"
"v_mfma_f32_16x16x32_bf16 a[40:43], a[176:179], v[176:179], a[40:43]\n"
"ds_read_b64_tr_b16 a[172:173], v220 offset:58088\n"
"v_mov_b32 v242, v214\n"
"v_mfma_f32_16x16x32_bf16 a[40:43], a[180:183], v[180:183], a[40:43]\n"
"ds_read_b128 v[144:147], v230 offset:16448\n"
"v_mov_b32 v243, v215\n"
"v_mfma_f32_16x16x32_bf16 a[44:47], a[180:183], v[188:191], a[44:47]\n"
"s_add_u32 s58, s87, 256\n"
"ds_read_b64_tr_b16 a[174:175], v220 offset:59112\n"
"v_permlane32_swap_b32 v212, v240\n"
"v_mfma_f32_16x16x32_bf16 a[44:47], a[176:179], v[184:187], a[44:47]\n"
"s_waitcnt lgkmcnt(13)\n"
"ds_read_b128 v[148:151], v231 offset:16448\n"
"v_permlane32_swap_b32 v213, v241\n"
"v_mfma_f32_16x16x32_bf16 a[60:63], a[184:187], v[184:187], a[60:63]\n"
"s_add_u32 m0, s61, 49152\n"
"ds_read_b64_tr_b16 a[176:177], v220 offset:50112\n"
"v_permlane32_swap_b32 v214, v242\n"
"v_mfma_f32_16x16x32_bf16 a[60:63], a[188:191], v[188:191], a[60:63]\n"
"buffer_load_dwordx4 v226, s[44:47], s58 offen lds\n"
"v_mfma_f32_16x16x32_bf16 a[56:59], a[188:191], v[180:183], a[56:59]\n"
"ds_read_b64_tr_b16 a[178:179], v220 offset:51136\n"
"v_permlane32_swap_b32 v215, v243\n"
"v_mfma_f32_16x16x32_bf16 a[56:59], a[184:187], v[176:179], a[56:59]\n"
"ds_read_b128 v[152:155], v230 offset:18496\n"
"v_mov_b32 v240, v212\n"
"v_mfma_f32_16x16x32_bf16 a[52:55], a[184:187], v[168:171], a[52:55]\n"
"ds_read_b64_tr_b16 a[180:181], v220 offset:58336\n"
"v_mov_b32 v241, v213\n"
"v_mfma_f32_16x16x32_bf16 a[52:55], a[188:191], v[172:175], a[52:55]\n"
"ds_read_b128 v[156:159], v231 offset:18496\n"
"v_mov_b32 v242, v214\n"
"v_mfma_f32_16x16x32_bf16 a[48:51], a[188:191], v[164:167], a[48:51]\n"
"s_add_u32 m0, s61, 50176\n"
"ds_read_b64_tr_b16 a[182:183], v220 offset:59360\n"
"v_mov_b32 v243, v215\n"
"v_permlane16_swap_b32 v212, v240\n"
"v_permlane16_swap_b32 v213, v241\n"
"v_permlane16_swap_b32 v214, v242\n"
"v_permlane16_swap_b32 v215, v243\n"
"v_add_f32 v252, v212, v213\n"
"v_add_f32 v253, v214, v215\n"
"v_add_f32 v252, v252, v253\n"
"v_cmp_nge_f32 vcc, 0x5d800000, v252\n"
"v_mfma_f32_16x16x32_bf16 a[48:51], a[184:187], v[160:163], a[48:51]\n"
"s_waitcnt lgkmcnt(14)\n"
"buffer_load_dwordx4 v227, s[44:47], s58 offen lds\n"
"v_mfma_f32_16x16x32_bf16 a[64:67], a[160:163], v[160:163], a[64:67]\n"
"s_cmp_lg_u64 vcc, 0\n"
"s_cselect_b32 s75, 1, 0\n"
"ds_read_b64_tr_b16 a[184:185], v220 offset:50120\n"
"v_mfma_f32_16x16x32_bf16 a[64:67], a[164:167], v[164:167], a[64:67]\n"
"s_add_u32 s58, s68, 256\n"
"v_mov_b32 v252, s75\n"
"v_mfma_f32_16x16x32_bf16 a[68:71], a[164:167], v[172:175], a[68:71]\n"
"s_add_u32 m0, s61, 51200\n"
"ds_read_b64_tr_b16 a[186:187], v220 offset:51144\n"
"v_mfma_f32_16x16x32_bf16 a[68:71], a[160:163], v[168:171], a[68:71]\n"
"buffer_load_dwordx4 v226, s[44:47], s58 offen lds\n"
"v_mfma_f32_16x16x32_bf16 a[72:75], a[160:163], v[176:179], a[72:75]\n"
"ds_read_b64_tr_b16 a[188:189], v220 offset:58344\n"
"v_mfma_f32_16x16x32_bf16 a[72:75], a[164:167], v[180:183], a[72:75]\n"
"ds_write_b32 v233, v252\n"
"v_mfma_f32_16x16x32_bf16 a[76:79], a[164:167], v[188:191], a[76:79]\n"
"s_add_u32 m0, s61, 52224\n"
"ds_read_b64_tr_b16 a[190:191], v220 offset:59368\n"
"v_mfma_f32_16x16x32_bf16 a[76:79], a[160:163], v[184:187], a[76:79]\n"
"s_waitcnt lgkmcnt(12)\n"
"buffer_load_dwordx4 v227, s[44:47], s58 offen lds\n"
"v_mfma_f32_16x16x32_bf16 a[92:95], a[168:171], v[184:187], a[92:95]\n"
"v_rcp_f32 v212, v212\n"
"v_mfma_f32_16x16x32_bf16 a[92:95], a[172:175], v[188:191], a[92:95]\n"
"v_rcp_f32 v213, v213\n"
"v_mfma_f32_16x16x32_bf16 a[88:91], a[172:175], v[180:183], a[88:91]\n"
"v_rcp_f32 v214, v214\n"
"v_mfma_f32_16x16x32_bf16 a[88:91], a[168:171], v[176:179], a[88:91]\n"
"s_waitcnt vmcnt(22) lgkmcnt(0)\n"
"v_rcp_f32 v215, v215\n"
"v_mfma_f32_16x16x32_bf16 a[84:87], a[168:171], v[168:171], a[84:87]\n"
"s_barrier\n"
"v_mfma_f32_16x16x32_bf16 a[84:87], a[172:175], v[172:175], a[84:87]\n"
"s_add_u32 s60, s59, 1048576\n"
"ds_read_b64_tr_b16 a[160:161], v221 offset:33024\n"
"ds_read_b64_tr_b16 a[162:163], v221 offset:34048\n"
"v_mfma_f32_16x16x32_bf16 a[80:83], a[172:175], v[164:167], a[80:83]\n"
"s_add_u32 m0, s62, 49344\n"
"ds_read_b64_tr_b16 a[164:165], v221 offset:41248\n"
"v_mfma_f32_16x16x32_bf16 a[80:83], a[168:171], v[160:163], a[80:83]\n"
"buffer_load_dwordx4 v225, s[48:51], s60 offen lds\n"
"v_mfma_f32_16x16x32_bf16 a[96:99], a[176:179], v[160:163], a[96:99]\n"
"ds_read_b64_tr_b16 a[166:167], v221 offset:42272\n"
"ds_read_b64_tr_b16 a[168:169], v221 offset:33032\n"
"v_mfma_f32_16x16x32_bf16 a[96:99], a[180:183], v[164:167], a[96:99]\n"
"v_mfma_f32_16x16x32_bf16 a[100:103], a[180:183], v[172:175], a[100:103]\n"
"ds_read_b64_tr_b16 a[170:171], v221 offset:34056\n"
"v_mfma_f32_16x16x32_bf16 a[100:103], a[176:179], v[168:171], a[100:103]\n"
"s_add_u32 s60, s59, 1081344\n"
"ds_read_b128 v[236:239], v234\n"
"v_mfma_f32_16x16x32_bf16 a[104:107], a[176:179], v[176:179], a[104:107]\n"
"s_add_u32 m0, s62, 50368\n"
"ds_read_b64_tr_b16 a[172:173], v221 offset:41256\n"
"v_mfma_f32_16x16x32_bf16 a[104:107], a[180:183], v[180:183], a[104:107]\n"
"buffer_load_dwordx4 v225, s[48:51], s60 offen lds\n"
"v_mfma_f32_16x16x32_bf16 a[108:111], a[180:183], v[188:191], a[108:111]\n"
"ds_read_b64_tr_b16 a[174:175], v221 offset:42280\n"
"v_mfma_f32_16x16x32_bf16 a[108:111], a[176:179], v[184:187], a[108:111]\n"
"s_add_u32 s60, s59, 1114112\n"
"v_mfma_f32_16x16x32_bf16 a[124:127], a[184:187], v[184:187], a[124:127]\n"
"s_add_u32 m0, s62, 51400\n"
"ds_read_b64_tr_b16 a[176:177], v221 offset:33280\n"
"v_mfma_f32_16x16x32_bf16 a[124:127], a[188:191], v[188:191], a[124:127]\n"
"buffer_load_dwordx4 v225, s[48:51], s60 offen lds\n"
"v_mfma_f32_16x16x32_bf16 a[120:123], a[188:191], v[180:183], a[120:123]\n"
"s_waitcnt lgkmcnt(3)\n"
"ds_read_b64_tr_b16 a[178:179], v221 offset:34304\n"
"v_or3_b32 v236, v236, v237, v238\n"
"v_mfma_f32_16x16x32_bf16 a[120:123], a[184:187], v[176:179], a[120:123]\n"
"v_mfma_f32_16x16x32_bf16 a[116:119], a[184:187], v[168:171], a[116:119]\n"
"ds_read_b64_tr_b16 a[180:181], v221 offset:41504\n"
"v_or_b32 v236, v236, v239\n"
"v_mfma_f32_16x16x32_bf16 a[116:119], a[188:191], v[172:175], a[116:119]\n"
"s_add_u32 s60, s59, 1146880\n"
"v_mfma_f32_16x16x32_bf16 a[112:115], a[188:191], v[164:167], a[112:115]\n"
"s_add_u32 m0, s62, 52424\n"
"ds_read_b64_tr_b16 a[182:183], v221 offset:42528\n"
"v_readfirstlane_b32 s75, v236\n"
"v_mfma_f32_16x16x32_bf16 a[112:115], a[184:187], v[160:163], a[112:115]\n"
"s_waitcnt lgkmcnt(9)\n"
"buffer_load_dwordx4 v225, s[48:51], s60 offen lds\n"
"s_nop 0\n"
"s_cmp_lg_u32 s75, 0\n"
"s_cbranch_scc1 L_slow%=\n"
"s_mov_b32 s63, 1\n"
"s_branch L_head%=\n"
"L_tble%=:\n"
"v_mfma_f32_16x16x32_bf16 v[0:3], a[128:131], v[64:67], v[192:195]\n"
"s_add_u32 m0, s62, 49344\n"
"v_exp_f32 v62, v62\n"
"v_mfma_f32_16x16x32_bf16 v[0:3], a[132:135], v[68:71], v[0:3]\n"
"s_waitcnt lgkmcnt(4)\n"
"buffer_load_dwordx4 v224, s[40:43], s57 offen nt lds\n"
"v_mfma_f32_16x16x32_bf16 v[0:3], a[136:139], v[72:75], v[0:3]\n"
"v_accvgpr_read_b32 v250, a221\n"
"v_mfma_f32_16x16x32_bf16 v[0:3], a[140:143], v[76:79], v[0:3]\n"
"v_exp_f32 v63, v63\n"
"v_mfma_f32_16x16x32_bf16 v[4:7], a[140:143], v[92:95], v[196:199]\n"
"v_exp_f32 v56, v56\n"
"v_mfma_f32_16x16x32_bf16 v[4:7], a[136:139], v[88:91], v[4:7]\n"
"buffer_load_dwordx4 v224, s[40:43], s57 offen offset:1024 nt lds\n"
"v_mfma_f32_16x16x32_bf16 v[4:7], a[132:135], v[84:87], v[4:7]\n"
"v_exp_f32 v57, v57\n"
"v_mfma_f32_16x16x32_bf16 v[4:7], a[128:131], v[80:83], v[4:7]\n"
"v_cvt_pk_bf16_f32 v191, v62, v63\n"
"v_cvt_pk_bf16_f32 v182, v56, v57\n"
"v_mfma_f32_16x16x32_bf16 v[8:11], a[128:131], v[96:99], v[200:203]\n"
"s_add_u32 m0, s62, 49352\n"
"v_exp_f32 v58, v58\n"
"v_mfma_f32_16x16x32_bf16 v[8:11], a[132:135], v[100:103], v[8:11]\n"
"buffer_load_dwordx4 v224, s[40:43], s57 offen offset:2048 nt lds\n"
"v_mfma_f32_16x16x32_bf16 v[8:11], a[136:139], v[104:107], v[8:11]\n"
"v_exp_f32 v59, v59\n"
"v_mfma_f32_16x16x32_bf16 v[8:11], a[140:143], v[108:111], v[8:11]\n"
"v_exp_f32 v52, v52\n"
"v_mfma_f32_16x16x32_bf16 v[12:15], a[140:143], v[124:127], v[204:207]\n"
"v_exp_f32 v53, v53\n"
"v_mfma_f32_16x16x32_bf16 v[12:15], a[136:139], v[120:123], v[12:15]\n"
"buffer_load_dwordx4 v224, s[40:43], s57 offen offset:3072 nt lds\n"
"v_mfma_f32_16x16x32_bf16 v[12:15], a[132:135], v[116:119], v[12:15]\n"
"s_sub_u32 s57, s57, 159744\n"
"v_exp_f32 v54, v54\n"
"v_mfma_f32_16x16x32_bf16 v[12:15], a[128:131], v[112:115], v[12:15]\n"
"s_waitcnt lgkmcnt(0)\n"
"ds_read_b128 a[128:131], v216 offset:40960\n"
"v_cvt_pk_bf16_f32 v183, v58, v59\n"
"v_mfma_f32_16x16x32_bf16 v[28:31], a[144:147], v[112:115], v[204:207]\n"
"s_and_b32 s57, s57, 159744\n"
"v_exp_f32 v55, v55\n"
"v_mfma_f32_16x16x32_bf16 v[28:31], a[148:151], v[116:119], v[28:31]\n"
"ds_read_b128 a[132:135], v217 offset:40960\n"
"v_cvt_pk_bf16_f32 v174, v52, v53\n"
"v_mfma_f32_16x16x32_bf16 v[28:31], a[152:155], v[120:123], v[28:31]\n"
"v_exp_f32 v48, v48\n"
"v_mfma_f32_16x16x32_bf16 v[28:31], a[156:159], v[124:127], v[28:31]\n"
"ds_read_b128 a[136:139], v218 offset:40960\n"
"v_cvt_pk_bf16_f32 v175, v54, v55\n"
"v_mfma_f32_16x16x32_bf16 v[24:27], a[156:159], v[108:111], v[200:203]\n"
"v_exp_f32 v49, v49\n"
"v_mfma_f32_16x16x32_bf16 v[24:27], a[152:155], v[104:107], v[24:27]\n"
"buffer_load_dword v251, v250, s[96:99], 0 offen sc1\n"
"v_mfma_f32_16x16x32_bf16 v[24:27], a[148:151], v[100:103], v[24:27]\n"
"v_exp_f32 v50, v50\n"
"v_mfma_f32_16x16x32_bf16 v[24:27], a[144:147], v[96:99], v[24:27]\n"
"ds_read_b128 a[140:143], v219 offset:40960\n"
"v_cvt_pk_bf16_f32 v166, v48, v49\n"
"v_mfma_f32_16x16x32_bf16 v[20:23], a[144:147], v[80:83], v[196:199]\n"
"v_exp_f32 v51, v51\n"
"v_mfma_f32_16x16x32_bf16 v[20:23], a[148:151], v[84:87], v[20:23]\n"
"v_exp_f32 v0, v0\n"
"v_mfma_f32_16x16x32_bf16 v[20:23], a[152:155], v[88:91], v[20:23]\n"
"v_accvgpr_read_b32 v253, a207\n"
"v_cvt_pk_bf16_f32 v167, v50, v51\n"
"v_mfma_f32_16x16x32_bf16 v[20:23], a[156:159], v[92:95], v[20:23]\n"
"buffer_load_dword a206, v253, s[92:95], 0 offen\n"
"v_mfma_f32_16x16x32_bf16 v[16:19], a[156:159], v[76:79], v[192:195]\n"
"v_exp_f32 v1, v1\n"
"v_mfma_f32_16x16x32_bf16 v[16:19], a[152:155], v[72:75], v[16:19]\n"
"v_exp_f32 v2, v2\n"
"v_mfma_f32_16x16x32_bf16 v[16:19], a[148:151], v[68:71], v[16:19]\n"
"v_exp_f32 v3, v3\n"
"v_mfma_f32_16x16x32_bf16 v[16:19], a[144:147], v[64:67], v[16:19]\n"
"s_waitcnt lgkmcnt(2)\n"
"ds_read_b128 a[144:147], v216 offset:41984\n"
"v_cvt_pk_bf16_f32 v128, v0, v1\n"
"v_mfma_f32_16x16x32_bf16 v[32:35], a[128:131], v[64:67], v[192:195]\n"
"v_exp_f32 v4, v4\n"
"v_mfma_f32_16x16x32_bf16 v[32:35], a[132:135], v[68:71], v[32:35]\n"
"s_waitcnt lgkmcnt(1)\n"
"ds_read_b128 a[148:151], v217 offset:41984\n"
"v_cvt_pk_bf16_f32 v129, v2, v3\n"
"v_mfma_f32_16x16x32_bf16 v[32:35], a[136:139], v[72:75], v[32:35]\n"
"v_exp_f32 v5, v5\n"
"v_mfma_f32_16x16x32_bf16 v[32:35], a[140:143], v[76:79], v[32:35]\n"
"ds_read_b128 a[152:155], v218 offset:41984\n"
"v_cvt_pk_bf16_f32 v136, v4, v5\n"
"v_mfma_f32_16x16x32_bf16 v[36:39], a[140:143], v[92:95], v[196:199]\n"
"buffer_load_dword a206, v253, s[92:95], s78 offen\n"
"v_mfma_f32_16x16x32_bf16 v[36:39], a[136:139], v[88:91], v[36:39]\n"
"v_exp_f32 v6, v6\n"
"v_mfma_f32_16x16x32_bf16 v[36:39], a[132:135], v[84:87], v[36:39]\n"
"v_exp_f32 v7, v7\n"
"v_mfma_f32_16x16x32_bf16 v[36:39], a[128:131], v[80:83], v[36:39]\n"
"ds_read_b128 a[156:159], v219 offset:41984\n"
"v_cvt_pk_bf16_f32 v137, v6, v7\n"
"v_mfma_f32_16x16x32_bf16 v[40:43], a[128:131], v[96:99], v[200:203]\n"
"v_exp_f32 v8, v8\n"
"v_mfma_f32_16x16x32_bf16 v[40:43], a[132:135], v[100:103], v[40:43]\n"
"ds_read_b64_tr_b16 a[160:161], v220 offset:16448\n"
"v_mfma_f32_16x16x32_bf16 v[40:43], a[136:139], v[104:107], v[40:43]\n"
"v_exp_f32 v9, v9\n"
"v_mfma_f32_16x16x32_bf16 v[40:43], a[140:143], v[108:111], v[40:43]\n"
"ds_read_b64_tr_b16 a[162:163], v220 offset:17472\n"
"v_cvt_pk_bf16_f32 v144, v8, v9\n"
"v_mfma_f32_16x16x32_bf16 v[44:47], a[140:143], v[124:127], v[204:207]\n"
"v_exp_f32 v10, v10\n"
"v_mfma_f32_16x16x32_bf16 v[44:47], a[136:139], v[120:123], v[44:47]\n"
"ds_read_b64_tr_b16 a[164:165], v220 offset:24672\n"
"ds_read_b64_tr_b16 a[166:167], v220 offset:25696\n"
"v_mfma_f32_16x16x32_bf16 v[44:47], a[132:135], v[116:119], v[44:47]\n"
"v_exp_f32 v11, v11\n"
"v_mfma_f32_16x16x32_bf16 v[44:47], a[128:131], v[112:115], v[44:47]\n"
"s_waitcnt lgkmcnt(6)\n"
"v_exp_f32 v12, v12\n"
"v_mfma_f32_16x16x32_bf16 v[60:63], a[144:147], v[112:115], v[204:207]\n"
"v_exp_f32 v13, v13\n"
"v_mfma_f32_16x16x32_bf16 v[60:63], a[148:151], v[116:119], v[60:63]\n"
"s_waitcnt lgkmcnt(4)\n"
"ds_read_b64_tr_b16 a[168:169], v220 offset:16456\n"
"ds_read_b32 v249, v234 offset:16\n"
"v_mfma_f32_16x16x32_bf16 v[60:63], a[152:155], v[120:123], v[60:63]\n"
"v_exp_f32 v14, v14\n"
"v_mfma_f32_16x16x32_bf16 v[60:63], a[156:159], v[124:127], v[60:63]\n"
"ds_read_b64_tr_b16 a[170:171], v220 offset:17480\n"
"v_cvt_pk_bf16_f32 v145, v10, v11\n"
"v_mfma_f32_16x16x32_bf16 v[56:59], a[156:159], v[108:111], v[200:203]\n"
"s_and_b32 s59, s23, 3\n"
"v_exp_f32 v15, v15\n"
"v_mfma_f32_16x16x32_bf16 v[56:59], a[152:155], v[104:107], v[56:59]\n"
"s_mul_i32 s60, s69, 9216\n"
"ds_read_b64_tr_b16 a[172:173], v220 offset:24680\n"
"ds_read_b64_tr_b16 a[174:175], v220 offset:25704\n"
"v_mfma_f32_16x16x32_bf16 v[56:59], a[148:151], v[100:103], v[56:59]\n"
"s_mov_b32 s64, 0x80000\n"
"v_accvgpr_read_b32 v248, a220\n"
"v_cvt_pk_bf16_f32 v152, v12, v13\n"
"v_mfma_f32_16x16x32_bf16 v[56:59], a[144:147], v[96:99], v[56:59]\n"
"s_and_b32 s72, s23, 12\n"
"v_exp_f32 v28, v28\n"
"v_mfma_f32_16x16x32_bf16 v[52:55], a[144:147], v[80:83], v[196:199]\n"
"s_lshl3_add_u32 s76, s72, s69\n"
"v_exp_f32 v29, v29\n"
"v_mfma_f32_16x16x32_bf16 v[52:55], a[148:151], v[84:87], v[52:55]\n"
"s_mul_i32 s76, s76, 16384\n"
"v_exp_f32 v30, v30\n"
"v_mfma_f32_16x16x32_bf16 v[52:55], a[152:155], v[88:91], v[52:55]\n"
"s_lshl_b32 s72, s59, 1\n"
"v_exp_f32 v31, v31\n"
"v_mfma_f32_16x16x32_bf16 v[52:55], a[156:159], v[92:95], v[52:55]\n"
"s_add_u32 s72, s72, 1\n"
"ds_read_b64_tr_b16 a[176:177], v220 offset:16704\n"
"v_accvgpr_read_b32 v221, a208\n"
"v_mfma_f32_16x16x32_bf16 v[48:51], a[156:159], v[76:79], v[192:195]\n"
"s_mul_i32 s58, s72, 147456\n"
"v_exp_f32 v24, v24\n"
"v_mfma_f32_16x16x32_bf16 v[48:51], a[152:155], v[72:75], v[48:51]\n"
"s_mov_b32 s72, 2\n"
"ds_read_b64_tr_b16 a[178:179], v220 offset:17728\n"
"v_cvt_pk_bf16_f32 v153, v14, v15\n"
"v_mfma_f32_16x16x32_bf16 v[48:51], a[148:151], v[68:71], v[48:51]\n"
"s_mul_i32 s72, s72, 65536\n"
"v_exp_f32 v25, v25\n"
"v_mfma_f32_16x16x32_bf16 v[48:51], a[144:147], v[64:67], v[48:51]\n"
"s_add_u32 s77, s76, s72\n"
"ds_read_b64_tr_b16 a[180:181], v220 offset:24928\n"
"ds_read_b64_tr_b16 a[182:183], v220 offset:25952\n"
"v_smfmac_f32_16x16x64_bf16 a[228:231], a[200:203], v[160:167], v208\n"
"v_accvgpr_read_b32 v225, a209\n"
"v_smfmac_f32_16x16x64_bf16 a[232:235], a[200:203], v[168:175], v208\n"
"v_cvt_pk_bf16_f32 v154, v28, v29\n"
"v_smfmac_f32_16x16x64_bf16 a[236:239], a[200:203], v[176:183], v208\n"
"v_cvt_pk_bf16_f32 v155, v30, v31\n"
"v_smfmac_f32_16x16x64_bf16 a[240:243], a[200:203], v[184:191], v208\n"
"s_waitcnt lgkmcnt(9)\n"
"ds_read_b64_tr_b16 a[184:185], v220 offset:16712\n"
"v_mfma_f32_16x16x32_bf16 a[0:3], a[160:163], v[160:163], a[0:3]\n"
"v_exp_f32 v26, v26\n"
"v_mfma_f32_16x16x32_bf16 a[0:3], a[164:167], v[164:167], a[0:3]\n"
"ds_read_b64_tr_b16 a[186:187], v220 offset:17736\n"
"v_accvgpr_read_b32 v226, a210\n"
"v_mfma_f32_16x16x32_bf16 a[4:7], a[164:167], v[172:175], a[4:7]\n"
"v_exp_f32 v27, v27\n"
"v_mfma_f32_16x16x32_bf16 a[4:7], a[160:163], v[168:171], a[4:7]\n"
"v_exp_f32 v20, v20\n"
"v_mfma_f32_16x16x32_bf16 a[8:11], a[160:163], v[176:179], a[8:11]\n"
"v_exp_f32 v21, v21\n"
"v_mfma_f32_16x16x32_bf16 a[8:11], a[164:167], v[180:183], a[8:11]\n"
"ds_read_b64_tr_b16 a[188:189], v220 offset:24936\n"
"ds_read_b64_tr_b16 a[190:191], v220 offset:25960\n"
"v_mfma_f32_16x16x32_bf16 a[12:15], a[164:167], v[188:191], a[12:15]\n"
"v_accvgpr_read_b32 v227, a211\n"
"v_cvt_pk_bf16_f32 v146, v24, v25\n"
"v_mfma_f32_16x16x32_bf16 a[12:15], a[160:163], v[184:187], a[12:15]\n"
"s_waitcnt lgkmcnt(8)\n"
"v_exp_f32 v22, v22\n"
"v_mfma_f32_16x16x32_bf16 a[28:31], a[168:171], v[184:187], a[28:31]\n"
"v_exp_f32 v23, v23\n"
"v_mfma_f32_16x16x32_bf16 a[28:31], a[172:175], v[188:191], a[28:31]\n"
"ds_read_b64_tr_b16 a[160:161], v220 offset:16960\n"
"v_cvt_pk_bf16_f32 v147, v26, v27\n"
"v_mfma_f32_16x16x32_bf16 a[24:27], a[172:175], v[180:183], a[24:27]\n"
"v_cvt_pk_bf16_f32 v138, v20, v21\n"
"v_cvt_pk_bf16_f32 v139, v22, v23\n"
"v_mfma_f32_16x16x32_bf16 a[24:27], a[168:171], v[176:179], a[24:27]\n"
"ds_read_b64_tr_b16 a[162:163], v220 offset:17984\n"
"v_accvgpr_read_b32 v228, a212\n"
"v_mfma_f32_16x16x32_bf16 a[20:23], a[168:171], v[168:171], a[20:23]\n"
"v_exp_f32 v16, v16\n"
"v_mfma_f32_16x16x32_bf16 a[20:23], a[172:175], v[172:175], a[20:23]\n"
"ds_read_b64_tr_b16 a[164:165], v220 offset:25184\n"
"ds_read_b64_tr_b16 a[166:167], v220 offset:26208\n"
"v_mfma_f32_16x16x32_bf16 a[16:19], a[172:175], v[164:167], a[16:19]\n"
"v_exp_f32 v17, v17\n"
"v_mfma_f32_16x16x32_bf16 a[16:19], a[168:171], v[160:163], a[16:19]\n"
"s_waitcnt lgkmcnt(8)\n"
"v_exp_f32 v18, v18\n"
"v_mfma_f32_16x16x32_bf16 a[32:35], a[176:179], v[160:163], a[32:35]\n"
"v_exp_f32 v19, v19\n"
"v_mfma_f32_16x16x32_bf16 a[32:35], a[180:183], v[164:167], a[32:35]\n"
"ds_read_b64_tr_b16 a[168:169], v220 offset:16968\n"
"v_accvgpr_read_b32 v229, a213\n"
"v_mfma_f32_16x16x32_bf16 a[36:39], a[180:183], v[172:175], a[36:39]\n"
"v_cvt_pk_bf16_f32 v130, v16, v17\n"
"v_cvt_pk_bf16_f32 v131, v18, v19\n"
"v_mfma_f32_16x16x32_bf16 a[36:39], a[176:179], v[168:171], a[36:39]\n"
"v_exp_f32 v32, v32\n"
"v_mfma_f32_16x16x32_bf16 a[40:43], a[176:179], v[176:179], a[40:43]\n"
"v_exp_f32 v33, v33\n"
"v_mfma_f32_16x16x32_bf16 a[40:43], a[180:183], v[180:183], a[40:43]\n"
"ds_read_b64_tr_b16 a[170:171], v220 offset:17992\n"
"v_accvgpr_read_b32 v230, a214\n"
"v_mfma_f32_16x16x32_bf16 a[44:47], a[180:183], v[188:191], a[44:47]\n"
"v_exp_f32 v34, v34\n"
"v_mfma_f32_16x16x32_bf16 a[44:47], a[176:179], v[184:187], a[44:47]\n"
"s_waitcnt lgkmcnt(6)\n"
"ds_read_b64_tr_b16 a[172:173], v220 offset:25192\n"
"ds_read_b64_tr_b16 a[174:175], v220 offset:26216\n"
"v_mfma_f32_16x16x32_bf16 a[60:63], a[184:187], v[184:187], a[60:63]\n"
"v_exp_f32 v35, v35\n"
"v_mfma_f32_16x16x32_bf16 a[60:63], a[188:191], v[188:191], a[60:63]\n"
"v_exp_f32 v36, v36\n"
"v_mfma_f32_16x16x32_bf16 a[56:59], a[188:191], v[180:183], a[56:59]\n"
"v_exp_f32 v37, v37\n"
"v_mfma_f32_16x16x32_bf16 a[56:59], a[184:187], v[176:179], a[56:59]\n"
"ds_read_b64_tr_b16 a[176:177], v220 offset:17216\n"
"v_accvgpr_read_b32 v231, a215\n"
"v_mfma_f32_16x16x32_bf16 a[52:55], a[184:187], v[168:171], a[52:55]\n"
"v_exp_f32 v38, v38\n"
"v_mfma_f32_16x16x32_bf16 a[52:55], a[188:191], v[172:175], a[52:55]\n"
"ds_read_b64_tr_b16 a[178:179], v220 offset:18240\n"
"v_cvt_pk_bf16_f32 v132, v32, v33\n"
"v_mfma_f32_16x16x32_bf16 a[48:51], a[188:191], v[164:167], a[48:51]\n"
"v_exp_f32 v39, v39\n"
"v_mfma_f32_16x16x32_bf16 a[48:51], a[184:187], v[160:163], a[48:51]\n"
"s_waitcnt lgkmcnt(6)\n"
"ds_read_b64_tr_b16 a[180:181], v220 offset:25440\n"
"ds_read_b64_tr_b16 a[182:183], v220 offset:26464\n"
"v_mfma_f32_16x16x32_bf16 a[64:67], a[160:163], v[160:163], a[64:67]\n"
"v_accvgpr_read_b32 v235, a216\n"
"v_cvt_pk_bf16_f32 v133, v34, v35\n"
"v_mfma_f32_16x16x32_bf16 a[64:67], a[164:167], v[164:167], a[64:67]\n"
"v_exp_f32 v40, v40\n"
"v_mfma_f32_16x16x32_bf16 a[68:71], a[164:167], v[172:175], a[68:71]\n"
"v_exp_f32 v41, v41\n"
"v_mfma_f32_16x16x32_bf16 a[68:71], a[160:163], v[168:171], a[68:71]\n"
"v_cvt_pk_bf16_f32 v140, v36, v37\n"
"v_cvt_pk_bf16_f32 v141, v38, v39\n"
"v_mfma_f32_16x16x32_bf16 a[72:75], a[160:163], v[176:179], a[72:75]\n"
"v_exp_f32 v42, v42\n"
"v_mfma_f32_16x16x32_bf16 a[72:75], a[164:167], v[180:183], a[72:75]\n"
"ds_read_b64_tr_b16 a[184:185], v220 offset:17224\n"
"v_readfirstlane_b32 s78, v249\n"
"v_mfma_f32_16x16x32_bf16 a[76:79], a[164:167], v[188:191], a[76:79]\n"
"v_exp_f32 v43, v43\n"
"v_mfma_f32_16x16x32_bf16 a[76:79], a[160:163], v[184:187], a[76:79]\n"
"s_waitcnt lgkmcnt(5)\n"
"ds_read_b64_tr_b16 a[186:187], v220 offset:18248\n"
"v_cvt_pk_bf16_f32 v148, v40, v41\n"
"v_mfma_f32_16x16x32_bf16 a[92:95], a[168:171], v[184:187], a[92:95]\n"
"v_exp_f32 v44, v44\n"
"v_mfma_f32_16x16x32_bf16 a[92:95], a[172:175], v[188:191], a[92:95]\n"
"ds_read_b64_tr_b16 a[188:189], v220 offset:25448\n"
"ds_read_b64_tr_b16 a[190:191], v220 offset:26472\n"
"v_mfma_f32_16x16x32_bf16 a[88:91], a[172:175], v[180:183], a[88:91]\n"
"v_exp_f32 v45, v45\n"
"v_mfma_f32_16x16x32_bf16 a[88:91], a[168:171], v[176:179], a[88:91]\n"
"v_cvt_pk_bf16_f32 v149, v42, v43\n"
"v_cvt_pk_bf16_f32 v156, v44, v45\n"
"v_mfma_f32_16x16x32_bf16 a[84:87], a[168:171], v[168:171], a[84:87]\n"
"v_exp_f32 v46, v46\n"
"v_mfma_f32_16x16x32_bf16 a[84:87], a[172:175], v[172:175], a[84:87]\n"
"v_exp_f32 v47, v47\n"
"v_mfma_f32_16x16x32_bf16 a[80:83], a[172:175], v[164:167], a[80:83]\n"
"v_exp_f32 v60, v60\n"
"v_mfma_f32_16x16x32_bf16 a[80:83], a[168:171], v[160:163], a[80:83]\n"
"s_waitcnt lgkmcnt(4)\n"
"v_exp_f32 v61, v61\n"
"v_mfma_f32_16x16x32_bf16 a[96:99], a[176:179], v[160:163], a[96:99]\n"
"s_waitcnt vmcnt(7) lgkmcnt(0)\n"
"v_exp_f32 v62, v62\n"
"v_mfma_f32_16x16x32_bf16 a[96:99], a[180:183], v[164:167], a[96:99]\n"
"s_barrier\n"
"v_mfma_f32_16x16x32_bf16 a[100:103], a[180:183], v[172:175], a[100:103]\n"
"ds_read_b128 a[128:131], v216 offset:49152\n"
"v_cvt_pk_bf16_f32 v157, v46, v47\n"
"v_mfma_f32_16x16x32_bf16 a[100:103], a[176:179], v[168:171], a[100:103]\n"
"ds_read_b128 a[132:135], v217 offset:49152\n"
"v_cvt_pk_bf16_f32 v158, v60, v61\n"
"v_mfma_f32_16x16x32_bf16 a[104:107], a[176:179], v[176:179], a[104:107]\n"
"v_exp_f32 v63, v63\n"
"v_mfma_f32_16x16x32_bf16 a[104:107], a[180:183], v[180:183], a[104:107]\n"
"v_exp_f32 v56, v56\n"
"v_mfma_f32_16x16x32_bf16 a[108:111], a[180:183], v[188:191], a[108:111]\n"
"v_exp_f32 v57, v57\n"
"v_mfma_f32_16x16x32_bf16 a[108:111], a[176:179], v[184:187], a[108:111]\n"
"ds_read_b128 a[136:139], v218 offset:49152\n"
"v_cvt_pk_bf16_f32 v159, v62, v63\n"
"v_mfma_f32_16x16x32_bf16 a[124:127], a[184:187], v[184:187], a[124:127]\n"
"v_exp_f32 v58, v58\n"
"v_mfma_f32_16x16x32_bf16 a[124:127], a[188:191], v[188:191], a[124:127]\n"
"ds_read_b128 a[140:143], v219 offset:49152\n"
"v_cvt_pk_bf16_f32 v150, v56, v57\n"
"v_mfma_f32_16x16x32_bf16 a[120:123], a[188:191], v[180:183], a[120:123]\n"
"v_exp_f32 v59, v59\n"
"v_mfma_f32_16x16x32_bf16 a[120:123], a[184:187], v[176:179], a[120:123]\n"
"ds_read_b128 a[144:147], v216 offset:50176\n"
"v_cvt_pk_bf16_f32 v151, v58, v59\n"
"v_mfma_f32_16x16x32_bf16 a[116:119], a[184:187], v[168:171], a[116:119]\n"
"v_exp_f32 v52, v52\n"
"v_mfma_f32_16x16x32_bf16 a[116:119], a[188:191], v[172:175], a[116:119]\n"
"ds_read_b128 a[148:151], v217 offset:50176\n"
"v_mfma_f32_16x16x32_bf16 a[112:115], a[188:191], v[164:167], a[112:115]\n"
"v_exp_f32 v53, v53\n"
"v_mfma_f32_16x16x32_bf16 a[112:115], a[184:187], v[160:163], a[112:115]\n"
"s_waitcnt lgkmcnt(4)\n"
"ds_read_b128 a[152:155], v218 offset:50176\n"
"ds_read_b128 a[156:159], v219 offset:50176\n"
"L_i15%=:\n"
"v_mfma_f32_16x16x32_bf16 v[0:3], a[128:131], v[64:67], v[192:195]\n"
"v_exp_f32 v54, v54\n"
"v_mfma_f32_16x16x32_bf16 v[0:3], a[132:135], v[68:71], v[0:3]\n"
"s_waitcnt lgkmcnt(4)\n"
"v_exp_f32 v55, v55\n"
"v_mfma_f32_16x16x32_bf16 v[0:3], a[136:139], v[72:75], v[0:3]\n"
"v_cvt_pk_bf16_f32 v142, v52, v53\n"
"v_cvt_pk_bf16_f32 v143, v54, v55\n"
"v_mfma_f32_16x16x32_bf16 v[0:3], a[140:143], v[76:79], v[0:3]\n"
"v_exp_f32 v48, v48\n"
"v_mfma_f32_16x16x32_bf16 v[4:7], a[140:143], v[92:95], v[196:199]\n"
"s_add_u32 m0, s60, 0\n"
"v_exp_f32 v49, v49\n"
"v_mfma_f32_16x16x32_bf16 v[4:7], a[136:139], v[88:91], v[4:7]\n"
"buffer_load_dwordx4 v221, s[92:95], s58 offen lds\n"
"v_mfma_f32_16x16x32_bf16 v[4:7], a[132:135], v[84:87], v[4:7]\n"
"v_exp_f32 v50, v50\n"
"v_mfma_f32_16x16x32_bf16 v[4:7], a[128:131], v[80:83], v[4:7]\n"
"v_exp_f32 v51, v51\n"
"v_mfma_f32_16x16x32_bf16 v[8:11], a[128:131], v[96:99], v[200:203]\n"
"v_cvt_pk_bf16_f32 v134, v48, v49\n"
"v_cvt_pk_bf16_f32 v135, v50, v51\n"
"v_mfma_f32_16x16x32_bf16 v[8:11], a[132:135], v[100:103], v[8:11]\n"
"v_exp_f32 v0, v0\n"
"v_mfma_f32_16x16x32_bf16 v[8:11], a[136:139], v[104:107], v[8:11]\n"
"v_exp_f32 v1, v1\n"
"v_mfma_f32_16x16x32_bf16 v[8:11], a[140:143], v[108:111], v[8:11]\n"
"v_exp_f32 v2, v2\n"
"v_mfma_f32_16x16x32_bf16 v[12:15], a[140:143], v[124:127], v[204:207]\n"
"buffer_load_dwordx4 v225, s[92:95], s58 offen offset:1024 lds\n"
"v_mfma_f32_16x16x32_bf16 v[12:15], a[136:139], v[120:123], v[12:15]\n"
"v_exp_f32 v3, v3\n"
"v_mfma_f32_16x16x32_bf16 v[12:15], a[132:135], v[116:119], v[12:15]\n"
"v_exp_f32 v4, v4\n"
"v_mfma_f32_16x16x32_bf16 v[12:15], a[128:131], v[112:115], v[12:15]\n"
"s_waitcnt lgkmcnt(0)\n"
"ds_read_b128 a[128:131], v216 offset:57344\n"
"v_cvt_pk_bf16_f32 v160, v0, v1\n"
"v_mfma_f32_16x16x32_bf16 v[28:31], a[144:147], v[112:115], v[204:207]\n"
"buffer_load_dwordx4 v226, s[92:95], s58 offen offset:2048 lds\n"
"v_mfma_f32_16x16x32_bf16 v[28:31], a[148:151], v[116:119], v[28:31]\n"
"ds_read_b128 a[132:135], v217 offset:57344\n"
"v_cvt_pk_bf16_f32 v161, v2, v3\n"
"v_mfma_f32_16x16x32_bf16 v[28:31], a[152:155], v[120:123], v[28:31]\n"
"v_exp_f32 v5, v5\n"
"v_mfma_f32_16x16x32_bf16 v[28:31], a[156:159], v[124:127], v[28:31]\n"
"ds_read_b128 a[136:139], v218 offset:57344\n"
"v_cvt_pk_bf16_f32 v168, v4, v5\n"
"v_mfma_f32_16x16x32_bf16 v[24:27], a[156:159], v[108:111], v[200:203]\n"
"v_exp_f32 v6, v6\n"
"v_mfma_f32_16x16x32_bf16 v[24:27], a[152:155], v[104:107], v[24:27]\n"
"buffer_load_dwordx4 v227, s[92:95], s58 offen offset:3072 lds\n"
"v_mfma_f32_16x16x32_bf16 v[24:27], a[148:151], v[100:103], v[24:27]\n"
"v_exp_f32 v7, v7\n"
"v_mfma_f32_16x16x32_bf16 v[24:27], a[144:147], v[96:99], v[24:27]\n"
"ds_read_b128 a[140:143], v219 offset:57344\n"
"v_cvt_pk_bf16_f32 v169, v6, v7\n"
"v_mfma_f32_16x16x32_bf16 v[20:23], a[144:147], v[80:83], v[196:199]\n"
"v_exp_f32 v8, v8\n"
"v_mfma_f32_16x16x32_bf16 v[20:23], a[148:151], v[84:87], v[20:23]\n"
"v_exp_f32 v9, v9\n"
"v_mfma_f32_16x16x32_bf16 v[20:23], a[152:155], v[88:91], v[20:23]\n"
"v_exp_f32 v10, v10\n"
"v_mfma_f32_16x16x32_bf16 v[20:23], a[156:159], v[92:95], v[20:23]\n"
"s_add_u32 m0, s60, 4096\n"
"v_exp_f32 v11, v11\n"
"v_mfma_f32_16x16x32_bf16 v[16:19], a[156:159], v[76:79], v[192:195]\n"
"buffer_load_dwordx4 v228, s[92:95], s58 offen lds\n"
"v_mfma_f32_16x16x32_bf16 v[16:19], a[152:155], v[72:75], v[16:19]\n"
"v_exp_f32 v12, v12\n"
"v_mfma_f32_16x16x32_bf16 v[16:19], a[148:151], v[68:71], v[16:19]\n"
"v_exp_f32 v13, v13\n"
"v_mfma_f32_16x16x32_bf16 v[16:19], a[144:147], v[64:67], v[16:19]\n"
"s_waitcnt lgkmcnt(2)\n"
"ds_read_b128 a[144:147], v216 offset:58368\n"
"v_cvt_pk_bf16_f32 v176, v8, v9\n"
"v_mfma_f32_16x16x32_bf16 v[32:35], a[128:131], v[64:67], v[192:195]\n"
"v_exp_f32 v14, v14\n"
"v_mfma_f32_16x16x32_bf16 v[32:35], a[132:135], v[68:71], v[32:35]\n"
"s_waitcnt lgkmcnt(1)\n"
"ds_read_b128 a[148:151], v217 offset:58368\n"
"v_cvt_pk_bf16_f32 v177, v10, v11\n"
"v_mfma_f32_16x16x32_bf16 v[32:35], a[136:139], v[72:75], v[32:35]\n"
"v_exp_f32 v15, v15\n"
"v_mfma_f32_16x16x32_bf16 v[32:35], a[140:143], v[76:79], v[32:35]\n"
"ds_read_b128 a[152:155], v218 offset:58368\n"
"v_cvt_pk_bf16_f32 v184, v12, v13\n"
"v_mfma_f32_16x16x32_bf16 v[36:39], a[140:143], v[92:95], v[196:199]\n"
"v_exp_f32 v28, v28\n"
"v_mfma_f32_16x16x32_bf16 v[36:39], a[136:139], v[88:91], v[36:39]\n"
"buffer_load_dwordx4 v229, s[92:95], s58 offen offset:1024 lds\n"
"v_mfma_f32_16x16x32_bf16 v[36:39], a[132:135], v[84:87], v[36:39]\n"
"v_exp_f32 v29, v29\n"
"v_mfma_f32_16x16x32_bf16 v[36:39], a[128:131], v[80:83], v[36:39]\n"
"ds_read_b128 a[156:159], v219 offset:58368\n"
"v_cvt_pk_bf16_f32 v185, v14, v15\n"
"v_mfma_f32_16x16x32_bf16 v[40:43], a[128:131], v[96:99], v[200:203]\n"
"v_exp_f32 v30, v30\n"
"v_mfma_f32_16x16x32_bf16 v[40:43], a[132:135], v[100:103], v[40:43]\n"
"ds_read_b64_tr_b16 a[160:161], v220 offset:32896\n"
"v_cvt_pk_bf16_f32 v186, v28, v29\n"
"v_mfma_f32_16x16x32_bf16 v[40:43], a[136:139], v[104:107], v[40:43]\n"
"buffer_load_dwordx4 v230, s[92:95], s58 offen offset:2048 lds\n"
"v_mfma_f32_16x16x32_bf16 v[40:43], a[140:143], v[108:111], v[40:43]\n"
"ds_read_b64_tr_b16 a[162:163], v220 offset:33920\n"
"v_mfma_f32_16x16x32_bf16 v[44:47], a[140:143], v[124:127], v[204:207]\n"
"v_exp_f32 v31, v31\n"
"v_mfma_f32_16x16x32_bf16 v[44:47], a[136:139], v[120:123], v[44:47]\n"
"ds_read_b64_tr_b16 a[164:165], v220 offset:41120\n"
"ds_read_b64_tr_b16 a[166:167], v220 offset:42144\n"
"v_mfma_f32_16x16x32_bf16 v[44:47], a[132:135], v[116:119], v[44:47]\n"
"v_exp_f32 v24, v24\n"
"v_mfma_f32_16x16x32_bf16 v[44:47], a[128:131], v[112:115], v[44:47]\n"
"s_waitcnt lgkmcnt(6)\n"
"v_exp_f32 v25, v25\n"
"v_mfma_f32_16x16x32_bf16 v[60:63], a[144:147], v[112:115], v[204:207]\n"
"buffer_load_dwordx4 v231, s[92:95], s58 offen offset:3072 lds\n"
"v_mfma_f32_16x16x32_bf16 v[60:63], a[148:151], v[116:119], v[60:63]\n"
"s_waitcnt lgkmcnt(4)\n"
"ds_read_b64_tr_b16 a[168:169], v220 offset:32904\n"
"v_cvt_pk_bf16_f32 v187, v30, v31\n"
"v_mfma_f32_16x16x32_bf16 v[60:63], a[152:155], v[120:123], v[60:63]\n"
"v_exp_f32 v26, v26\n"
"v_mfma_f32_16x16x32_bf16 v[60:63], a[156:159], v[124:127], v[60:63]\n"
"ds_read_b64_tr_b16 a[170:171], v220 offset:33928\n"
"v_cvt_pk_bf16_f32 v178, v24, v25\n"
"v_mfma_f32_16x16x32_bf16 v[56:59], a[156:159], v[108:111], v[200:203]\n"
"v_exp_f32 v27, v27\n"
"v_mfma_f32_16x16x32_bf16 v[56:59], a[152:155], v[104:107], v[56:59]\n"
"ds_read_b64_tr_b16 a[172:173], v220 offset:41128\n"
"ds_read_b64_tr_b16 a[174:175], v220 offset:42152\n"
"v_mfma_f32_16x16x32_bf16 v[56:59], a[148:151], v[100:103], v[56:59]\n"
"s_add_u32 m0, s60, 8192\n"
"v_exp_f32 v20, v20\n"
"v_mfma_f32_16x16x32_bf16 v[56:59], a[144:147], v[96:99], v[56:59]\n"
"buffer_load_dwordx4 v235, s[92:95], s58 offen lds\n"
"v_mfma_f32_16x16x32_bf16 v[52:55], a[144:147], v[80:83], v[196:199]\n"
"v_exp_f32 v21, v21\n"
"v_mfma_f32_16x16x32_bf16 v[52:55], a[148:151], v[84:87], v[52:55]\n"
"v_exp_f32 v22, v22\n"
"v_mfma_f32_16x16x32_bf16 v[52:55], a[152:155], v[88:91], v[52:55]\n"
"v_exp_f32 v23, v23\n"
"v_mfma_f32_16x16x32_bf16 v[52:55], a[156:159], v[92:95], v[52:55]\n"
"ds_read_b64_tr_b16 a[176:177], v220 offset:33152\n"
"v_cvt_pk_bf16_f32 v179, v26, v27\n"
"v_mfma_f32_16x16x32_bf16 v[48:51], a[156:159], v[76:79], v[192:195]\n"
"v_exp_f32 v16, v16\n"
"L_chk1%=:\n"
"s_waitcnt vmcnt(11)\n"
"v_cmp_eq_u32 vcc, s78, v251\n"
"s_nop 3\n"
"s_mov_b32 s72, vcc_lo\n"
"s_and_b32 s72, s72, 65535\n"
"s_cmp_eq_u32 s72, 65535\n"
"s_cbranch_scc0 L_rt1%=\n"
"L_ok1%=:\n"
"v_mfma_f32_16x16x32_bf16 v[48:51], a[152:155], v[72:75], v[48:51]\n"
"ds_read_b64_tr_b16 a[178:179], v220 offset:34176\n"
"v_lshrrev_b32 v247, 2, v232\n"
"v_mfma_f32_16x16x32_bf16 v[48:51], a[148:151], v[68:71], v[48:51]\n"
"buffer_load_dword v247, v247, s[96:99], 0 offen sc1\n"
"v_mfma_f32_16x16x32_bf16 v[48:51], a[144:147], v[64:67], v[48:51]\n"
"ds_read_b64_tr_b16 a[180:181], v220 offset:41376\n"
"ds_read_b64_tr_b16 a[182:183], v220 offset:42400\n"
"v_smfmac_f32_16x16x64_bf16 a[228:231], a[200:203], v[128:135], v208\n"
"v_cvt_pk_bf16_f32 v170, v20, v21\n"
"v_smfmac_f32_16x16x64_bf16 a[232:235], a[200:203], v[136:143], v208\n"
"v_cvt_pk_bf16_f32 v171, v22, v23\n"
"v_smfmac_f32_16x16x64_bf16 a[236:239], a[200:203], v[144:151], v208\n"
"v_smfmac_f32_16x16x64_bf16 a[240:243], a[200:203], v[152:159], v208\n"
"s_waitcnt lgkmcnt(8)\n"
"ds_read_b64_tr_b16 a[184:185], v220 offset:33160\n"
"v_mfma_f32_16x16x32_bf16 a[0:3], a[160:163], v[128:131], a[0:3]\n"
"v_exp_f32 v17, v17\n"
"v_mfma_f32_16x16x32_bf16 a[0:3], a[164:167], v[132:135], a[0:3]\n"
"ds_read_b64_tr_b16 a[186:187], v220 offset:34184\n"
"v_cvt_pk_bf16_f32 v162, v16, v17\n"
"v_mfma_f32_16x16x32_bf16 a[4:7], a[164:167], v[140:143], a[4:7]\n"
"s_add_u32 s66, s77, 0\n"
"v_exp_f32 v18, v18\n"
"v_mfma_f32_16x16x32_bf16 a[4:7], a[160:163], v[136:139], a[4:7]\n"
"buffer_load_dwordx4 a[128:131], v232, s[52:55], s66 offen sc1\n"
"v_mfma_f32_16x16x32_bf16 a[8:11], a[160:163], v[144:147], a[8:11]\n"
"v_exp_f32 v19, v19\n"
"v_mfma_f32_16x16x32_bf16 a[8:11], a[164:167], v[148:151], a[8:11]\n"
"ds_read_b64_tr_b16 a[188:189], v220 offset:41384\n"
"ds_read_b64_tr_b16 a[190:191], v220 offset:42408\n"
"v_mfma_f32_16x16x32_bf16 a[12:15], a[164:167], v[156:159], a[12:15]\n"
"v_exp_f32 v32, v32\n"
"v_mfma_f32_16x16x32_bf16 a[12:15], a[160:163], v[152:155], a[12:15]\n"
"s_waitcnt lgkmcnt(8)\n"
"v_exp_f32 v33, v33\n"
"v_mfma_f32_16x16x32_bf16 a[28:31], a[168:171], v[152:155], a[28:31]\n"
"v_exp_f32 v34, v34\n"
"v_mfma_f32_16x16x32_bf16 a[28:31], a[172:175], v[156:159], a[28:31]\n"
"ds_read_b64_tr_b16 a[160:161], v220 offset:33408\n"
"v_cvt_pk_bf16_f32 v163, v18, v19\n"
"v_mfma_f32_16x16x32_bf16 a[24:27], a[172:175], v[148:151], a[24:27]\n"
"buffer_load_dwordx4 a[132:135], v232, s[52:55], s66 offen offset:1024 sc1\n"
"v_mfma_f32_16x16x32_bf16 a[24:27], a[168:171], v[144:147], a[24:27]\n"
"ds_read_b64_tr_b16 a[162:163], v220 offset:34432\n"
"v_cvt_pk_bf16_f32 v164, v32, v33\n"
"v_mfma_f32_16x16x32_bf16 a[20:23], a[168:171], v[136:139], a[20:23]\n"
"v_exp_f32 v35, v35\n"
"v_mfma_f32_16x16x32_bf16 a[20:23], a[172:175], v[140:143], a[20:23]\n"
"ds_read_b64_tr_b16 a[164:165], v220 offset:41632\n"
"ds_read_b64_tr_b16 a[166:167], v220 offset:42656\n"
"v_mfma_f32_16x16x32_bf16 a[16:19], a[172:175], v[132:135], a[16:19]\n"
"v_exp_f32 v36, v36\n"
"v_mfma_f32_16x16x32_bf16 a[16:19], a[168:171], v[128:131], a[16:19]\n"
"s_waitcnt lgkmcnt(8)\n"
"v_exp_f32 v37, v37\n"
"v_mfma_f32_16x16x32_bf16 a[32:35], a[176:179], v[128:131], a[32:35]\n"
"v_exp_f32 v38, v38\n"
"v_mfma_f32_16x16x32_bf16 a[32:35], a[180:183], v[132:135], a[32:35]\n"
"ds_read_b64_tr_b16 a[168:169], v220 offset:33416\n"
"v_cvt_pk_bf16_f32 v165, v34, v35\n"
"v_mfma_f32_16x16x32_bf16 a[36:39], a[180:183], v[140:143], a[36:39]\n"
"v_exp_f32 v39, v39\n"
"v_mfma_f32_16x16x32_bf16 a[36:39], a[176:179], v[136:139], a[36:39]\n"
"buffer_load_dwordx4 a[136:139], v232, s[52:55], s66 offen offset:2048 sc1\n"
"v_mfma_f32_16x16x32_bf16 a[40:43], a[176:179], v[144:147], a[40:43]\n"
"v_exp_f32 v40, v40\n"
"v_mfma_f32_16x16x32_bf16 a[40:43], a[180:183], v[148:151], a[40:43]\n"
"ds_read_b64_tr_b16 a[170:171], v220 offset:34440\n"
"v_cvt_pk_bf16_f32 v172, v36, v37\n"
"v_mfma_f32_16x16x32_bf16 a[44:47], a[180:183], v[156:159], a[44:47]\n"
"v_exp_f32 v41, v41\n"
"v_mfma_f32_16x16x32_bf16 a[44:47], a[176:179], v[152:155], a[44:47]\n"
"s_waitcnt lgkmcnt(6)\n"
"ds_read_b64_tr_b16 a[172:173], v220 offset:41640\n"
"ds_read_b64_tr_b16 a[174:175], v220 offset:42664\n"
"v_mfma_f32_16x16x32_bf16 a[60:63], a[184:187], v[152:155], a[60:63]\n"
"v_exp_f32 v42, v42\n"
"v_mfma_f32_16x16x32_bf16 a[60:63], a[188:191], v[156:159], a[60:63]\n"
"v_exp_f32 v43, v43\n"
"v_mfma_f32_16x16x32_bf16 a[56:59], a[188:191], v[148:151], a[56:59]\n"
"buffer_load_dwordx4 a[140:143], v232, s[52:55], s66 offen offset:3072 sc1\n"
"v_mfma_f32_16x16x32_bf16 a[56:59], a[184:187], v[144:147], a[56:59]\n"
"ds_read_b64_tr_b16 a[176:177], v220 offset:33664\n"
"v_cvt_pk_bf16_f32 v173, v38, v39\n"
"v_mfma_f32_16x16x32_bf16 a[52:55], a[184:187], v[136:139], a[52:55]\n"
"v_exp_f32 v44, v44\n"
"v_mfma_f32_16x16x32_bf16 a[52:55], a[188:191], v[140:143], a[52:55]\n"
"ds_read_b64_tr_b16 a[178:179], v220 offset:34688\n"
"v_cvt_pk_bf16_f32 v180, v40, v41\n"
"v_mfma_f32_16x16x32_bf16 a[48:51], a[188:191], v[132:135], a[48:51]\n"
"v_exp_f32 v45, v45\n"
"v_mfma_f32_16x16x32_bf16 a[48:51], a[184:187], v[128:131], a[48:51]\n"
"s_waitcnt lgkmcnt(6)\n"
"ds_read_b64_tr_b16 a[180:181], v220 offset:41888\n"
"ds_read_b64_tr_b16 a[182:183], v220 offset:42912\n"
"v_mfma_f32_16x16x32_bf16 a[64:67], a[160:163], v[128:131], a[64:67]\n"
"v_exp_f32 v46, v46\n"
"v_mfma_f32_16x16x32_bf16 a[64:67], a[164:167], v[132:135], a[64:67]\n"
"v_exp_f32 v47, v47\n"
"v_mfma_f32_16x16x32_bf16 a[68:71], a[164:167], v[140:143], a[68:71]\n"
"s_add_u32 s66, s77, 4096\n"
"v_exp_f32 v60, v60\n"
"v_mfma_f32_16x16x32_bf16 a[68:71], a[160:163], v[136:139], a[68:71]\n"
"buffer_load_dwordx4 a[144:147], v232, s[52:55], s66 offen sc1\n"
"v_mfma_f32_16x16x32_bf16 a[72:75], a[160:163], v[144:147], a[72:75]\n"
"v_exp_f32 v61, v61\n"
"v_mfma_f32_16x16x32_bf16 a[72:75], a[164:167], v[148:151], a[72:75]\n"
"ds_read_b64_tr_b16 a[184:185], v220 offset:33672\n"
"v_cvt_pk_bf16_f32 v181, v42, v43\n"
"v_mfma_f32_16x16x32_bf16 a[76:79], a[164:167], v[156:159], a[76:79]\n"
"v_cvt_pk_bf16_f32 v188, v44, v45\n"
"v_cvt_pk_bf16_f32 v189, v46, v47\n"
"v_mfma_f32_16x16x32_bf16 a[76:79], a[160:163], v[152:155], a[76:79]\n"
"s_waitcnt lgkmcnt(5)\n"
"ds_read_b64_tr_b16 a[186:187], v220 offset:34696\n"
"v_cvt_pk_bf16_f32 v190, v60, v61\n"
"v_mfma_f32_16x16x32_bf16 a[92:95], a[168:171], v[152:155], a[92:95]\n"
"v_exp_f32 v62, v62\n"
"v_mfma_f32_16x16x32_bf16 a[92:95], a[172:175], v[156:159], a[92:95]\n"
"ds_read_b64_tr_b16 a[188:189], v220 offset:41896\n"
"ds_read_b64_tr_b16 a[190:191], v220 offset:42920\n"
"v_mfma_f32_16x16x32_bf16 a[88:91], a[172:175], v[148:151], a[88:91]\n"
"buffer_load_dwordx4 a[148:151], v232, s[52:55], s66 offen offset:1024 sc1\n"
"v_mfma_f32_16x16x32_bf16 a[88:91], a[168:171], v[144:147], a[88:91]\n"
"v_exp_f32 v63, v63\n"
"v_mfma_f32_16x16x32_bf16 a[84:87], a[168:171], v[136:139], a[84:87]\n"
"v_exp_f32 v56, v56\n"
"v_mfma_f32_16x16x32_bf16 a[84:87], a[172:175], v[140:143], a[84:87]\n"
"v_exp_f32 v57, v57\n"
"v_mfma_f32_16x16x32_bf16 a[80:83], a[172:175], v[132:135], a[80:83]\n"
"v_exp_f32 v58, v58\n"
"v_mfma_f32_16x16x32_bf16 a[80:83], a[168:171], v[128:131], a[80:83]\n"
"s_waitcnt lgkmcnt(4)\n"
"v_exp_f32 v59, v59\n"
"v_mfma_f32_16x16x32_bf16 a[96:99], a[176:179], v[128:131], a[96:99]\n"
"s_waitcnt vmcnt(19) lgkmcnt(0)\n"
"v_exp_f32 v52, v52\n"
"v_mfma_f32_16x16x32_bf16 a[96:99], a[180:183], v[132:135], a[96:99]\n"
"s_barrier\n"
"v_mfma_f32_16x16x32_bf16 a[100:103], a[180:183], v[140:143], a[100:103]\n"
"ds_read_b64_tr_b16 a[160:161], v220 offset:49344\n"
"v_cvt_pk_bf16_f32 v191, v62, v63\n"
"v_mfma_f32_16x16x32_bf16 a[100:103], a[176:179], v[136:139], a[100:103]\n"
"ds_read_b64_tr_b16 a[162:163], v220 offset:50368\n"
"v_cvt_pk_bf16_f32 v182, v56, v57\n"
"v_mfma_f32_16x16x32_bf16 a[104:107], a[176:179], v[144:147], a[104:107]\n"
"v_exp_f32 v53, v53\n"
"v_mfma_f32_16x16x32_bf16 a[104:107], a[180:183], v[148:151], a[104:107]\n"
"buffer_load_dwordx4 a[152:155], v232, s[52:55], s66 offen offset:2048 sc1\n"
"v_mfma_f32_16x16x32_bf16 a[108:111], a[180:183], v[156:159], a[108:111]\n"
"v_exp_f32 v54, v54\n"
"v_mfma_f32_16x16x32_bf16 a[108:111], a[176:179], v[152:155], a[108:111]\n"
"ds_read_b64_tr_b16 a[164:165], v220 offset:57568\n"
"v_cvt_pk_bf16_f32 v183, v58, v59\n"
"v_mfma_f32_16x16x32_bf16 a[124:127], a[184:187], v[152:155], a[124:127]\n"
"v_exp_f32 v55, v55\n"
"v_mfma_f32_16x16x32_bf16 a[124:127], a[188:191], v[156:159], a[124:127]\n"
"ds_read_b64_tr_b16 a[166:167], v220 offset:58592\n"
"v_cvt_pk_bf16_f32 v174, v52, v53\n"
"v_mfma_f32_16x16x32_bf16 a[120:123], a[188:191], v[148:151], a[120:123]\n"
"v_exp_f32 v48, v48\n"
"v_mfma_f32_16x16x32_bf16 a[120:123], a[184:187], v[144:147], a[120:123]\n"
"ds_read_b64_tr_b16 a[168:169], v220 offset:49352\n"
"v_cvt_pk_bf16_f32 v175, v54, v55\n"
"v_mfma_f32_16x16x32_bf16 a[116:119], a[184:187], v[136:139], a[116:119]\n"
"buffer_load_dwordx4 a[156:159], v232, s[52:55], s66 offen offset:3072 sc1\n"
"v_mfma_f32_16x16x32_bf16 a[116:119], a[188:191], v[140:143], a[116:119]\n"
"ds_read_b64_tr_b16 a[170:171], v220 offset:50376\n"
"v_mfma_f32_16x16x32_bf16 a[112:115], a[188:191], v[132:135], a[112:115]\n"
"v_exp_f32 v49, v49\n"
"v_mfma_f32_16x16x32_bf16 a[112:115], a[184:187], v[128:131], a[112:115]\n"
"ds_read_b64_tr_b16 a[172:173], v220 offset:57576\n"
"ds_read_b64_tr_b16 a[174:175], v220 offset:58600\n"
"v_exp_f32 v50, v50\n"
"v_cvt_pk_bf16_f32 v166, v48, v49\n"
"v_exp_f32 v51, v51\n"
"s_nop 0\n"
"v_cvt_pk_bf16_f32 v167, v50, v51\n"
"s_nop 1\n"
"v_smfmac_f32_16x16x64_bf16 a[228:231], a[200:203], v[160:167], v208\n"
"ds_read_b64_tr_b16 a[176:177], v220 offset:49600\n"
"v_smfmac_f32_16x16x64_bf16 a[232:235], a[200:203], v[168:175], v208\n"
"ds_read_b64_tr_b16 a[178:179], v220 offset:50624\n"
"v_smfmac_f32_16x16x64_bf16 a[236:239], a[200:203], v[176:183], v208\n"
"ds_read_b64_tr_b16 a[180:181], v220 offset:57824\n"
"v_smfmac_f32_16x16x64_bf16 a[240:243], a[200:203], v[184:191], v208\n"
"s_waitcnt lgkmcnt(7)\n"
"ds_read_b64_tr_b16 a[182:183], v220 offset:58848\n"
"v_mfma_f32_16x16x32_bf16 a[0:3], a[160:163], v[160:163], a[0:3]\n"
"ds_read_b64_tr_b16 a[184:185], v220 offset:49608\n"
"v_mfma_f32_16x16x32_bf16 a[0:3], a[164:167], v[164:167], a[0:3]\n"
"v_mfma_f32_16x16x32_bf16 a[4:7], a[164:167], v[172:175], a[4:7]\n"
"s_mov_b32 s72, 2\n"
"s_mul_i32 s58, s72, 147456\n"
"s_add_u32 m0, s60, 36864\n"
"ds_read_b64_tr_b16 a[186:187], v220 offset:50632\n"
"v_mfma_f32_16x16x32_bf16 a[4:7], a[160:163], v[168:171], a[4:7]\n"
"buffer_load_dwordx4 v221, s[92:95], s58 offen lds\n"
"v_mfma_f32_16x16x32_bf16 a[8:11], a[160:163], v[176:179], a[8:11]\n"
"ds_read_b64_tr_b16 a[188:189], v220 offset:57832\n"
"v_accvgpr_read_b32 v212, a228\n"
"v_mfma_f32_16x16x32_bf16 a[8:11], a[164:167], v[180:183], a[8:11]\n"
"v_accvgpr_read_b32 v213, a232\n"
"v_accvgpr_read_b32 v214, a236\n"
"v_mfma_f32_16x16x32_bf16 a[12:15], a[164:167], v[188:191], a[12:15]\n"
"ds_read_b64_tr_b16 a[190:191], v220 offset:58856\n"
"v_accvgpr_read_b32 v215, a240\n"
"v_mfma_f32_16x16x32_bf16 a[12:15], a[160:163], v[184:187], a[12:15]\n"
"s_waitcnt lgkmcnt(8)\n"
"buffer_load_dwordx4 v225, s[92:95], s58 offen offset:1024 lds\n"
"v_mfma_f32_16x16x32_bf16 a[28:31], a[168:171], v[184:187], a[28:31]\n"
"ds_read_b64_tr_b16 a[160:161], v220 offset:49856\n"
"v_accvgpr_read_b32 v240, a229\n"
"v_mfma_f32_16x16x32_bf16 a[28:31], a[172:175], v[188:191], a[28:31]\n"
"v_accvgpr_read_b32 v241, a233\n"
"v_accvgpr_read_b32 v242, a237\n"
"v_mfma_f32_16x16x32_bf16 a[24:27], a[172:175], v[180:183], a[24:27]\n"
"ds_read_b64_tr_b16 a[162:163], v220 offset:50880\n"
"v_accvgpr_read_b32 v243, a241\n"
"v_mfma_f32_16x16x32_bf16 a[24:27], a[168:171], v[176:179], a[24:27]\n"
"buffer_load_dwordx4 v226, s[92:95], s58 offen offset:2048 lds\n"
"v_mfma_f32_16x16x32_bf16 a[20:23], a[168:171], v[168:171], a[20:23]\n"
"ds_read_b64_tr_b16 a[164:165], v220 offset:58080\n"
"v_add_f32 v212, v212, v240\n"
"v_mfma_f32_16x16x32_bf16 a[20:23], a[172:175], v[172:175], a[20:23]\n"
"v_add_f32 v213, v213, v241\n"
"v_add_f32 v214, v214, v242\n"
"v_mfma_f32_16x16x32_bf16 a[16:19], a[172:175], v[164:167], a[16:19]\n"
"ds_read_b64_tr_b16 a[166:167], v220 offset:59104\n"
"v_add_f32 v215, v215, v243\n"
"v_mfma_f32_16x16x32_bf16 a[16:19], a[168:171], v[160:163], a[16:19]\n"
"s_waitcnt lgkmcnt(8)\n"
"buffer_load_dwordx4 v227, s[92:95], s58 offen offset:3072 lds\n"
"v_mfma_f32_16x16x32_bf16 a[32:35], a[176:179], v[160:163], a[32:35]\n"
"ds_read_b64_tr_b16 a[168:169], v220 offset:49864\n"
"v_mov_b32 v240, v212\n"
"v_mfma_f32_16x16x32_bf16 a[32:35], a[180:183], v[164:167], a[32:35]\n"
"v_mov_b32 v241, v213\n"
"v_mov_b32 v242, v214\n"
"v_mfma_f32_16x16x32_bf16 a[36:39], a[180:183], v[172:175], a[36:39]\n"
"s_add_u32 m0, s60, 40960\n"
"ds_read_b64_tr_b16 a[170:171], v220 offset:50888\n"
"v_mov_b32 v243, v215\n"
"v_mfma_f32_16x16x32_bf16 a[36:39], a[176:179], v[168:171], a[36:39]\n"
"buffer_load_dwordx4 v228, s[92:95], s58 offen lds\n"
"v_mfma_f32_16x16x32_bf16 a[40:43], a[176:179], v[176:179], a[40:43]\n"
"ds_read_b64_tr_b16 a[172:173], v220 offset:58088\n"
"v_permlane32_swap_b32 v212, v240\n"
"v_mfma_f32_16x16x32_bf16 a[40:43], a[180:183], v[180:183], a[40:43]\n"
"v_permlane32_swap_b32 v213, v241\n"
"v_permlane32_swap_b32 v214, v242\n"
"v_mfma_f32_16x16x32_bf16 a[44:47], a[180:183], v[188:191], a[44:47]\n"
"ds_read_b64_tr_b16 a[174:175], v220 offset:59112\n"
"v_permlane32_swap_b32 v215, v243\n"
"v_mfma_f32_16x16x32_bf16 a[44:47], a[176:179], v[184:187], a[44:47]\n"
"s_waitcnt lgkmcnt(8)\n"
"buffer_load_dwordx4 v229, s[92:95], s58 offen offset:1024 lds\n"
"v_mfma_f32_16x16x32_bf16 a[60:63], a[184:187], v[184:187], a[60:63]\n"
"ds_read_b64_tr_b16 a[176:177], v220 offset:50112\n"
"v_mov_b32 v240, v212\n"
"v_mfma_f32_16x16x32_bf16 a[60:63], a[188:191], v[188:191], a[60:63]\n"
"v_mov_b32 v241, v213\n"
"v_mov_b32 v242, v214\n"
"v_mfma_f32_16x16x32_bf16 a[56:59], a[188:191], v[180:183], a[56:59]\n"
"ds_read_b64_tr_b16 a[178:179], v220 offset:51136\n"
"v_mov_b32 v243, v215\n"
"v_mfma_f32_16x16x32_bf16 a[56:59], a[184:187], v[176:179], a[56:59]\n"
"buffer_load_dwordx4 v230, s[92:95], s58 offen offset:2048 lds\n"
"v_mfma_f32_16x16x32_bf16 a[52:55], a[184:187], v[168:171], a[52:55]\n"
"ds_read_b64_tr_b16 a[180:181], v220 offset:58336\n"
"v_permlane16_swap_b32 v212, v240\n"
"v_mfma_f32_16x16x32_bf16 a[52:55], a[188:191], v[172:175], a[52:55]\n"
"v_permlane16_swap_b32 v213, v241\n"
"v_permlane16_swap_b32 v214, v242\n"
"v_mfma_f32_16x16x32_bf16 a[48:51], a[188:191], v[164:167], a[48:51]\n"
"ds_read_b64_tr_b16 a[182:183], v220 offset:59360\n"
"v_permlane16_swap_b32 v215, v243\n"
"v_add_f32 v252, v212, v213\n"
"v_add_f32 v253, v214, v215\n"
"v_add_f32 v252, v252, v253\n"
"v_cmp_nge_f32 vcc, 0x5d800000, v252\n"
"v_mfma_f32_16x16x32_bf16 a[48:51], a[184:187], v[160:163], a[48:51]\n"
"s_waitcnt lgkmcnt(8)\n"
"buffer_load_dwordx4 v231, s[92:95], s58 offen offset:3072 lds\n"
"v_mfma_f32_16x16x32_bf16 a[64:67], a[160:163], v[160:163], a[64:67]\n"
"s_cmp_lg_u64 vcc, 0\n"
"s_cselect_b32 s75, 1, 0\n"
"ds_read_b64_tr_b16 a[184:185], v220 offset:50120\n"
"v_mfma_f32_16x16x32_bf16 a[64:67], a[164:167], v[164:167], a[64:67]\n"
"v_mov_b32 v252, s75\n"
"v_mfma_f32_16x16x32_bf16 a[68:71], a[164:167], v[172:175], a[68:71]\n"
"s_add_u32 m0, s60, 45056\n"
"ds_read_b64_tr_b16 a[186:187], v220 offset:51144\n"
"v_mfma_f32_16x16x32_bf16 a[68:71], a[160:163], v[168:171], a[68:71]\n"
"buffer_load_dwordx4 v235, s[92:95], s58 offen lds\n"
"v_mfma_f32_16x16x32_bf16 a[72:75], a[160:163], v[176:179], a[72:75]\n"
"ds_read_b64_tr_b16 a[188:189], v220 offset:58344\n"
"v_mfma_f32_16x16x32_bf16 a[72:75], a[164:167], v[180:183], a[72:75]\n"
"ds_write_b32 v233, v252\n"
"v_mfma_f32_16x16x32_bf16 a[76:79], a[164:167], v[188:191], a[76:79]\n"
"s_mov_b32 s72, 4\n"
"s_mul_i32 s58, s72, 147456\n"
"s_add_u32 m0, s60, 73728\n"
"ds_read_b64_tr_b16 a[190:191], v220 offset:59368\n"
"v_mfma_f32_16x16x32_bf16 a[76:79], a[160:163], v[184:187], a[76:79]\n"
"s_waitcnt lgkmcnt(9)\n"
"buffer_load_dwordx4 v221, s[92:95], s58 offen lds\n"
"v_mfma_f32_16x16x32_bf16 a[92:95], a[168:171], v[184:187], a[92:95]\n"
"v_rcp_f32 v212, v212\n"
"v_mfma_f32_16x16x32_bf16 a[92:95], a[172:175], v[188:191], a[92:95]\n"
"v_rcp_f32 v213, v213\n"
"v_mfma_f32_16x16x32_bf16 a[88:91], a[172:175], v[180:183], a[88:91]\n"
"v_rcp_f32 v214, v214\n"
"v_mfma_f32_16x16x32_bf16 a[88:91], a[168:171], v[176:179], a[88:91]\n"
"s_waitcnt lgkmcnt(0)\n"
"buffer_load_dwordx4 v225, s[92:95], s58 offen offset:1024 lds\n"
"v_mfma_f32_16x16x32_bf16 a[84:87], a[168:171], v[168:171], a[84:87]\n"
"s_barrier\n"
"v_mfma_f32_16x16x32_bf16 a[84:87], a[172:175], v[172:175], a[84:87]\n"
"v_rcp_f32 v215, v215\n"
"v_mfma_f32_16x16x32_bf16 a[80:83], a[172:175], v[164:167], a[80:83]\n"
"v_mfma_f32_16x16x32_bf16 a[80:83], a[168:171], v[160:163], a[80:83]\n"
"buffer_load_dwordx4 v226, s[92:95], s58 offen offset:2048 lds\n"
"v_mfma_f32_16x16x32_bf16 a[96:99], a[176:179], v[160:163], a[96:99]\n"
"v_mfma_f32_16x16x32_bf16 a[96:99], a[180:183], v[164:167], a[96:99]\n"
"v_mfma_f32_16x16x32_bf16 a[100:103], a[180:183], v[172:175], a[100:103]\n"
"v_mfma_f32_16x16x32_bf16 a[100:103], a[176:179], v[168:171], a[100:103]\n"
"ds_read_b128 v[236:239], v234\n"
"v_mfma_f32_16x16x32_bf16 a[104:107], a[176:179], v[176:179], a[104:107]\n"
"v_mfma_f32_16x16x32_bf16 a[104:107], a[180:183], v[180:183], a[104:107]\n"
"buffer_load_dwordx4 v227, s[92:95], s58 offen offset:3072 lds\n"
"v_mfma_f32_16x16x32_bf16 a[108:111], a[180:183], v[188:191], a[108:111]\n"
"v_mfma_f32_16x16x32_bf16 a[108:111], a[176:179], v[184:187], a[108:111]\n"
"v_mfma_f32_16x16x32_bf16 a[124:127], a[184:187], v[184:187], a[124:127]\n"
"s_add_u32 m0, s60, 77824\n"
"v_mfma_f32_16x16x32_bf16 a[124:127], a[188:191], v[188:191], a[124:127]\n"
"buffer_load_dwordx4 v228, s[92:95], s58 offen lds\n"
"v_mfma_f32_16x16x32_bf16 a[120:123], a[188:191], v[180:183], a[120:123]\n"
"s_waitcnt lgkmcnt(0)\n"
"v_or3_b32 v236, v236, v237, v238\n"
"v_mfma_f32_16x16x32_bf16 a[120:123], a[184:187], v[176:179], a[120:123]\n"
"v_mfma_f32_16x16x32_bf16 a[116:119], a[184:187], v[168:171], a[116:119]\n"
"v_or_b32 v236, v236, v239\n"
"v_mfma_f32_16x16x32_bf16 a[116:119], a[188:191], v[172:175], a[116:119]\n"
"buffer_load_dwordx4 v229, s[92:95], s58 offen offset:1024 lds\n"
"v_mfma_f32_16x16x32_bf16 a[112:115], a[188:191], v[164:167], a[112:115]\n"
"v_readfirstlane_b32 s75, v236\n"
"v_mfma_f32_16x16x32_bf16 a[112:115], a[184:187], v[160:163], a[112:115]\n"
"buffer_load_dwordx4 v230, s[92:95], s58 offen offset:2048 lds\n"
"s_nop 1\n"
"s_cmp_lg_u32 s75, 0\n"
"s_cbranch_scc1 L_slow%=\n"
"L_fin%=:\n"
"v_accvgpr_read_b32 v216, a217\n"
"v_accvgpr_read_b32 v217, a218\n"
"v_accvgpr_read_b32 v220, a219\n"
"v_accvgpr_read_b32 v222, a205\n"
"v_accvgpr_read_b32 v224, a223\n"
"v_add_u32 v218, 73728, v216\n"
"v_add_u32 v219, 73728, v217\n"
"s_lshl2_add_u32 s73, s23, s69\n"
"s_lshl_b32 s73, s73, 11\n"
"s_add_u32 s73, s73, 8192\n"
"v_min_u32 v250, 240, v232\n"
"v_lshl_add_u32 v250, v250, 3, s73\n"
"v_accvgpr_read_b32 v0, a0\n"
"v_accvgpr_read_b32 v1, a1\n"
"v_accvgpr_read_b32 v2, a2\n"
"v_accvgpr_read_b32 v3, a3\n"
"v_accvgpr_read_b32 v4, a16\n"
"v_accvgpr_read_b32 v5, a17\n"
"v_accvgpr_read_b32 v6, a18\n"
"v_accvgpr_read_b32 v7, a19\n"
"s_add_u32 s66, s77, 8192\n"
"buffer_load_dwordx4 a[160:163], v232, s[52:55], s66 offen sc1\n"
"v_pk_mul_f32 v[0:1], v[0:1], v[212:213] op_sel:[0,0] op_sel_hi:[1,0]\n"
"v_pk_mul_f32 v[2:3], v[2:3], v[212:213] op_sel:[0,0] op_sel_hi:[1,0]\n"
"v_pk_mul_f32 v[4:5], v[4:5], v[212:213] op_sel:[0,0] op_sel_hi:[1,0]\n"
"v_pk_mul_f32 v[6:7], v[6:7], v[212:213] op_sel:[0,0] op_sel_hi:[1,0]\n"
"v_cvt_pk_bf16_f32 v144, v0, v1\n"
"v_cvt_pk_bf16_f32 v145, v2, v3\n"
"v_cvt_pk_bf16_f32 v146, v4, v5\n"
"v_cvt_pk_bf16_f32 v147, v6, v7\n"
"v_accvgpr_read_b32 v8, a4\n"
"v_accvgpr_read_b32 v9, a5\n"
"v_accvgpr_read_b32 v10, a6\n"
"v_accvgpr_read_b32 v11, a7\n"
"v_accvgpr_read_b32 v12, a20\n"
"v_accvgpr_read_b32 v13, a21\n"
"v_accvgpr_read_b32 v14, a22\n"
"v_accvgpr_read_b32 v15, a23\n"
"buffer_load_dwordx4 a[164:167], v232, s[52:55], s66 offen offset:1024 sc1\n"
"v_pk_mul_f32 v[8:9], v[8:9], v[212:213] op_sel:[0,1] op_sel_hi:[1,1]\n"
"v_pk_mul_f32 v[10:11], v[10:11], v[212:213] op_sel:[0,1] op_sel_hi:[1,1]\n"
"v_pk_mul_f32 v[12:13], v[12:13], v[212:213] op_sel:[0,1] op_sel_hi:[1,1]\n"
"v_pk_mul_f32 v[14:15], v[14:15], v[212:213] op_sel:[0,1] op_sel_hi:[1,1]\n"
"v_cvt_pk_bf16_f32 v160, v8, v9\n"
"v_cvt_pk_bf16_f32 v161, v10, v11\n"
"v_cvt_pk_bf16_f32 v162, v12, v13\n"
"v_cvt_pk_bf16_f32 v163, v14, v15\n"
"v_accvgpr_read_b32 v0, a8\n"
"v_accvgpr_read_b32 v1, a9\n"
"v_accvgpr_read_b32 v2, a10\n"
"v_accvgpr_read_b32 v3, a11\n"
"v_accvgpr_read_b32 v4, a24\n"
"v_accvgpr_read_b32 v5, a25\n"
"v_accvgpr_read_b32 v6, a26\n"
"v_accvgpr_read_b32 v7, a27\n"
"buffer_load_dwordx4 a[168:171], v232, s[52:55], s66 offen offset:2048 sc1\n"
"v_pk_mul_f32 v[0:1], v[0:1], v[214:215] op_sel:[0,0] op_sel_hi:[1,0]\n"
"v_pk_mul_f32 v[2:3], v[2:3], v[214:215] op_sel:[0,0] op_sel_hi:[1,0]\n"
"v_pk_mul_f32 v[4:5], v[4:5], v[214:215] op_sel:[0,0] op_sel_hi:[1,0]\n"
"v_pk_mul_f32 v[6:7], v[6:7], v[214:215] op_sel:[0,0] op_sel_hi:[1,0]\n"
"v_cvt_pk_bf16_f32 v176, v0, v1\n"
"v_cvt_pk_bf16_f32 v177, v2, v3\n"
"v_cvt_pk_bf16_f32 v178, v4, v5\n"
"v_cvt_pk_bf16_f32 v179, v6, v7\n"
"v_accvgpr_read_b32 v8, a12\n"
"v_accvgpr_read_b32 v9, a13\n"
"v_accvgpr_read_b32 v10, a14\n"
"v_accvgpr_read_b32 v11, a15\n"
"v_accvgpr_read_b32 v12, a28\n"
"v_accvgpr_read_b32 v13, a29\n"
"v_accvgpr_read_b32 v14, a30\n"
"v_accvgpr_read_b32 v15, a31\n"
"buffer_load_dwordx4 a[172:175], v232, s[52:55], s66 offen offset:3072 sc1\n"
"v_pk_mul_f32 v[8:9], v[8:9], v[214:215] op_sel:[0,1] op_sel_hi:[1,1]\n"
"v_pk_mul_f32 v[10:11], v[10:11], v[214:215] op_sel:[0,1] op_sel_hi:[1,1]\n"
"v_pk_mul_f32 v[12:13], v[12:13], v[214:215] op_sel:[0,1] op_sel_hi:[1,1]\n"
"v_pk_mul_f32 v[14:15], v[14:15], v[214:215] op_sel:[0,1] op_sel_hi:[1,1]\n"
"v_cvt_pk_bf16_f32 v192, v8, v9\n"
"v_cvt_pk_bf16_f32 v193, v10, v11\n"
"v_cvt_pk_bf16_f32 v194, v12, v13\n"
"v_cvt_pk_bf16_f32 v195, v14, v15\n"
"v_accvgpr_read_b32 v0, a32\n"
"v_accvgpr_read_b32 v1, a33\n"
"v_accvgpr_read_b32 v2, a34\n"
"v_accvgpr_read_b32 v3, a35\n"
"v_accvgpr_read_b32 v4, a48\n"
"v_accvgpr_read_b32 v5, a49\n"
"v_accvgpr_read_b32 v6, a50\n"
"v_accvgpr_read_b32 v7, a51\n"
"s_add_u32 s66, s77, 12288\n"
"buffer_load_dwordx4 a[176:179], v232, s[52:55], s66 offen sc1\n"
"v_pk_mul_f32 v[0:1], v[0:1], v[212:213] op_sel:[0,0] op_sel_hi:[1,0]\n"
"v_pk_mul_f32 v[2:3], v[2:3], v[212:213] op_sel:[0,0] op_sel_hi:[1,0]\n"
"v_pk_mul_f32 v[4:5], v[4:5], v[212:213] op_sel:[0,0] op_sel_hi:[1,0]\n"
"v_pk_mul_f32 v[6:7], v[6:7], v[212:213] op_sel:[0,0] op_sel_hi:[1,0]\n"
"v_cvt_pk_bf16_f32 v148, v0, v1\n"
"v_cvt_pk_bf16_f32 v149, v2, v3\n"
"v_cvt_pk_bf16_f32 v150, v4, v5\n"
"v_cvt_pk_bf16_f32 v151, v6, v7\n"
"v_accvgpr_read_b32 v8, a36\n"
"v_accvgpr_read_b32 v9, a37\n"
"v_accvgpr_read_b32 v10, a38\n"
"v_accvgpr_read_b32 v11, a39\n"
"v_accvgpr_read_b32 v12, a52\n"
"v_accvgpr_read_b32 v13, a53\n"
"v_accvgpr_read_b32 v14, a54\n"
"v_accvgpr_read_b32 v15, a55\n"
"buffer_load_dwordx4 a[180:183], v232, s[52:55], s66 offen offset:1024 sc1\n"
"v_pk_mul_f32 v[8:9], v[8:9], v[212:213] op_sel:[0,1] op_sel_hi:[1,1]\n"
"v_pk_mul_f32 v[10:11], v[10:11], v[212:213] op_sel:[0,1] op_sel_hi:[1,1]\n"
"v_pk_mul_f32 v[12:13], v[12:13], v[212:213] op_sel:[0,1] op_sel_hi:[1,1]\n"
"v_pk_mul_f32 v[14:15], v[14:15], v[212:213] op_sel:[0,1] op_sel_hi:[1,1]\n"
"v_cvt_pk_bf16_f32 v164, v8, v9\n"
"v_cvt_pk_bf16_f32 v165, v10, v11\n"
"v_cvt_pk_bf16_f32 v166, v12, v13\n"
"v_cvt_pk_bf16_f32 v167, v14, v15\n"
"v_accvgpr_read_b32 v0, a40\n"
"v_accvgpr_read_b32 v1, a41\n"
"v_accvgpr_read_b32 v2, a42\n"
"v_accvgpr_read_b32 v3, a43\n"
"v_accvgpr_read_b32 v4, a56\n"
"v_accvgpr_read_b32 v5, a57\n"
"v_accvgpr_read_b32 v6, a58\n"
"v_accvgpr_read_b32 v7, a59\n"
"buffer_load_dwordx4 a[184:187], v232, s[52:55], s66 offen offset:2048 sc1\n"
"v_pk_mul_f32 v[0:1], v[0:1], v[214:215] op_sel:[0,0] op_sel_hi:[1,0]\n"
"v_pk_mul_f32 v[2:3], v[2:3], v[214:215] op_sel:[0,0] op_sel_hi:[1,0]\n"
"v_pk_mul_f32 v[4:5], v[4:5], v[214:215] op_sel:[0,0] op_sel_hi:[1,0]\n"
"v_pk_mul_f32 v[6:7], v[6:7], v[214:215] op_sel:[0,0] op_sel_hi:[1,0]\n"
"v_cvt_pk_bf16_f32 v180, v0, v1\n"
"v_cvt_pk_bf16_f32 v181, v2, v3\n"
"v_cvt_pk_bf16_f32 v182, v4, v5\n"
"v_cvt_pk_bf16_f32 v183, v6, v7\n"
"v_accvgpr_read_b32 v8, a44\n"
"v_accvgpr_read_b32 v9, a45\n"
"v_accvgpr_read_b32 v10, a46\n"
"v_accvgpr_read_b32 v11, a47\n"
"v_accvgpr_read_b32 v12, a60\n"
"v_accvgpr_read_b32 v13, a61\n"
"v_accvgpr_read_b32 v14, a62\n"
"v_accvgpr_read_b32 v15, a63\n"
"buffer_load_dwordx4 a[188:191], v232, s[52:55], s66 offen offset:3072 sc1\n"
"v_pk_mul_f32 v[8:9], v[8:9], v[214:215] op_sel:[0,1] op_sel_hi:[1,1]\n"
"v_pk_mul_f32 v[10:11], v[10:11], v[214:215] op_sel:[0,1] op_sel_hi:[1,1]\n"
"v_pk_mul_f32 v[12:13], v[12:13], v[214:215] op_sel:[0,1] op_sel_hi:[1,1]\n"
"v_pk_mul_f32 v[14:15], v[14:15], v[214:215] op_sel:[0,1] op_sel_hi:[1,1]\n"
"v_cvt_pk_bf16_f32 v196, v8, v9\n"
"v_cvt_pk_bf16_f32 v197, v10, v11\n"
"v_cvt_pk_bf16_f32 v198, v12, v13\n"
"v_cvt_pk_bf16_f32 v199, v14, v15\n"
"s_add_u32 s61, s65, 0\n"
"buffer_store_dwordx4 v[144:147], v232, s[52:55], s61 offen sc1\n"
"v_accvgpr_read_b32 v0, a64\n"
"v_accvgpr_read_b32 v1, a65\n"
"v_accvgpr_read_b32 v2, a66\n"
"v_accvgpr_read_b32 v3, a67\n"
"v_accvgpr_read_b32 v4, a80\n"
"v_accvgpr_read_b32 v5, a81\n"
"v_accvgpr_read_b32 v6, a82\n"
"v_accvgpr_read_b32 v7, a83\n"
"buffer_load_dwordx4 v231, s[92:95], s58 offen offset:3072 lds\n"
"v_pk_mul_f32 v[0:1], v[0:1], v[212:213] op_sel:[0,0] op_sel_hi:[1,0]\n"
"v_pk_mul_f32 v[2:3], v[2:3], v[212:213] op_sel:[0,0] op_sel_hi:[1,0]\n"
"v_pk_mul_f32 v[4:5], v[4:5], v[212:213] op_sel:[0,0] op_sel_hi:[1,0]\n"
"v_pk_mul_f32 v[6:7], v[6:7], v[212:213] op_sel:[0,0] op_sel_hi:[1,0]\n"
"v_cvt_pk_bf16_f32 v152, v0, v1\n"
"v_cvt_pk_bf16_f32 v153, v2, v3\n"
"v_cvt_pk_bf16_f32 v154, v4, v5\n"
"v_cvt_pk_bf16_f32 v155, v6, v7\n"
"v_accvgpr_read_b32 v8, a68\n"
"v_accvgpr_read_b32 v9, a69\n"
"v_accvgpr_read_b32 v10, a70\n"
"v_accvgpr_read_b32 v11, a71\n"
"v_accvgpr_read_b32 v12, a84\n"
"v_accvgpr_read_b32 v13, a85\n"
"v_accvgpr_read_b32 v14, a86\n"
"v_accvgpr_read_b32 v15, a87\n"
"s_add_u32 m0, s60, 81920\n"
"s_nop 0\n"
"buffer_load_dwordx4 v235, s[92:95], s58 offen lds\n"
"v_pk_mul_f32 v[8:9], v[8:9], v[212:213] op_sel:[0,1] op_sel_hi:[1,1]\n"
"v_pk_mul_f32 v[10:11], v[10:11], v[212:213] op_sel:[0,1] op_sel_hi:[1,1]\n"
"v_pk_mul_f32 v[12:13], v[12:13], v[212:213] op_sel:[0,1] op_sel_hi:[1,1]\n"
"v_pk_mul_f32 v[14:15], v[14:15], v[212:213] op_sel:[0,1] op_sel_hi:[1,1]\n"
"v_cvt_pk_bf16_f32 v168, v8, v9\n"
"v_cvt_pk_bf16_f32 v169, v10, v11\n"
"v_cvt_pk_bf16_f32 v170, v12, v13\n"
"v_cvt_pk_bf16_f32 v171, v14, v15\n"
"v_accvgpr_read_b32 v0, a72\n"
"v_accvgpr_read_b32 v1, a73\n"
"v_accvgpr_read_b32 v2, a74\n"
"v_accvgpr_read_b32 v3, a75\n"
"v_accvgpr_read_b32 v4, a88\n"
"v_accvgpr_read_b32 v5, a89\n"
"v_accvgpr_read_b32 v6, a90\n"
"v_accvgpr_read_b32 v7, a91\n"
"s_mov_b32 s72, 4\n"
"s_mul_i32 s72, s72, 65536\n"
"s_add_u32 s77, s76, s72\n"
"s_add_u32 s66, s77, 0\n"
"buffer_load_dwordx4 a[0:3], v232, s[52:55], s66 offen sc1\n"
"v_pk_mul_f32 v[0:1], v[0:1], v[214:215] op_sel:[0,0] op_sel_hi:[1,0]\n"
"v_pk_mul_f32 v[2:3], v[2:3], v[214:215] op_sel:[0,0] op_sel_hi:[1,0]\n"
"v_pk_mul_f32 v[4:5], v[4:5], v[214:215] op_sel:[0,0] op_sel_hi:[1,0]\n"
"v_pk_mul_f32 v[6:7], v[6:7], v[214:215] op_sel:[0,0] op_sel_hi:[1,0]\n"
"v_cvt_pk_bf16_f32 v184, v0, v1\n"
"v_cvt_pk_bf16_f32 v185, v2, v3\n"
"v_cvt_pk_bf16_f32 v186, v4, v5\n"
"v_cvt_pk_bf16_f32 v187, v6, v7\n"
"v_accvgpr_read_b32 v8, a76\n"
"v_accvgpr_read_b32 v9, a77\n"
"v_accvgpr_read_b32 v10, a78\n"
"v_accvgpr_read_b32 v11, a79\n"
"v_accvgpr_read_b32 v12, a92\n"
"v_accvgpr_read_b32 v13, a93\n"
"v_accvgpr_read_b32 v14, a94\n"
"v_accvgpr_read_b32 v15, a95\n"
"buffer_load_dwordx4 a[4:7], v232, s[52:55], s66 offen offset:1024 sc1\n"
"v_pk_mul_f32 v[8:9], v[8:9], v[214:215] op_sel:[0,1] op_sel_hi:[1,1]\n"
"v_pk_mul_f32 v[10:11], v[10:11], v[214:215] op_sel:[0,1] op_sel_hi:[1,1]\n"
"v_pk_mul_f32 v[12:13], v[12:13], v[214:215] op_sel:[0,1] op_sel_hi:[1,1]\n"
"v_pk_mul_f32 v[14:15], v[14:15], v[214:215] op_sel:[0,1] op_sel_hi:[1,1]\n"
"v_cvt_pk_bf16_f32 v200, v8, v9\n"
"v_cvt_pk_bf16_f32 v201, v10, v11\n"
"v_cvt_pk_bf16_f32 v202, v12, v13\n"
"v_cvt_pk_bf16_f32 v203, v14, v15\n"
"s_add_u32 s61, s65, 4096\n"
"buffer_store_dwordx4 v[160:163], v232, s[52:55], s61 offen sc1\n"
"s_waitcnt vmcnt(39)\n"
"s_barrier\n"
"ds_read_b64_tr_b16 a[192:193], v216\n"
"ds_read_b64_tr_b16 a[194:195], v217\n"
"ds_read_b64_tr_b16 a[196:197], v216 offset:9216\n"
"ds_read_b64_tr_b16 a[198:199], v217 offset:9216\n"
"ds_read_b64_tr_b16 a[200:201], v216 offset:18432\n"
"ds_read_b64_tr_b16 a[202:203], v217 offset:18432\n"
"ds_read_b64_tr_b16 a[204:205], v216 offset:27648\n"
"ds_read_b64_tr_b16 a[206:207], v217 offset:27648\n"
"ds_read_b64_tr_b16 a[208:209], v216 offset:32\n"
"ds_read_b64_tr_b16 a[210:211], v217 offset:32\n"
"ds_read_b64_tr_b16 a[212:213], v216 offset:9248\n"
"ds_read_b64_tr_b16 a[214:215], v217 offset:9248\n"
"ds_read_b64_tr_b16 a[216:217], v216 offset:18464\n"
"ds_read_b64_tr_b16 a[218:219], v217 offset:18464\n"
"ds_read_b64_tr_b16 a[220:221], v216 offset:27680\n"
"ds_read_b64_tr_b16 a[222:223], v217 offset:27680\n"
"v_accvgpr_read_b32 v0, a96\n"
"v_accvgpr_read_b32 v1, a97\n"
"v_accvgpr_read_b32 v2, a98\n"
"v_accvgpr_read_b32 v3, a99\n"
"v_accvgpr_read_b32 v4, a112\n"
"v_accvgpr_read_b32 v5, a113\n"
"v_accvgpr_read_b32 v6, a114\n"
"v_accvgpr_read_b32 v7, a115\n"
"buffer_load_dwordx4 a[8:11], v232, s[52:55], s66 offen offset:2048 sc1\n"
"v_pk_mul_f32 v[0:1], v[0:1], v[212:213] op_sel:[0,0] op_sel_hi:[1,0]\n"
"v_pk_mul_f32 v[2:3], v[2:3], v[212:213] op_sel:[0,0] op_sel_hi:[1,0]\n"
"v_pk_mul_f32 v[4:5], v[4:5], v[212:213] op_sel:[0,0] op_sel_hi:[1,0]\n"
"v_pk_mul_f32 v[6:7], v[6:7], v[212:213] op_sel:[0,0] op_sel_hi:[1,0]\n"
"v_cvt_pk_bf16_f32 v156, v0, v1\n"
"v_cvt_pk_bf16_f32 v157, v2, v3\n"
"v_cvt_pk_bf16_f32 v158, v4, v5\n"
"v_cvt_pk_bf16_f32 v159, v6, v7\n"
"v_accvgpr_read_b32 v8, a100\n"
"v_accvgpr_read_b32 v9, a101\n"
"v_accvgpr_read_b32 v10, a102\n"
"v_accvgpr_read_b32 v11, a103\n"
"v_accvgpr_read_b32 v12, a116\n"
"v_accvgpr_read_b32 v13, a117\n"
"v_accvgpr_read_b32 v14, a118\n"
"v_accvgpr_read_b32 v15, a119\n"
"buffer_load_dwordx4 a[12:15], v232, s[52:55], s66 offen offset:3072 sc1\n"
"v_pk_mul_f32 v[8:9], v[8:9], v[212:213] op_sel:[0,1] op_sel_hi:[1,1]\n"
"v_pk_mul_f32 v[10:11], v[10:11], v[212:213] op_sel:[0,1] op_sel_hi:[1,1]\n"
"v_pk_mul_f32 v[12:13], v[12:13], v[212:213] op_sel:[0,1] op_sel_hi:[1,1]\n"
"v_pk_mul_f32 v[14:15], v[14:15], v[212:213] op_sel:[0,1] op_sel_hi:[1,1]\n"
"v_cvt_pk_bf16_f32 v172, v8, v9\n"
"v_cvt_pk_bf16_f32 v173, v10, v11\n"
"v_cvt_pk_bf16_f32 v174, v12, v13\n"
"v_cvt_pk_bf16_f32 v175, v14, v15\n"
"v_accvgpr_read_b32 v0, a104\n"
"v_accvgpr_read_b32 v1, a105\n"
"v_accvgpr_read_b32 v2, a106\n"
"v_accvgpr_read_b32 v3, a107\n"
"v_accvgpr_read_b32 v4, a120\n"
"v_accvgpr_read_b32 v5, a121\n"
"v_accvgpr_read_b32 v6, a122\n"
"v_accvgpr_read_b32 v7, a123\n"
"s_add_u32 s66, s77, 4096\n"
"buffer_load_dwordx4 a[16:19], v232, s[52:55], s66 offen sc1\n"
"v_pk_mul_f32 v[0:1], v[0:1], v[214:215] op_sel:[0,0] op_sel_hi:[1,0]\n"
"v_pk_mul_f32 v[2:3], v[2:3], v[214:215] op_sel:[0,0] op_sel_hi:[1,0]\n"
"v_pk_mul_f32 v[4:5], v[4:5], v[214:215] op_sel:[0,0] op_sel_hi:[1,0]\n"
"v_pk_mul_f32 v[6:7], v[6:7], v[214:215] op_sel:[0,0] op_sel_hi:[1,0]\n"
"v_cvt_pk_bf16_f32 v188, v0, v1\n"
"v_cvt_pk_bf16_f32 v189, v2, v3\n"
"v_cvt_pk_bf16_f32 v190, v4, v5\n"
"v_cvt_pk_bf16_f32 v191, v6, v7\n"
"v_accvgpr_read_b32 v8, a108\n"
"v_accvgpr_read_b32 v9, a109\n"
"v_accvgpr_read_b32 v10, a110\n"
"v_accvgpr_read_b32 v11, a111\n"
"v_accvgpr_read_b32 v12, a124\n"
"v_accvgpr_read_b32 v13, a125\n"
"v_accvgpr_read_b32 v14, a126\n"
"v_accvgpr_read_b32 v15, a127\n"
"buffer_load_dwordx4 a[20:23], v232, s[52:55], s66 offen offset:1024 sc1\n"
"v_pk_mul_f32 v[8:9], v[8:9], v[214:215] op_sel:[0,1] op_sel_hi:[1,1]\n"
"v_pk_mul_f32 v[10:11], v[10:11], v[214:215] op_sel:[0,1] op_sel_hi:[1,1]\n"
"v_pk_mul_f32 v[12:13], v[12:13], v[214:215] op_sel:[0,1] op_sel_hi:[1,1]\n"
"v_pk_mul_f32 v[14:15], v[14:15], v[214:215] op_sel:[0,1] op_sel_hi:[1,1]\n"
"v_cvt_pk_bf16_f32 v204, v8, v9\n"
"v_cvt_pk_bf16_f32 v205, v10, v11\n"
"v_cvt_pk_bf16_f32 v206, v12, v13\n"
"v_cvt_pk_bf16_f32 v207, v14, v15\n"
"s_add_u32 s61, s65, 8192\n"
"buffer_store_dwordx4 v[176:179], v232, s[52:55], s61 offen sc1\n"
"s_bitcmp1_b32 s23, 2\n"
"s_waitcnt lgkmcnt(8)\n"
"s_cbranch_scc1 L_odd%=\n"
"v_mfma_f32_16x16x32_bf16 v[0:3], a[192:195], v[144:147], 0\n"
"ds_read_b64_tr_b16 a[224:225], v216 offset:64\n"
"v_mfma_f32_16x16x32_bf16 v[0:3], a[196:199], v[148:151], v[0:3]\n"
"v_mfma_f32_16x16x32_bf16 v[0:3], a[200:203], v[152:155], v[0:3]\n"
"ds_read_b64_tr_b16 a[226:227], v217 offset:64\n"
"v_mfma_f32_16x16x32_bf16 v[0:3], a[204:207], v[156:159], v[0:3]\n"
"buffer_load_dwordx4 a[24:27], v232, s[52:55], s66 offen offset:2048 sc1\n"
"v_mfma_f32_16x16x32_bf16 v[4:7], a[204:207], v[172:175], 0\n"
"ds_read_b64_tr_b16 a[228:229], v216 offset:9280\n"
"v_mfma_f32_16x16x32_bf16 v[4:7], a[200:203], v[168:171], v[4:7]\n"
"v_mfma_f32_16x16x32_bf16 v[4:7], a[196:199], v[164:167], v[4:7]\n"
"ds_read_b64_tr_b16 a[230:231], v217 offset:9280\n"
"v_mfma_f32_16x16x32_bf16 v[4:7], a[192:195], v[160:163], v[4:7]\n"
"v_mfma_f32_16x16x32_bf16 v[8:11], a[192:195], v[176:179], 0\n"
"ds_read_b64_tr_b16 a[232:233], v216 offset:18496\n"
"v_mfma_f32_16x16x32_bf16 v[8:11], a[196:199], v[180:183], v[8:11]\n"
"buffer_load_dwordx4 a[28:31], v232, s[52:55], s66 offen offset:3072 sc1\n"
"v_mfma_f32_16x16x32_bf16 v[8:11], a[200:203], v[184:187], v[8:11]\n"
"ds_read_b64_tr_b16 a[234:235], v217 offset:18496\n"
"v_mfma_f32_16x16x32_bf16 v[8:11], a[204:207], v[188:191], v[8:11]\n"
"v_mfma_f32_16x16x32_bf16 v[12:15], a[204:207], v[204:207], 0\n"
"ds_read_b64_tr_b16 a[236:237], v216 offset:27712\n"
"v_mfma_f32_16x16x32_bf16 v[12:15], a[200:203], v[200:203], v[12:15]\n"
"v_mfma_f32_16x16x32_bf16 v[12:15], a[196:199], v[196:199], v[12:15]\n"
"s_add_u32 s61, s65, 12288\n"
"ds_read_b64_tr_b16 a[238:239], v217 offset:27712\n"
"v_mfma_f32_16x16x32_bf16 v[12:15], a[192:195], v[192:195], v[12:15]\n"
"s_waitcnt lgkmcnt(8)\n"
"buffer_store_dwordx4 v[192:195], v232, s[52:55], s61 offen sc1\n"
"v_mfma_f32_16x16x32_bf16 v[28:31], a[208:211], v[192:195], 0\n"
"s_add_u32 s66, s77, 8192\n"
"ds_read_b64_tr_b16 a[192:193], v216 offset:96\n"
"v_mfma_f32_16x16x32_bf16 v[28:31], a[212:215], v[196:199], v[28:31]\n"
"buffer_load_dwordx4 a[32:35], v232, s[52:55], s66 offen sc1\n"
"v_mfma_f32_16x16x32_bf16 v[28:31], a[216:219], v[200:203], v[28:31]\n"
"ds_read_b64_tr_b16 a[194:195], v217 offset:96\n"
"v_mfma_f32_16x16x32_bf16 v[28:31], a[220:223], v[204:207], v[28:31]\n"
"v_mfma_f32_16x16x32_bf16 v[24:27], a[220:223], v[188:191], 0\n"
"ds_read_b64_tr_b16 a[196:197], v216 offset:9312\n"
"v_mfma_f32_16x16x32_bf16 v[24:27], a[216:219], v[184:187], v[24:27]\n"
"buffer_load_dwordx4 a[36:39], v232, s[52:55], s66 offen offset:1024 sc1\n"
"v_mfma_f32_16x16x32_bf16 v[24:27], a[212:215], v[180:183], v[24:27]\n"
"ds_read_b64_tr_b16 a[198:199], v217 offset:9312\n"
"v_mfma_f32_16x16x32_bf16 v[24:27], a[208:211], v[176:179], v[24:27]\n"
"v_mfma_f32_16x16x32_bf16 v[20:23], a[208:211], v[160:163], 0\n"
"ds_read_b64_tr_b16 a[200:201], v216 offset:18528\n"
"v_mfma_f32_16x16x32_bf16 v[20:23], a[212:215], v[164:167], v[20:23]\n"
"v_mfma_f32_16x16x32_bf16 v[20:23], a[216:219], v[168:171], v[20:23]\n"
"ds_read_b64_tr_b16 a[202:203], v217 offset:18528\n"
"v_mfma_f32_16x16x32_bf16 v[20:23], a[220:223], v[172:175], v[20:23]\n"
"buffer_load_dwordx4 a[40:43], v232, s[52:55], s66 offen offset:2048 sc1\n"
"v_mfma_f32_16x16x32_bf16 v[16:19], a[220:223], v[156:159], 0\n"
"ds_read_b64_tr_b16 a[204:205], v216 offset:27744\n"
"v_mfma_f32_16x16x32_bf16 v[16:19], a[216:219], v[152:155], v[16:19]\n"
"v_mfma_f32_16x16x32_bf16 v[16:19], a[212:215], v[148:151], v[16:19]\n"
"ds_read_b64_tr_b16 a[206:207], v217 offset:27744\n"
"v_mfma_f32_16x16x32_bf16 v[16:19], a[208:211], v[144:147], v[16:19]\n"
"s_waitcnt lgkmcnt(8)\n"
"v_mfma_f32_16x16x32_bf16 v[32:35], a[224:227], v[144:147], 0\n"
"s_add_u32 s61, s65, 0\n"
"ds_read_b64_tr_b16 a[208:209], v216 offset:128\n"
"v_mfma_f32_16x16x32_bf16 v[32:35], a[228:231], v[148:151], v[32:35]\n"
"buffer_store_dwordx4 v[148:151], v232, s[52:55], s61 offen offset:1024 sc1\n"
"v_mfma_f32_16x16x32_bf16 v[32:35], a[232:235], v[152:155], v[32:35]\n"
"ds_read_b64_tr_b16 a[210:211], v217 offset:128\n"
"v_mfma_f32_16x16x32_bf16 v[32:35], a[236:239], v[156:159], v[32:35]\n"
"buffer_load_dwordx4 a[44:47], v232, s[52:55], s66 offen offset:3072 sc1\n"
"v_mfma_f32_16x16x32_bf16 v[36:39], a[236:239], v[172:175], 0\n"
"ds_read_b64_tr_b16 a[212:213], v216 offset:9344\n"
"v_mfma_f32_16x16x32_bf16 v[36:39], a[232:235], v[168:171], v[36:39]\n"
"v_mfma_f32_16x16x32_bf16 v[36:39], a[228:231], v[164:167], v[36:39]\n"
"s_add_u32 s66, s77, 12288\n"
"ds_read_b64_tr_b16 a[214:215], v217 offset:9344\n"
"v_mfma_f32_16x16x32_bf16 v[36:39], a[224:227], v[160:163], v[36:39]\n"
"buffer_load_dwordx4 a[48:51], v232, s[52:55], s66 offen sc1\n"
"v_mfma_f32_16x16x32_bf16 v[40:43], a[224:227], v[176:179], 0\n"
"ds_read_b64_tr_b16 a[216:217], v216 offset:18560\n"
"v_mfma_f32_16x16x32_bf16 v[40:43], a[228:231], v[180:183], v[40:43]\n"
"v_mfma_f32_16x16x32_bf16 v[40:43], a[232:235], v[184:187], v[40:43]\n"
"ds_read_b64_tr_b16 a[218:219], v217 offset:18560\n"
"v_mfma_f32_16x16x32_bf16 v[40:43], a[236:239], v[188:191], v[40:43]\n"
"v_mfma_f32_16x16x32_bf16 v[44:47], a[236:239], v[204:207], 0\n"
"ds_read_b64_tr_b16 a[220:221], v216 offset:27776\n"
"v_mfma_f32_16x16x32_bf16 v[44:47], a[232:235], v[200:203], v[44:47]\n"
"buffer_load_dwordx4 a[52:55], v232, s[52:55], s66 offen offset:1024 sc1\n"
"v_mfma_f32_16x16x32_bf16 v[44:47], a[228:231], v[196:199], v[44:47]\n"
"ds_read_b64_tr_b16 a[222:223], v217 offset:27776\n"
"v_mfma_f32_16x16x32_bf16 v[44:47], a[224:227], v[192:195], v[44:47]\n"
"s_waitcnt lgkmcnt(8)\n"
"v_mfma_f32_16x16x32_bf16 v[60:63], a[192:195], v[192:195], 0\n"
"ds_read_b64_tr_b16 a[224:225], v216 offset:160\n"
"v_mfma_f32_16x16x32_bf16 v[60:63], a[196:199], v[196:199], v[60:63]\n"
"v_mfma_f32_16x16x32_bf16 v[60:63], a[200:203], v[200:203], v[60:63]\n"
"s_add_u32 s61, s65, 4096\n"
"ds_read_b64_tr_b16 a[226:227], v217 offset:160\n"
"v_mfma_f32_16x16x32_bf16 v[60:63], a[204:207], v[204:207], v[60:63]\n"
"buffer_store_dwordx4 v[164:167], v232, s[52:55], s61 offen offset:1024 sc1\n"
"v_mfma_f32_16x16x32_bf16 v[56:59], a[204:207], v[188:191], 0\n"
"ds_read_b64_tr_b16 a[228:229], v216 offset:9376\n"
"v_mfma_f32_16x16x32_bf16 v[56:59], a[200:203], v[184:187], v[56:59]\n"
"buffer_load_dwordx4 a[56:59], v232, s[52:55], s66 offen offset:2048 sc1\n"
"v_mfma_f32_16x16x32_bf16 v[56:59], a[196:199], v[180:183], v[56:59]\n"
"ds_read_b64_tr_b16 a[230:231], v217 offset:9376\n"
"v_mfma_f32_16x16x32_bf16 v[56:59], a[192:195], v[176:179], v[56:59]\n"
"v_mfma_f32_16x16x32_bf16 v[52:55], a[192:195], v[160:163], 0\n"
"ds_read_b64_tr_b16 a[232:233], v216 offset:18592\n"
"v_mfma_f32_16x16x32_bf16 v[52:55], a[196:199], v[164:167], v[52:55]\n"
"buffer_load_dwordx4 a[60:63], v232, s[52:55], s66 offen offset:3072 sc1\n"
"v_mfma_f32_16x16x32_bf16 v[52:55], a[200:203], v[168:171], v[52:55]\n"
"ds_read_b64_tr_b16 a[234:235], v217 offset:18592\n"
"v_mfma_f32_16x16x32_bf16 v[52:55], a[204:207], v[172:175], v[52:55]\n"
"v_mfma_f32_16x16x32_bf16 v[48:51], a[204:207], v[156:159], 0\n"
"ds_read_b64_tr_b16 a[236:237], v216 offset:27808\n"
"v_mfma_f32_16x16x32_bf16 v[48:51], a[200:203], v[152:155], v[48:51]\n"
"v_mfma_f32_16x16x32_bf16 v[48:51], a[196:199], v[148:151], v[48:51]\n"
"ds_read_b64_tr_b16 a[238:239], v217 offset:27808\n"
"v_mfma_f32_16x16x32_bf16 v[48:51], a[192:195], v[144:147], v[48:51]\n"
"s_waitcnt lgkmcnt(8)\n"
"v_mfma_f32_16x16x32_bf16 v[64:67], a[208:211], v[144:147], 0\n"
"ds_read_b64_tr_b16 a[192:193], v216 offset:192\n"
"v_mfma_f32_16x16x32_bf16 v[64:67], a[212:215], v[148:151], v[64:67]\n"
"v_mfma_f32_16x16x32_bf16 v[64:67], a[216:219], v[152:155], v[64:67]\n"
"ds_read_b64_tr_b16 a[194:195], v217 offset:192\n"
"v_mfma_f32_16x16x32_bf16 v[64:67], a[220:223], v[156:159], v[64:67]\n"
"v_mfma_f32_16x16x32_bf16 v[68:71], a[220:223], v[172:175], 0\n"
"s_add_u32 s61, s65, 8192\n"
"ds_read_b64_tr_b16 a[196:197], v216 offset:9408\n"
"v_mfma_f32_16x16x32_bf16 v[68:71], a[216:219], v[168:171], v[68:71]\n"
"buffer_store_dwordx4 v[180:183], v232, s[52:55], s61 offen offset:1024 sc1\n"
"v_mfma_f32_16x16x32_bf16 v[68:71], a[212:215], v[164:167], v[68:71]\n"
"ds_read_b64_tr_b16 a[198:199], v217 offset:9408\n"
"v_mfma_f32_16x16x32_bf16 v[68:71], a[208:211], v[160:163], v[68:71]\n"
"v_mfma_f32_16x16x32_bf16 v[72:75], a[208:211], v[176:179], 0\n"
"ds_read_b64_tr_b16 a[200:201], v216 offset:18624\n"
"v_mfma_f32_16x16x32_bf16 v[72:75], a[212:215], v[180:183], v[72:75]\n"
"v_mfma_f32_16x16x32_bf16 v[72:75], a[216:219], v[184:187], v[72:75]\n"
"s_mov_b32 s98, s79\n"
"ds_read_b64_tr_b16 a[202:203], v217 offset:18624\n"
"v_mfma_f32_16x16x32_bf16 v[72:75], a[220:223], v[188:191], v[72:75]\n"
"s_waitcnt vmcnt(57)\n"
"buffer_store_dword v247, v248, s[96:99], 0 offen sc1\n"
"v_mfma_f32_16x16x32_bf16 v[76:79], a[220:223], v[204:207], 0\n"
"s_mov_b32 s98, 139264\n"
"ds_read_b64_tr_b16 a[204:205], v216 offset:27840\n"
"v_mfma_f32_16x16x32_bf16 v[76:79], a[216:219], v[200:203], v[76:79]\n"
"v_mfma_f32_16x16x32_bf16 v[76:79], a[212:215], v[196:199], v[76:79]\n"
"ds_read_b64_tr_b16 a[206:207], v217 offset:27840\n"
"v_mfma_f32_16x16x32_bf16 v[76:79], a[208:211], v[192:195], v[76:79]\n"
"s_waitcnt lgkmcnt(8)\n"
"v_mfma_f32_16x16x32_bf16 v[92:95], a[224:227], v[192:195], 0\n"
"ds_read_b64_tr_b16 a[208:209], v216 offset:224\n"
"v_mfma_f32_16x16x32_bf16 v[92:95], a[228:231], v[196:199], v[92:95]\n"
"v_mfma_f32_16x16x32_bf16 v[92:95], a[232:235], v[200:203], v[92:95]\n"
"ds_read_b64_tr_b16 a[210:211], v217 offset:224\n"
"v_mfma_f32_16x16x32_bf16 v[92:95], a[236:239], v[204:207], v[92:95]\n"
"v_mfma_f32_16x16x32_bf16 v[88:91], a[236:239], v[188:191], 0\n"
"ds_read_b64_tr_b16 a[212:213], v216 offset:9440\n"
"v_mfma_f32_16x16x32_bf16 v[88:91], a[232:235], v[184:187], v[88:91]\n"
"v_mfma_f32_16x16x32_bf16 v[88:91], a[228:231], v[180:183], v[88:91]\n"
"s_add_u32 s61, s65, 12288\n"
"ds_read_b64_tr_b16 a[214:215], v217 offset:9440\n"
"v_mfma_f32_16x16x32_bf16 v[88:91], a[224:227], v[176:179], v[88:91]\n"
"buffer_store_dwordx4 v[196:199], v232, s[52:55], s61 offen offset:1024 sc1\n"
"v_mfma_f32_16x16x32_bf16 v[84:87], a[224:227], v[160:163], 0\n"
"ds_read_b64_tr_b16 a[216:217], v216 offset:18656\n"
"v_mfma_f32_16x16x32_bf16 v[84:87], a[228:231], v[164:167], v[84:87]\n"
"v_mfma_f32_16x16x32_bf16 v[84:87], a[232:235], v[168:171], v[84:87]\n"
"ds_read_b64_tr_b16 a[218:219], v217 offset:18656\n"
"v_mfma_f32_16x16x32_bf16 v[84:87], a[236:239], v[172:175], v[84:87]\n"
"v_mfma_f32_16x16x32_bf16 v[80:83], a[236:239], v[156:159], 0\n"
"ds_read_b64_tr_b16 a[220:221], v216 offset:27872\n"
"v_mfma_f32_16x16x32_bf16 v[80:83], a[232:235], v[152:155], v[80:83]\n"
"v_mfma_f32_16x16x32_bf16 v[80:83], a[228:231], v[148:151], v[80:83]\n"
"ds_read_b64_tr_b16 a[222:223], v217 offset:27872\n"
"v_mfma_f32_16x16x32_bf16 v[80:83], a[224:227], v[144:147], v[80:83]\n"
"s_waitcnt lgkmcnt(8)\n"
"v_mfma_f32_16x16x32_bf16 v[96:99], a[192:195], v[144:147], 0\n"
"ds_read_b64_tr_b16 a[224:225], v216 offset:256\n"
"v_mfma_f32_16x16x32_bf16 v[96:99], a[196:199], v[148:151], v[96:99]\n"
"v_mfma_f32_16x16x32_bf16 v[96:99], a[200:203], v[152:155], v[96:99]\n"
"ds_read_b64_tr_b16 a[226:227], v217 offset:256\n"
"v_mfma_f32_16x16x32_bf16 v[96:99], a[204:207], v[156:159], v[96:99]\n"
"v_mfma_f32_16x16x32_bf16 v[100:103], a[204:207], v[172:175], 0\n"
"ds_read_b64_tr_b16 a[228:229], v216 offset:9472\n"
"v_mfma_f32_16x16x32_bf16 v[100:103], a[200:203], v[168:171], v[100:103]\n"
"v_mfma_f32_16x16x32_bf16 v[100:103], a[196:199], v[164:167], v[100:103]\n"
"ds_read_b64_tr_b16 a[230:231], v217 offset:9472\n"
"v_mfma_f32_16x16x32_bf16 v[100:103], a[192:195], v[160:163], v[100:103]\n"
"v_mfma_f32_16x16x32_bf16 v[104:107], a[192:195], v[176:179], 0\n"
"s_add_u32 s61, s65, 0\n"
"ds_read_b64_tr_b16 a[232:233], v216 offset:18688\n"
"v_mfma_f32_16x16x32_bf16 v[104:107], a[196:199], v[180:183], v[104:107]\n"
"buffer_store_dwordx4 v[152:155], v232, s[52:55], s61 offen offset:2048 sc1\n"
"v_mfma_f32_16x16x32_bf16 v[104:107], a[200:203], v[184:187], v[104:107]\n"
"ds_read_b64_tr_b16 a[234:235], v217 offset:18688\n"
"v_mfma_f32_16x16x32_bf16 v[104:107], a[204:207], v[188:191], v[104:107]\n"
"v_mfma_f32_16x16x32_bf16 v[108:111], a[204:207], v[204:207], 0\n"
"ds_read_b64_tr_b16 a[236:237], v216 offset:27904\n"
"v_mfma_f32_16x16x32_bf16 v[108:111], a[200:203], v[200:203], v[108:111]\n"
"v_mfma_f32_16x16x32_bf16 v[108:111], a[196:199], v[196:199], v[108:111]\n"
"ds_read_b64_tr_b16 a[238:239], v217 offset:27904\n"
"v_mfma_f32_16x16x32_bf16 v[108:111], a[192:195], v[192:195], v[108:111]\n"
"s_waitcnt lgkmcnt(8)\n"
"v_mfma_f32_16x16x32_bf16 v[124:127], a[208:211], v[192:195], 0\n"
"v_mfma_f32_16x16x32_bf16 v[124:127], a[212:215], v[196:199], v[124:127]\n"
"v_mfma_f32_16x16x32_bf16 v[124:127], a[216:219], v[200:203], v[124:127]\n"
"v_mfma_f32_16x16x32_bf16 v[124:127], a[220:223], v[204:207], v[124:127]\n"
"s_waitcnt vmcnt(43) lgkmcnt(0)\n"
"v_mfma_f32_16x16x32_bf16 v[120:123], a[220:223], v[188:191], 0\n"
"s_barrier\n"
"v_mfma_f32_16x16x32_bf16 v[120:123], a[216:219], v[184:187], v[120:123]\n"
"v_mfma_f32_16x16x32_bf16 v[120:123], a[212:215], v[180:183], v[120:123]\n"
"ds_read_b64_tr_b16 a[192:193], v216 offset:36864\n"
"ds_read_b64_tr_b16 a[194:195], v217 offset:36864\n"
"v_mfma_f32_16x16x32_bf16 v[120:123], a[208:211], v[176:179], v[120:123]\n"
"v_mfma_f32_16x16x32_bf16 v[116:119], a[208:211], v[160:163], 0\n"
"ds_read_b64_tr_b16 a[196:197], v216 offset:46080\n"
"ds_read_b64_tr_b16 a[198:199], v217 offset:46080\n"
"v_mfma_f32_16x16x32_bf16 v[116:119], a[212:215], v[164:167], v[116:119]\n"
"v_mfma_f32_16x16x32_bf16 v[116:119], a[216:219], v[168:171], v[116:119]\n"
"s_add_u32 s61, s65, 4096\n"
"ds_read_b64_tr_b16 a[200:201], v216 offset:55296\n"
"ds_read_b64_tr_b16 a[202:203], v217 offset:55296\n"
"v_mfma_f32_16x16x32_bf16 v[116:119], a[220:223], v[172:175], v[116:119]\n"
"buffer_store_dwordx4 v[168:171], v232, s[52:55], s61 offen offset:2048 sc1\n"
"v_mfma_f32_16x16x32_bf16 v[112:115], a[220:223], v[156:159], 0\n"
"ds_read_b64_tr_b16 a[204:205], v216 offset:64512\n"
"v_mfma_f32_16x16x32_bf16 v[112:115], a[216:219], v[152:155], v[112:115]\n"
"v_mfma_f32_16x16x32_bf16 v[112:115], a[212:215], v[148:151], v[112:115]\n"
"ds_read_b64_tr_b16 a[206:207], v217 offset:64512\n"
"v_mfma_f32_16x16x32_bf16 v[112:115], a[208:211], v[144:147], v[112:115]\n"
"v_mfma_f32_16x16x32_bf16 v[128:131], a[224:227], v[144:147], 0\n"
"ds_read_b64_tr_b16 a[208:209], v216 offset:36896\n"
"v_mfma_f32_16x16x32_bf16 v[128:131], a[228:231], v[148:151], v[128:131]\n"
"v_mfma_f32_16x16x32_bf16 v[128:131], a[232:235], v[152:155], v[128:131]\n"
"ds_read_b64_tr_b16 a[210:211], v217 offset:36896\n"
"v_mfma_f32_16x16x32_bf16 v[128:131], a[236:239], v[156:159], v[128:131]\n"
"v_mfma_f32_16x16x32_bf16 v[132:135], a[236:239], v[172:175], 0\n"
"ds_read_b64_tr_b16 a[212:213], v216 offset:46112\n"
"v_mfma_f32_16x16x32_bf16 v[132:135], a[232:235], v[168:171], v[132:135]\n"
"v_mfma_f32_16x16x32_bf16 v[132:135], a[228:231], v[164:167], v[132:135]\n"
"ds_read_b64_tr_b16 a[214:215], v217 offset:46112\n"
"v_mfma_f32_16x16x32_bf16 v[132:135], a[224:227], v[160:163], v[132:135]\n"
"v_mfma_f32_16x16x32_bf16 v[136:139], a[224:227], v[176:179], 0\n"
"ds_read_b64_tr_b16 a[216:217], v216 offset:55328\n"
"v_mfma_f32_16x16x32_bf16 v[136:139], a[228:231], v[180:183], v[136:139]\n"
"v_mfma_f32_16x16x32_bf16 v[136:139], a[232:235], v[184:187], v[136:139]\n"
"ds_read_b64_tr_b16 a[218:219], v217 offset:55328\n"
"v_mfma_f32_16x16x32_bf16 v[136:139], a[236:239], v[188:191], v[136:139]\n"
"v_mfma_f32_16x16x32_bf16 v[140:143], a[236:239], v[204:207], 0\n"
"s_add_u32 s61, s65, 8192\n"
"ds_read_b64_tr_b16 a[220:221], v216 offset:64544\n"
"v_mfma_f32_16x16x32_bf16 v[140:143], a[232:235], v[200:203], v[140:143]\n"
"buffer_store_dwordx4 v[184:187], v232, s[52:55], s61 offen offset:2048 sc1\n"
"v_mfma_f32_16x16x32_bf16 v[140:143], a[228:231], v[196:199], v[140:143]\n"
"ds_read_b64_tr_b16 a[222:223], v217 offset:64544\n"
"v_mfma_f32_16x16x32_bf16 v[140:143], a[224:227], v[192:195], v[140:143]\n"
"s_waitcnt vmcnt(30) lgkmcnt(8)\n"
"v_mfma_f32_16x16x32_bf16 v[12:15], a[192:195], a[176:179], v[12:15]\n"
"ds_read_b64_tr_b16 a[224:225], v216 offset:36928\n"
"v_mfma_f32_16x16x32_bf16 v[12:15], a[196:199], a[180:183], v[12:15]\n"
"v_mfma_f32_16x16x32_bf16 v[12:15], a[200:203], a[184:187], v[12:15]\n"
"ds_read_b64_tr_b16 a[226:227], v217 offset:36928\n"
"v_mfma_f32_16x16x32_bf16 v[12:15], a[204:207], a[188:191], v[12:15]\n"
"v_mfma_f32_16x16x32_bf16 v[8:11], a[204:207], a[172:175], v[8:11]\n"
"ds_read_b64_tr_b16 a[228:229], v216 offset:46144\n"
"v_mfma_f32_16x16x32_bf16 v[8:11], a[200:203], a[168:171], v[8:11]\n"
"v_mfma_f32_16x16x32_bf16 v[8:11], a[196:199], a[164:167], v[8:11]\n"
"ds_read_b64_tr_b16 a[230:231], v217 offset:46144\n"
"v_mfma_f32_16x16x32_bf16 v[8:11], a[192:195], a[160:163], v[8:11]\n"
"v_mfma_f32_16x16x32_bf16 v[4:7], a[192:195], a[144:147], v[4:7]\n"
"ds_read_b64_tr_b16 a[232:233], v216 offset:55360\n"
"v_mfma_f32_16x16x32_bf16 v[4:7], a[196:199], a[148:151], v[4:7]\n"
"v_mfma_f32_16x16x32_bf16 v[4:7], a[200:203], a[152:155], v[4:7]\n"
"ds_read_b64_tr_b16 a[234:235], v217 offset:55360\n"
"v_mfma_f32_16x16x32_bf16 v[4:7], a[204:207], a[156:159], v[4:7]\n"
"v_mfma_f32_16x16x32_bf16 v[0:3], a[204:207], a[140:143], v[0:3]\n"
"ds_read_b64_tr_b16 a[236:237], v216 offset:64576\n"
"v_mfma_f32_16x16x32_bf16 v[0:3], a[200:203], a[136:139], v[0:3]\n"
"v_mfma_f32_16x16x32_bf16 v[0:3], a[196:199], a[132:135], v[0:3]\n"
"s_add_u32 s61, s65, 12288\n"
"ds_read_b64_tr_b16 a[238:239], v217 offset:64576\n"
"v_mfma_f32_16x16x32_bf16 v[0:3], a[192:195], a[128:131], v[0:3]\n"
"s_waitcnt lgkmcnt(8)\n"
"buffer_store_dwordx4 v[200:203], v232, s[52:55], s61 offen offset:2048 sc1\n"
"v_mfma_f32_16x16x32_bf16 v[16:19], a[208:211], a[128:131], v[16:19]\n"
"ds_read_b64_tr_b16 a[192:193], v216 offset:36960\n"
"v_mfma_f32_16x16x32_bf16 v[16:19], a[212:215], a[132:135], v[16:19]\n"
"v_mfma_f32_16x16x32_bf16 v[16:19], a[216:219], a[136:139], v[16:19]\n"
"ds_read_b64_tr_b16 a[194:195], v217 offset:36960\n"
"v_mfma_f32_16x16x32_bf16 v[16:19], a[220:223], a[140:143], v[16:19]\n"
"v_mfma_f32_16x16x32_bf16 v[20:23], a[220:223], a[156:159], v[20:23]\n"
"ds_read_b64_tr_b16 a[196:197], v216 offset:46176\n"
"v_mfma_f32_16x16x32_bf16 v[20:23], a[216:219], a[152:155], v[20:23]\n"
"v_mfma_f32_16x16x32_bf16 v[20:23], a[212:215], a[148:151], v[20:23]\n"
"ds_read_b64_tr_b16 a[198:199], v217 offset:46176\n"
"v_mfma_f32_16x16x32_bf16 v[20:23], a[208:211], a[144:147], v[20:23]\n"
"v_mfma_f32_16x16x32_bf16 v[24:27], a[208:211], a[160:163], v[24:27]\n"
"ds_read_b64_tr_b16 a[200:201], v216 offset:55392\n"
"v_mfma_f32_16x16x32_bf16 v[24:27], a[212:215], a[164:167], v[24:27]\n"
"v_mfma_f32_16x16x32_bf16 v[24:27], a[216:219], a[168:171], v[24:27]\n"
"ds_read_b64_tr_b16 a[202:203], v217 offset:55392\n"
"v_mfma_f32_16x16x32_bf16 v[24:27], a[220:223], a[172:175], v[24:27]\n"
"v_mfma_f32_16x16x32_bf16 v[28:31], a[220:223], a[188:191], v[28:31]\n"
"ds_read_b64_tr_b16 a[204:205], v216 offset:64608\n"
"v_mfma_f32_16x16x32_bf16 v[28:31], a[216:219], a[184:187], v[28:31]\n"
"v_mfma_f32_16x16x32_bf16 v[28:31], a[212:215], a[180:183], v[28:31]\n"
"ds_read_b64_tr_b16 a[206:207], v217 offset:64608\n"
"v_mfma_f32_16x16x32_bf16 v[28:31], a[208:211], a[176:179], v[28:31]\n"
"s_waitcnt lgkmcnt(8)\n"
"v_mfma_f32_16x16x32_bf16 v[44:47], a[224:227], a[176:179], v[44:47]\n"
"s_add_u32 s61, s65, 0\n"
"ds_read_b64_tr_b16 a[208:209], v216 offset:36992\n"
"v_mfma_f32_16x16x32_bf16 v[44:47], a[228:231], a[180:183], v[44:47]\n"
"buffer_store_dwordx4 v[156:159], v232, s[52:55], s61 offen offset:3072 sc1\n"
"v_mfma_f32_16x16x32_bf16 v[44:47], a[232:235], a[184:187], v[44:47]\n"
"ds_read_b64_tr_b16 a[210:211], v217 offset:36992\n"
"v_mfma_f32_16x16x32_bf16 v[44:47], a[236:239], a[188:191], v[44:47]\n"
"v_mfma_f32_16x16x32_bf16 v[40:43], a[236:239], a[172:175], v[40:43]\n"
"ds_read_b64_tr_b16 a[212:213], v216 offset:46208\n"
"v_mfma_f32_16x16x32_bf16 v[40:43], a[232:235], a[168:171], v[40:43]\n"
"v_mfma_f32_16x16x32_bf16 v[40:43], a[228:231], a[164:167], v[40:43]\n"
"ds_read_b64_tr_b16 a[214:215], v217 offset:46208\n"
"v_mfma_f32_16x16x32_bf16 v[40:43], a[224:227], a[160:163], v[40:43]\n"
"v_mfma_f32_16x16x32_bf16 v[36:39], a[224:227], a[144:147], v[36:39]\n"
"ds_read_b64_tr_b16 a[216:217], v216 offset:55424\n"
"v_mfma_f32_16x16x32_bf16 v[36:39], a[228:231], a[148:151], v[36:39]\n"
"v_mfma_f32_16x16x32_bf16 v[36:39], a[232:235], a[152:155], v[36:39]\n"
"ds_read_b64_tr_b16 a[218:219], v217 offset:55424\n"
"v_mfma_f32_16x16x32_bf16 v[36:39], a[236:239], a[156:159], v[36:39]\n"
"v_mfma_f32_16x16x32_bf16 v[32:35], a[236:239], a[140:143], v[32:35]\n"
"ds_read_b64_tr_b16 a[220:221], v216 offset:64640\n"
"v_mfma_f32_16x16x32_bf16 v[32:35], a[232:235], a[136:139], v[32:35]\n"
"v_mfma_f32_16x16x32_bf16 v[32:35], a[228:231], a[132:135], v[32:35]\n"
"ds_read_b64_tr_b16 a[222:223], v217 offset:64640\n"
"v_mfma_f32_16x16x32_bf16 v[32:35], a[224:227], a[128:131], v[32:35]\n"
"s_waitcnt lgkmcnt(8)\n"
"v_mfma_f32_16x16x32_bf16 v[48:51], a[192:195], a[128:131], v[48:51]\n"
"ds_read_b64_tr_b16 a[224:225], v216 offset:37024\n"
"v_mfma_f32_16x16x32_bf16 v[48:51], a[196:199], a[132:135], v[48:51]\n"
"v_mfma_f32_16x16x32_bf16 v[48:51], a[200:203], a[136:139], v[48:51]\n"
"s_add_u32 s61, s65, 4096\n"
"ds_read_b64_tr_b16 a[226:227], v217 offset:37024\n"
"v_mfma_f32_16x16x32_bf16 v[48:51], a[204:207], a[140:143], v[48:51]\n"
"buffer_store_dwordx4 v[172:175], v232, s[52:55], s61 offen offset:3072 sc1\n"
"v_mfma_f32_16x16x32_bf16 v[52:55], a[204:207], a[156:159], v[52:55]\n"
"ds_read_b64_tr_b16 a[228:229], v216 offset:46240\n"
"v_mfma_f32_16x16x32_bf16 v[52:55], a[200:203], a[152:155], v[52:55]\n"
"v_mfma_f32_16x16x32_bf16 v[52:55], a[196:199], a[148:151], v[52:55]\n"
"ds_read_b64_tr_b16 a[230:231], v217 offset:46240\n"
"v_mfma_f32_16x16x32_bf16 v[52:55], a[192:195], a[144:147], v[52:55]\n"
"v_mfma_f32_16x16x32_bf16 v[56:59], a[192:195], a[160:163], v[56:59]\n"
"s_mov_b32 s72, 6\n"
"s_mul_i32 s58, s72, 147456\n"
"s_add_u32 m0, s60, 110592\n"
"ds_read_b64_tr_b16 a[232:233], v216 offset:55456\n"
"v_mfma_f32_16x16x32_bf16 v[56:59], a[196:199], a[164:167], v[56:59]\n"
"buffer_load_dwordx4 v221, s[92:95], s58 offen lds\n"
"v_mfma_f32_16x16x32_bf16 v[56:59], a[200:203], a[168:171], v[56:59]\n"
"ds_read_b64_tr_b16 a[234:235], v217 offset:55456\n"
"v_mfma_f32_16x16x32_bf16 v[56:59], a[204:207], a[172:175], v[56:59]\n"
"v_mfma_f32_16x16x32_bf16 v[60:63], a[204:207], a[188:191], v[60:63]\n"
"ds_read_b64_tr_b16 a[236:237], v216 offset:64672\n"
"v_mfma_f32_16x16x32_bf16 v[60:63], a[200:203], a[184:187], v[60:63]\n"
"buffer_load_dwordx4 v225, s[92:95], s58 offen offset:1024 lds\n"
"v_mfma_f32_16x16x32_bf16 v[60:63], a[196:199], a[180:183], v[60:63]\n"
"ds_read_b64_tr_b16 a[238:239], v217 offset:64672\n"
"v_mfma_f32_16x16x32_bf16 v[60:63], a[192:195], a[176:179], v[60:63]\n"
"s_waitcnt lgkmcnt(8)\n"
"v_mfma_f32_16x16x32_bf16 v[76:79], a[208:211], a[176:179], v[76:79]\n"
"ds_read_b64_tr_b16 a[192:193], v216 offset:37056\n"
"v_mfma_f32_16x16x32_bf16 v[76:79], a[212:215], a[180:183], v[76:79]\n"
"v_mfma_f32_16x16x32_bf16 v[76:79], a[216:219], a[184:187], v[76:79]\n"
"ds_read_b64_tr_b16 a[194:195], v217 offset:37056\n"
"v_mfma_f32_16x16x32_bf16 v[76:79], a[220:223], a[188:191], v[76:79]\n"
"buffer_load_dwordx4 v226, s[92:95], s58 offen offset:2048 lds\n"
"v_mfma_f32_16x16x32_bf16 v[72:75], a[220:223], a[172:175], v[72:75]\n"
"s_add_u32 s61, s65, 8192\n"
"ds_read_b64_tr_b16 a[196:197], v216 offset:46272\n"
"v_mfma_f32_16x16x32_bf16 v[72:75], a[216:219], a[168:171], v[72:75]\n"
"buffer_store_dwordx4 v[188:191], v232, s[52:55], s61 offen offset:3072 sc1\n"
"v_mfma_f32_16x16x32_bf16 v[72:75], a[212:215], a[164:167], v[72:75]\n"
"ds_read_b64_tr_b16 a[198:199], v217 offset:46272\n"
"v_mfma_f32_16x16x32_bf16 v[72:75], a[208:211], a[160:163], v[72:75]\n"
"v_mfma_f32_16x16x32_bf16 v[68:71], a[208:211], a[144:147], v[68:71]\n"
"ds_read_b64_tr_b16 a[200:201], v216 offset:55488\n"
"v_mfma_f32_16x16x32_bf16 v[68:71], a[212:215], a[148:151], v[68:71]\n"
"buffer_load_dwordx4 v227, s[92:95], s58 offen offset:3072 lds\n"
"v_mfma_f32_16x16x32_bf16 v[68:71], a[216:219], a[152:155], v[68:71]\n"
"ds_read_b64_tr_b16 a[202:203], v217 offset:55488\n"
"v_mfma_f32_16x16x32_bf16 v[68:71], a[220:223], a[156:159], v[68:71]\n"
"v_mfma_f32_16x16x32_bf16 v[64:67], a[220:223], a[140:143], v[64:67]\n"
"ds_read_b64_tr_b16 a[204:205], v216 offset:64704\n"
"v_mfma_f32_16x16x32_bf16 v[64:67], a[216:219], a[136:139], v[64:67]\n"
"v_mfma_f32_16x16x32_bf16 v[64:67], a[212:215], a[132:135], v[64:67]\n"
"s_add_u32 m0, s60, 114688\n"
"ds_read_b64_tr_b16 a[206:207], v217 offset:64704\n"
"v_mfma_f32_16x16x32_bf16 v[64:67], a[208:211], a[128:131], v[64:67]\n"
"s_waitcnt lgkmcnt(8)\n"
"buffer_load_dwordx4 v228, s[92:95], s58 offen lds\n"
"v_mfma_f32_16x16x32_bf16 v[80:83], a[224:227], a[128:131], v[80:83]\n"
"ds_read_b64_tr_b16 a[208:209], v216 offset:37088\n"
"v_mfma_f32_16x16x32_bf16 v[80:83], a[228:231], a[132:135], v[80:83]\n"
"v_mfma_f32_16x16x32_bf16 v[80:83], a[232:235], a[136:139], v[80:83]\n"
"ds_read_b64_tr_b16 a[210:211], v217 offset:37088\n"
"v_mfma_f32_16x16x32_bf16 v[80:83], a[236:239], a[140:143], v[80:83]\n"
"buffer_load_dwordx4 v229, s[92:95], s58 offen offset:1024 lds\n"
"v_mfma_f32_16x16x32_bf16 v[84:87], a[236:239], a[156:159], v[84:87]\n"
"ds_read_b64_tr_b16 a[212:213], v216 offset:46304\n"
"v_mfma_f32_16x16x32_bf16 v[84:87], a[232:235], a[152:155], v[84:87]\n"
"v_mfma_f32_16x16x32_bf16 v[84:87], a[228:231], a[148:151], v[84:87]\n"
"s_add_u32 s61, s65, 12288\n"
"ds_read_b64_tr_b16 a[214:215], v217 offset:46304\n"
"v_mfma_f32_16x16x32_bf16 v[84:87], a[224:227], a[144:147], v[84:87]\n"
"buffer_store_dwordx4 v[204:207], v232, s[52:55], s61 offen offset:3072 sc1\n"
"v_mfma_f32_16x16x32_bf16 v[88:91], a[224:227], a[160:163], v[88:91]\n"
"ds_read_b64_tr_b16 a[216:217], v216 offset:55520\n"
"v_mfma_f32_16x16x32_bf16 v[88:91], a[228:231], a[164:167], v[88:91]\n"
"buffer_load_dwordx4 v230, s[92:95], s58 offen offset:2048 lds\n"
"v_mfma_f32_16x16x32_bf16 v[88:91], a[232:235], a[168:171], v[88:91]\n"
"ds_read_b64_tr_b16 a[218:219], v217 offset:55520\n"
"v_mfma_f32_16x16x32_bf16 v[88:91], a[236:239], a[172:175], v[88:91]\n"
"v_mfma_f32_16x16x32_bf16 v[92:95], a[236:239], a[188:191], v[92:95]\n"
"ds_read_b64_tr_b16 a[220:221], v216 offset:64736\n"
"v_mfma_f32_16x16x32_bf16 v[92:95], a[232:235], a[184:187], v[92:95]\n"
"v_mfma_f32_16x16x32_bf16 v[92:95], a[228:231], a[180:183], v[92:95]\n"
"ds_read_b64_tr_b16 a[222:223], v217 offset:64736\n"
"v_mfma_f32_16x16x32_bf16 v[92:95], a[224:227], a[176:179], v[92:95]\n"
"s_waitcnt lgkmcnt(8)\n"
"buffer_load_dwordx4 v231, s[92:95], s58 offen offset:3072 lds\n"
"v_mfma_f32_16x16x32_bf16 v[108:111], a[192:195], a[176:179], v[108:111]\n"
"ds_read_b64_tr_b16 a[224:225], v216 offset:37120\n"
"v_mfma_f32_16x16x32_bf16 v[108:111], a[196:199], a[180:183], v[108:111]\n"
"v_mfma_f32_16x16x32_bf16 v[108:111], a[200:203], a[184:187], v[108:111]\n"
"ds_read_b64_tr_b16 a[226:227], v217 offset:37120\n"
"v_mfma_f32_16x16x32_bf16 v[108:111], a[204:207], a[188:191], v[108:111]\n"
"v_mfma_f32_16x16x32_bf16 v[104:107], a[204:207], a[172:175], v[104:107]\n"
"s_add_u32 m0, s60, 118784\n"
"ds_read_b64_tr_b16 a[228:229], v216 offset:46336\n"
"v_mfma_f32_16x16x32_bf16 v[104:107], a[200:203], a[168:171], v[104:107]\n"
"buffer_load_dwordx4 v235, s[92:95], s58 offen lds\n"
"v_mfma_f32_16x16x32_bf16 v[104:107], a[196:199], a[164:167], v[104:107]\n"
"ds_read_b64_tr_b16 a[230:231], v217 offset:46336\n"
"v_mfma_f32_16x16x32_bf16 v[104:107], a[192:195], a[160:163], v[104:107]\n"
"v_mfma_f32_16x16x32_bf16 v[100:103], a[192:195], a[144:147], v[100:103]\n"
"s_mov_b32 s72, 6\n"
"s_mul_i32 s72, s72, 65536\n"
"s_add_u32 s77, s76, s72\n"
"s_add_u32 s66, s77, 0\n"
"ds_read_b64_tr_b16 a[232:233], v216 offset:55552\n"
"v_mfma_f32_16x16x32_bf16 v[100:103], a[196:199], a[148:151], v[100:103]\n"
"buffer_load_dwordx4 a[64:67], v232, s[52:55], s66 offen sc1\n"
"v_mfma_f32_16x16x32_bf16 v[100:103], a[200:203], a[152:155], v[100:103]\n"
"ds_read_b64_tr_b16 a[234:235], v217 offset:55552\n"
"v_mfma_f32_16x16x32_bf16 v[100:103], a[204:207], a[156:159], v[100:103]\n"
"v_mfma_f32_16x16x32_bf16 v[96:99], a[204:207], a[140:143], v[96:99]\n"
"ds_read_b64_tr_b16 a[236:237], v216 offset:64768\n"
"v_mfma_f32_16x16x32_bf16 v[96:99], a[200:203], a[136:139], v[96:99]\n"
"v_mfma_f32_16x16x32_bf16 v[96:99], a[196:199], a[132:135], v[96:99]\n"
"ds_read_b64_tr_b16 a[238:239], v217 offset:64768\n"
"v_mfma_f32_16x16x32_bf16 v[96:99], a[192:195], a[128:131], v[96:99]\n"
"s_waitcnt lgkmcnt(8)\n"
"buffer_load_dwordx4 a[68:71], v232, s[52:55], s66 offen offset:1024 sc1\n"
"v_mfma_f32_16x16x32_bf16 v[112:115], a[208:211], a[128:131], v[112:115]\n"
"v_mfma_f32_16x16x32_bf16 v[112:115], a[212:215], a[132:135], v[112:115]\n"
"v_mfma_f32_16x16x32_bf16 v[112:115], a[216:219], a[136:139], v[112:115]\n"
"v_mfma_f32_16x16x32_bf16 v[112:115], a[220:223], a[140:143], v[112:115]\n"
"s_waitcnt vmcnt(43) lgkmcnt(0)\n"
"v_mfma_f32_16x16x32_bf16 v[116:119], a[220:223], a[156:159], v[116:119]\n"
"s_barrier\n"
"v_mfma_f32_16x16x32_bf16 v[116:119], a[216:219], a[152:155], v[116:119]\n"
"buffer_load_dwordx4 a[72:75], v232, s[52:55], s66 offen offset:2048 sc1\n"
"v_mfma_f32_16x16x32_bf16 v[116:119], a[212:215], a[148:151], v[116:119]\n"
"ds_read_b64_tr_b16 a[192:193], v218\n"
"ds_read_b64_tr_b16 a[194:195], v219\n"
"v_mfma_f32_16x16x32_bf16 v[116:119], a[208:211], a[144:147], v[116:119]\n"
"v_mfma_f32_16x16x32_bf16 v[120:123], a[208:211], a[160:163], v[120:123]\n"
"ds_read_b64_tr_b16 a[196:197], v218 offset:9216\n"
"ds_read_b64_tr_b16 a[198:199], v219 offset:9216\n"
"v_mfma_f32_16x16x32_bf16 v[120:123], a[212:215], a[164:167], v[120:123]\n"
"v_mfma_f32_16x16x32_bf16 v[120:123], a[216:219], a[168:171], v[120:123]\n"
"ds_read_b64_tr_b16 a[200:201], v218 offset:18432\n"
"ds_read_b64_tr_b16 a[202:203], v219 offset:18432\n"
"v_mfma_f32_16x16x32_bf16 v[120:123], a[220:223], a[172:175], v[120:123]\n"
"buffer_load_dwordx4 a[76:79], v232, s[52:55], s66 offen offset:3072 sc1\n"
"v_mfma_f32_16x16x32_bf16 v[124:127], a[220:223], a[188:191], v[124:127]\n"
"ds_read_b64_tr_b16 a[204:205], v218 offset:27648\n"
"v_mfma_f32_16x16x32_bf16 v[124:127], a[216:219], a[184:187], v[124:127]\n"
"v_mfma_f32_16x16x32_bf16 v[124:127], a[212:215], a[180:183], v[124:127]\n"
"s_add_u32 s66, s77, 4096\n"
"ds_read_b64_tr_b16 a[206:207], v219 offset:27648\n"
"v_mfma_f32_16x16x32_bf16 v[124:127], a[208:211], a[176:179], v[124:127]\n"
"buffer_load_dwordx4 a[80:83], v232, s[52:55], s66 offen sc1\n"
"v_mfma_f32_16x16x32_bf16 v[140:143], a[224:227], a[176:179], v[140:143]\n"
"ds_read_b64_tr_b16 a[208:209], v218 offset:32\n"
"v_mfma_f32_16x16x32_bf16 v[140:143], a[228:231], a[180:183], v[140:143]\n"
"v_mfma_f32_16x16x32_bf16 v[140:143], a[232:235], a[184:187], v[140:143]\n"
"ds_read_b64_tr_b16 a[210:211], v219 offset:32\n"
"v_mfma_f32_16x16x32_bf16 v[140:143], a[236:239], a[188:191], v[140:143]\n"
"v_mfma_f32_16x16x32_bf16 v[136:139], a[236:239], a[172:175], v[136:139]\n"
"ds_read_b64_tr_b16 a[212:213], v218 offset:9248\n"
"v_mfma_f32_16x16x32_bf16 v[136:139], a[232:235], a[168:171], v[136:139]\n"
"buffer_load_dwordx4 a[84:87], v232, s[52:55], s66 offen offset:1024 sc1\n"
"v_mfma_f32_16x16x32_bf16 v[136:139], a[228:231], a[164:167], v[136:139]\n"
"ds_read_b64_tr_b16 a[214:215], v219 offset:9248\n"
"v_mfma_f32_16x16x32_bf16 v[136:139], a[224:227], a[160:163], v[136:139]\n"
"v_mfma_f32_16x16x32_bf16 v[132:135], a[224:227], a[144:147], v[132:135]\n"
"ds_read_b64_tr_b16 a[216:217], v218 offset:18464\n"
"v_mfma_f32_16x16x32_bf16 v[132:135], a[228:231], a[148:151], v[132:135]\n"
"v_mfma_f32_16x16x32_bf16 v[132:135], a[232:235], a[152:155], v[132:135]\n"
"ds_read_b64_tr_b16 a[218:219], v219 offset:18464\n"
"v_mfma_f32_16x16x32_bf16 v[132:135], a[236:239], a[156:159], v[132:135]\n"
"buffer_load_dwordx4 a[88:91], v232, s[52:55], s66 offen offset:2048 sc1\n"
"v_mfma_f32_16x16x32_bf16 v[128:131], a[236:239], a[140:143], v[128:131]\n"
"ds_read_b64_tr_b16 a[220:221], v218 offset:27680\n"
"v_mfma_f32_16x16x32_bf16 v[128:131], a[232:235], a[136:139], v[128:131]\n"
"v_mfma_f32_16x16x32_bf16 v[128:131], a[228:231], a[132:135], v[128:131]\n"
"ds_read_b64_tr_b16 a[222:223], v219 offset:27680\n"
"v_mfma_f32_16x16x32_bf16 v[128:131], a[224:227], a[128:131], v[128:131]\n"
"s_waitcnt vmcnt(27) lgkmcnt(8)\n"
"v_mfma_f32_16x16x32_bf16 v[0:3], a[192:195], a[0:3], v[0:3]\n"
"ds_read_b64_tr_b16 a[224:225], v218 offset:64\n"
"v_mfma_f32_16x16x32_bf16 v[0:3], a[196:199], a[4:7], v[0:3]\n"
"buffer_load_dwordx4 a[92:95], v232, s[52:55], s66 offen offset:3072 sc1\n"
"v_mfma_f32_16x16x32_bf16 v[0:3], a[200:203], a[8:11], v[0:3]\n"
"ds_read_b64_tr_b16 a[226:227], v219 offset:64\n"
"v_mfma_f32_16x16x32_bf16 v[0:3], a[204:207], a[12:15], v[0:3]\n"
"v_mfma_f32_16x16x32_bf16 v[4:7], a[204:207], a[28:31], v[4:7]\n"
"s_add_u32 s66, s77, 8192\n"
"ds_read_b64_tr_b16 a[228:229], v218 offset:9280\n"
"v_mfma_f32_16x16x32_bf16 v[4:7], a[200:203], a[24:27], v[4:7]\n"
"buffer_load_dwordx4 a[96:99], v232, s[52:55], s66 offen sc1\n"
"v_mfma_f32_16x16x32_bf16 v[4:7], a[196:199], a[20:23], v[4:7]\n"
"ds_read_b64_tr_b16 a[230:231], v219 offset:9280\n"
"v_mfma_f32_16x16x32_bf16 v[4:7], a[192:195], a[16:19], v[4:7]\n"
"v_mfma_f32_16x16x32_bf16 v[8:11], a[192:195], a[32:35], v[8:11]\n"
"ds_read_b64_tr_b16 a[232:233], v218 offset:18496\n"
"v_mfma_f32_16x16x32_bf16 v[8:11], a[196:199], a[36:39], v[8:11]\n"
"v_mfma_f32_16x16x32_bf16 v[8:11], a[200:203], a[40:43], v[8:11]\n"
"ds_read_b64_tr_b16 a[234:235], v219 offset:18496\n"
"v_mfma_f32_16x16x32_bf16 v[8:11], a[204:207], a[44:47], v[8:11]\n"
"buffer_load_dwordx4 a[100:103], v232, s[52:55], s66 offen offset:1024 sc1\n"
"v_mfma_f32_16x16x32_bf16 v[12:15], a[204:207], a[60:63], v[12:15]\n"
"ds_read_b64_tr_b16 a[236:237], v218 offset:27712\n"
"v_mfma_f32_16x16x32_bf16 v[12:15], a[200:203], a[56:59], v[12:15]\n"
"v_mfma_f32_16x16x32_bf16 v[12:15], a[196:199], a[52:55], v[12:15]\n"
"ds_read_b64_tr_b16 a[238:239], v219 offset:27712\n"
"v_mfma_f32_16x16x32_bf16 v[12:15], a[192:195], a[48:51], v[12:15]\n"
"s_waitcnt lgkmcnt(8)\n"
"v_mfma_f32_16x16x32_bf16 v[28:31], a[208:211], a[48:51], v[28:31]\n"
"ds_read_b64_tr_b16 a[192:193], v218 offset:96\n"
"v_mfma_f32_16x16x32_bf16 v[28:31], a[212:215], a[52:55], v[28:31]\n"
"buffer_load_dwordx4 a[104:107], v232, s[52:55], s66 offen offset:2048 sc1\n"
"v_mfma_f32_16x16x32_bf16 v[28:31], a[216:219], a[56:59], v[28:31]\n"
"ds_read_b64_tr_b16 a[194:195], v219 offset:96\n"
"v_mfma_f32_16x16x32_bf16 v[28:31], a[220:223], a[60:63], v[28:31]\n"
"v_mfma_f32_16x16x32_bf16 v[24:27], a[220:223], a[44:47], v[24:27]\n"
"ds_read_b64_tr_b16 a[196:197], v218 offset:9312\n"
"v_mfma_f32_16x16x32_bf16 v[24:27], a[216:219], a[40:43], v[24:27]\n"
"v_mfma_f32_16x16x32_bf16 v[24:27], a[212:215], a[36:39], v[24:27]\n"
"ds_read_b64_tr_b16 a[198:199], v219 offset:9312\n"
"v_mfma_f32_16x16x32_bf16 v[24:27], a[208:211], a[32:35], v[24:27]\n"
"buffer_load_dwordx4 a[108:111], v232, s[52:55], s66 offen offset:3072 sc1\n"
"v_mfma_f32_16x16x32_bf16 v[20:23], a[208:211], a[16:19], v[20:23]\n"
"ds_read_b64_tr_b16 a[200:201], v218 offset:18528\n"
"v_mfma_f32_16x16x32_bf16 v[20:23], a[212:215], a[20:23], v[20:23]\n"
"v_mfma_f32_16x16x32_bf16 v[20:23], a[216:219], a[24:27], v[20:23]\n"
"s_add_u32 s66, s77, 12288\n"
"ds_read_b64_tr_b16 a[202:203], v219 offset:18528\n"
"v_mfma_f32_16x16x32_bf16 v[20:23], a[220:223], a[28:31], v[20:23]\n"
"buffer_load_dwordx4 a[112:115], v232, s[52:55], s66 offen sc1\n"
"v_mfma_f32_16x16x32_bf16 v[16:19], a[220:223], a[12:15], v[16:19]\n"
"ds_read_b64_tr_b16 a[204:205], v218 offset:27744\n"
"v_mfma_f32_16x16x32_bf16 v[16:19], a[216:219], a[8:11], v[16:19]\n"
"v_mfma_f32_16x16x32_bf16 v[16:19], a[212:215], a[4:7], v[16:19]\n"
"ds_read_b64_tr_b16 a[206:207], v219 offset:27744\n"
"v_mfma_f32_16x16x32_bf16 v[16:19], a[208:211], a[0:3], v[16:19]\n"
"s_waitcnt lgkmcnt(8)\n"
"v_mfma_f32_16x16x32_bf16 v[32:35], a[224:227], a[0:3], v[32:35]\n"
"ds_read_b64_tr_b16 a[208:209], v218 offset:128\n"
"v_mfma_f32_16x16x32_bf16 v[32:35], a[228:231], a[4:7], v[32:35]\n"
"buffer_load_dwordx4 a[116:119], v232, s[52:55], s66 offen offset:1024 sc1\n"
"v_mfma_f32_16x16x32_bf16 v[32:35], a[232:235], a[8:11], v[32:35]\n"
"ds_read_b64_tr_b16 a[210:211], v219 offset:128\n"
"v_mfma_f32_16x16x32_bf16 v[32:35], a[236:239], a[12:15], v[32:35]\n"
"v_mfma_f32_16x16x32_bf16 v[36:39], a[236:239], a[28:31], v[36:39]\n"
"ds_read_b64_tr_b16 a[212:213], v218 offset:9344\n"
"v_mfma_f32_16x16x32_bf16 v[36:39], a[232:235], a[24:27], v[36:39]\n"
"v_mfma_f32_16x16x32_bf16 v[36:39], a[228:231], a[20:23], v[36:39]\n"
"ds_read_b64_tr_b16 a[214:215], v219 offset:9344\n"
"v_mfma_f32_16x16x32_bf16 v[36:39], a[224:227], a[16:19], v[36:39]\n"
"buffer_load_dwordx4 a[120:123], v232, s[52:55], s66 offen offset:2048 sc1\n"
"v_mfma_f32_16x16x32_bf16 v[40:43], a[224:227], a[32:35], v[40:43]\n"
"ds_read_b64_tr_b16 a[216:217], v218 offset:18560\n"
"v_mfma_f32_16x16x32_bf16 v[40:43], a[228:231], a[36:39], v[40:43]\n"
"v_mfma_f32_16x16x32_bf16 v[40:43], a[232:235], a[40:43], v[40:43]\n"
"s_waitcnt vmcnt(18)\n"
"ds_read_b64_tr_b16 a[218:219], v219 offset:18560\n"
"v_mfma_f32_16x16x32_bf16 v[40:43], a[236:239], a[44:47], v[40:43]\n"
"buffer_store_dword v247, v222, s[96:99], 0 offen sc1\n"
"v_mfma_f32_16x16x32_bf16 v[44:47], a[236:239], a[60:63], v[44:47]\n"
"ds_read_b64_tr_b16 a[220:221], v218 offset:27776\n"
"v_mfma_f32_16x16x32_bf16 v[44:47], a[232:235], a[56:59], v[44:47]\n"
"buffer_load_dwordx4 a[124:127], v232, s[52:55], s66 offen offset:3072 sc1\n"
"v_mfma_f32_16x16x32_bf16 v[44:47], a[228:231], a[52:55], v[44:47]\n"
"ds_read_b64_tr_b16 a[222:223], v219 offset:27776\n"
"v_mfma_f32_16x16x32_bf16 v[44:47], a[224:227], a[48:51], v[44:47]\n"
"s_waitcnt lgkmcnt(8)\n"
"v_mfma_f32_16x16x32_bf16 v[60:63], a[192:195], a[48:51], v[60:63]\n"
"s_mov_b32 s72, 0\n"
"s_mul_i32 s58, s72, 147456\n"
"s_add_u32 m0, s60, 0\n"
"ds_read_b64_tr_b16 a[224:225], v218 offset:160\n"
"v_mfma_f32_16x16x32_bf16 v[60:63], a[196:199], a[52:55], v[60:63]\n"
"buffer_load_dwordx4 v221, s[92:95], s58 offen lds\n"
"v_mfma_f32_16x16x32_bf16 v[60:63], a[200:203], a[56:59], v[60:63]\n"
"ds_read_b64_tr_b16 a[226:227], v219 offset:160\n"
"v_mfma_f32_16x16x32_bf16 v[60:63], a[204:207], a[60:63], v[60:63]\n"
"v_mfma_f32_16x16x32_bf16 v[56:59], a[204:207], a[44:47], v[56:59]\n"
"ds_read_b64_tr_b16 a[228:229], v218 offset:9376\n"
"v_mfma_f32_16x16x32_bf16 v[56:59], a[200:203], a[40:43], v[56:59]\n"
"v_mfma_f32_16x16x32_bf16 v[56:59], a[196:199], a[36:39], v[56:59]\n"
"ds_read_b64_tr_b16 a[230:231], v219 offset:9376\n"
"v_mfma_f32_16x16x32_bf16 v[56:59], a[192:195], a[32:35], v[56:59]\n"
"buffer_load_dwordx4 v225, s[92:95], s58 offen offset:1024 lds\n"
"v_mfma_f32_16x16x32_bf16 v[52:55], a[192:195], a[16:19], v[52:55]\n"
"ds_read_b64_tr_b16 a[232:233], v218 offset:18592\n"
"v_mfma_f32_16x16x32_bf16 v[52:55], a[196:199], a[20:23], v[52:55]\n"
"v_mfma_f32_16x16x32_bf16 v[52:55], a[200:203], a[24:27], v[52:55]\n"
"ds_read_b64_tr_b16 a[234:235], v219 offset:18592\n"
"v_mfma_f32_16x16x32_bf16 v[52:55], a[204:207], a[28:31], v[52:55]\n"
"v_mfma_f32_16x16x32_bf16 v[48:51], a[204:207], a[12:15], v[48:51]\n"
"ds_read_b64_tr_b16 a[236:237], v218 offset:27808\n"
"v_mfma_f32_16x16x32_bf16 v[48:51], a[200:203], a[8:11], v[48:51]\n"
"buffer_load_dwordx4 v226, s[92:95], s58 offen offset:2048 lds\n"
"v_mfma_f32_16x16x32_bf16 v[48:51], a[196:199], a[4:7], v[48:51]\n"
"ds_read_b64_tr_b16 a[238:239], v219 offset:27808\n"
"v_mfma_f32_16x16x32_bf16 v[48:51], a[192:195], a[0:3], v[48:51]\n"
"s_waitcnt lgkmcnt(8)\n"
"v_mfma_f32_16x16x32_bf16 v[64:67], a[208:211], a[0:3], v[64:67]\n"
"ds_read_b64_tr_b16 a[192:193], v218 offset:192\n"
"v_mfma_f32_16x16x32_bf16 v[64:67], a[212:215], a[4:7], v[64:67]\n"
"v_mfma_f32_16x16x32_bf16 v[64:67], a[216:219], a[8:11], v[64:67]\n"
"ds_read_b64_tr_b16 a[194:195], v219 offset:192\n"
"v_mfma_f32_16x16x32_bf16 v[64:67], a[220:223], a[12:15], v[64:67]\n"
"buffer_load_dwordx4 v227, s[92:95], s58 offen offset:3072 lds\n"
"v_mfma_f32_16x16x32_bf16 v[68:71], a[220:223], a[28:31], v[68:71]\n"
"ds_read_b64_tr_b16 a[196:197], v218 offset:9408\n"
"v_mfma_f32_16x16x32_bf16 v[68:71], a[216:219], a[24:27], v[68:71]\n"
"v_mfma_f32_16x16x32_bf16 v[68:71], a[212:215], a[20:23], v[68:71]\n"
"s_add_u32 m0, s60, 4096\n"
"ds_read_b64_tr_b16 a[198:199], v219 offset:9408\n"
"v_mfma_f32_16x16x32_bf16 v[68:71], a[208:211], a[16:19], v[68:71]\n"
"buffer_load_dwordx4 v228, s[92:95], s58 offen lds\n"
"v_mfma_f32_16x16x32_bf16 v[72:75], a[208:211], a[32:35], v[72:75]\n"
"ds_read_b64_tr_b16 a[200:201], v218 offset:18624\n"
"v_mfma_f32_16x16x32_bf16 v[72:75], a[212:215], a[36:39], v[72:75]\n"
"v_mfma_f32_16x16x32_bf16 v[72:75], a[216:219], a[40:43], v[72:75]\n"
"ds_read_b64_tr_b16 a[202:203], v219 offset:18624\n"
"v_mfma_f32_16x16x32_bf16 v[72:75], a[220:223], a[44:47], v[72:75]\n"
"v_mfma_f32_16x16x32_bf16 v[76:79], a[220:223], a[60:63], v[76:79]\n"
"ds_read_b64_tr_b16 a[204:205], v218 offset:27840\n"
"v_mfma_f32_16x16x32_bf16 v[76:79], a[216:219], a[56:59], v[76:79]\n"
"buffer_load_dwordx4 v229, s[92:95], s58 offen offset:1024 lds\n"
"v_mfma_f32_16x16x32_bf16 v[76:79], a[212:215], a[52:55], v[76:79]\n"
"ds_read_b64_tr_b16 a[206:207], v219 offset:27840\n"
"v_mfma_f32_16x16x32_bf16 v[76:79], a[208:211], a[48:51], v[76:79]\n"
"s_waitcnt lgkmcnt(8)\n"
"v_mfma_f32_16x16x32_bf16 v[92:95], a[224:227], a[48:51], v[92:95]\n"
"ds_read_b64_tr_b16 a[208:209], v218 offset:224\n"
"v_mfma_f32_16x16x32_bf16 v[92:95], a[228:231], a[52:55], v[92:95]\n"
"v_mfma_f32_16x16x32_bf16 v[92:95], a[232:235], a[56:59], v[92:95]\n"
"ds_read_b64_tr_b16 a[210:211], v219 offset:224\n"
"v_mfma_f32_16x16x32_bf16 v[92:95], a[236:239], a[60:63], v[92:95]\n"
"buffer_load_dwordx4 v230, s[92:95], s58 offen offset:2048 lds\n"
"v_mfma_f32_16x16x32_bf16 v[88:91], a[236:239], a[44:47], v[88:91]\n"
"ds_read_b64_tr_b16 a[212:213], v218 offset:9440\n"
"v_mfma_f32_16x16x32_bf16 v[88:91], a[232:235], a[40:43], v[88:91]\n"
"v_mfma_f32_16x16x32_bf16 v[88:91], a[228:231], a[36:39], v[88:91]\n"
"ds_read_b64_tr_b16 a[214:215], v219 offset:9440\n"
"v_mfma_f32_16x16x32_bf16 v[88:91], a[224:227], a[32:35], v[88:91]\n"
"v_mfma_f32_16x16x32_bf16 v[84:87], a[224:227], a[16:19], v[84:87]\n"
"ds_read_b64_tr_b16 a[216:217], v218 offset:18656\n"
"v_mfma_f32_16x16x32_bf16 v[84:87], a[228:231], a[20:23], v[84:87]\n"
"buffer_load_dwordx4 v231, s[92:95], s58 offen offset:3072 lds\n"
"v_mfma_f32_16x16x32_bf16 v[84:87], a[232:235], a[24:27], v[84:87]\n"
"ds_read_b64_tr_b16 a[218:219], v219 offset:18656\n"
"v_mfma_f32_16x16x32_bf16 v[84:87], a[236:239], a[28:31], v[84:87]\n"
"v_mfma_f32_16x16x32_bf16 v[80:83], a[236:239], a[12:15], v[80:83]\n"
"s_add_u32 m0, s60, 8192\n"
"ds_read_b64_tr_b16 a[220:221], v218 offset:27872\n"
"v_mfma_f32_16x16x32_bf16 v[80:83], a[232:235], a[8:11], v[80:83]\n"
"buffer_load_dwordx4 v235, s[92:95], s58 offen lds\n"
"v_mfma_f32_16x16x32_bf16 v[80:83], a[228:231], a[4:7], v[80:83]\n"
"ds_read_b64_tr_b16 a[222:223], v219 offset:27872\n"
"v_mfma_f32_16x16x32_bf16 v[80:83], a[224:227], a[0:3], v[80:83]\n"
"s_waitcnt lgkmcnt(8)\n"
"v_mfma_f32_16x16x32_bf16 v[96:99], a[192:195], a[0:3], v[96:99]\n"
"ds_read_b64_tr_b16 a[224:225], v218 offset:256\n"
"v_mfma_f32_16x16x32_bf16 v[96:99], a[196:199], a[4:7], v[96:99]\n"
"v_mfma_f32_16x16x32_bf16 v[96:99], a[200:203], a[8:11], v[96:99]\n"
"s_mov_b32 s72, 0\n"
"s_mul_i32 s72, s72, 65536\n"
"s_add_u32 s77, s76, s72\n"
"s_add_u32 s66, s77, 0\n"
"ds_read_b64_tr_b16 a[226:227], v219 offset:256\n"
"v_mfma_f32_16x16x32_bf16 v[96:99], a[204:207], a[12:15], v[96:99]\n"
"buffer_load_dwordx4 v[144:147], v232, s[52:55], s66 offen sc1\n"
"v_mfma_f32_16x16x32_bf16 v[100:103], a[204:207], a[28:31], v[100:103]\n"
"ds_read_b64_tr_b16 a[228:229], v218 offset:9472\n"
"v_mfma_f32_16x16x32_bf16 v[100:103], a[200:203], a[24:27], v[100:103]\n"
"v_mfma_f32_16x16x32_bf16 v[100:103], a[196:199], a[20:23], v[100:103]\n"
"ds_read_b64_tr_b16 a[230:231], v219 offset:9472\n"
"v_mfma_f32_16x16x32_bf16 v[100:103], a[192:195], a[16:19], v[100:103]\n"
"v_mfma_f32_16x16x32_bf16 v[104:107], a[192:195], a[32:35], v[104:107]\n"
"ds_read_b64_tr_b16 a[232:233], v218 offset:18688\n"
"v_mfma_f32_16x16x32_bf16 v[104:107], a[196:199], a[36:39], v[104:107]\n"
"buffer_load_dwordx4 v[148:151], v232, s[52:55], s66 offen offset:1024 sc1\n"
"v_mfma_f32_16x16x32_bf16 v[104:107], a[200:203], a[40:43], v[104:107]\n"
"ds_read_b64_tr_b16 a[234:235], v219 offset:18688\n"
"v_mfma_f32_16x16x32_bf16 v[104:107], a[204:207], a[44:47], v[104:107]\n"
"v_mfma_f32_16x16x32_bf16 v[108:111], a[204:207], a[60:63], v[108:111]\n"
"ds_read_b64_tr_b16 a[236:237], v218 offset:27904\n"
"v_mfma_f32_16x16x32_bf16 v[108:111], a[200:203], a[56:59], v[108:111]\n"
"v_mfma_f32_16x16x32_bf16 v[108:111], a[196:199], a[52:55], v[108:111]\n"
"ds_read_b64_tr_b16 a[238:239], v219 offset:27904\n"
"v_mfma_f32_16x16x32_bf16 v[108:111], a[192:195], a[48:51], v[108:111]\n"
"s_waitcnt lgkmcnt(8)\n"
"buffer_load_dwordx4 v[152:155], v232, s[52:55], s66 offen offset:2048 sc1\n"
"v_mfma_f32_16x16x32_bf16 v[124:127], a[208:211], a[48:51], v[124:127]\n"
"v_mfma_f32_16x16x32_bf16 v[124:127], a[212:215], a[52:55], v[124:127]\n"
"v_mfma_f32_16x16x32_bf16 v[124:127], a[216:219], a[56:59], v[124:127]\n"
"v_mfma_f32_16x16x32_bf16 v[124:127], a[220:223], a[60:63], v[124:127]\n"
"s_waitcnt vmcnt(29) lgkmcnt(0)\n"
"buffer_load_dwordx4 v[156:159], v232, s[52:55], s66 offen offset:3072 sc1\n"
"v_mfma_f32_16x16x32_bf16 v[120:123], a[220:223], a[44:47], v[120:123]\n"
"s_barrier\n"
"v_mfma_f32_16x16x32_bf16 v[120:123], a[216:219], a[40:43], v[120:123]\n"
"v_mfma_f32_16x16x32_bf16 v[120:123], a[212:215], a[36:39], v[120:123]\n"
"ds_read_b64_tr_b16 a[192:193], v218 offset:36864\n"
"ds_read_b64_tr_b16 a[194:195], v219 offset:36864\n"
"v_mfma_f32_16x16x32_bf16 v[120:123], a[208:211], a[32:35], v[120:123]\n"
"v_mfma_f32_16x16x32_bf16 v[116:119], a[208:211], a[16:19], v[116:119]\n"
"s_add_u32 s66, s77, 4096\n"
"ds_read_b64_tr_b16 a[196:197], v218 offset:46080\n"
"ds_read_b64_tr_b16 a[198:199], v219 offset:46080\n"
"v_mfma_f32_16x16x32_bf16 v[116:119], a[212:215], a[20:23], v[116:119]\n"
"buffer_load_dwordx4 v[160:163], v232, s[52:55], s66 offen sc1\n"
"v_mfma_f32_16x16x32_bf16 v[116:119], a[216:219], a[24:27], v[116:119]\n"
"ds_read_b64_tr_b16 a[200:201], v218 offset:55296\n"
"ds_read_b64_tr_b16 a[202:203], v219 offset:55296\n"
"v_mfma_f32_16x16x32_bf16 v[116:119], a[220:223], a[28:31], v[116:119]\n"
"v_mfma_f32_16x16x32_bf16 v[112:115], a[220:223], a[12:15], v[112:115]\n"
"ds_read_b64_tr_b16 a[204:205], v218 offset:64512\n"
"v_mfma_f32_16x16x32_bf16 v[112:115], a[216:219], a[8:11], v[112:115]\n"
"v_mfma_f32_16x16x32_bf16 v[112:115], a[212:215], a[4:7], v[112:115]\n"
"ds_read_b64_tr_b16 a[206:207], v219 offset:64512\n"
"v_mfma_f32_16x16x32_bf16 v[112:115], a[208:211], a[0:3], v[112:115]\n"
"buffer_load_dwordx4 v[164:167], v232, s[52:55], s66 offen offset:1024 sc1\n"
"v_mfma_f32_16x16x32_bf16 v[128:131], a[224:227], a[0:3], v[128:131]\n"
"ds_read_b64_tr_b16 a[208:209], v218 offset:36896\n"
"v_mfma_f32_16x16x32_bf16 v[128:131], a[228:231], a[4:7], v[128:131]\n"
"v_mfma_f32_16x16x32_bf16 v[128:131], a[232:235], a[8:11], v[128:131]\n"
"ds_read_b64_tr_b16 a[210:211], v219 offset:36896\n"
"v_mfma_f32_16x16x32_bf16 v[128:131], a[236:239], a[12:15], v[128:131]\n"
"v_mfma_f32_16x16x32_bf16 v[132:135], a[236:239], a[28:31], v[132:135]\n"
"ds_read_b64_tr_b16 a[212:213], v218 offset:46112\n"
"v_mfma_f32_16x16x32_bf16 v[132:135], a[232:235], a[24:27], v[132:135]\n"
"buffer_load_dwordx4 v[168:171], v232, s[52:55], s66 offen offset:2048 sc1\n"
"v_mfma_f32_16x16x32_bf16 v[132:135], a[228:231], a[20:23], v[132:135]\n"
"ds_read_b64_tr_b16 a[214:215], v219 offset:46112\n"
"v_mfma_f32_16x16x32_bf16 v[132:135], a[224:227], a[16:19], v[132:135]\n"
"v_mfma_f32_16x16x32_bf16 v[136:139], a[224:227], a[32:35], v[136:139]\n"
"ds_read_b64_tr_b16 a[216:217], v218 offset:55328\n"
"v_mfma_f32_16x16x32_bf16 v[136:139], a[228:231], a[36:39], v[136:139]\n"
"buffer_load_dwordx4 v[172:175], v232, s[52:55], s66 offen offset:3072 sc1\n"
"v_mfma_f32_16x16x32_bf16 v[136:139], a[232:235], a[40:43], v[136:139]\n"
"ds_read_b64_tr_b16 a[218:219], v219 offset:55328\n"
"v_mfma_f32_16x16x32_bf16 v[136:139], a[236:239], a[44:47], v[136:139]\n"
"v_mfma_f32_16x16x32_bf16 v[140:143], a[236:239], a[60:63], v[140:143]\n"
"ds_read_b64_tr_b16 a[220:221], v218 offset:64544\n"
"v_mfma_f32_16x16x32_bf16 v[140:143], a[232:235], a[56:59], v[140:143]\n"
"v_mfma_f32_16x16x32_bf16 v[140:143], a[228:231], a[52:55], v[140:143]\n"
"s_add_u32 s66, s77, 8192\n"
"ds_read_b64_tr_b16 a[222:223], v219 offset:64544\n"
"v_mfma_f32_16x16x32_bf16 v[140:143], a[224:227], a[48:51], v[140:143]\n"
"s_waitcnt vmcnt(17) lgkmcnt(8)\n"
"buffer_load_dwordx4 v[176:179], v232, s[52:55], s66 offen sc1\n"
"v_mfma_f32_16x16x32_bf16 v[12:15], a[192:195], a[112:115], v[12:15]\n"
"ds_read_b64_tr_b16 a[224:225], v218 offset:36928\n"
"v_mfma_f32_16x16x32_bf16 v[12:15], a[196:199], a[116:119], v[12:15]\n"
"v_mfma_f32_16x16x32_bf16 v[12:15], a[200:203], a[120:123], v[12:15]\n"
"ds_read_b64_tr_b16 a[226:227], v219 offset:36928\n"
"v_mfma_f32_16x16x32_bf16 v[12:15], a[204:207], a[124:127], v[12:15]\n"
"v_mfma_f32_16x16x32_bf16 v[8:11], a[204:207], a[108:111], v[8:11]\n"
"ds_read_b64_tr_b16 a[228:229], v218 offset:46144\n"
"v_mfma_f32_16x16x32_bf16 v[8:11], a[200:203], a[104:107], v[8:11]\n"
"buffer_load_dwordx4 v[180:183], v232, s[52:55], s66 offen offset:1024 sc1\n"
"v_mfma_f32_16x16x32_bf16 v[8:11], a[196:199], a[100:103], v[8:11]\n"
"ds_read_b64_tr_b16 a[230:231], v219 offset:46144\n"
"v_mfma_f32_16x16x32_bf16 v[8:11], a[192:195], a[96:99], v[8:11]\n"
"v_mfma_f32_16x16x32_bf16 v[4:7], a[192:195], a[80:83], v[4:7]\n"
"ds_read_b64_tr_b16 a[232:233], v218 offset:55360\n"
"v_mfma_f32_16x16x32_bf16 v[4:7], a[196:199], a[84:87], v[4:7]\n"
"v_mfma_f32_16x16x32_bf16 v[4:7], a[200:203], a[88:91], v[4:7]\n"
"ds_read_b64_tr_b16 a[234:235], v219 offset:55360\n"
"v_mfma_f32_16x16x32_bf16 v[4:7], a[204:207], a[92:95], v[4:7]\n"
"buffer_load_dwordx4 v[184:187], v232, s[52:55], s66 offen offset:2048 sc1\n"
"v_mfma_f32_16x16x32_bf16 v[0:3], a[204:207], a[76:79], v[0:3]\n"
"ds_read_b64_tr_b16 a[236:237], v218 offset:64576\n"
"v_mfma_f32_16x16x32_bf16 v[0:3], a[200:203], a[72:75], v[0:3]\n"
"v_mfma_f32_16x16x32_bf16 v[0:3], a[196:199], a[68:71], v[0:3]\n"
"ds_read_b64_tr_b16 a[238:239], v219 offset:64576\n"
"v_mfma_f32_16x16x32_bf16 v[0:3], a[192:195], a[64:67], v[0:3]\n"
"s_waitcnt lgkmcnt(8)\n"
"buffer_load_dwordx4 v[188:191], v232, s[52:55], s66 offen offset:3072 sc1\n"
"v_mfma_f32_16x16x32_bf16 v[16:19], a[208:211], a[64:67], v[16:19]\n"
"ds_read_b64_tr_b16 a[192:193], v218 offset:36960\n"
"v_mfma_f32_16x16x32_bf16 v[16:19], a[212:215], a[68:71], v[16:19]\n"
"v_mfma_f32_16x16x32_bf16 v[16:19], a[216:219], a[72:75], v[16:19]\n"
"ds_read_b64_tr_b16 a[194:195], v219 offset:36960\n"
"v_mfma_f32_16x16x32_bf16 v[16:19], a[220:223], a[76:79], v[16:19]\n"
"v_mfma_f32_16x16x32_bf16 v[20:23], a[220:223], a[92:95], v[20:23]\n"
"s_add_u32 s66, s77, 12288\n"
"ds_read_b64_tr_b16 a[196:197], v218 offset:46176\n"
"v_mfma_f32_16x16x32_bf16 v[20:23], a[216:219], a[88:91], v[20:23]\n"
"buffer_load_dwordx4 v[192:195], v232, s[52:55], s66 offen sc1\n"
"v_mfma_f32_16x16x32_bf16 v[20:23], a[212:215], a[84:87], v[20:23]\n"
"ds_read_b64_tr_b16 a[198:199], v219 offset:46176\n"
"v_mfma_f32_16x16x32_bf16 v[20:23], a[208:211], a[80:83], v[20:23]\n"
"v_mfma_f32_16x16x32_bf16 v[24:27], a[208:211], a[96:99], v[24:27]\n"
"ds_read_b64_tr_b16 a[200:201], v218 offset:55392\n"
"v_mfma_f32_16x16x32_bf16 v[24:27], a[212:215], a[100:103], v[24:27]\n"
"v_mfma_f32_16x16x32_bf16 v[24:27], a[216:219], a[104:107], v[24:27]\n"
"ds_read_b64_tr_b16 a[202:203], v219 offset:55392\n"
"v_mfma_f32_16x16x32_bf16 v[24:27], a[220:223], a[108:111], v[24:27]\n"
"buffer_load_dwordx4 v[196:199], v232, s[52:55], s66 offen offset:1024 sc1\n"
"v_mfma_f32_16x16x32_bf16 v[28:31], a[220:223], a[124:127], v[28:31]\n"
"ds_read_b64_tr_b16 a[204:205], v218 offset:64608\n"
"v_mfma_f32_16x16x32_bf16 v[28:31], a[216:219], a[120:123], v[28:31]\n"
"v_mfma_f32_16x16x32_bf16 v[28:31], a[212:215], a[116:119], v[28:31]\n"
"ds_read_b64_tr_b16 a[206:207], v219 offset:64608\n"
"v_mfma_f32_16x16x32_bf16 v[28:31], a[208:211], a[112:115], v[28:31]\n"
"s_waitcnt lgkmcnt(8)\n"
"v_mfma_f32_16x16x32_bf16 v[44:47], a[224:227], a[112:115], v[44:47]\n"
"ds_read_b64_tr_b16 a[208:209], v218 offset:36992\n"
"v_mfma_f32_16x16x32_bf16 v[44:47], a[228:231], a[116:119], v[44:47]\n"
"buffer_load_dwordx4 v[200:203], v232, s[52:55], s66 offen offset:2048 sc1\n"
"v_mfma_f32_16x16x32_bf16 v[44:47], a[232:235], a[120:123], v[44:47]\n"
"ds_read_b64_tr_b16 a[210:211], v219 offset:36992\n"
"v_mfma_f32_16x16x32_bf16 v[44:47], a[236:239], a[124:127], v[44:47]\n"
"v_mfma_f32_16x16x32_bf16 v[40:43], a[236:239], a[108:111], v[40:43]\n"
"ds_read_b64_tr_b16 a[212:213], v218 offset:46208\n"
"v_mfma_f32_16x16x32_bf16 v[40:43], a[232:235], a[104:107], v[40:43]\n"
"buffer_load_dwordx4 v[204:207], v232, s[52:55], s66 offen offset:3072 sc1\n"
"v_mfma_f32_16x16x32_bf16 v[40:43], a[228:231], a[100:103], v[40:43]\n"
"ds_read_b64_tr_b16 a[214:215], v219 offset:46208\n"
"v_mfma_f32_16x16x32_bf16 v[40:43], a[224:227], a[96:99], v[40:43]\n"
"v_mfma_f32_16x16x32_bf16 v[36:39], a[224:227], a[80:83], v[36:39]\n"
"ds_read_b64_tr_b16 a[216:217], v218 offset:55424\n"
"v_mfma_f32_16x16x32_bf16 v[36:39], a[228:231], a[84:87], v[36:39]\n"
"v_mfma_f32_16x16x32_bf16 v[36:39], a[232:235], a[88:91], v[36:39]\n"
"s_mov_b32 s72, 8\n"
"s_mul_i32 s58, s72, 147456\n"
"s_add_u32 m0, s60, 36864\n"
"ds_read_b64_tr_b16 a[218:219], v219 offset:55424\n"
"v_mfma_f32_16x16x32_bf16 v[36:39], a[236:239], a[92:95], v[36:39]\n"
"buffer_load_dwordx4 v221, s[92:95], s58 offen lds\n"
"v_mfma_f32_16x16x32_bf16 v[32:35], a[236:239], a[76:79], v[32:35]\n"
"ds_read_b64_tr_b16 a[220:221], v218 offset:64640\n"
"v_mfma_f32_16x16x32_bf16 v[32:35], a[232:235], a[72:75], v[32:35]\n"
"v_mfma_f32_16x16x32_bf16 v[32:35], a[228:231], a[68:71], v[32:35]\n"
"ds_read_b64_tr_b16 a[222:223], v219 offset:64640\n"
"v_mfma_f32_16x16x32_bf16 v[32:35], a[224:227], a[64:67], v[32:35]\n"
"s_waitcnt lgkmcnt(8)\n"
"v_mfma_f32_16x16x32_bf16 v[48:51], a[192:195], a[64:67], v[48:51]\n"
"ds_read_b64_tr_b16 a[224:225], v218 offset:37024\n"
"v_mfma_f32_16x16x32_bf16 v[48:51], a[196:199], a[68:71], v[48:51]\n"
"buffer_load_dwordx4 v225, s[92:95], s58 offen offset:1024 lds\n"
"v_mfma_f32_16x16x32_bf16 v[48:51], a[200:203], a[72:75], v[48:51]\n"
"ds_read_b64_tr_b16 a[226:227], v219 offset:37024\n"
"v_mfma_f32_16x16x32_bf16 v[48:51], a[204:207], a[76:79], v[48:51]\n"
"v_mfma_f32_16x16x32_bf16 v[52:55], a[204:207], a[92:95], v[52:55]\n"
"ds_read_b64_tr_b16 a[228:229], v218 offset:46240\n"
"v_mfma_f32_16x16x32_bf16 v[52:55], a[200:203], a[88:91], v[52:55]\n"
"v_mfma_f32_16x16x32_bf16 v[52:55], a[196:199], a[84:87], v[52:55]\n"
"ds_read_b64_tr_b16 a[230:231], v219 offset:46240\n"
"v_mfma_f32_16x16x32_bf16 v[52:55], a[192:195], a[80:83], v[52:55]\n"
"buffer_load_dwordx4 v226, s[92:95], s58 offen offset:2048 lds\n"
"v_mfma_f32_16x16x32_bf16 v[56:59], a[192:195], a[96:99], v[56:59]\n"
"ds_read_b64_tr_b16 a[232:233], v218 offset:55456\n"
"v_mfma_f32_16x16x32_bf16 v[56:59], a[196:199], a[100:103], v[56:59]\n"
"v_mfma_f32_16x16x32_bf16 v[56:59], a[200:203], a[104:107], v[56:59]\n"
"ds_read_b64_tr_b16 a[234:235], v219 offset:55456\n"
"v_mfma_f32_16x16x32_bf16 v[56:59], a[204:207], a[108:111], v[56:59]\n"
"buffer_load_dwordx4 v227, s[92:95], s58 offen offset:3072 lds\n"
"v_mfma_f32_16x16x32_bf16 v[60:63], a[204:207], a[124:127], v[60:63]\n"
"ds_read_b64_tr_b16 a[236:237], v218 offset:64672\n"
"v_mfma_f32_16x16x32_bf16 v[60:63], a[200:203], a[120:123], v[60:63]\n"
"v_mfma_f32_16x16x32_bf16 v[60:63], a[196:199], a[116:119], v[60:63]\n"
"ds_read_b64_tr_b16 a[238:239], v219 offset:64672\n"
"v_mfma_f32_16x16x32_bf16 v[60:63], a[192:195], a[112:115], v[60:63]\n"
"s_waitcnt lgkmcnt(8)\n"
"v_mfma_f32_16x16x32_bf16 v[76:79], a[208:211], a[112:115], v[76:79]\n"
"s_add_u32 m0, s60, 40960\n"
"ds_read_b64_tr_b16 a[192:193], v218 offset:37056\n"
"v_mfma_f32_16x16x32_bf16 v[76:79], a[212:215], a[116:119], v[76:79]\n"
"buffer_load_dwordx4 v228, s[92:95], s58 offen lds\n"
"v_mfma_f32_16x16x32_bf16 v[76:79], a[216:219], a[120:123], v[76:79]\n"
"ds_read_b64_tr_b16 a[194:195], v219 offset:37056\n"
"v_mfma_f32_16x16x32_bf16 v[76:79], a[220:223], a[124:127], v[76:79]\n"
"v_mfma_f32_16x16x32_bf16 v[72:75], a[220:223], a[108:111], v[72:75]\n"
"ds_read_b64_tr_b16 a[196:197], v218 offset:46272\n"
"v_mfma_f32_16x16x32_bf16 v[72:75], a[216:219], a[104:107], v[72:75]\n"
"v_mfma_f32_16x16x32_bf16 v[72:75], a[212:215], a[100:103], v[72:75]\n"
"ds_read_b64_tr_b16 a[198:199], v219 offset:46272\n"
"v_mfma_f32_16x16x32_bf16 v[72:75], a[208:211], a[96:99], v[72:75]\n"
"buffer_load_dwordx4 v229, s[92:95], s58 offen offset:1024 lds\n"
"v_mfma_f32_16x16x32_bf16 v[68:71], a[208:211], a[80:83], v[68:71]\n"
"ds_read_b64_tr_b16 a[200:201], v218 offset:55488\n"
"v_mfma_f32_16x16x32_bf16 v[68:71], a[212:215], a[84:87], v[68:71]\n"
"v_mfma_f32_16x16x32_bf16 v[68:71], a[216:219], a[88:91], v[68:71]\n"
"ds_read_b64_tr_b16 a[202:203], v219 offset:55488\n"
"v_mfma_f32_16x16x32_bf16 v[68:71], a[220:223], a[92:95], v[68:71]\n"
"v_mfma_f32_16x16x32_bf16 v[64:67], a[220:223], a[76:79], v[64:67]\n"
"ds_read_b64_tr_b16 a[204:205], v218 offset:64704\n"
"v_mfma_f32_16x16x32_bf16 v[64:67], a[216:219], a[72:75], v[64:67]\n"
"buffer_load_dwordx4 v230, s[92:95], s58 offen offset:2048 lds\n"
"v_mfma_f32_16x16x32_bf16 v[64:67], a[212:215], a[68:71], v[64:67]\n"
"ds_read_b64_tr_b16 a[206:207], v219 offset:64704\n"
"v_mfma_f32_16x16x32_bf16 v[64:67], a[208:211], a[64:67], v[64:67]\n"
"s_waitcnt lgkmcnt(8)\n"
"v_mfma_f32_16x16x32_bf16 v[80:83], a[224:227], a[64:67], v[80:83]\n"
"ds_read_b64_tr_b16 a[208:209], v218 offset:37088\n"
"v_mfma_f32_16x16x32_bf16 v[80:83], a[228:231], a[68:71], v[80:83]\n"
"buffer_load_dwordx4 v231, s[92:95], s58 offen offset:3072 lds\n"
"v_mfma_f32_16x16x32_bf16 v[80:83], a[232:235], a[72:75], v[80:83]\n"
"ds_read_b64_tr_b16 a[210:211], v219 offset:37088\n"
"v_mfma_f32_16x16x32_bf16 v[80:83], a[236:239], a[76:79], v[80:83]\n"
"v_mfma_f32_16x16x32_bf16 v[84:87], a[236:239], a[92:95], v[84:87]\n"
"ds_read_b64_tr_b16 a[212:213], v218 offset:46304\n"
"v_mfma_f32_16x16x32_bf16 v[84:87], a[232:235], a[88:91], v[84:87]\n"
"v_mfma_f32_16x16x32_bf16 v[84:87], a[228:231], a[84:87], v[84:87]\n"
"s_add_u32 m0, s60, 45056\n"
"ds_read_b64_tr_b16 a[214:215], v219 offset:46304\n"
"v_mfma_f32_16x16x32_bf16 v[84:87], a[224:227], a[80:83], v[84:87]\n"
"buffer_load_dwordx4 v235, s[92:95], s58 offen lds\n"
"v_mfma_f32_16x16x32_bf16 v[88:91], a[224:227], a[96:99], v[88:91]\n"
"ds_read_b64_tr_b16 a[216:217], v218 offset:55520\n"
"v_mfma_f32_16x16x32_bf16 v[88:91], a[228:231], a[100:103], v[88:91]\n"
"v_mfma_f32_16x16x32_bf16 v[88:91], a[232:235], a[104:107], v[88:91]\n"
"ds_read_b64_tr_b16 a[218:219], v219 offset:55520\n"
"v_mfma_f32_16x16x32_bf16 v[88:91], a[236:239], a[108:111], v[88:91]\n"
"v_mfma_f32_16x16x32_bf16 v[92:95], a[236:239], a[124:127], v[92:95]\n"
"s_mov_b32 s72, 8\n"
"s_mul_i32 s72, s72, 65536\n"
"s_add_u32 s77, s76, s72\n"
"s_add_u32 s66, s77, 12288\n"
"ds_read_b64_tr_b16 a[220:221], v218 offset:64736\n"
"v_mfma_f32_16x16x32_bf16 v[92:95], a[232:235], a[120:123], v[92:95]\n"
"buffer_load_dwordx4 a[0:3], v232, s[52:55], s66 offen sc1\n"
"v_mfma_f32_16x16x32_bf16 v[92:95], a[228:231], a[116:119], v[92:95]\n"
"ds_read_b64_tr_b16 a[222:223], v219 offset:64736\n"
"v_mfma_f32_16x16x32_bf16 v[92:95], a[224:227], a[112:115], v[92:95]\n"
"s_waitcnt lgkmcnt(8)\n"
"v_mfma_f32_16x16x32_bf16 v[108:111], a[192:195], a[112:115], v[108:111]\n"
"ds_read_b64_tr_b16 a[224:225], v218 offset:37120\n"
"v_mfma_f32_16x16x32_bf16 v[108:111], a[196:199], a[116:119], v[108:111]\n"
"v_mfma_f32_16x16x32_bf16 v[108:111], a[200:203], a[120:123], v[108:111]\n"
"ds_read_b64_tr_b16 a[226:227], v219 offset:37120\n"
"v_mfma_f32_16x16x32_bf16 v[108:111], a[204:207], a[124:127], v[108:111]\n"
"buffer_load_dwordx4 a[4:7], v232, s[52:55], s66 offen offset:1024 sc1\n"
"v_mfma_f32_16x16x32_bf16 v[104:107], a[204:207], a[108:111], v[104:107]\n"
"ds_read_b64_tr_b16 a[228:229], v218 offset:46336\n"
"v_mfma_f32_16x16x32_bf16 v[104:107], a[200:203], a[104:107], v[104:107]\n"
"v_mfma_f32_16x16x32_bf16 v[104:107], a[196:199], a[100:103], v[104:107]\n"
"ds_read_b64_tr_b16 a[230:231], v219 offset:46336\n"
"v_mfma_f32_16x16x32_bf16 v[104:107], a[192:195], a[96:99], v[104:107]\n"
"buffer_load_dwordx4 a[8:11], v232, s[52:55], s66 offen offset:2048 sc1\n"
"v_mfma_f32_16x16x32_bf16 v[100:103], a[192:195], a[80:83], v[100:103]\n"
"ds_read_b64_tr_b16 a[232:233], v218 offset:55552\n"
"v_mfma_f32_16x16x32_bf16 v[100:103], a[196:199], a[84:87], v[100:103]\n"
"v_mfma_f32_16x16x32_bf16 v[100:103], a[200:203], a[88:91], v[100:103]\n"
"ds_read_b64_tr_b16 a[234:235], v219 offset:55552\n"
"v_mfma_f32_16x16x32_bf16 v[100:103], a[204:207], a[92:95], v[100:103]\n"
"v_mfma_f32_16x16x32_bf16 v[96:99], a[204:207], a[76:79], v[96:99]\n"
"ds_read_b64_tr_b16 a[236:237], v218 offset:64768\n"
"v_mfma_f32_16x16x32_bf16 v[96:99], a[200:203], a[72:75], v[96:99]\n"
"buffer_load_dwordx4 a[12:15], v232, s[52:55], s66 offen offset:3072 sc1\n"
"v_mfma_f32_16x16x32_bf16 v[96:99], a[196:199], a[68:71], v[96:99]\n"
"ds_read_b64_tr_b16 a[238:239], v219 offset:64768\n"
"v_mfma_f32_16x16x32_bf16 v[96:99], a[192:195], a[64:67], v[96:99]\n"
"s_waitcnt lgkmcnt(8)\n"
"v_mfma_f32_16x16x32_bf16 v[112:115], a[208:211], a[64:67], v[112:115]\n"
"v_mfma_f32_16x16x32_bf16 v[112:115], a[212:215], a[68:71], v[112:115]\n"
"v_mfma_f32_16x16x32_bf16 v[112:115], a[216:219], a[72:75], v[112:115]\n"
"s_add_u32 s66, s77, 0\n"
"v_mfma_f32_16x16x32_bf16 v[112:115], a[220:223], a[76:79], v[112:115]\n"
"s_waitcnt vmcnt(29) lgkmcnt(0)\n"
"buffer_load_dwordx4 a[16:19], v232, s[52:55], s66 offen sc1\n"
"v_mfma_f32_16x16x32_bf16 v[116:119], a[220:223], a[92:95], v[116:119]\n"
"s_barrier\n"
"v_mfma_f32_16x16x32_bf16 v[116:119], a[216:219], a[88:91], v[116:119]\n"
"v_mfma_f32_16x16x32_bf16 v[116:119], a[212:215], a[84:87], v[116:119]\n"
"ds_read_b64_tr_b16 a[192:193], v216\n"
"ds_read_b64_tr_b16 a[194:195], v217\n"
"v_mfma_f32_16x16x32_bf16 v[116:119], a[208:211], a[80:83], v[116:119]\n"
"v_mfma_f32_16x16x32_bf16 v[120:123], a[208:211], a[96:99], v[120:123]\n"
"ds_read_b64_tr_b16 a[196:197], v216 offset:9216\n"
"ds_read_b64_tr_b16 a[198:199], v217 offset:9216\n"
"v_mfma_f32_16x16x32_bf16 v[120:123], a[212:215], a[100:103], v[120:123]\n"
"buffer_load_dwordx4 a[20:23], v232, s[52:55], s66 offen offset:1024 sc1\n"
"v_mfma_f32_16x16x32_bf16 v[120:123], a[216:219], a[104:107], v[120:123]\n"
"ds_read_b64_tr_b16 a[200:201], v216 offset:18432\n"
"ds_read_b64_tr_b16 a[202:203], v217 offset:18432\n"
"v_mfma_f32_16x16x32_bf16 v[120:123], a[220:223], a[108:111], v[120:123]\n"
"v_mfma_f32_16x16x32_bf16 v[124:127], a[220:223], a[124:127], v[124:127]\n"
"ds_read_b64_tr_b16 a[204:205], v216 offset:27648\n"
"v_mfma_f32_16x16x32_bf16 v[124:127], a[216:219], a[120:123], v[124:127]\n"
"buffer_load_dwordx4 a[24:27], v232, s[52:55], s66 offen offset:2048 sc1\n"
"v_mfma_f32_16x16x32_bf16 v[124:127], a[212:215], a[116:119], v[124:127]\n"
"ds_read_b64_tr_b16 a[206:207], v217 offset:27648\n"
"v_mfma_f32_16x16x32_bf16 v[124:127], a[208:211], a[112:115], v[124:127]\n"
"v_mfma_f32_16x16x32_bf16 v[140:143], a[224:227], a[112:115], v[140:143]\n"
"ds_read_b64_tr_b16 a[208:209], v216 offset:32\n"
"v_mfma_f32_16x16x32_bf16 v[140:143], a[228:231], a[116:119], v[140:143]\n"
"v_mfma_f32_16x16x32_bf16 v[140:143], a[232:235], a[120:123], v[140:143]\n"
"ds_read_b64_tr_b16 a[210:211], v217 offset:32\n"
"v_mfma_f32_16x16x32_bf16 v[140:143], a[236:239], a[124:127], v[140:143]\n"
"buffer_load_dwordx4 a[28:31], v232, s[52:55], s66 offen offset:3072 sc1\n"
"v_mfma_f32_16x16x32_bf16 v[136:139], a[236:239], a[108:111], v[136:139]\n"
"ds_read_b64_tr_b16 a[212:213], v216 offset:9248\n"
"v_mfma_f32_16x16x32_bf16 v[136:139], a[232:235], a[104:107], v[136:139]\n"
"v_mfma_f32_16x16x32_bf16 v[136:139], a[228:231], a[100:103], v[136:139]\n"
"ds_read_b64_tr_b16 a[214:215], v217 offset:9248\n"
"v_mfma_f32_16x16x32_bf16 v[136:139], a[224:227], a[96:99], v[136:139]\n"
"v_mfma_f32_16x16x32_bf16 v[132:135], a[224:227], a[80:83], v[132:135]\n"
"s_add_u32 s66, s77, 4096\n"
"ds_read_b64_tr_b16 a[216:217], v216 offset:18464\n"
"v_mfma_f32_16x16x32_bf16 v[132:135], a[228:231], a[84:87], v[132:135]\n"
"buffer_load_dwordx4 a[32:35], v232, s[52:55], s66 offen sc1\n"
"v_mfma_f32_16x16x32_bf16 v[132:135], a[232:235], a[88:91], v[132:135]\n"
"ds_read_b64_tr_b16 a[218:219], v217 offset:18464\n"
"v_mfma_f32_16x16x32_bf16 v[132:135], a[236:239], a[92:95], v[132:135]\n"
"v_mfma_f32_16x16x32_bf16 v[128:131], a[236:239], a[76:79], v[128:131]\n"
"ds_read_b64_tr_b16 a[220:221], v216 offset:27680\n"
"v_mfma_f32_16x16x32_bf16 v[128:131], a[232:235], a[72:75], v[128:131]\n"
"v_mfma_f32_16x16x32_bf16 v[128:131], a[228:231], a[68:71], v[128:131]\n"
"ds_read_b64_tr_b16 a[222:223], v217 offset:27680\n"
"v_mfma_f32_16x16x32_bf16 v[128:131], a[224:227], a[64:67], v[128:131]\n"
"s_waitcnt vmcnt(18) lgkmcnt(8)\n"
"buffer_load_dwordx4 a[36:39], v232, s[52:55], s66 offen offset:1024 sc1\n"
"v_mfma_f32_16x16x32_bf16 v[0:3], a[192:195], v[144:147], v[0:3]\n"
"ds_read_b64_tr_b16 a[224:225], v216 offset:64\n"
"v_mfma_f32_16x16x32_bf16 v[0:3], a[196:199], v[148:151], v[0:3]\n"
"v_mfma_f32_16x16x32_bf16 v[0:3], a[200:203], v[152:155], v[0:3]\n"
"ds_read_b64_tr_b16 a[226:227], v217 offset:64\n"
"v_mfma_f32_16x16x32_bf16 v[0:3], a[204:207], v[156:159], v[0:3]\n"
"buffer_load_dwordx4 a[40:43], v232, s[52:55], s66 offen offset:2048 sc1\n"
"v_mfma_f32_16x16x32_bf16 v[4:7], a[204:207], v[172:175], v[4:7]\n"
"ds_read_b64_tr_b16 a[228:229], v216 offset:9280\n"
"v_mfma_f32_16x16x32_bf16 v[4:7], a[200:203], v[168:171], v[4:7]\n"
"v_mfma_f32_16x16x32_bf16 v[4:7], a[196:199], v[164:167], v[4:7]\n"
"ds_read_b64_tr_b16 a[230:231], v217 offset:9280\n"
"v_mfma_f32_16x16x32_bf16 v[4:7], a[192:195], v[160:163], v[4:7]\n"
"v_mfma_f32_16x16x32_bf16 v[8:11], a[192:195], v[176:179], v[8:11]\n"
"ds_read_b64_tr_b16 a[232:233], v216 offset:18496\n"
"v_mfma_f32_16x16x32_bf16 v[8:11], a[196:199], v[180:183], v[8:11]\n"
"buffer_load_dwordx4 a[44:47], v232, s[52:55], s66 offen offset:3072 sc1\n"
"v_mfma_f32_16x16x32_bf16 v[8:11], a[200:203], v[184:187], v[8:11]\n"
"ds_read_b64_tr_b16 a[234:235], v217 offset:18496\n"
"v_mfma_f32_16x16x32_bf16 v[8:11], a[204:207], v[188:191], v[8:11]\n"
"v_mfma_f32_16x16x32_bf16 v[12:15], a[204:207], v[204:207], v[12:15]\n"
"ds_read_b64_tr_b16 a[236:237], v216 offset:27712\n"
"v_mfma_f32_16x16x32_bf16 v[12:15], a[200:203], v[200:203], v[12:15]\n"
"v_mfma_f32_16x16x32_bf16 v[12:15], a[196:199], v[196:199], v[12:15]\n"
"s_add_u32 s66, s77, 8192\n"
"ds_read_b64_tr_b16 a[238:239], v217 offset:27712\n"
"v_mfma_f32_16x16x32_bf16 v[12:15], a[192:195], v[192:195], v[12:15]\n"
"s_waitcnt lgkmcnt(8)\n"
"buffer_load_dwordx4 a[48:51], v232, s[52:55], s66 offen sc1\n"
"v_mfma_f32_16x16x32_bf16 v[28:31], a[208:211], v[192:195], v[28:31]\n"
"ds_read_b64_tr_b16 a[192:193], v216 offset:96\n"
"v_mfma_f32_16x16x32_bf16 v[28:31], a[212:215], v[196:199], v[28:31]\n"
"v_mfma_f32_16x16x32_bf16 v[28:31], a[216:219], v[200:203], v[28:31]\n"
"ds_read_b64_tr_b16 a[194:195], v217 offset:96\n"
"v_mfma_f32_16x16x32_bf16 v[28:31], a[220:223], v[204:207], v[28:31]\n"
"v_mfma_f32_16x16x32_bf16 v[24:27], a[220:223], v[188:191], v[24:27]\n"
"ds_read_b64_tr_b16 a[196:197], v216 offset:9312\n"
"v_mfma_f32_16x16x32_bf16 v[24:27], a[216:219], v[184:187], v[24:27]\n"
"buffer_load_dwordx4 a[52:55], v232, s[52:55], s66 offen offset:1024 sc1\n"
"v_mfma_f32_16x16x32_bf16 v[24:27], a[212:215], v[180:183], v[24:27]\n"
"ds_read_b64_tr_b16 a[198:199], v217 offset:9312\n"
"v_mfma_f32_16x16x32_bf16 v[24:27], a[208:211], v[176:179], v[24:27]\n"
"v_mfma_f32_16x16x32_bf16 v[20:23], a[208:211], v[160:163], v[20:23]\n"
"ds_read_b64_tr_b16 a[200:201], v216 offset:18528\n"
"v_mfma_f32_16x16x32_bf16 v[20:23], a[212:215], v[164:167], v[20:23]\n"
"buffer_load_dwordx4 a[56:59], v232, s[52:55], s66 offen offset:2048 sc1\n"
"v_mfma_f32_16x16x32_bf16 v[20:23], a[216:219], v[168:171], v[20:23]\n"
"ds_read_b64_tr_b16 a[202:203], v217 offset:18528\n"
"v_mfma_f32_16x16x32_bf16 v[20:23], a[220:223], v[172:175], v[20:23]\n"
"buffer_load_dword v251, v250, s[96:99], 0 offen sc1\n"
"v_mfma_f32_16x16x32_bf16 v[16:19], a[220:223], v[156:159], v[16:19]\n"
"ds_read_b64_tr_b16 a[204:205], v216 offset:27744\n"
"v_mfma_f32_16x16x32_bf16 v[16:19], a[216:219], v[152:155], v[16:19]\n"
"v_mfma_f32_16x16x32_bf16 v[16:19], a[212:215], v[148:151], v[16:19]\n"
"ds_read_b64_tr_b16 a[206:207], v217 offset:27744\n"
"v_mfma_f32_16x16x32_bf16 v[16:19], a[208:211], v[144:147], v[16:19]\n"
"s_waitcnt lgkmcnt(8)\n"
"buffer_load_dwordx4 a[60:63], v232, s[52:55], s66 offen offset:3072 sc1\n"
"v_mfma_f32_16x16x32_bf16 v[32:35], a[224:227], v[144:147], v[32:35]\n"
"ds_read_b64_tr_b16 a[208:209], v216 offset:128\n"
"v_mfma_f32_16x16x32_bf16 v[32:35], a[228:231], v[148:151], v[32:35]\n"
"v_mfma_f32_16x16x32_bf16 v[32:35], a[232:235], v[152:155], v[32:35]\n"
"ds_read_b64_tr_b16 a[210:211], v217 offset:128\n"
"v_mfma_f32_16x16x32_bf16 v[32:35], a[236:239], v[156:159], v[32:35]\n"
"v_mfma_f32_16x16x32_bf16 v[36:39], a[236:239], v[172:175], v[36:39]\n"
"s_cmp_le_u32 s59, 0\n"
"s_cselect_b32 s72, 3, 1\n"
"s_mul_i32 s58, s72, 147456\n"
"s_add_u32 m0, s60, 73728\n"
"ds_read_b64_tr_b16 a[212:213], v216 offset:9344\n"
"v_mfma_f32_16x16x32_bf16 v[36:39], a[232:235], v[168:171], v[36:39]\n"
"buffer_load_dwordx4 v221, s[92:95], s58 offen lds\n"
"v_mfma_f32_16x16x32_bf16 v[36:39], a[228:231], v[164:167], v[36:39]\n"
"ds_read_b64_tr_b16 a[214:215], v217 offset:9344\n"
"v_mfma_f32_16x16x32_bf16 v[36:39], a[224:227], v[160:163], v[36:39]\n"
"v_mfma_f32_16x16x32_bf16 v[40:43], a[224:227], v[176:179], v[40:43]\n"
"ds_read_b64_tr_b16 a[216:217], v216 offset:18560\n"
"v_mfma_f32_16x16x32_bf16 v[40:43], a[228:231], v[180:183], v[40:43]\n"
"v_mfma_f32_16x16x32_bf16 v[40:43], a[232:235], v[184:187], v[40:43]\n"
"ds_read_b64_tr_b16 a[218:219], v217 offset:18560\n"
"v_mfma_f32_16x16x32_bf16 v[40:43], a[236:239], v[188:191], v[40:43]\n"
"buffer_load_dwordx4 v225, s[92:95], s58 offen offset:1024 lds\n"
"v_mfma_f32_16x16x32_bf16 v[44:47], a[236:239], v[204:207], v[44:47]\n"
"ds_read_b64_tr_b16 a[220:221], v216 offset:27776\n"
"v_mfma_f32_16x16x32_bf16 v[44:47], a[232:235], v[200:203], v[44:47]\n"
"v_mfma_f32_16x16x32_bf16 v[44:47], a[228:231], v[196:199], v[44:47]\n"
"ds_read_b64_tr_b16 a[222:223], v217 offset:27776\n"
"v_mfma_f32_16x16x32_bf16 v[44:47], a[224:227], v[192:195], v[44:47]\n"
"s_waitcnt lgkmcnt(8)\n"
"buffer_load_dwordx4 v226, s[92:95], s58 offen offset:2048 lds\n"
"v_mfma_f32_16x16x32_bf16 v[60:63], a[192:195], v[192:195], v[60:63]\n"
"ds_read_b64_tr_b16 a[224:225], v216 offset:160\n"
"v_mfma_f32_16x16x32_bf16 v[60:63], a[196:199], v[196:199], v[60:63]\n"
"v_mfma_f32_16x16x32_bf16 v[60:63], a[200:203], v[200:203], v[60:63]\n"
"ds_read_b64_tr_b16 a[226:227], v217 offset:160\n"
"v_mfma_f32_16x16x32_bf16 v[60:63], a[204:207], v[204:207], v[60:63]\n"
"v_mfma_f32_16x16x32_bf16 v[56:59], a[204:207], v[188:191], v[56:59]\n"
"ds_read_b64_tr_b16 a[228:229], v216 offset:9376\n"
"v_mfma_f32_16x16x32_bf16 v[56:59], a[200:203], v[184:187], v[56:59]\n"
"buffer_load_dwordx4 v227, s[92:95], s58 offen offset:3072 lds\n"
"v_mfma_f32_16x16x32_bf16 v[56:59], a[196:199], v[180:183], v[56:59]\n"
"ds_read_b64_tr_b16 a[230:231], v217 offset:9376\n"
"v_mfma_f32_16x16x32_bf16 v[56:59], a[192:195], v[176:179], v[56:59]\n"
"v_mfma_f32_16x16x32_bf16 v[52:55], a[192:195], v[160:163], v[52:55]\n"
"ds_read_b64_tr_b16 a[232:233], v216 offset:18592\n"
"v_mfma_f32_16x16x32_bf16 v[52:55], a[196:199], v[164:167], v[52:55]\n"
"v_mfma_f32_16x16x32_bf16 v[52:55], a[200:203], v[168:171], v[52:55]\n"
"s_add_u32 m0, s60, 77824\n"
"ds_read_b64_tr_b16 a[234:235], v217 offset:18592\n"
"v_mfma_f32_16x16x32_bf16 v[52:55], a[204:207], v[172:175], v[52:55]\n"
"buffer_load_dwordx4 v228, s[92:95], s58 offen lds\n"
"v_mfma_f32_16x16x32_bf16 v[48:51], a[204:207], v[156:159], v[48:51]\n"
"ds_read_b64_tr_b16 a[236:237], v216 offset:27808\n"
"v_mfma_f32_16x16x32_bf16 v[48:51], a[200:203], v[152:155], v[48:51]\n"
"v_mfma_f32_16x16x32_bf16 v[48:51], a[196:199], v[148:151], v[48:51]\n"
"ds_read_b64_tr_b16 a[238:239], v217 offset:27808\n"
"v_mfma_f32_16x16x32_bf16 v[48:51], a[192:195], v[144:147], v[48:51]\n"
"s_waitcnt lgkmcnt(8)\n"
"v_mfma_f32_16x16x32_bf16 v[64:67], a[208:211], v[144:147], v[64:67]\n"
"ds_read_b64_tr_b16 a[192:193], v216 offset:192\n"
"v_mfma_f32_16x16x32_bf16 v[64:67], a[212:215], v[148:151], v[64:67]\n"
"buffer_load_dwordx4 v229, s[92:95], s58 offen offset:1024 lds\n"
"v_mfma_f32_16x16x32_bf16 v[64:67], a[216:219], v[152:155], v[64:67]\n"
"ds_read_b64_tr_b16 a[194:195], v217 offset:192\n"
"v_mfma_f32_16x16x32_bf16 v[64:67], a[220:223], v[156:159], v[64:67]\n"
"v_mfma_f32_16x16x32_bf16 v[68:71], a[220:223], v[172:175], v[68:71]\n"
"ds_read_b64_tr_b16 a[196:197], v216 offset:9408\n"
"v_mfma_f32_16x16x32_bf16 v[68:71], a[216:219], v[168:171], v[68:71]\n"
"buffer_load_dwordx4 v230, s[92:95], s58 offen offset:2048 lds\n"
"v_mfma_f32_16x16x32_bf16 v[68:71], a[212:215], v[164:167], v[68:71]\n"
"ds_read_b64_tr_b16 a[198:199], v217 offset:9408\n"
"v_mfma_f32_16x16x32_bf16 v[68:71], a[208:211], v[160:163], v[68:71]\n"
"v_mfma_f32_16x16x32_bf16 v[72:75], a[208:211], v[176:179], v[72:75]\n"
"ds_read_b64_tr_b16 a[200:201], v216 offset:18624\n"
"v_mfma_f32_16x16x32_bf16 v[72:75], a[212:215], v[180:183], v[72:75]\n"
"v_mfma_f32_16x16x32_bf16 v[72:75], a[216:219], v[184:187], v[72:75]\n"
"ds_read_b64_tr_b16 a[202:203], v217 offset:18624\n"
"v_mfma_f32_16x16x32_bf16 v[72:75], a[220:223], v[188:191], v[72:75]\n"
"buffer_load_dwordx4 v231, s[92:95], s58 offen offset:3072 lds\n"
"v_mfma_f32_16x16x32_bf16 v[76:79], a[220:223], v[204:207], v[76:79]\n"
"ds_read_b64_tr_b16 a[204:205], v216 offset:27840\n"
"v_mfma_f32_16x16x32_bf16 v[76:79], a[216:219], v[200:203], v[76:79]\n"
"v_mfma_f32_16x16x32_bf16 v[76:79], a[212:215], v[196:199], v[76:79]\n"
"ds_read_b64_tr_b16 a[206:207], v217 offset:27840\n"
"v_mfma_f32_16x16x32_bf16 v[76:79], a[208:211], v[192:195], v[76:79]\n"
"s_waitcnt lgkmcnt(8)\n"
"v_mfma_f32_16x16x32_bf16 v[92:95], a[224:227], v[192:195], v[92:95]\n"
"s_add_u32 m0, s60, 81920\n"
"ds_read_b64_tr_b16 a[208:209], v216 offset:224\n"
"v_mfma_f32_16x16x32_bf16 v[92:95], a[228:231], v[196:199], v[92:95]\n"
"buffer_load_dwordx4 v235, s[92:95], s58 offen lds\n"
"v_mfma_f32_16x16x32_bf16 v[92:95], a[232:235], v[200:203], v[92:95]\n"
"ds_read_b64_tr_b16 a[210:211], v217 offset:224\n"
"v_mfma_f32_16x16x32_bf16 v[92:95], a[236:239], v[204:207], v[92:95]\n"
"v_mfma_f32_16x16x32_bf16 v[88:91], a[236:239], v[188:191], v[88:91]\n"
"ds_read_b64_tr_b16 a[212:213], v216 offset:9440\n"
"L_chk2e%=:\n"
"s_waitcnt vmcnt(10)\n"
"v_cmp_eq_u32 vcc, s78, v251\n"
"s_nop 3\n"
"s_lshr_b32 s72, vcc_lo, 12\n"
"s_and_b32 s72, s72, 15\n"
"s_cmp_eq_u32 s72, 15\n"
"s_cbranch_scc0 L_rt2e%=\n"
"L_ok2e%=:\n"
"v_mfma_f32_16x16x32_bf16 v[88:91], a[232:235], v[184:187], v[88:91]\n"
"v_mfma_f32_16x16x32_bf16 v[88:91], a[228:231], v[180:183], v[88:91]\n"
"s_cmp_le_u32 s59, 0\n"
"s_cselect_b32 s72, 3, 1\n"
"s_mul_i32 s72, s72, 65536\n"
"s_add_u32 s77, s76, s72\n"
"s_add_u32 s66, s77, 0\n"
"ds_read_b64_tr_b16 a[214:215], v217 offset:9440\n"
"v_mfma_f32_16x16x32_bf16 v[88:91], a[224:227], v[176:179], v[88:91]\n"
"buffer_load_dwordx4 a[128:131], v232, s[52:55], s66 offen sc1\n"
"v_mfma_f32_16x16x32_bf16 v[84:87], a[224:227], v[160:163], v[84:87]\n"
"ds_read_b64_tr_b16 a[216:217], v216 offset:18656\n"
"v_mfma_f32_16x16x32_bf16 v[84:87], a[228:231], v[164:167], v[84:87]\n"
"v_mfma_f32_16x16x32_bf16 v[84:87], a[232:235], v[168:171], v[84:87]\n"
"ds_read_b64_tr_b16 a[218:219], v217 offset:18656\n"
"v_mfma_f32_16x16x32_bf16 v[84:87], a[236:239], v[172:175], v[84:87]\n"
"buffer_load_dwordx4 a[132:135], v232, s[52:55], s66 offen offset:1024 sc1\n"
"v_mfma_f32_16x16x32_bf16 v[80:83], a[236:239], v[156:159], v[80:83]\n"
"ds_read_b64_tr_b16 a[220:221], v216 offset:27872\n"
"v_mfma_f32_16x16x32_bf16 v[80:83], a[232:235], v[152:155], v[80:83]\n"
"v_mfma_f32_16x16x32_bf16 v[80:83], a[228:231], v[148:151], v[80:83]\n"
"ds_read_b64_tr_b16 a[222:223], v217 offset:27872\n"
"v_mfma_f32_16x16x32_bf16 v[80:83], a[224:227], v[144:147], v[80:83]\n"
"s_waitcnt lgkmcnt(8)\n"
"v_mfma_f32_16x16x32_bf16 v[96:99], a[192:195], v[144:147], v[96:99]\n"
"ds_read_b64_tr_b16 a[224:225], v216 offset:256\n"
"v_mfma_f32_16x16x32_bf16 v[96:99], a[196:199], v[148:151], v[96:99]\n"
"buffer_load_dwordx4 a[136:139], v232, s[52:55], s66 offen offset:2048 sc1\n"
"v_mfma_f32_16x16x32_bf16 v[96:99], a[200:203], v[152:155], v[96:99]\n"
"ds_read_b64_tr_b16 a[226:227], v217 offset:256\n"
"v_mfma_f32_16x16x32_bf16 v[96:99], a[204:207], v[156:159], v[96:99]\n"
"v_mfma_f32_16x16x32_bf16 v[100:103], a[204:207], v[172:175], v[100:103]\n"
"ds_read_b64_tr_b16 a[228:229], v216 offset:9472\n"
"v_mfma_f32_16x16x32_bf16 v[100:103], a[200:203], v[168:171], v[100:103]\n"
"v_mfma_f32_16x16x32_bf16 v[100:103], a[196:199], v[164:167], v[100:103]\n"
"ds_read_b64_tr_b16 a[230:231], v217 offset:9472\n"
"v_mfma_f32_16x16x32_bf16 v[100:103], a[192:195], v[160:163], v[100:103]\n"
"buffer_load_dwordx4 a[140:143], v232, s[52:55], s66 offen offset:3072 sc1\n"
"v_mfma_f32_16x16x32_bf16 v[104:107], a[192:195], v[176:179], v[104:107]\n"
"ds_read_b64_tr_b16 a[232:233], v216 offset:18688\n"
"v_mfma_f32_16x16x32_bf16 v[104:107], a[196:199], v[180:183], v[104:107]\n"
"v_mfma_f32_16x16x32_bf16 v[104:107], a[200:203], v[184:187], v[104:107]\n"
"ds_read_b64_tr_b16 a[234:235], v217 offset:18688\n"
"v_mfma_f32_16x16x32_bf16 v[104:107], a[204:207], v[188:191], v[104:107]\n"
"v_mfma_f32_16x16x32_bf16 v[108:111], a[204:207], v[204:207], v[108:111]\n"
"s_add_u32 s66, s77, 4096\n"
"ds_read_b64_tr_b16 a[236:237], v216 offset:27904\n"
"v_mfma_f32_16x16x32_bf16 v[108:111], a[200:203], v[200:203], v[108:111]\n"
"buffer_load_dwordx4 a[144:147], v232, s[52:55], s66 offen sc1\n"
"v_mfma_f32_16x16x32_bf16 v[108:111], a[196:199], v[196:199], v[108:111]\n"
"ds_read_b64_tr_b16 a[238:239], v217 offset:27904\n"
"v_mfma_f32_16x16x32_bf16 v[108:111], a[192:195], v[192:195], v[108:111]\n"
"s_waitcnt lgkmcnt(8)\n"
"v_mfma_f32_16x16x32_bf16 v[124:127], a[208:211], v[192:195], v[124:127]\n"
"v_mfma_f32_16x16x32_bf16 v[124:127], a[212:215], v[196:199], v[124:127]\n"
"buffer_load_dwordx4 a[148:151], v232, s[52:55], s66 offen offset:1024 sc1\n"
"v_mfma_f32_16x16x32_bf16 v[124:127], a[216:219], v[200:203], v[124:127]\n"
"v_mfma_f32_16x16x32_bf16 v[124:127], a[220:223], v[204:207], v[124:127]\n"
"s_waitcnt vmcnt(32) lgkmcnt(0)\n"
"v_mfma_f32_16x16x32_bf16 v[120:123], a[220:223], v[188:191], v[120:123]\n"
"s_barrier\n"
"v_mfma_f32_16x16x32_bf16 v[120:123], a[216:219], v[184:187], v[120:123]\n"
"v_mfma_f32_16x16x32_bf16 v[120:123], a[212:215], v[180:183], v[120:123]\n"
"ds_read_b64_tr_b16 a[192:193], v216 offset:36864\n"
"ds_read_b64_tr_b16 a[194:195], v217 offset:36864\n"
"v_mfma_f32_16x16x32_bf16 v[120:123], a[208:211], v[176:179], v[120:123]\n"
"buffer_load_dwordx4 a[152:155], v232, s[52:55], s66 offen offset:2048 sc1\n"
"v_mfma_f32_16x16x32_bf16 v[116:119], a[208:211], v[160:163], v[116:119]\n"
"ds_read_b64_tr_b16 a[196:197], v216 offset:46080\n"
"ds_read_b64_tr_b16 a[198:199], v217 offset:46080\n"
"v_mfma_f32_16x16x32_bf16 v[116:119], a[212:215], v[164:167], v[116:119]\n"
"v_mfma_f32_16x16x32_bf16 v[116:119], a[216:219], v[168:171], v[116:119]\n"
"ds_read_b64_tr_b16 a[200:201], v216 offset:55296\n"
"ds_read_b64_tr_b16 a[202:203], v217 offset:55296\n"
"v_mfma_f32_16x16x32_bf16 v[116:119], a[220:223], v[172:175], v[116:119]\n"
"v_mfma_f32_16x16x32_bf16 v[112:115], a[220:223], v[156:159], v[112:115]\n"
"ds_read_b64_tr_b16 a[204:205], v216 offset:64512\n"
"v_mfma_f32_16x16x32_bf16 v[112:115], a[216:219], v[152:155], v[112:115]\n"
"buffer_load_dwordx4 a[156:159], v232, s[52:55], s66 offen offset:3072 sc1\n"
"v_mfma_f32_16x16x32_bf16 v[112:115], a[212:215], v[148:151], v[112:115]\n"
"ds_read_b64_tr_b16 a[206:207], v217 offset:64512\n"
"v_mfma_f32_16x16x32_bf16 v[112:115], a[208:211], v[144:147], v[112:115]\n"
"v_mfma_f32_16x16x32_bf16 v[128:131], a[224:227], v[144:147], v[128:131]\n"
"ds_read_b64_tr_b16 a[208:209], v216 offset:36896\n"
"v_mfma_f32_16x16x32_bf16 v[128:131], a[228:231], v[148:151], v[128:131]\n"
"v_mfma_f32_16x16x32_bf16 v[128:131], a[232:235], v[152:155], v[128:131]\n"
"s_add_u32 s66, s77, 8192\n"
"ds_read_b64_tr_b16 a[210:211], v217 offset:36896\n"
"v_mfma_f32_16x16x32_bf16 v[128:131], a[236:239], v[156:159], v[128:131]\n"
"buffer_load_dwordx4 a[160:163], v232, s[52:55], s66 offen sc1\n"
"v_mfma_f32_16x16x32_bf16 v[132:135], a[236:239], v[172:175], v[132:135]\n"
"ds_read_b64_tr_b16 a[212:213], v216 offset:46112\n"
"v_mfma_f32_16x16x32_bf16 v[132:135], a[232:235], v[168:171], v[132:135]\n"
"v_mfma_f32_16x16x32_bf16 v[132:135], a[228:231], v[164:167], v[132:135]\n"
"ds_read_b64_tr_b16 a[214:215], v217 offset:46112\n"
"v_mfma_f32_16x16x32_bf16 v[132:135], a[224:227], v[160:163], v[132:135]\n"
"buffer_load_dwordx4 a[164:167], v232, s[52:55], s66 offen offset:1024 sc1\n"
"v_mfma_f32_16x16x32_bf16 v[136:139], a[224:227], v[176:179], v[136:139]\n"
"ds_read_b64_tr_b16 a[216:217], v216 offset:55328\n"
"v_mfma_f32_16x16x32_bf16 v[136:139], a[228:231], v[180:183], v[136:139]\n"
"v_mfma_f32_16x16x32_bf16 v[136:139], a[232:235], v[184:187], v[136:139]\n"
"ds_read_b64_tr_b16 a[218:219], v217 offset:55328\n"
"v_mfma_f32_16x16x32_bf16 v[136:139], a[236:239], v[188:191], v[136:139]\n"
"v_mfma_f32_16x16x32_bf16 v[140:143], a[236:239], v[204:207], v[140:143]\n"
"ds_read_b64_tr_b16 a[220:221], v216 offset:64544\n"
"v_mfma_f32_16x16x32_bf16 v[140:143], a[232:235], v[200:203], v[140:143]\n"
"buffer_load_dwordx4 a[168:171], v232, s[52:55], s66 offen offset:2048 sc1\n"
"v_mfma_f32_16x16x32_bf16 v[140:143], a[228:231], v[196:199], v[140:143]\n"
"ds_read_b64_tr_b16 a[222:223], v217 offset:64544\n"
"v_mfma_f32_16x16x32_bf16 v[140:143], a[224:227], v[192:195], v[140:143]\n"
"s_waitcnt vmcnt(20) lgkmcnt(8)\n"
"v_mfma_f32_16x16x32_bf16 v[12:15], a[192:195], a[48:51], v[12:15]\n"
"ds_read_b64_tr_b16 a[224:225], v216 offset:36928\n"
"v_mfma_f32_16x16x32_bf16 v[12:15], a[196:199], a[52:55], v[12:15]\n"
"v_mfma_f32_16x16x32_bf16 v[12:15], a[200:203], a[56:59], v[12:15]\n"
"ds_read_b64_tr_b16 a[226:227], v217 offset:36928\n"
"v_mfma_f32_16x16x32_bf16 v[12:15], a[204:207], a[60:63], v[12:15]\n"
"buffer_load_dwordx4 a[172:175], v232, s[52:55], s66 offen offset:3072 sc1\n"
"v_mfma_f32_16x16x32_bf16 v[8:11], a[204:207], a[44:47], v[8:11]\n"
"ds_read_b64_tr_b16 a[228:229], v216 offset:46144\n"
"v_mfma_f32_16x16x32_bf16 v[8:11], a[200:203], a[40:43], v[8:11]\n"
"v_mfma_f32_16x16x32_bf16 v[8:11], a[196:199], a[36:39], v[8:11]\n"
"ds_read_b64_tr_b16 a[230:231], v217 offset:46144\n"
"v_mfma_f32_16x16x32_bf16 v[8:11], a[192:195], a[32:35], v[8:11]\n"
"v_mfma_f32_16x16x32_bf16 v[4:7], a[192:195], a[16:19], v[4:7]\n"
"s_add_u32 s66, s77, 12288\n"
"ds_read_b64_tr_b16 a[232:233], v216 offset:55360\n"
"v_mfma_f32_16x16x32_bf16 v[4:7], a[196:199], a[20:23], v[4:7]\n"
"buffer_load_dwordx4 a[176:179], v232, s[52:55], s66 offen sc1\n"
"v_mfma_f32_16x16x32_bf16 v[4:7], a[200:203], a[24:27], v[4:7]\n"
"ds_read_b64_tr_b16 a[234:235], v217 offset:55360\n"
"v_mfma_f32_16x16x32_bf16 v[4:7], a[204:207], a[28:31], v[4:7]\n"
"v_mfma_f32_16x16x32_bf16 v[0:3], a[204:207], a[12:15], v[0:3]\n"
"ds_read_b64_tr_b16 a[236:237], v216 offset:64576\n"
"v_mfma_f32_16x16x32_bf16 v[0:3], a[200:203], a[8:11], v[0:3]\n"
"buffer_load_dwordx4 a[180:183], v232, s[52:55], s66 offen offset:1024 sc1\n"
"v_mfma_f32_16x16x32_bf16 v[0:3], a[196:199], a[4:7], v[0:3]\n"
"ds_read_b64_tr_b16 a[238:239], v217 offset:64576\n"
"v_mfma_f32_16x16x32_bf16 v[0:3], a[192:195], a[0:3], v[0:3]\n"
"s_waitcnt lgkmcnt(8)\n"
"v_mfma_f32_16x16x32_bf16 v[16:19], a[208:211], a[0:3], v[16:19]\n"
"ds_read_b64_tr_b16 a[192:193], v216 offset:36960\n"
"v_mfma_f32_16x16x32_bf16 v[16:19], a[212:215], a[4:7], v[16:19]\n"
"v_mfma_f32_16x16x32_bf16 v[16:19], a[216:219], a[8:11], v[16:19]\n"
"ds_read_b64_tr_b16 a[194:195], v217 offset:36960\n"
"v_mfma_f32_16x16x32_bf16 v[16:19], a[220:223], a[12:15], v[16:19]\n"
"buffer_load_dwordx4 a[184:187], v232, s[52:55], s66 offen offset:2048 sc1\n"
"v_mfma_f32_16x16x32_bf16 v[20:23], a[220:223], a[28:31], v[20:23]\n"
"ds_read_b64_tr_b16 a[196:197], v216 offset:46176\n"
"v_mfma_f32_16x16x32_bf16 v[20:23], a[216:219], a[24:27], v[20:23]\n"
"v_mfma_f32_16x16x32_bf16 v[20:23], a[212:215], a[20:23], v[20:23]\n"
"ds_read_b64_tr_b16 a[198:199], v217 offset:46176\n"
"v_mfma_f32_16x16x32_bf16 v[20:23], a[208:211], a[16:19], v[20:23]\n"
"v_mfma_f32_16x16x32_bf16 v[24:27], a[208:211], a[32:35], v[24:27]\n"
"ds_read_b64_tr_b16 a[200:201], v216 offset:55392\n"
"v_mfma_f32_16x16x32_bf16 v[24:27], a[212:215], a[36:39], v[24:27]\n"
"buffer_load_dwordx4 a[188:191], v232, s[52:55], s66 offen offset:3072 sc1\n"
"v_mfma_f32_16x16x32_bf16 v[24:27], a[216:219], a[40:43], v[24:27]\n"
"ds_read_b64_tr_b16 a[202:203], v217 offset:55392\n"
"v_mfma_f32_16x16x32_bf16 v[24:27], a[220:223], a[44:47], v[24:27]\n"
"v_mfma_f32_16x16x32_bf16 v[28:31], a[220:223], a[60:63], v[28:31]\n"
"ds_read_b64_tr_b16 a[204:205], v216 offset:64608\n"
"v_mfma_f32_16x16x32_bf16 v[28:31], a[216:219], a[56:59], v[28:31]\n"
"v_mfma_f32_16x16x32_bf16 v[28:31], a[212:215], a[52:55], v[28:31]\n"
"s_cmp_le_u32 s59, 1\n"
"s_cselect_b32 s72, 5, 3\n"
"s_mul_i32 s58, s72, 147456\n"
"s_add_u32 m0, s60, 110592\n"
"ds_read_b64_tr_b16 a[206:207], v217 offset:64608\n"
"v_mfma_f32_16x16x32_bf16 v[28:31], a[208:211], a[48:51], v[28:31]\n"
"s_waitcnt lgkmcnt(8)\n"
"buffer_load_dwordx4 v221, s[92:95], s58 offen lds\n"
"v_mfma_f32_16x16x32_bf16 v[44:47], a[224:227], a[48:51], v[44:47]\n"
"ds_read_b64_tr_b16 a[208:209], v216 offset:36992\n"
"v_mfma_f32_16x16x32_bf16 v[44:47], a[228:231], a[52:55], v[44:47]\n"
"v_mfma_f32_16x16x32_bf16 v[44:47], a[232:235], a[56:59], v[44:47]\n"
"ds_read_b64_tr_b16 a[210:211], v217 offset:36992\n"
"v_mfma_f32_16x16x32_bf16 v[44:47], a[236:239], a[60:63], v[44:47]\n"
"buffer_load_dwordx4 v225, s[92:95], s58 offen offset:1024 lds\n"
"v_mfma_f32_16x16x32_bf16 v[40:43], a[236:239], a[44:47], v[40:43]\n"
"ds_read_b64_tr_b16 a[212:213], v216 offset:46208\n"
"v_mfma_f32_16x16x32_bf16 v[40:43], a[232:235], a[40:43], v[40:43]\n"
"v_mfma_f32_16x16x32_bf16 v[40:43], a[228:231], a[36:39], v[40:43]\n"
"ds_read_b64_tr_b16 a[214:215], v217 offset:46208\n"
"v_mfma_f32_16x16x32_bf16 v[40:43], a[224:227], a[32:35], v[40:43]\n"
"v_mfma_f32_16x16x32_bf16 v[36:39], a[224:227], a[16:19], v[36:39]\n"
"ds_read_b64_tr_b16 a[216:217], v216 offset:55424\n"
"v_mfma_f32_16x16x32_bf16 v[36:39], a[228:231], a[20:23], v[36:39]\n"
"buffer_load_dwordx4 v226, s[92:95], s58 offen offset:2048 lds\n"
"v_mfma_f32_16x16x32_bf16 v[36:39], a[232:235], a[24:27], v[36:39]\n"
"ds_read_b64_tr_b16 a[218:219], v217 offset:55424\n"
"v_mfma_f32_16x16x32_bf16 v[36:39], a[236:239], a[28:31], v[36:39]\n"
"v_mfma_f32_16x16x32_bf16 v[32:35], a[236:239], a[12:15], v[32:35]\n"
"ds_read_b64_tr_b16 a[220:221], v216 offset:64640\n"
"v_mfma_f32_16x16x32_bf16 v[32:35], a[232:235], a[8:11], v[32:35]\n"
"v_mfma_f32_16x16x32_bf16 v[32:35], a[228:231], a[4:7], v[32:35]\n"
"ds_read_b64_tr_b16 a[222:223], v217 offset:64640\n"
"v_mfma_f32_16x16x32_bf16 v[32:35], a[224:227], a[0:3], v[32:35]\n"
"s_waitcnt lgkmcnt(8)\n"
"buffer_load_dwordx4 v227, s[92:95], s58 offen offset:3072 lds\n"
"v_mfma_f32_16x16x32_bf16 v[48:51], a[192:195], a[0:3], v[48:51]\n"
"ds_read_b64_tr_b16 a[224:225], v216 offset:37024\n"
"v_mfma_f32_16x16x32_bf16 v[48:51], a[196:199], a[4:7], v[48:51]\n"
"v_mfma_f32_16x16x32_bf16 v[48:51], a[200:203], a[8:11], v[48:51]\n"
"ds_read_b64_tr_b16 a[226:227], v217 offset:37024\n"
"v_mfma_f32_16x16x32_bf16 v[48:51], a[204:207], a[12:15], v[48:51]\n"
"v_mfma_f32_16x16x32_bf16 v[52:55], a[204:207], a[28:31], v[52:55]\n"
"s_add_u32 m0, s60, 114688\n"
"ds_read_b64_tr_b16 a[228:229], v216 offset:46240\n"
"v_mfma_f32_16x16x32_bf16 v[52:55], a[200:203], a[24:27], v[52:55]\n"
"buffer_load_dwordx4 v228, s[92:95], s58 offen lds\n"
"v_mfma_f32_16x16x32_bf16 v[52:55], a[196:199], a[20:23], v[52:55]\n"
"ds_read_b64_tr_b16 a[230:231], v217 offset:46240\n"
"v_mfma_f32_16x16x32_bf16 v[52:55], a[192:195], a[16:19], v[52:55]\n"
"v_mfma_f32_16x16x32_bf16 v[56:59], a[192:195], a[32:35], v[56:59]\n"
"ds_read_b64_tr_b16 a[232:233], v216 offset:55456\n"
"v_mfma_f32_16x16x32_bf16 v[56:59], a[196:199], a[36:39], v[56:59]\n"
"buffer_load_dwordx4 v229, s[92:95], s58 offen offset:1024 lds\n"
"v_mfma_f32_16x16x32_bf16 v[56:59], a[200:203], a[40:43], v[56:59]\n"
"ds_read_b64_tr_b16 a[234:235], v217 offset:55456\n"
"v_mfma_f32_16x16x32_bf16 v[56:59], a[204:207], a[44:47], v[56:59]\n"
"v_mfma_f32_16x16x32_bf16 v[60:63], a[204:207], a[60:63], v[60:63]\n"
"ds_read_b64_tr_b16 a[236:237], v216 offset:64672\n"
"v_mfma_f32_16x16x32_bf16 v[60:63], a[200:203], a[56:59], v[60:63]\n"
"v_mfma_f32_16x16x32_bf16 v[60:63], a[196:199], a[52:55], v[60:63]\n"
"ds_read_b64_tr_b16 a[238:239], v217 offset:64672\n"
"v_mfma_f32_16x16x32_bf16 v[60:63], a[192:195], a[48:51], v[60:63]\n"
"s_waitcnt lgkmcnt(8)\n"
"buffer_load_dwordx4 v230, s[92:95], s58 offen offset:2048 lds\n"
"v_mfma_f32_16x16x32_bf16 v[76:79], a[208:211], a[48:51], v[76:79]\n"
"ds_read_b64_tr_b16 a[192:193], v216 offset:37056\n"
"v_mfma_f32_16x16x32_bf16 v[76:79], a[212:215], a[52:55], v[76:79]\n"
"v_mfma_f32_16x16x32_bf16 v[76:79], a[216:219], a[56:59], v[76:79]\n"
"ds_read_b64_tr_b16 a[194:195], v217 offset:37056\n"
"v_mfma_f32_16x16x32_bf16 v[76:79], a[220:223], a[60:63], v[76:79]\n"
"v_mfma_f32_16x16x32_bf16 v[72:75], a[220:223], a[44:47], v[72:75]\n"
"ds_read_b64_tr_b16 a[196:197], v216 offset:46272\n"
"v_mfma_f32_16x16x32_bf16 v[72:75], a[216:219], a[40:43], v[72:75]\n"
"buffer_load_dwordx4 v231, s[92:95], s58 offen offset:3072 lds\n"
"v_mfma_f32_16x16x32_bf16 v[72:75], a[212:215], a[36:39], v[72:75]\n"
"ds_read_b64_tr_b16 a[198:199], v217 offset:46272\n"
"v_mfma_f32_16x16x32_bf16 v[72:75], a[208:211], a[32:35], v[72:75]\n"
"v_mfma_f32_16x16x32_bf16 v[68:71], a[208:211], a[16:19], v[68:71]\n"
"ds_read_b64_tr_b16 a[200:201], v216 offset:55488\n"
"v_mfma_f32_16x16x32_bf16 v[68:71], a[212:215], a[20:23], v[68:71]\n"
"v_mfma_f32_16x16x32_bf16 v[68:71], a[216:219], a[24:27], v[68:71]\n"
"s_add_u32 m0, s60, 118784\n"
"ds_read_b64_tr_b16 a[202:203], v217 offset:55488\n"
"v_mfma_f32_16x16x32_bf16 v[68:71], a[220:223], a[28:31], v[68:71]\n"
"buffer_load_dwordx4 v235, s[92:95], s58 offen lds\n"
"v_mfma_f32_16x16x32_bf16 v[64:67], a[220:223], a[12:15], v[64:67]\n"
"ds_read_b64_tr_b16 a[204:205], v216 offset:64704\n"
"v_mfma_f32_16x16x32_bf16 v[64:67], a[216:219], a[8:11], v[64:67]\n"
"v_mfma_f32_16x16x32_bf16 v[64:67], a[212:215], a[4:7], v[64:67]\n"
"s_cmp_le_u32 s59, 1\n"
"s_cselect_b32 s72, 5, 3\n"
"s_mul_i32 s72, s72, 65536\n"
"s_add_u32 s77, s76, s72\n"
"s_add_u32 s66, s77, 0\n"
"ds_read_b64_tr_b16 a[206:207], v217 offset:64704\n"
"v_mfma_f32_16x16x32_bf16 v[64:67], a[208:211], a[0:3], v[64:67]\n"
"s_waitcnt lgkmcnt(8)\n"
"buffer_load_dwordx4 a[64:67], v232, s[52:55], s66 offen sc1\n"
"v_mfma_f32_16x16x32_bf16 v[80:83], a[224:227], a[0:3], v[80:83]\n"
"ds_read_b64_tr_b16 a[208:209], v216 offset:37088\n"
"v_mfma_f32_16x16x32_bf16 v[80:83], a[228:231], a[4:7], v[80:83]\n"
"v_mfma_f32_16x16x32_bf16 v[80:83], a[232:235], a[8:11], v[80:83]\n"
"ds_read_b64_tr_b16 a[210:211], v217 offset:37088\n"
"v_mfma_f32_16x16x32_bf16 v[80:83], a[236:239], a[12:15], v[80:83]\n"
"v_mfma_f32_16x16x32_bf16 v[84:87], a[236:239], a[28:31], v[84:87]\n"
"ds_read_b64_tr_b16 a[212:213], v216 offset:46304\n"
"v_mfma_f32_16x16x32_bf16 v[84:87], a[232:235], a[24:27], v[84:87]\n"
"buffer_load_dwordx4 a[68:71], v232, s[52:55], s66 offen offset:1024 sc1\n"
"v_mfma_f32_16x16x32_bf16 v[84:87], a[228:231], a[20:23], v[84:87]\n"
"ds_read_b64_tr_b16 a[214:215], v217 offset:46304\n"
"v_mfma_f32_16x16x32_bf16 v[84:87], a[224:227], a[16:19], v[84:87]\n"
"v_mfma_f32_16x16x32_bf16 v[88:91], a[224:227], a[32:35], v[88:91]\n"
"ds_read_b64_tr_b16 a[216:217], v216 offset:55520\n"
"v_mfma_f32_16x16x32_bf16 v[88:91], a[228:231], a[36:39], v[88:91]\n"
"v_mfma_f32_16x16x32_bf16 v[88:91], a[232:235], a[40:43], v[88:91]\n"
"ds_read_b64_tr_b16 a[218:219], v217 offset:55520\n"
"v_mfma_f32_16x16x32_bf16 v[88:91], a[236:239], a[44:47], v[88:91]\n"
"buffer_load_dwordx4 a[72:75], v232, s[52:55], s66 offen offset:2048 sc1\n"
"v_mfma_f32_16x16x32_bf16 v[92:95], a[236:239], a[60:63], v[92:95]\n"
"ds_read_b64_tr_b16 a[220:221], v216 offset:64736\n"
"v_mfma_f32_16x16x32_bf16 v[92:95], a[232:235], a[56:59], v[92:95]\n"
"v_mfma_f32_16x16x32_bf16 v[92:95], a[228:231], a[52:55], v[92:95]\n"
"ds_read_b64_tr_b16 a[222:223], v217 offset:64736\n"
"v_mfma_f32_16x16x32_bf16 v[92:95], a[224:227], a[48:51], v[92:95]\n"
"s_waitcnt lgkmcnt(8)\n"
"v_mfma_f32_16x16x32_bf16 v[108:111], a[192:195], a[48:51], v[108:111]\n"
"ds_read_b64_tr_b16 a[224:225], v216 offset:37120\n"
"v_mfma_f32_16x16x32_bf16 v[108:111], a[196:199], a[52:55], v[108:111]\n"
"buffer_load_dwordx4 a[76:79], v232, s[52:55], s66 offen offset:3072 sc1\n"
"v_mfma_f32_16x16x32_bf16 v[108:111], a[200:203], a[56:59], v[108:111]\n"
"ds_read_b64_tr_b16 a[226:227], v217 offset:37120\n"
"v_mfma_f32_16x16x32_bf16 v[108:111], a[204:207], a[60:63], v[108:111]\n"
"v_mfma_f32_16x16x32_bf16 v[104:107], a[204:207], a[44:47], v[104:107]\n"
"s_add_u32 s66, s77, 4096\n"
"ds_read_b64_tr_b16 a[228:229], v216 offset:46336\n"
"v_mfma_f32_16x16x32_bf16 v[104:107], a[200:203], a[40:43], v[104:107]\n"
"buffer_load_dwordx4 a[80:83], v232, s[52:55], s66 offen sc1\n"
"v_mfma_f32_16x16x32_bf16 v[104:107], a[196:199], a[36:39], v[104:107]\n"
"ds_read_b64_tr_b16 a[230:231], v217 offset:46336\n"
"v_mfma_f32_16x16x32_bf16 v[104:107], a[192:195], a[32:35], v[104:107]\n"
"v_mfma_f32_16x16x32_bf16 v[100:103], a[192:195], a[16:19], v[100:103]\n"
"ds_read_b64_tr_b16 a[232:233], v216 offset:55552\n"
"v_mfma_f32_16x16x32_bf16 v[100:103], a[196:199], a[20:23], v[100:103]\n"
"v_mfma_f32_16x16x32_bf16 v[100:103], a[200:203], a[24:27], v[100:103]\n"
"ds_read_b64_tr_b16 a[234:235], v217 offset:55552\n"
"v_mfma_f32_16x16x32_bf16 v[100:103], a[204:207], a[28:31], v[100:103]\n"
"buffer_load_dwordx4 a[84:87], v232, s[52:55], s66 offen offset:1024 sc1\n"
"v_mfma_f32_16x16x32_bf16 v[96:99], a[204:207], a[12:15], v[96:99]\n"
"ds_read_b64_tr_b16 a[236:237], v216 offset:64768\n"
"v_mfma_f32_16x16x32_bf16 v[96:99], a[200:203], a[8:11], v[96:99]\n"
"v_mfma_f32_16x16x32_bf16 v[96:99], a[196:199], a[4:7], v[96:99]\n"
"ds_read_b64_tr_b16 a[238:239], v217 offset:64768\n"
"v_mfma_f32_16x16x32_bf16 v[96:99], a[192:195], a[0:3], v[96:99]\n"
"s_waitcnt lgkmcnt(8)\n"
"v_mfma_f32_16x16x32_bf16 v[112:115], a[208:211], a[0:3], v[112:115]\n"
"v_mfma_f32_16x16x32_bf16 v[112:115], a[212:215], a[4:7], v[112:115]\n"
"buffer_load_dwordx4 a[88:91], v232, s[52:55], s66 offen offset:2048 sc1\n"
"v_mfma_f32_16x16x32_bf16 v[112:115], a[216:219], a[8:11], v[112:115]\n"
"v_mfma_f32_16x16x32_bf16 v[112:115], a[220:223], a[12:15], v[112:115]\n"
"s_waitcnt vmcnt(32) lgkmcnt(0)\n"
"v_mfma_f32_16x16x32_bf16 v[116:119], a[220:223], a[28:31], v[116:119]\n"
"s_barrier\n"
"v_mfma_f32_16x16x32_bf16 v[116:119], a[216:219], a[24:27], v[116:119]\n"
"v_mfma_f32_16x16x32_bf16 v[116:119], a[212:215], a[20:23], v[116:119]\n"
"ds_read_b64_tr_b16 a[192:193], v218\n"
"ds_read_b64_tr_b16 a[194:195], v219\n"
"v_mfma_f32_16x16x32_bf16 v[116:119], a[208:211], a[16:19], v[116:119]\n"
"buffer_load_dwordx4 a[92:95], v232, s[52:55], s66 offen offset:3072 sc1\n"
"v_mfma_f32_16x16x32_bf16 v[120:123], a[208:211], a[32:35], v[120:123]\n"
"ds_read_b64_tr_b16 a[196:197], v218 offset:9216\n"
"ds_read_b64_tr_b16 a[198:199], v219 offset:9216\n"
"v_mfma_f32_16x16x32_bf16 v[120:123], a[212:215], a[36:39], v[120:123]\n"
"v_mfma_f32_16x16x32_bf16 v[120:123], a[216:219], a[40:43], v[120:123]\n"
"s_add_u32 s66, s77, 8192\n"
"ds_read_b64_tr_b16 a[200:201], v218 offset:18432\n"
"ds_read_b64_tr_b16 a[202:203], v219 offset:18432\n"
"v_mfma_f32_16x16x32_bf16 v[120:123], a[220:223], a[44:47], v[120:123]\n"
"buffer_load_dwordx4 a[96:99], v232, s[52:55], s66 offen sc1\n"
"v_mfma_f32_16x16x32_bf16 v[124:127], a[220:223], a[60:63], v[124:127]\n"
"ds_read_b64_tr_b16 a[204:205], v218 offset:27648\n"
"v_mfma_f32_16x16x32_bf16 v[124:127], a[216:219], a[56:59], v[124:127]\n"
"v_mfma_f32_16x16x32_bf16 v[124:127], a[212:215], a[52:55], v[124:127]\n"
"ds_read_b64_tr_b16 a[206:207], v219 offset:27648\n"
"v_mfma_f32_16x16x32_bf16 v[124:127], a[208:211], a[48:51], v[124:127]\n"
"v_mfma_f32_16x16x32_bf16 v[140:143], a[224:227], a[48:51], v[140:143]\n"
"ds_read_b64_tr_b16 a[208:209], v218 offset:32\n"
"v_mfma_f32_16x16x32_bf16 v[140:143], a[228:231], a[52:55], v[140:143]\n"
"buffer_load_dwordx4 a[100:103], v232, s[52:55], s66 offen offset:1024 sc1\n"
"v_mfma_f32_16x16x32_bf16 v[140:143], a[232:235], a[56:59], v[140:143]\n"
"ds_read_b64_tr_b16 a[210:211], v219 offset:32\n"
"v_mfma_f32_16x16x32_bf16 v[140:143], a[236:239], a[60:63], v[140:143]\n"
"v_mfma_f32_16x16x32_bf16 v[136:139], a[236:239], a[44:47], v[136:139]\n"
"ds_read_b64_tr_b16 a[212:213], v218 offset:9248\n"
"v_mfma_f32_16x16x32_bf16 v[136:139], a[232:235], a[40:43], v[136:139]\n"
"v_mfma_f32_16x16x32_bf16 v[136:139], a[228:231], a[36:39], v[136:139]\n"
"ds_read_b64_tr_b16 a[214:215], v219 offset:9248\n"
"v_mfma_f32_16x16x32_bf16 v[136:139], a[224:227], a[32:35], v[136:139]\n"
"buffer_load_dwordx4 a[104:107], v232, s[52:55], s66 offen offset:2048 sc1\n"
"v_mfma_f32_16x16x32_bf16 v[132:135], a[224:227], a[16:19], v[132:135]\n"
"ds_read_b64_tr_b16 a[216:217], v218 offset:18464\n"
"v_mfma_f32_16x16x32_bf16 v[132:135], a[228:231], a[20:23], v[132:135]\n"
"v_mfma_f32_16x16x32_bf16 v[132:135], a[232:235], a[24:27], v[132:135]\n"
"ds_read_b64_tr_b16 a[218:219], v219 offset:18464\n"
"v_mfma_f32_16x16x32_bf16 v[132:135], a[236:239], a[28:31], v[132:135]\n"
"v_mfma_f32_16x16x32_bf16 v[128:131], a[236:239], a[12:15], v[128:131]\n"
"ds_read_b64_tr_b16 a[220:221], v218 offset:27680\n"
"v_mfma_f32_16x16x32_bf16 v[128:131], a[232:235], a[8:11], v[128:131]\n"
"buffer_load_dwordx4 a[108:111], v232, s[52:55], s66 offen offset:3072 sc1\n"
"v_mfma_f32_16x16x32_bf16 v[128:131], a[228:231], a[4:7], v[128:131]\n"
"ds_read_b64_tr_b16 a[222:223], v219 offset:27680\n"
"v_mfma_f32_16x16x32_bf16 v[128:131], a[224:227], a[0:3], v[128:131]\n"
"s_waitcnt vmcnt(21) lgkmcnt(8)\n"
"v_mfma_f32_16x16x32_bf16 v[0:3], a[192:195], a[128:131], v[0:3]\n"
"s_add_u32 s66, s77, 12288\n"
"ds_read_b64_tr_b16 a[224:225], v218 offset:64\n"
"v_mfma_f32_16x16x32_bf16 v[0:3], a[196:199], a[132:135], v[0:3]\n"
"buffer_load_dwordx4 a[112:115], v232, s[52:55], s66 offen sc1\n"
"v_mfma_f32_16x16x32_bf16 v[0:3], a[200:203], a[136:139], v[0:3]\n"
"ds_read_b64_tr_b16 a[226:227], v219 offset:64\n"
"v_mfma_f32_16x16x32_bf16 v[0:3], a[204:207], a[140:143], v[0:3]\n"
"v_mfma_f32_16x16x32_bf16 v[4:7], a[204:207], a[156:159], v[4:7]\n"
"ds_read_b64_tr_b16 a[228:229], v218 offset:9280\n"
"v_mfma_f32_16x16x32_bf16 v[4:7], a[200:203], a[152:155], v[4:7]\n"
"v_mfma_f32_16x16x32_bf16 v[4:7], a[196:199], a[148:151], v[4:7]\n"
"ds_read_b64_tr_b16 a[230:231], v219 offset:9280\n"
"v_mfma_f32_16x16x32_bf16 v[4:7], a[192:195], a[144:147], v[4:7]\n"
"buffer_load_dwordx4 a[116:119], v232, s[52:55], s66 offen offset:1024 sc1\n"
"v_mfma_f32_16x16x32_bf16 v[8:11], a[192:195], a[160:163], v[8:11]\n"
"ds_read_b64_tr_b16 a[232:233], v218 offset:18496\n"
"v_mfma_f32_16x16x32_bf16 v[8:11], a[196:199], a[164:167], v[8:11]\n"
"v_mfma_f32_16x16x32_bf16 v[8:11], a[200:203], a[168:171], v[8:11]\n"
"ds_read_b64_tr_b16 a[234:235], v219 offset:18496\n"
"v_mfma_f32_16x16x32_bf16 v[8:11], a[204:207], a[172:175], v[8:11]\n"
"v_mfma_f32_16x16x32_bf16 v[12:15], a[204:207], a[188:191], v[12:15]\n"
"ds_read_b64_tr_b16 a[236:237], v218 offset:27712\n"
"v_mfma_f32_16x16x32_bf16 v[12:15], a[200:203], a[184:187], v[12:15]\n"
"buffer_load_dwordx4 a[120:123], v232, s[52:55], s66 offen offset:2048 sc1\n"
"v_mfma_f32_16x16x32_bf16 v[12:15], a[196:199], a[180:183], v[12:15]\n"
"ds_read_b64_tr_b16 a[238:239], v219 offset:27712\n"
"v_mfma_f32_16x16x32_bf16 v[12:15], a[192:195], a[176:179], v[12:15]\n"
"s_waitcnt lgkmcnt(8)\n"
"v_mfma_f32_16x16x32_bf16 v[28:31], a[208:211], a[176:179], v[28:31]\n"
"ds_read_b64_tr_b16 a[192:193], v218 offset:96\n"
"v_mfma_f32_16x16x32_bf16 v[28:31], a[212:215], a[180:183], v[28:31]\n"
"v_mfma_f32_16x16x32_bf16 v[28:31], a[216:219], a[184:187], v[28:31]\n"
"ds_read_b64_tr_b16 a[194:195], v219 offset:96\n"
"v_mfma_f32_16x16x32_bf16 v[28:31], a[220:223], a[188:191], v[28:31]\n"
"buffer_load_dwordx4 a[124:127], v232, s[52:55], s66 offen offset:3072 sc1\n"
"v_mfma_f32_16x16x32_bf16 v[24:27], a[220:223], a[172:175], v[24:27]\n"
"ds_read_b64_tr_b16 a[196:197], v218 offset:9312\n"
"v_mfma_f32_16x16x32_bf16 v[24:27], a[216:219], a[168:171], v[24:27]\n"
"v_mfma_f32_16x16x32_bf16 v[24:27], a[212:215], a[164:167], v[24:27]\n"
"s_cmp_le_u32 s59, 2\n"
"s_cselect_b32 s72, 7, 5\n"
"s_mul_i32 s58, s72, 147456\n"
"s_add_u32 m0, s60, 0\n"
"ds_read_b64_tr_b16 a[198:199], v219 offset:9312\n"
"v_mfma_f32_16x16x32_bf16 v[24:27], a[208:211], a[160:163], v[24:27]\n"
"buffer_load_dwordx4 v221, s[92:95], s58 offen lds\n"
"v_mfma_f32_16x16x32_bf16 v[20:23], a[208:211], a[144:147], v[20:23]\n"
"ds_read_b64_tr_b16 a[200:201], v218 offset:18528\n"
"v_mfma_f32_16x16x32_bf16 v[20:23], a[212:215], a[148:151], v[20:23]\n"
"v_mfma_f32_16x16x32_bf16 v[20:23], a[216:219], a[152:155], v[20:23]\n"
"ds_read_b64_tr_b16 a[202:203], v219 offset:18528\n"
"v_mfma_f32_16x16x32_bf16 v[20:23], a[220:223], a[156:159], v[20:23]\n"
"v_mfma_f32_16x16x32_bf16 v[16:19], a[220:223], a[140:143], v[16:19]\n"
"ds_read_b64_tr_b16 a[204:205], v218 offset:27744\n"
"v_mfma_f32_16x16x32_bf16 v[16:19], a[216:219], a[136:139], v[16:19]\n"
"buffer_load_dwordx4 v225, s[92:95], s58 offen offset:1024 lds\n"
"v_mfma_f32_16x16x32_bf16 v[16:19], a[212:215], a[132:135], v[16:19]\n"
"ds_read_b64_tr_b16 a[206:207], v219 offset:27744\n"
"v_mfma_f32_16x16x32_bf16 v[16:19], a[208:211], a[128:131], v[16:19]\n"
"s_waitcnt lgkmcnt(8)\n"
"v_mfma_f32_16x16x32_bf16 v[32:35], a[224:227], a[128:131], v[32:35]\n"
"ds_read_b64_tr_b16 a[208:209], v218 offset:128\n"
"v_mfma_f32_16x16x32_bf16 v[32:35], a[228:231], a[132:135], v[32:35]\n"
"v_mfma_f32_16x16x32_bf16 v[32:35], a[232:235], a[136:139], v[32:35]\n"
"ds_read_b64_tr_b16 a[210:211], v219 offset:128\n"
"v_mfma_f32_16x16x32_bf16 v[32:35], a[236:239], a[140:143], v[32:35]\n"
"buffer_load_dwordx4 v226, s[92:95], s58 offen offset:2048 lds\n"
"v_mfma_f32_16x16x32_bf16 v[36:39], a[236:239], a[156:159], v[36:39]\n"
"ds_read_b64_tr_b16 a[212:213], v218 offset:9344\n"
"v_mfma_f32_16x16x32_bf16 v[36:39], a[232:235], a[152:155], v[36:39]\n"
"v_mfma_f32_16x16x32_bf16 v[36:39], a[228:231], a[148:151], v[36:39]\n"
"s_add_u32 s74, s69, 128\n"
"ds_read_b64_tr_b16 a[214:215], v219 offset:9344\n"
"v_mfma_f32_16x16x32_bf16 v[36:39], a[224:227], a[144:147], v[36:39]\n"
"v_mfma_f32_16x16x32_bf16 v[40:43], a[224:227], a[160:163], v[40:43]\n"
"ds_read_b64_tr_b16 a[216:217], v218 offset:18560\n"
"v_mfma_f32_16x16x32_bf16 v[40:43], a[228:231], a[164:167], v[40:43]\n"
"buffer_load_dwordx4 v227, s[92:95], s58 offen offset:3072 lds\n"
"v_mfma_f32_16x16x32_bf16 v[40:43], a[232:235], a[168:171], v[40:43]\n"
"s_add_u32 s84, s23, 0\n"
"ds_read_b64_tr_b16 a[218:219], v219 offset:18560\n"
"v_mfma_f32_16x16x32_bf16 v[40:43], a[236:239], a[172:175], v[40:43]\n"
"v_mfma_f32_16x16x32_bf16 v[44:47], a[236:239], a[188:191], v[44:47]\n"
"s_add_u32 m0, s60, 4096\n"
"ds_read_b64_tr_b16 a[220:221], v218 offset:27776\n"
"v_mfma_f32_16x16x32_bf16 v[44:47], a[232:235], a[184:187], v[44:47]\n"
"buffer_load_dwordx4 v228, s[92:95], s58 offen lds\n"
"v_mfma_f32_16x16x32_bf16 v[44:47], a[228:231], a[180:183], v[44:47]\n"
"s_and_b32 s84, s84, 15\n"
"ds_read_b64_tr_b16 a[222:223], v219 offset:27776\n"
"v_mfma_f32_16x16x32_bf16 v[44:47], a[224:227], a[176:179], v[44:47]\n"
"s_waitcnt lgkmcnt(8)\n"
"v_mfma_f32_16x16x32_bf16 v[60:63], a[192:195], a[176:179], v[60:63]\n"
"ds_read_b64_tr_b16 a[224:225], v218 offset:160\n"
"v_mfma_f32_16x16x32_bf16 v[60:63], a[196:199], a[180:183], v[60:63]\n"
"v_mfma_f32_16x16x32_bf16 v[60:63], a[200:203], a[184:187], v[60:63]\n"
"s_lshl2_add_u32 s84, s84, s74\n"
"ds_read_b64_tr_b16 a[226:227], v219 offset:160\n"
"v_mfma_f32_16x16x32_bf16 v[60:63], a[204:207], a[188:191], v[60:63]\n"
"buffer_load_dwordx4 v229, s[92:95], s58 offen offset:1024 lds\n"
"v_mfma_f32_16x16x32_bf16 v[56:59], a[204:207], a[172:175], v[56:59]\n"
"ds_read_b64_tr_b16 a[228:229], v218 offset:9376\n"
"v_mfma_f32_16x16x32_bf16 v[56:59], a[200:203], a[168:171], v[56:59]\n"
"v_mfma_f32_16x16x32_bf16 v[56:59], a[196:199], a[164:167], v[56:59]\n"
"s_mul_i32 s84, s84, 16384\n"
"ds_read_b64_tr_b16 a[230:231], v219 offset:9376\n"
"v_mfma_f32_16x16x32_bf16 v[56:59], a[192:195], a[160:163], v[56:59]\n"
"v_mfma_f32_16x16x32_bf16 v[52:55], a[192:195], a[144:147], v[52:55]\n"
"ds_read_b64_tr_b16 a[232:233], v218 offset:18592\n"
"v_mfma_f32_16x16x32_bf16 v[52:55], a[196:199], a[148:151], v[52:55]\n"
"buffer_load_dwordx4 v230, s[92:95], s58 offen offset:2048 lds\n"
"v_mfma_f32_16x16x32_bf16 v[52:55], a[200:203], a[152:155], v[52:55]\n"
"s_add_u32 s85, s23, 4\n"
"ds_read_b64_tr_b16 a[234:235], v219 offset:18592\n"
"v_mfma_f32_16x16x32_bf16 v[52:55], a[204:207], a[156:159], v[52:55]\n"
"v_mfma_f32_16x16x32_bf16 v[48:51], a[204:207], a[140:143], v[48:51]\n"
"ds_read_b64_tr_b16 a[236:237], v218 offset:27808\n"
"v_mfma_f32_16x16x32_bf16 v[48:51], a[200:203], a[136:139], v[48:51]\n"
"v_mfma_f32_16x16x32_bf16 v[48:51], a[196:199], a[132:135], v[48:51]\n"
"s_and_b32 s85, s85, 15\n"
"ds_read_b64_tr_b16 a[238:239], v219 offset:27808\n"
"v_mfma_f32_16x16x32_bf16 v[48:51], a[192:195], a[128:131], v[48:51]\n"
"s_waitcnt lgkmcnt(8)\n"
"buffer_load_dwordx4 v231, s[92:95], s58 offen offset:3072 lds\n"
"v_mfma_f32_16x16x32_bf16 v[64:67], a[208:211], a[128:131], v[64:67]\n"
"ds_read_b64_tr_b16 a[192:193], v218 offset:192\n"
"v_mfma_f32_16x16x32_bf16 v[64:67], a[212:215], a[132:135], v[64:67]\n"
"s_add_u32 m0, s60, 8192\n"
"v_mfma_f32_16x16x32_bf16 v[64:67], a[216:219], a[136:139], v[64:67]\n"
"s_lshl2_add_u32 s85, s85, s74\n"
"ds_read_b64_tr_b16 a[194:195], v219 offset:192\n"
"v_mfma_f32_16x16x32_bf16 v[64:67], a[220:223], a[140:143], v[64:67]\n"
"buffer_load_dwordx4 v235, s[92:95], s58 offen lds\n"
"v_mfma_f32_16x16x32_bf16 v[68:71], a[220:223], a[156:159], v[68:71]\n"
"ds_read_b64_tr_b16 a[196:197], v218 offset:9408\n"
"v_mfma_f32_16x16x32_bf16 v[68:71], a[216:219], a[152:155], v[68:71]\n"
"v_mfma_f32_16x16x32_bf16 v[68:71], a[212:215], a[148:151], v[68:71]\n"
"s_mul_i32 s85, s85, 16384\n"
"ds_read_b64_tr_b16 a[198:199], v219 offset:9408\n"
"v_mfma_f32_16x16x32_bf16 v[68:71], a[208:211], a[144:147], v[68:71]\n"
"v_mfma_f32_16x16x32_bf16 v[72:75], a[208:211], a[160:163], v[72:75]\n"
"s_cmp_le_u32 s59, 2\n"
"s_cselect_b32 s72, 7, 5\n"
"s_mul_i32 s72, s72, 65536\n"
"s_add_u32 s77, s76, s72\n"
"s_add_u32 s66, s77, 0\n"
"ds_read_b64_tr_b16 a[200:201], v218 offset:18624\n"
"v_mfma_f32_16x16x32_bf16 v[72:75], a[212:215], a[164:167], v[72:75]\n"
"buffer_load_dwordx4 a[0:3], v232, s[52:55], s66 offen sc1\n"
"v_mfma_f32_16x16x32_bf16 v[72:75], a[216:219], a[168:171], v[72:75]\n"
"s_add_u32 s87, s23, 8\n"
"ds_read_b64_tr_b16 a[202:203], v219 offset:18624\n"
"v_mfma_f32_16x16x32_bf16 v[72:75], a[220:223], a[172:175], v[72:75]\n"
"v_mfma_f32_16x16x32_bf16 v[76:79], a[220:223], a[188:191], v[76:79]\n"
"ds_read_b64_tr_b16 a[204:205], v218 offset:27840\n"
"v_mfma_f32_16x16x32_bf16 v[76:79], a[216:219], a[184:187], v[76:79]\n"
"v_mfma_f32_16x16x32_bf16 v[76:79], a[212:215], a[180:183], v[76:79]\n"
"s_and_b32 s87, s87, 15\n"
"ds_read_b64_tr_b16 a[206:207], v219 offset:27840\n"
"v_mfma_f32_16x16x32_bf16 v[76:79], a[208:211], a[176:179], v[76:79]\n"
"s_waitcnt lgkmcnt(8)\n"
"buffer_load_dwordx4 a[4:7], v232, s[52:55], s66 offen offset:1024 sc1\n"
"v_mfma_f32_16x16x32_bf16 v[92:95], a[224:227], a[176:179], v[92:95]\n"
"ds_read_b64_tr_b16 a[208:209], v218 offset:224\n"
"v_mfma_f32_16x16x32_bf16 v[92:95], a[228:231], a[180:183], v[92:95]\n"
"v_mfma_f32_16x16x32_bf16 v[92:95], a[232:235], a[184:187], v[92:95]\n"
"s_lshl2_add_u32 s87, s87, s74\n"
"ds_read_b64_tr_b16 a[210:211], v219 offset:224\n"
"v_mfma_f32_16x16x32_bf16 v[92:95], a[236:239], a[188:191], v[92:95]\n"
"v_mfma_f32_16x16x32_bf16 v[88:91], a[236:239], a[172:175], v[88:91]\n"
"ds_read_b64_tr_b16 a[212:213], v218 offset:9440\n"
"v_mfma_f32_16x16x32_bf16 v[88:91], a[232:235], a[168:171], v[88:91]\n"
"buffer_load_dwordx4 a[8:11], v232, s[52:55], s66 offen offset:2048 sc1\n"
"v_mfma_f32_16x16x32_bf16 v[88:91], a[228:231], a[164:167], v[88:91]\n"
"s_mul_i32 s87, s87, 16384\n"
"ds_read_b64_tr_b16 a[214:215], v219 offset:9440\n"
"v_mfma_f32_16x16x32_bf16 v[88:91], a[224:227], a[160:163], v[88:91]\n"
"v_mfma_f32_16x16x32_bf16 v[84:87], a[224:227], a[144:147], v[84:87]\n"
"ds_read_b64_tr_b16 a[216:217], v218 offset:18656\n"
"v_mfma_f32_16x16x32_bf16 v[84:87], a[228:231], a[148:151], v[84:87]\n"
"buffer_load_dwordx4 a[12:15], v232, s[52:55], s66 offen offset:3072 sc1\n"
"v_mfma_f32_16x16x32_bf16 v[84:87], a[232:235], a[152:155], v[84:87]\n"
"s_add_u32 s68, s23, 12\n"
"ds_read_b64_tr_b16 a[218:219], v219 offset:18656\n"
"v_mfma_f32_16x16x32_bf16 v[84:87], a[236:239], a[156:159], v[84:87]\n"
"v_mfma_f32_16x16x32_bf16 v[80:83], a[236:239], a[140:143], v[80:83]\n"
"ds_read_b64_tr_b16 a[220:221], v218 offset:27872\n"
"v_mfma_f32_16x16x32_bf16 v[80:83], a[232:235], a[136:139], v[80:83]\n"
"s_add_u32 s66, s77, 4096\n"
"v_mfma_f32_16x16x32_bf16 v[80:83], a[228:231], a[132:135], v[80:83]\n"
"s_and_b32 s68, s68, 15\n"
"ds_read_b64_tr_b16 a[222:223], v219 offset:27872\n"
"v_mfma_f32_16x16x32_bf16 v[80:83], a[224:227], a[128:131], v[80:83]\n"
"s_waitcnt lgkmcnt(8)\n"
"buffer_load_dwordx4 a[16:19], v232, s[52:55], s66 offen sc1\n"
"v_mfma_f32_16x16x32_bf16 v[96:99], a[192:195], a[128:131], v[96:99]\n"
"ds_read_b64_tr_b16 a[224:225], v218 offset:256\n"
"v_mfma_f32_16x16x32_bf16 v[96:99], a[196:199], a[132:135], v[96:99]\n"
"v_mfma_f32_16x16x32_bf16 v[96:99], a[200:203], a[136:139], v[96:99]\n"
"s_lshl2_add_u32 s68, s68, s74\n"
"ds_read_b64_tr_b16 a[226:227], v219 offset:256\n"
"v_mfma_f32_16x16x32_bf16 v[96:99], a[204:207], a[140:143], v[96:99]\n"
"v_mfma_f32_16x16x32_bf16 v[100:103], a[204:207], a[156:159], v[100:103]\n"
"ds_read_b64_tr_b16 a[228:229], v218 offset:9472\n"
"v_mfma_f32_16x16x32_bf16 v[100:103], a[200:203], a[152:155], v[100:103]\n"
"buffer_load_dwordx4 a[20:23], v232, s[52:55], s66 offen offset:1024 sc1\n"
"v_mfma_f32_16x16x32_bf16 v[100:103], a[196:199], a[148:151], v[100:103]\n"
"s_mul_i32 s68, s68, 16384\n"
"ds_read_b64_tr_b16 a[230:231], v219 offset:9472\n"
"v_mfma_f32_16x16x32_bf16 v[100:103], a[192:195], a[144:147], v[100:103]\n"
"v_mfma_f32_16x16x32_bf16 v[104:107], a[192:195], a[160:163], v[104:107]\n"
"ds_read_b64_tr_b16 a[232:233], v218 offset:18688\n"
"v_mfma_f32_16x16x32_bf16 v[104:107], a[196:199], a[164:167], v[104:107]\n"
"v_mfma_f32_16x16x32_bf16 v[104:107], a[200:203], a[168:171], v[104:107]\n"
"s_mov_b64 s[36:37], s[18:19]\n"
"ds_read_b64_tr_b16 a[234:235], v219 offset:18688\n"
"v_mfma_f32_16x16x32_bf16 v[104:107], a[204:207], a[172:175], v[104:107]\n"
"buffer_load_dwordx4 a[24:27], v232, s[52:55], s66 offen offset:2048 sc1\n"
"v_mfma_f32_16x16x32_bf16 v[108:111], a[204:207], a[188:191], v[108:111]\n"
"ds_read_b64_tr_b16 a[236:237], v218 offset:27904\n"
"v_mfma_f32_16x16x32_bf16 v[108:111], a[200:203], a[184:187], v[108:111]\n"
"v_mfma_f32_16x16x32_bf16 v[108:111], a[196:199], a[180:183], v[108:111]\n"
"s_and_b32 s37, s37, 0xffff\n"
"ds_read_b64_tr_b16 a[238:239], v219 offset:27904\n"
"v_mfma_f32_16x16x32_bf16 v[108:111], a[192:195], a[176:179], v[108:111]\n"
"s_waitcnt lgkmcnt(8)\n"
"buffer_load_dwordx4 a[28:31], v232, s[52:55], s66 offen offset:3072 sc1\n"
"v_mfma_f32_16x16x32_bf16 v[124:127], a[208:211], a[176:179], v[124:127]\n"
"v_mfma_f32_16x16x32_bf16 v[124:127], a[212:215], a[180:183], v[124:127]\n"
"s_add_u32 s66, s77, 8192\n"
"v_mfma_f32_16x16x32_bf16 v[124:127], a[216:219], a[184:187], v[124:127]\n"
"s_mov_b32 s38, 477216\n"
"v_mfma_f32_16x16x32_bf16 v[124:127], a[220:223], a[188:191], v[124:127]\n"
"s_waitcnt vmcnt(33) lgkmcnt(0)\n"
"v_mfma_f32_16x16x32_bf16 v[120:123], a[220:223], a[172:175], v[120:123]\n"
"s_barrier\n"
"v_mfma_f32_16x16x32_bf16 v[120:123], a[216:219], a[168:171], v[120:123]\n"
"buffer_load_dwordx4 a[32:35], v232, s[52:55], s66 offen sc1\n"
"v_mfma_f32_16x16x32_bf16 v[120:123], a[212:215], a[164:167], v[120:123]\n"
"s_mov_b64 s[40:41], s[52:53]\n"
"ds_read_b64_tr_b16 a[192:193], v218 offset:36864\n"
"ds_read_b64_tr_b16 a[194:195], v219 offset:36864\n"
"v_mfma_f32_16x16x32_bf16 v[120:123], a[208:211], a[160:163], v[120:123]\n"
"v_mfma_f32_16x16x32_bf16 v[116:119], a[208:211], a[144:147], v[116:119]\n"
"ds_read_b64_tr_b16 a[196:197], v218 offset:46080\n"
"ds_read_b64_tr_b16 a[198:199], v219 offset:46080\n"
"v_mfma_f32_16x16x32_bf16 v[116:119], a[212:215], a[148:151], v[116:119]\n"
"v_mfma_f32_16x16x32_bf16 v[116:119], a[216:219], a[152:155], v[116:119]\n"
"s_mov_b32 s62, 0xee\n"
"ds_read_b64_tr_b16 a[200:201], v218 offset:55296\n"
"ds_read_b64_tr_b16 a[202:203], v219 offset:55296\n"
"v_mfma_f32_16x16x32_bf16 v[116:119], a[220:223], a[156:159], v[116:119]\n"
"buffer_load_dwordx4 a[36:39], v232, s[52:55], s66 offen offset:1024 sc1\n"
"v_mfma_f32_16x16x32_bf16 v[112:115], a[220:223], a[140:143], v[112:115]\n"
"ds_read_b64_tr_b16 a[204:205], v218 offset:64512\n"
"v_mfma_f32_16x16x32_bf16 v[112:115], a[216:219], a[136:139], v[112:115]\n"
"v_mfma_f32_16x16x32_bf16 v[112:115], a[212:215], a[132:135], v[112:115]\n"
"ds_read_b64_tr_b16 a[206:207], v219 offset:64512\n"
"v_mfma_f32_16x16x32_bf16 v[112:115], a[208:211], a[128:131], v[112:115]\n"
"v_mfma_f32_16x16x32_bf16 v[128:131], a[224:227], a[128:131], v[128:131]\n"
"ds_read_b64_tr_b16 a[208:209], v218 offset:36896\n"
"v_mfma_f32_16x16x32_bf16 v[128:131], a[228:231], a[132:135], v[128:131]\n"
"buffer_load_dwordx4 a[40:43], v232, s[52:55], s66 offen offset:2048 sc1\n"
"v_mfma_f32_16x16x32_bf16 v[128:131], a[232:235], a[136:139], v[128:131]\n"
"ds_read_b64_tr_b16 a[210:211], v219 offset:36896\n"
"v_mfma_f32_16x16x32_bf16 v[128:131], a[236:239], a[140:143], v[128:131]\n"
"v_mfma_f32_16x16x32_bf16 v[132:135], a[236:239], a[156:159], v[132:135]\n"
"ds_read_b64_tr_b16 a[212:213], v218 offset:46112\n"
"v_mfma_f32_16x16x32_bf16 v[132:135], a[232:235], a[152:155], v[132:135]\n"
"buffer_load_dwordx4 a[44:47], v232, s[52:55], s66 offen offset:3072 sc1\n"
"v_mfma_f32_16x16x32_bf16 v[132:135], a[228:231], a[148:151], v[132:135]\n"
"ds_read_b64_tr_b16 a[214:215], v219 offset:46112\n"
"v_mfma_f32_16x16x32_bf16 v[132:135], a[224:227], a[144:147], v[132:135]\n"
"v_mfma_f32_16x16x32_bf16 v[136:139], a[224:227], a[160:163], v[136:139]\n"
"ds_read_b64_tr_b16 a[216:217], v218 offset:55328\n"
"v_mfma_f32_16x16x32_bf16 v[136:139], a[228:231], a[164:167], v[136:139]\n"
"v_mfma_f32_16x16x32_bf16 v[136:139], a[232:235], a[168:171], v[136:139]\n"
"s_add_u32 s66, s77, 12288\n"
"ds_read_b64_tr_b16 a[218:219], v219 offset:55328\n"
"v_mfma_f32_16x16x32_bf16 v[136:139], a[236:239], a[172:175], v[136:139]\n"
"buffer_load_dwordx4 a[48:51], v232, s[52:55], s66 offen sc1\n"
"v_mfma_f32_16x16x32_bf16 v[140:143], a[236:239], a[188:191], v[140:143]\n"
"ds_read_b64_tr_b16 a[220:221], v218 offset:64544\n"
"v_mfma_f32_16x16x32_bf16 v[140:143], a[232:235], a[184:187], v[140:143]\n"
"v_mfma_f32_16x16x32_bf16 v[140:143], a[228:231], a[180:183], v[140:143]\n"
"ds_read_b64_tr_b16 a[222:223], v219 offset:64544\n"
"v_mfma_f32_16x16x32_bf16 v[140:143], a[224:227], a[176:179], v[140:143]\n"
"s_waitcnt vmcnt(22) lgkmcnt(8)\n"
"v_mfma_f32_16x16x32_bf16 v[12:15], a[192:195], a[112:115], v[12:15]\n"
"ds_read_b64_tr_b16 a[224:225], v218 offset:36928\n"
"v_mfma_f32_16x16x32_bf16 v[12:15], a[196:199], a[116:119], v[12:15]\n"
"buffer_load_dwordx4 a[52:55], v232, s[52:55], s66 offen offset:1024 sc1\n"
"v_mfma_f32_16x16x32_bf16 v[12:15], a[200:203], a[120:123], v[12:15]\n"
"ds_read_b64_tr_b16 a[226:227], v219 offset:36928\n"
"v_mfma_f32_16x16x32_bf16 v[12:15], a[204:207], a[124:127], v[12:15]\n"
"v_mfma_f32_16x16x32_bf16 v[8:11], a[204:207], a[108:111], v[8:11]\n"
"ds_read_b64_tr_b16 a[228:229], v218 offset:46144\n"
"v_lshrrev_b32 v236, 4, v232\n"
"v_mfma_f32_16x16x32_bf16 v[8:11], a[200:203], a[104:107], v[8:11]\n"
"v_lshrrev_b32 v238, 4, v236\n"
"v_bfe_u32 v239, v236, 2, 2\n"
"v_mfma_f32_16x16x32_bf16 v[8:11], a[196:199], a[100:103], v[8:11]\n"
"ds_read_b64_tr_b16 a[230:231], v219 offset:46144\n"
"v_cmp_eq_u32 vcc, v238, v239\n"
"v_mfma_f32_16x16x32_bf16 v[8:11], a[192:195], a[96:99], v[8:11]\n"
"buffer_load_dwordx4 a[56:59], v232, s[52:55], s66 offen offset:2048 sc1\n"
"v_mfma_f32_16x16x32_bf16 v[4:7], a[192:195], a[80:83], v[4:7]\n"
"ds_read_b64_tr_b16 a[232:233], v218 offset:55360\n"
"v_and_b32 v238, 1, v236\n"
"v_mfma_f32_16x16x32_bf16 v[4:7], a[196:199], a[84:87], v[4:7]\n"
"v_lshlrev_b32 v238, 4, v238\n"
"v_mov_b32 v239, 0x3f80\n"
"v_mfma_f32_16x16x32_bf16 v[4:7], a[200:203], a[88:91], v[4:7]\n"
"ds_read_b64_tr_b16 a[234:235], v219 offset:55360\n"
"v_lshlrev_b32 v238, v238, v239\n"
"v_mfma_f32_16x16x32_bf16 v[4:7], a[204:207], a[92:95], v[4:7]\n"
"buffer_load_dwordx4 a[60:63], v232, s[52:55], s66 offen offset:3072 sc1\n"
"v_mfma_f32_16x16x32_bf16 v[0:3], a[204:207], a[76:79], v[0:3]\n"
"ds_read_b64_tr_b16 a[236:237], v218 offset:64576\n"
"v_cndmask_b32 v238, 0, v238, vcc\n"
"v_mfma_f32_16x16x32_bf16 v[0:3], a[200:203], a[72:75], v[0:3]\n"
"v_and_b32 v239, 2, v236\n"
"v_cmp_ne_u32 vcc, 0, v239\n"
"v_mfma_f32_16x16x32_bf16 v[0:3], a[196:199], a[68:71], v[0:3]\n"
"ds_read_b64_tr_b16 a[238:239], v219 offset:64576\n"
"v_mov_b32 v208, 0\n"
"v_mfma_f32_16x16x32_bf16 v[0:3], a[192:195], a[64:67], v[0:3]\n"
"s_waitcnt lgkmcnt(8)\n"
"v_mov_b32 v209, 0\n"
"v_mov_b32 v212, 0\n"
"v_mfma_f32_16x16x32_bf16 v[16:19], a[208:211], a[64:67], v[16:19]\n"
"ds_read_b64_tr_b16 a[192:193], v218 offset:36960\n"
"v_mov_b32 v213, 0\n"
"v_mfma_f32_16x16x32_bf16 v[16:19], a[212:215], a[68:71], v[16:19]\n"
"v_lshrrev_b32 v223, 1, v232\n"
"v_and_b32 v250, 0xfffffbff, v250\n"
"v_mfma_f32_16x16x32_bf16 v[16:19], a[216:219], a[72:75], v[16:19]\n"
"ds_read_b64_tr_b16 a[194:195], v219 offset:36960\n"
"v_cndmask_b32 v211, 0, v238, vcc\n"
"v_mfma_f32_16x16x32_bf16 v[16:19], a[220:223], a[76:79], v[16:19]\n"
"v_xor_b32 v210, v211, v238\n"
"v_accvgpr_write_b32 a146, 0\n"
"v_mfma_f32_16x16x32_bf16 v[20:23], a[220:223], a[92:95], v[20:23]\n"
"ds_read_b64_tr_b16 a[196:197], v218 offset:46176\n"
"v_accvgpr_write_b32 a147, 0\n"
"v_mfma_f32_16x16x32_bf16 v[20:23], a[216:219], a[88:91], v[20:23]\n"
"v_accvgpr_write_b32 a166, 0\n"
"v_accvgpr_write_b32 a167, 0\n"
"v_mfma_f32_16x16x32_bf16 v[20:23], a[212:215], a[84:87], v[20:23]\n"
"ds_read_b64_tr_b16 a[198:199], v219 offset:46176\n"
"v_accvgpr_write_b32 a186, 0\n"
"v_mfma_f32_16x16x32_bf16 v[20:23], a[208:211], a[80:83], v[20:23]\n"
"v_accvgpr_write_b32 a187, 0\n"
"v_mfma_f32_16x16x32_bf16 v[24:27], a[208:211], a[96:99], v[24:27]\n"
"ds_read_b64_tr_b16 a[200:201], v218 offset:55392\n"
"v_mfma_f32_16x16x32_bf16 v[24:27], a[212:215], a[100:103], v[24:27]\n"
"v_mfma_f32_16x16x32_bf16 v[24:27], a[216:219], a[104:107], v[24:27]\n"
"ds_read_b64_tr_b16 a[202:203], v219 offset:55392\n"
"v_mfma_f32_16x16x32_bf16 v[24:27], a[220:223], a[108:111], v[24:27]\n"
"v_mfma_f32_16x16x32_bf16 v[28:31], a[220:223], a[124:127], v[28:31]\n"
"ds_read_b64_tr_b16 a[204:205], v218 offset:64608\n"
"v_mfma_f32_16x16x32_bf16 v[28:31], a[216:219], a[120:123], v[28:31]\n"
"v_mfma_f32_16x16x32_bf16 v[28:31], a[212:215], a[116:119], v[28:31]\n"
"ds_read_b64_tr_b16 a[206:207], v219 offset:64608\n"
"v_mfma_f32_16x16x32_bf16 v[28:31], a[208:211], a[112:115], v[28:31]\n"
"s_waitcnt lgkmcnt(8)\n"
"v_mfma_f32_16x16x32_bf16 v[44:47], a[224:227], a[112:115], v[44:47]\n"
"ds_read_b64_tr_b16 a[208:209], v218 offset:36992\n"
"v_mfma_f32_16x16x32_bf16 v[44:47], a[228:231], a[116:119], v[44:47]\n"
"v_mfma_f32_16x16x32_bf16 v[44:47], a[232:235], a[120:123], v[44:47]\n"
"ds_read_b64_tr_b16 a[210:211], v219 offset:36992\n"
"v_mfma_f32_16x16x32_bf16 v[44:47], a[236:239], a[124:127], v[44:47]\n"
"v_mfma_f32_16x16x32_bf16 v[40:43], a[236:239], a[108:111], v[40:43]\n"
"ds_read_b64_tr_b16 a[212:213], v218 offset:46208\n"
"v_mfma_f32_16x16x32_bf16 v[40:43], a[232:235], a[104:107], v[40:43]\n"
"v_mfma_f32_16x16x32_bf16 v[40:43], a[228:231], a[100:103], v[40:43]\n"
"ds_read_b64_tr_b16 a[214:215], v219 offset:46208\n"
"v_mfma_f32_16x16x32_bf16 v[40:43], a[224:227], a[96:99], v[40:43]\n"
"v_mfma_f32_16x16x32_bf16 v[36:39], a[224:227], a[80:83], v[36:39]\n"
"ds_read_b64_tr_b16 a[216:217], v218 offset:55424\n"
"v_mfma_f32_16x16x32_bf16 v[36:39], a[228:231], a[84:87], v[36:39]\n"
"v_mfma_f32_16x16x32_bf16 v[36:39], a[232:235], a[88:91], v[36:39]\n"
"ds_read_b64_tr_b16 a[218:219], v219 offset:55424\n"
"v_mfma_f32_16x16x32_bf16 v[36:39], a[236:239], a[92:95], v[36:39]\n"
"v_mfma_f32_16x16x32_bf16 v[32:35], a[236:239], a[76:79], v[32:35]\n"
"ds_read_b64_tr_b16 a[220:221], v218 offset:64640\n"
"v_mfma_f32_16x16x32_bf16 v[32:35], a[232:235], a[72:75], v[32:35]\n"
"v_mfma_f32_16x16x32_bf16 v[32:35], a[228:231], a[68:71], v[32:35]\n"
"ds_read_b64_tr_b16 a[222:223], v219 offset:64640\n"
"v_mfma_f32_16x16x32_bf16 v[32:35], a[224:227], a[64:67], v[32:35]\n"
"s_waitcnt lgkmcnt(8)\n"
"v_mfma_f32_16x16x32_bf16 v[48:51], a[192:195], a[64:67], v[48:51]\n"
"v_mfma_f32_16x16x32_bf16 v[48:51], a[196:199], a[68:71], v[48:51]\n"
"v_mfma_f32_16x16x32_bf16 v[48:51], a[200:203], a[72:75], v[48:51]\n"
"v_mfma_f32_16x16x32_bf16 v[48:51], a[204:207], a[76:79], v[48:51]\n"
"s_waitcnt vmcnt(16) lgkmcnt(0)\n"
"v_mfma_f32_16x16x32_bf16 v[52:55], a[204:207], a[92:95], v[52:55]\n"
"s_barrier\n"
"v_mfma_f32_16x16x32_bf16 v[52:55], a[200:203], a[88:91], v[52:55]\n"
"v_mfma_f32_16x16x32_bf16 v[52:55], a[196:199], a[84:87], v[52:55]\n"
"ds_read_b64_tr_b16 a[224:225], v216\n"
"ds_read_b64_tr_b16 a[226:227], v217\n"
"v_mfma_f32_16x16x32_bf16 v[52:55], a[192:195], a[80:83], v[52:55]\n"
"v_mfma_f32_16x16x32_bf16 v[56:59], a[192:195], a[96:99], v[56:59]\n"
"ds_read_b64_tr_b16 a[228:229], v216 offset:9216\n"
"ds_read_b64_tr_b16 a[230:231], v217 offset:9216\n"
"v_mfma_f32_16x16x32_bf16 v[56:59], a[196:199], a[100:103], v[56:59]\n"
"v_mfma_f32_16x16x32_bf16 v[56:59], a[200:203], a[104:107], v[56:59]\n"
"ds_read_b64_tr_b16 a[232:233], v216 offset:18432\n"
"ds_read_b64_tr_b16 a[234:235], v217 offset:18432\n"
"v_mfma_f32_16x16x32_bf16 v[56:59], a[204:207], a[108:111], v[56:59]\n"
"v_mfma_f32_16x16x32_bf16 v[60:63], a[204:207], a[124:127], v[60:63]\n"
"ds_read_b64_tr_b16 a[236:237], v216 offset:27648\n"
"v_mfma_f32_16x16x32_bf16 v[60:63], a[200:203], a[120:123], v[60:63]\n"
"v_mfma_f32_16x16x32_bf16 v[60:63], a[196:199], a[116:119], v[60:63]\n"
"ds_read_b64_tr_b16 a[238:239], v217 offset:27648\n"
"v_mfma_f32_16x16x32_bf16 v[60:63], a[192:195], a[112:115], v[60:63]\n"
"v_mfma_f32_16x16x32_bf16 v[76:79], a[208:211], a[112:115], v[76:79]\n"
"ds_read_b64_tr_b16 a[192:193], v216 offset:32\n"
"v_mfma_f32_16x16x32_bf16 v[76:79], a[212:215], a[116:119], v[76:79]\n"
"v_mfma_f32_16x16x32_bf16 v[76:79], a[216:219], a[120:123], v[76:79]\n"
"ds_read_b64_tr_b16 a[194:195], v217 offset:32\n"
"v_mfma_f32_16x16x32_bf16 v[76:79], a[220:223], a[124:127], v[76:79]\n"
"v_mfma_f32_16x16x32_bf16 v[72:75], a[220:223], a[108:111], v[72:75]\n"
"ds_read_b64_tr_b16 a[196:197], v216 offset:9248\n"
"v_mfma_f32_16x16x32_bf16 v[72:75], a[216:219], a[104:107], v[72:75]\n"
"v_mfma_f32_16x16x32_bf16 v[72:75], a[212:215], a[100:103], v[72:75]\n"
"ds_read_b64_tr_b16 a[198:199], v217 offset:9248\n"
"v_mfma_f32_16x16x32_bf16 v[72:75], a[208:211], a[96:99], v[72:75]\n"
"v_mfma_f32_16x16x32_bf16 v[68:71], a[208:211], a[80:83], v[68:71]\n"
"ds_read_b64_tr_b16 a[200:201], v216 offset:18464\n"
"v_mfma_f32_16x16x32_bf16 v[68:71], a[212:215], a[84:87], v[68:71]\n"
"v_mfma_f32_16x16x32_bf16 v[68:71], a[216:219], a[88:91], v[68:71]\n"
"ds_read_b64_tr_b16 a[202:203], v217 offset:18464\n"
"v_mfma_f32_16x16x32_bf16 v[68:71], a[220:223], a[92:95], v[68:71]\n"
"v_mfma_f32_16x16x32_bf16 v[64:67], a[220:223], a[76:79], v[64:67]\n"
"ds_read_b64_tr_b16 a[204:205], v216 offset:27680\n"
"v_mfma_f32_16x16x32_bf16 v[64:67], a[216:219], a[72:75], v[64:67]\n"
"v_mfma_f32_16x16x32_bf16 v[64:67], a[212:215], a[68:71], v[64:67]\n"
"ds_read_b64_tr_b16 a[206:207], v217 offset:27680\n"
"v_mfma_f32_16x16x32_bf16 v[64:67], a[208:211], a[64:67], v[64:67]\n"
"s_waitcnt vmcnt(0) lgkmcnt(8)\n"
"v_mfma_f32_16x16x32_bf16 v[4:7], a[224:227], a[16:19], v[4:7]\n"
"ds_read_b64_tr_b16 a[208:209], v216 offset:64\n"
"v_mfma_f32_16x16x32_bf16 v[4:7], a[228:231], a[20:23], v[4:7]\n"
"v_mfma_f32_16x16x32_bf16 v[4:7], a[232:235], a[24:27], v[4:7]\n"
"ds_read_b64_tr_b16 a[210:211], v217 offset:64\n"
"v_mfma_f32_16x16x32_bf16 v[4:7], a[236:239], a[28:31], v[4:7]\n"
"v_mfma_f32_16x16x32_bf16 v[8:11], a[236:239], a[44:47], v[8:11]\n"
"ds_read_b64_tr_b16 a[212:213], v216 offset:9280\n"
"v_mfma_f32_16x16x32_bf16 v[8:11], a[232:235], a[40:43], v[8:11]\n"
"v_mfma_f32_16x16x32_bf16 v[8:11], a[228:231], a[36:39], v[8:11]\n"
"ds_read_b64_tr_b16 a[214:215], v217 offset:9280\n"
"v_mfma_f32_16x16x32_bf16 v[8:11], a[224:227], a[32:35], v[8:11]\n"
"v_mfma_f32_16x16x32_bf16 v[12:15], a[224:227], a[48:51], v[12:15]\n"
"ds_read_b64_tr_b16 a[216:217], v216 offset:18496\n"
"v_mfma_f32_16x16x32_bf16 v[12:15], a[228:231], a[52:55], v[12:15]\n"
"v_mfma_f32_16x16x32_bf16 v[12:15], a[232:235], a[56:59], v[12:15]\n"
"ds_read_b64_tr_b16 a[218:219], v217 offset:18496\n"
"v_mfma_f32_16x16x32_bf16 v[12:15], a[236:239], a[60:63], v[12:15]\n"
"v_mfma_f32_16x16x32_bf16 v[0:3], a[236:239], a[12:15], v[0:3]\n"
"ds_read_b64_tr_b16 a[220:221], v216 offset:27712\n"
"v_mfma_f32_16x16x32_bf16 v[0:3], a[232:235], a[8:11], v[0:3]\n"
"v_mfma_f32_16x16x32_bf16 v[0:3], a[228:231], a[4:7], v[0:3]\n"
"ds_read_b64_tr_b16 a[222:223], v217 offset:27712\n"
"v_mfma_f32_16x16x32_bf16 v[0:3], a[224:227], a[0:3], v[0:3]\n"
"s_waitcnt lgkmcnt(8)\n"
"v_mfma_f32_16x16x32_bf16 v[20:23], a[192:195], a[16:19], v[20:23]\n"
"ds_read_b64_tr_b16 a[224:225], v216 offset:96\n"
"v_mfma_f32_16x16x32_bf16 v[20:23], a[196:199], a[20:23], v[20:23]\n"
"v_mfma_f32_16x16x32_bf16 v[20:23], a[200:203], a[24:27], v[20:23]\n"
"ds_read_b64_tr_b16 a[226:227], v217 offset:96\n"
"v_mfma_f32_16x16x32_bf16 v[20:23], a[204:207], a[28:31], v[20:23]\n"
"v_mfma_f32_16x16x32_bf16 v[24:27], a[204:207], a[44:47], v[24:27]\n"
"ds_read_b64_tr_b16 a[228:229], v216 offset:9312\n"
"v_mfma_f32_16x16x32_bf16 v[24:27], a[200:203], a[40:43], v[24:27]\n"
"v_mfma_f32_16x16x32_bf16 v[24:27], a[196:199], a[36:39], v[24:27]\n"
"ds_read_b64_tr_b16 a[230:231], v217 offset:9312\n"
"v_mfma_f32_16x16x32_bf16 v[24:27], a[192:195], a[32:35], v[24:27]\n"
"v_mfma_f32_16x16x32_bf16 v[28:31], a[192:195], a[48:51], v[28:31]\n"
"ds_read_b64_tr_b16 a[232:233], v216 offset:18528\n"
"v_mfma_f32_16x16x32_bf16 v[28:31], a[196:199], a[52:55], v[28:31]\n"
"v_mfma_f32_16x16x32_bf16 v[28:31], a[200:203], a[56:59], v[28:31]\n"
"ds_read_b64_tr_b16 a[234:235], v217 offset:18528\n"
"v_mfma_f32_16x16x32_bf16 v[28:31], a[204:207], a[60:63], v[28:31]\n"
"v_mfma_f32_16x16x32_bf16 v[16:19], a[204:207], a[12:15], v[16:19]\n"
"ds_read_b64_tr_b16 a[236:237], v216 offset:27744\n"
"v_cvt_pk_bf16_f32 v184, v4, v5\n"
"v_mfma_f32_16x16x32_bf16 v[16:19], a[200:203], a[8:11], v[16:19]\n"
"v_cvt_pk_bf16_f32 v185, v6, v7\n"
"v_cvt_pk_bf16_f32 v186, v20, v21\n"
"v_mfma_f32_16x16x32_bf16 v[16:19], a[196:199], a[4:7], v[16:19]\n"
"s_add_u32 s77, s85, 0\n"
"ds_read_b64_tr_b16 a[238:239], v217 offset:27744\n"
"v_cvt_pk_bf16_f32 v187, v22, v23\n"
"v_mfma_f32_16x16x32_bf16 v[16:19], a[192:195], a[0:3], v[16:19]\n"
"s_waitcnt lgkmcnt(8)\n"
"buffer_store_dwordx4 v[184:187], v232, s[52:55], s77 offen sc1\n"
"v_mfma_f32_16x16x32_bf16 v[36:39], a[208:211], a[16:19], v[36:39]\n"
"ds_read_b64_tr_b16 a[192:193], v216 offset:128\n"
"v_cvt_pk_bf16_f32 v188, v8, v9\n"
"v_mfma_f32_16x16x32_bf16 v[36:39], a[212:215], a[20:23], v[36:39]\n"
"v_cvt_pk_bf16_f32 v189, v10, v11\n"
"v_cvt_pk_bf16_f32 v190, v24, v25\n"
"v_mfma_f32_16x16x32_bf16 v[36:39], a[216:219], a[24:27], v[36:39]\n"
"s_add_u32 s77, s87, 0\n"
"ds_read_b64_tr_b16 a[194:195], v217 offset:128\n"
"v_cvt_pk_bf16_f32 v191, v26, v27\n"
"v_mfma_f32_16x16x32_bf16 v[36:39], a[220:223], a[28:31], v[36:39]\n"
"buffer_store_dwordx4 v[188:191], v232, s[52:55], s77 offen offset:1024 sc1\n"
"v_mfma_f32_16x16x32_bf16 v[40:43], a[220:223], a[44:47], v[40:43]\n"
"ds_read_b64_tr_b16 a[196:197], v216 offset:9344\n"
"v_cvt_pk_bf16_f32 v192, v12, v13\n"
"v_mfma_f32_16x16x32_bf16 v[40:43], a[216:219], a[40:43], v[40:43]\n"
"v_cvt_pk_bf16_f32 v193, v14, v15\n"
"v_cvt_pk_bf16_f32 v194, v28, v29\n"
"v_mfma_f32_16x16x32_bf16 v[40:43], a[212:215], a[36:39], v[40:43]\n"
"s_add_u32 s77, s68, 0\n"
"ds_read_b64_tr_b16 a[198:199], v217 offset:9344\n"
"v_cvt_pk_bf16_f32 v195, v30, v31\n"
"v_mfma_f32_16x16x32_bf16 v[40:43], a[208:211], a[32:35], v[40:43]\n"
"buffer_store_dwordx4 v[192:195], v232, s[52:55], s77 offen offset:2048 sc1\n"
"v_mfma_f32_16x16x32_bf16 v[44:47], a[208:211], a[48:51], v[44:47]\n"
"ds_read_b64_tr_b16 a[200:201], v216 offset:18560\n"
"v_mfma_f32_16x16x32_bf16 v[44:47], a[212:215], a[52:55], v[44:47]\n"
"v_mfma_f32_16x16x32_bf16 v[44:47], a[216:219], a[56:59], v[44:47]\n"
"ds_read_b64_tr_b16 a[202:203], v217 offset:18560\n"
"v_mfma_f32_16x16x32_bf16 v[44:47], a[220:223], a[60:63], v[44:47]\n"
"v_mfma_f32_16x16x32_bf16 v[32:35], a[220:223], a[12:15], v[32:35]\n"
"ds_read_b64_tr_b16 a[204:205], v216 offset:27776\n"
"v_mfma_f32_16x16x32_bf16 v[32:35], a[216:219], a[8:11], v[32:35]\n"
"v_mfma_f32_16x16x32_bf16 v[32:35], a[212:215], a[4:7], v[32:35]\n"
"ds_read_b64_tr_b16 a[206:207], v217 offset:27776\n"
"v_mfma_f32_16x16x32_bf16 v[32:35], a[208:211], a[0:3], v[32:35]\n"
"s_waitcnt lgkmcnt(8)\n"
"v_mfma_f32_16x16x32_bf16 v[52:55], a[224:227], a[16:19], v[52:55]\n"
"ds_read_b64_tr_b16 a[208:209], v218 offset:37024\n"
"v_mfma_f32_16x16x32_bf16 v[52:55], a[228:231], a[20:23], v[52:55]\n"
"v_mfma_f32_16x16x32_bf16 v[52:55], a[232:235], a[24:27], v[52:55]\n"
"ds_read_b64_tr_b16 a[210:211], v219 offset:37024\n"
"v_mfma_f32_16x16x32_bf16 v[52:55], a[236:239], a[28:31], v[52:55]\n"
"v_mfma_f32_16x16x32_bf16 v[56:59], a[236:239], a[44:47], v[56:59]\n"
"ds_read_b64_tr_b16 a[212:213], v218 offset:46240\n"
"v_mfma_f32_16x16x32_bf16 v[56:59], a[232:235], a[40:43], v[56:59]\n"
"v_mfma_f32_16x16x32_bf16 v[56:59], a[228:231], a[36:39], v[56:59]\n"
"ds_read_b64_tr_b16 a[214:215], v219 offset:46240\n"
"v_mfma_f32_16x16x32_bf16 v[56:59], a[224:227], a[32:35], v[56:59]\n"
"v_mfma_f32_16x16x32_bf16 v[60:63], a[224:227], a[48:51], v[60:63]\n"
"ds_read_b64_tr_b16 a[216:217], v218 offset:55456\n"
"v_mfma_f32_16x16x32_bf16 v[60:63], a[228:231], a[52:55], v[60:63]\n"
"v_mfma_f32_16x16x32_bf16 v[60:63], a[232:235], a[56:59], v[60:63]\n"
"ds_read_b64_tr_b16 a[218:219], v219 offset:55456\n"
"v_mfma_f32_16x16x32_bf16 v[60:63], a[236:239], a[60:63], v[60:63]\n"
"v_mfma_f32_16x16x32_bf16 v[48:51], a[236:239], a[12:15], v[48:51]\n"
"ds_read_b64_tr_b16 a[220:221], v218 offset:64672\n"
"v_cvt_pk_bf16_f32 v196, v36, v37\n"
"v_mfma_f32_16x16x32_bf16 v[48:51], a[232:235], a[8:11], v[48:51]\n"
"v_cvt_pk_bf16_f32 v197, v38, v39\n"
"v_cvt_pk_bf16_f32 v198, v52, v53\n"
"v_mfma_f32_16x16x32_bf16 v[48:51], a[228:231], a[4:7], v[48:51]\n"
"s_add_u32 s77, s85, 0\n"
"ds_read_b64_tr_b16 a[222:223], v219 offset:64672\n"
"v_cvt_pk_bf16_f32 v199, v54, v55\n"
"v_mfma_f32_16x16x32_bf16 v[48:51], a[224:227], a[0:3], v[48:51]\n"
"s_waitcnt lgkmcnt(8)\n"
"buffer_store_dwordx4 v[196:199], v232, s[52:55], s77 offen offset:3072 sc1\n"
"v_mfma_f32_16x16x32_bf16 v[68:71], a[192:195], a[16:19], v[68:71]\n"
"ds_read_b64_tr_b16 a[224:225], v216 offset:160\n"
"v_cvt_pk_bf16_f32 v200, v40, v41\n"
"v_mfma_f32_16x16x32_bf16 v[68:71], a[196:199], a[20:23], v[68:71]\n"
"v_cvt_pk_bf16_f32 v201, v42, v43\n"
"v_cvt_pk_bf16_f32 v202, v56, v57\n"
"v_mfma_f32_16x16x32_bf16 v[68:71], a[200:203], a[24:27], v[68:71]\n"
"s_add_u32 s77, s87, 4096\n"
"ds_read_b64_tr_b16 a[226:227], v217 offset:160\n"
"v_cvt_pk_bf16_f32 v203, v58, v59\n"
"v_mfma_f32_16x16x32_bf16 v[68:71], a[204:207], a[28:31], v[68:71]\n"
"buffer_store_dwordx4 v[200:203], v232, s[52:55], s77 offen sc1\n"
"v_mfma_f32_16x16x32_bf16 v[72:75], a[204:207], a[44:47], v[72:75]\n"
"ds_read_b64_tr_b16 a[228:229], v216 offset:9376\n"
"v_cvt_pk_bf16_f32 v180, v44, v45\n"
"v_mfma_f32_16x16x32_bf16 v[72:75], a[200:203], a[40:43], v[72:75]\n"
"v_cvt_pk_bf16_f32 v181, v46, v47\n"
"v_cvt_pk_bf16_f32 v182, v60, v61\n"
"v_mfma_f32_16x16x32_bf16 v[72:75], a[196:199], a[36:39], v[72:75]\n"
"s_add_u32 s77, s68, 4096\n"
"ds_read_b64_tr_b16 a[230:231], v217 offset:9376\n"
"v_cvt_pk_bf16_f32 v183, v62, v63\n"
"v_mfma_f32_16x16x32_bf16 v[72:75], a[192:195], a[32:35], v[72:75]\n"
"buffer_store_dwordx4 v[180:183], v232, s[52:55], s77 offen offset:1024 sc1\n"
"v_mfma_f32_16x16x32_bf16 v[76:79], a[192:195], a[48:51], v[76:79]\n"
"ds_read_b64_tr_b16 a[232:233], v216 offset:18592\n"
"v_mfma_f32_16x16x32_bf16 v[76:79], a[196:199], a[52:55], v[76:79]\n"
"v_mfma_f32_16x16x32_bf16 v[76:79], a[200:203], a[56:59], v[76:79]\n"
"ds_read_b64_tr_b16 a[234:235], v217 offset:18592\n"
"v_mfma_f32_16x16x32_bf16 v[76:79], a[204:207], a[60:63], v[76:79]\n"
"v_mfma_f32_16x16x32_bf16 v[64:67], a[204:207], a[12:15], v[64:67]\n"
"ds_read_b64_tr_b16 a[236:237], v216 offset:27808\n"
"v_mfma_f32_16x16x32_bf16 v[64:67], a[200:203], a[8:11], v[64:67]\n"
"v_mfma_f32_16x16x32_bf16 v[64:67], a[196:199], a[4:7], v[64:67]\n"
"ds_read_b64_tr_b16 a[238:239], v217 offset:27808\n"
"v_mfma_f32_16x16x32_bf16 v[64:67], a[192:195], a[0:3], v[64:67]\n"
"s_waitcnt lgkmcnt(8)\n"
"v_mfma_f32_16x16x32_bf16 v[92:95], a[208:211], a[112:115], v[92:95]\n"
"ds_read_b64_tr_b16 a[192:193], v218 offset:37056\n"
"v_mfma_f32_16x16x32_bf16 v[92:95], a[212:215], a[116:119], v[92:95]\n"
"v_mfma_f32_16x16x32_bf16 v[92:95], a[216:219], a[120:123], v[92:95]\n"
"ds_read_b64_tr_b16 a[194:195], v219 offset:37056\n"
"v_mfma_f32_16x16x32_bf16 v[92:95], a[220:223], a[124:127], v[92:95]\n"
"v_mfma_f32_16x16x32_bf16 v[88:91], a[220:223], a[108:111], v[88:91]\n"
"ds_read_b64_tr_b16 a[196:197], v218 offset:46272\n"
"v_mfma_f32_16x16x32_bf16 v[88:91], a[216:219], a[104:107], v[88:91]\n"
"v_mfma_f32_16x16x32_bf16 v[88:91], a[212:215], a[100:103], v[88:91]\n"
"ds_read_b64_tr_b16 a[198:199], v219 offset:46272\n"
"v_mfma_f32_16x16x32_bf16 v[88:91], a[208:211], a[96:99], v[88:91]\n"
"v_mfma_f32_16x16x32_bf16 v[84:87], a[208:211], a[80:83], v[84:87]\n"
"ds_read_b64_tr_b16 a[200:201], v218 offset:55488\n"
"v_mfma_f32_16x16x32_bf16 v[84:87], a[212:215], a[84:87], v[84:87]\n"
"v_mfma_f32_16x16x32_bf16 v[84:87], a[216:219], a[88:91], v[84:87]\n"
"ds_read_b64_tr_b16 a[202:203], v219 offset:55488\n"
"v_mfma_f32_16x16x32_bf16 v[84:87], a[220:223], a[92:95], v[84:87]\n"
"v_mfma_f32_16x16x32_bf16 v[80:83], a[220:223], a[76:79], v[80:83]\n"
"ds_read_b64_tr_b16 a[204:205], v218 offset:64704\n"
"v_mfma_f32_16x16x32_bf16 v[80:83], a[216:219], a[72:75], v[80:83]\n"
"v_mfma_f32_16x16x32_bf16 v[80:83], a[212:215], a[68:71], v[80:83]\n"
"ds_read_b64_tr_b16 a[206:207], v219 offset:64704\n"
"v_mfma_f32_16x16x32_bf16 v[80:83], a[208:211], a[64:67], v[80:83]\n"
"s_waitcnt lgkmcnt(8)\n"
"v_mfma_f32_16x16x32_bf16 v[84:87], a[224:227], a[16:19], v[84:87]\n"
"ds_read_b64_tr_b16 a[208:209], v216 offset:192\n"
"v_mfma_f32_16x16x32_bf16 v[84:87], a[228:231], a[20:23], v[84:87]\n"
"v_mfma_f32_16x16x32_bf16 v[84:87], a[232:235], a[24:27], v[84:87]\n"
"ds_read_b64_tr_b16 a[210:211], v217 offset:192\n"
"v_mfma_f32_16x16x32_bf16 v[84:87], a[236:239], a[28:31], v[84:87]\n"
"buffer_load_dword v251, v250, s[96:99], 0 offen sc1\n"
"v_mfma_f32_16x16x32_bf16 v[88:91], a[236:239], a[44:47], v[88:91]\n"
"ds_read_b64_tr_b16 a[212:213], v216 offset:9408\n"
"v_mfma_f32_16x16x32_bf16 v[88:91], a[232:235], a[40:43], v[88:91]\n"
"v_mfma_f32_16x16x32_bf16 v[88:91], a[228:231], a[36:39], v[88:91]\n"
"ds_read_b64_tr_b16 a[214:215], v217 offset:9408\n"
"v_mfma_f32_16x16x32_bf16 v[88:91], a[224:227], a[32:35], v[88:91]\n"
"v_mfma_f32_16x16x32_bf16 v[92:95], a[224:227], a[48:51], v[92:95]\n"
"ds_read_b64_tr_b16 a[216:217], v216 offset:18624\n"
"v_mfma_f32_16x16x32_bf16 v[92:95], a[228:231], a[52:55], v[92:95]\n"
"v_mfma_f32_16x16x32_bf16 v[92:95], a[232:235], a[56:59], v[92:95]\n"
"ds_read_b64_tr_b16 a[218:219], v217 offset:18624\n"
"v_mfma_f32_16x16x32_bf16 v[92:95], a[236:239], a[60:63], v[92:95]\n"
"v_mfma_f32_16x16x32_bf16 v[80:83], a[236:239], a[12:15], v[80:83]\n"
"ds_read_b64_tr_b16 a[220:221], v216 offset:27840\n"
"v_cvt_pk_bf16_f32 v184, v68, v69\n"
"v_mfma_f32_16x16x32_bf16 v[80:83], a[232:235], a[8:11], v[80:83]\n"
"v_cvt_pk_bf16_f32 v185, v70, v71\n"
"v_cvt_pk_bf16_f32 v186, v84, v85\n"
"v_mfma_f32_16x16x32_bf16 v[80:83], a[228:231], a[4:7], v[80:83]\n"
"s_add_u32 s77, s85, 4096\n"
"ds_read_b64_tr_b16 a[222:223], v217 offset:27840\n"
"v_cvt_pk_bf16_f32 v187, v86, v87\n"
"v_mfma_f32_16x16x32_bf16 v[80:83], a[224:227], a[0:3], v[80:83]\n"
"s_waitcnt lgkmcnt(8)\n"
"buffer_store_dwordx4 v[184:187], v232, s[52:55], s77 offen offset:2048 sc1\n"
"v_mfma_f32_16x16x32_bf16 v[108:111], a[192:195], a[112:115], v[108:111]\n"
"ds_read_b64_tr_b16 a[224:225], v218 offset:37088\n"
"v_cvt_pk_bf16_f32 v188, v72, v73\n"
"v_mfma_f32_16x16x32_bf16 v[108:111], a[196:199], a[116:119], v[108:111]\n"
"v_cvt_pk_bf16_f32 v189, v74, v75\n"
"v_cvt_pk_bf16_f32 v190, v88, v89\n"
"v_mfma_f32_16x16x32_bf16 v[108:111], a[200:203], a[120:123], v[108:111]\n"
"s_add_u32 s77, s87, 4096\n"
"ds_read_b64_tr_b16 a[226:227], v219 offset:37088\n"
"v_cvt_pk_bf16_f32 v191, v90, v91\n"
"v_mfma_f32_16x16x32_bf16 v[108:111], a[204:207], a[124:127], v[108:111]\n"
"buffer_store_dwordx4 v[188:191], v232, s[52:55], s77 offen offset:3072 sc1\n"
"v_mfma_f32_16x16x32_bf16 v[104:107], a[204:207], a[108:111], v[104:107]\n"
"ds_read_b64_tr_b16 a[228:229], v218 offset:46304\n"
"v_cvt_pk_bf16_f32 v192, v76, v77\n"
"v_mfma_f32_16x16x32_bf16 v[104:107], a[200:203], a[104:107], v[104:107]\n"
"v_cvt_pk_bf16_f32 v193, v78, v79\n"
"v_cvt_pk_bf16_f32 v194, v92, v93\n"
"v_mfma_f32_16x16x32_bf16 v[104:107], a[196:199], a[100:103], v[104:107]\n"
"s_add_u32 s77, s68, 8192\n"
"ds_read_b64_tr_b16 a[230:231], v219 offset:46304\n"
"v_cvt_pk_bf16_f32 v195, v94, v95\n"
"v_mfma_f32_16x16x32_bf16 v[104:107], a[192:195], a[96:99], v[104:107]\n"
"buffer_store_dwordx4 v[192:195], v232, s[52:55], s77 offen sc1\n"
"v_mfma_f32_16x16x32_bf16 v[100:103], a[192:195], a[80:83], v[100:103]\n"
"s_waitcnt vmcnt(4)\n"
"ds_read_b64_tr_b16 a[232:233], v218 offset:55520\n"
"v_mfma_f32_16x16x32_bf16 v[100:103], a[196:199], a[84:87], v[100:103]\n"
"buffer_store_dword v247, v224, s[96:99], 0 offen sc1\n"
"v_mfma_f32_16x16x32_bf16 v[100:103], a[200:203], a[88:91], v[100:103]\n"
"ds_read_b64_tr_b16 a[234:235], v219 offset:55520\n"
"v_mfma_f32_16x16x32_bf16 v[100:103], a[204:207], a[92:95], v[100:103]\n"
"v_mfma_f32_16x16x32_bf16 v[96:99], a[204:207], a[76:79], v[96:99]\n"
"ds_read_b64_tr_b16 a[236:237], v218 offset:64736\n"
"v_mfma_f32_16x16x32_bf16 v[96:99], a[200:203], a[72:75], v[96:99]\n"
"v_mfma_f32_16x16x32_bf16 v[96:99], a[196:199], a[68:71], v[96:99]\n"
"ds_read_b64_tr_b16 a[238:239], v219 offset:64736\n"
"v_mfma_f32_16x16x32_bf16 v[96:99], a[192:195], a[64:67], v[96:99]\n"
"s_waitcnt lgkmcnt(8)\n"
"v_mfma_f32_16x16x32_bf16 v[100:103], a[208:211], a[16:19], v[100:103]\n"
"ds_read_b64_tr_b16 a[192:193], v216 offset:224\n"
"v_mfma_f32_16x16x32_bf16 v[100:103], a[212:215], a[20:23], v[100:103]\n"
"v_mfma_f32_16x16x32_bf16 v[100:103], a[216:219], a[24:27], v[100:103]\n"
"ds_read_b64_tr_b16 a[194:195], v217 offset:224\n"
"v_mfma_f32_16x16x32_bf16 v[100:103], a[220:223], a[28:31], v[100:103]\n"
"v_mfma_f32_16x16x32_bf16 v[104:107], a[220:223], a[44:47], v[104:107]\n"
"ds_read_b64_tr_b16 a[196:197], v216 offset:9440\n"
"v_mfma_f32_16x16x32_bf16 v[104:107], a[216:219], a[40:43], v[104:107]\n"
"v_mfma_f32_16x16x32_bf16 v[104:107], a[212:215], a[36:39], v[104:107]\n"
"ds_read_b64_tr_b16 a[198:199], v217 offset:9440\n"
"v_mfma_f32_16x16x32_bf16 v[104:107], a[208:211], a[32:35], v[104:107]\n"
"v_mfma_f32_16x16x32_bf16 v[108:111], a[208:211], a[48:51], v[108:111]\n"
"ds_read_b64_tr_b16 a[200:201], v216 offset:18656\n"
"v_mfma_f32_16x16x32_bf16 v[108:111], a[212:215], a[52:55], v[108:111]\n"
"v_mfma_f32_16x16x32_bf16 v[108:111], a[216:219], a[56:59], v[108:111]\n"
"s_waitcnt vmcnt(4)\n"
"ds_read_b64_tr_b16 a[202:203], v217 offset:18656\n"
"v_cmp_eq_u32 vcc, s78, v251\n"
"s_nop 3\n"
"s_and_b32 s56, vcc_lo, 0xe\n"
"s_andn2_b32 s62, s62, s56\n"
"v_mfma_f32_16x16x32_bf16 v[108:111], a[220:223], a[60:63], v[108:111]\n"
"v_mfma_f32_16x16x32_bf16 v[96:99], a[220:223], a[12:15], v[96:99]\n"
"s_bitcmp1_b32 s56, 1\n"
"s_cselect_b32 s42, 3145728, 0\n"
"s_add_u32 s66, s84, 0\n"
"ds_read_b64_tr_b16 a[204:205], v216 offset:27872\n"
"v_mfma_f32_16x16x32_bf16 v[96:99], a[216:219], a[8:11], v[96:99]\n"
"buffer_load_dwordx4 a[128:131], v232, s[40:43], s66 offen sc1\n"
"v_mfma_f32_16x16x32_bf16 v[96:99], a[212:215], a[4:7], v[96:99]\n"
"ds_read_b64_tr_b16 a[206:207], v217 offset:27872\n"
"v_mfma_f32_16x16x32_bf16 v[96:99], a[208:211], a[0:3], v[96:99]\n"
"s_waitcnt lgkmcnt(8)\n"
"v_mfma_f32_16x16x32_bf16 v[124:127], a[224:227], a[112:115], v[124:127]\n"
"ds_read_b64_tr_b16 a[208:209], v218 offset:37120\n"
"v_mfma_f32_16x16x32_bf16 v[124:127], a[228:231], a[116:119], v[124:127]\n"
"v_mfma_f32_16x16x32_bf16 v[124:127], a[232:235], a[120:123], v[124:127]\n"
"s_add_u32 s66, s84, 0\n"
"ds_read_b64_tr_b16 a[210:211], v219 offset:37120\n"
"v_mfma_f32_16x16x32_bf16 v[124:127], a[236:239], a[124:127], v[124:127]\n"
"buffer_load_dwordx4 a[132:135], v232, s[40:43], s66 offen offset:3072 sc1\n"
"v_mfma_f32_16x16x32_bf16 v[120:123], a[236:239], a[108:111], v[120:123]\n"
"ds_read_b64_tr_b16 a[212:213], v218 offset:46336\n"
"v_mfma_f32_16x16x32_bf16 v[120:123], a[232:235], a[104:107], v[120:123]\n"
"v_mfma_f32_16x16x32_bf16 v[120:123], a[228:231], a[100:103], v[120:123]\n"
"ds_read_b64_tr_b16 a[214:215], v219 offset:46336\n"
"v_mfma_f32_16x16x32_bf16 v[120:123], a[224:227], a[96:99], v[120:123]\n"
"v_mfma_f32_16x16x32_bf16 v[116:119], a[224:227], a[80:83], v[116:119]\n"
"s_bitcmp1_b32 s56, 3\n"
"s_cselect_b32 s42, 3145728, 0\n"
"s_add_u32 s66, s84, 0\n"
"ds_read_b64_tr_b16 a[216:217], v218 offset:55552\n"
"v_mfma_f32_16x16x32_bf16 v[116:119], a[228:231], a[84:87], v[116:119]\n"
"buffer_load_dwordx4 a[168:171], v232, s[40:43], s66 offen offset:2048 sc1\n"
"v_mfma_f32_16x16x32_bf16 v[116:119], a[232:235], a[88:91], v[116:119]\n"
"ds_read_b64_tr_b16 a[218:219], v219 offset:55552\n"
"v_mfma_f32_16x16x32_bf16 v[116:119], a[236:239], a[92:95], v[116:119]\n"
"v_mfma_f32_16x16x32_bf16 v[112:115], a[236:239], a[76:79], v[112:115]\n"
"ds_read_b64_tr_b16 a[220:221], v218 offset:64768\n"
"v_mfma_f32_16x16x32_bf16 v[112:115], a[232:235], a[72:75], v[112:115]\n"
"v_mfma_f32_16x16x32_bf16 v[112:115], a[228:231], a[68:71], v[112:115]\n"
"ds_read_b64_tr_b16 a[222:223], v219 offset:64768\n"
"v_mfma_f32_16x16x32_bf16 v[112:115], a[224:227], a[64:67], v[112:115]\n"
"s_waitcnt lgkmcnt(8)\n"
"v_mfma_f32_16x16x32_bf16 v[116:119], a[192:195], a[16:19], v[116:119]\n"
"s_add_u32 s66, s84, 4096\n"
"ds_read_b64_tr_b16 a[224:225], v216 offset:256\n"
"v_mfma_f32_16x16x32_bf16 v[116:119], a[196:199], a[20:23], v[116:119]\n"
"buffer_load_dwordx4 a[172:175], v232, s[40:43], s66 offen offset:1024 sc1\n"
"v_mfma_f32_16x16x32_bf16 v[116:119], a[200:203], a[24:27], v[116:119]\n"
"ds_read_b64_tr_b16 a[226:227], v217 offset:256\n"
"v_mfma_f32_16x16x32_bf16 v[116:119], a[204:207], a[28:31], v[116:119]\n"
"v_mfma_f32_16x16x32_bf16 v[120:123], a[204:207], a[44:47], v[120:123]\n"
"ds_read_b64_tr_b16 a[228:229], v216 offset:9472\n"
"v_mfma_f32_16x16x32_bf16 v[120:123], a[200:203], a[40:43], v[120:123]\n"
"v_mfma_f32_16x16x32_bf16 v[120:123], a[196:199], a[36:39], v[120:123]\n"
"s_bitcmp1_b32 s56, 2\n"
"s_cselect_b32 s42, 3145728, 0\n"
"s_add_u32 s66, s84, 0\n"
"ds_read_b64_tr_b16 a[230:231], v217 offset:9472\n"
"v_mfma_f32_16x16x32_bf16 v[120:123], a[192:195], a[32:35], v[120:123]\n"
"buffer_load_dwordx4 a[148:151], v232, s[40:43], s66 offen offset:1024 sc1\n"
"v_mfma_f32_16x16x32_bf16 v[124:127], a[192:195], a[48:51], v[124:127]\n"
"ds_read_b64_tr_b16 a[232:233], v216 offset:18688\n"
"v_mfma_f32_16x16x32_bf16 v[124:127], a[196:199], a[52:55], v[124:127]\n"
"v_mfma_f32_16x16x32_bf16 v[124:127], a[200:203], a[56:59], v[124:127]\n"
"ds_read_b64_tr_b16 a[234:235], v217 offset:18688\n"
"v_mfma_f32_16x16x32_bf16 v[124:127], a[204:207], a[60:63], v[124:127]\n"
"v_mfma_f32_16x16x32_bf16 v[112:115], a[204:207], a[12:15], v[112:115]\n"
"ds_read_b64_tr_b16 a[236:237], v216 offset:27904\n"
"v_cvt_pk_bf16_f32 v196, v100, v101\n"
"v_mfma_f32_16x16x32_bf16 v[112:115], a[200:203], a[8:11], v[112:115]\n"
"v_cvt_pk_bf16_f32 v197, v102, v103\n"
"v_cvt_pk_bf16_f32 v198, v116, v117\n"
"v_mfma_f32_16x16x32_bf16 v[112:115], a[196:199], a[4:7], v[112:115]\n"
"s_add_u32 s77, s85, 8192\n"
"ds_read_b64_tr_b16 a[238:239], v217 offset:27904\n"
"v_cvt_pk_bf16_f32 v199, v118, v119\n"
"v_mfma_f32_16x16x32_bf16 v[112:115], a[192:195], a[0:3], v[112:115]\n"
"s_waitcnt lgkmcnt(8)\n"
"buffer_store_dwordx4 v[196:199], v232, s[52:55], s77 offen offset:1024 sc1\n"
"v_mfma_f32_16x16x32_bf16 v[140:143], a[208:211], a[112:115], v[140:143]\n"
"v_cvt_pk_bf16_f32 v200, v104, v105\n"
"v_cvt_pk_bf16_f32 v201, v106, v107\n"
"v_mfma_f32_16x16x32_bf16 v[140:143], a[212:215], a[116:119], v[140:143]\n"
"s_add_u32 s77, s87, 8192\n"
"v_cvt_pk_bf16_f32 v202, v120, v121\n"
"v_cvt_pk_bf16_f32 v203, v122, v123\n"
"v_mfma_f32_16x16x32_bf16 v[140:143], a[216:219], a[120:123], v[140:143]\n"
"buffer_store_dwordx4 v[200:203], v232, s[52:55], s77 offen offset:2048 sc1\n"
"v_mfma_f32_16x16x32_bf16 v[140:143], a[220:223], a[124:127], v[140:143]\n"
"v_cvt_pk_bf16_f32 v180, v108, v109\n"
"v_cvt_pk_bf16_f32 v181, v110, v111\n"
"v_mfma_f32_16x16x32_bf16 v[136:139], a[220:223], a[108:111], v[136:139]\n"
"s_add_u32 s77, s68, 8192\n"
"v_cvt_pk_bf16_f32 v182, v124, v125\n"
"v_cvt_pk_bf16_f32 v183, v126, v127\n"
"v_mfma_f32_16x16x32_bf16 v[136:139], a[216:219], a[104:107], v[136:139]\n"
"buffer_store_dwordx4 v[180:183], v232, s[52:55], s77 offen offset:3072 sc1\n"
"v_mfma_f32_16x16x32_bf16 v[136:139], a[212:215], a[100:103], v[136:139]\n"
"s_add_u32 s66, s84, 4096\n"
"v_mfma_f32_16x16x32_bf16 v[136:139], a[208:211], a[96:99], v[136:139]\n"
"buffer_load_dwordx4 a[152:155], v232, s[40:43], s66 offen sc1\n"
"v_mfma_f32_16x16x32_bf16 v[132:135], a[208:211], a[80:83], v[132:135]\n"
"v_mfma_f32_16x16x32_bf16 v[132:135], a[212:215], a[84:87], v[132:135]\n"
"v_mfma_f32_16x16x32_bf16 v[132:135], a[216:219], a[88:91], v[132:135]\n"
"v_mfma_f32_16x16x32_bf16 v[132:135], a[220:223], a[92:95], v[132:135]\n"
"v_mfma_f32_16x16x32_bf16 v[128:131], a[220:223], a[76:79], v[128:131]\n"
"v_mfma_f32_16x16x32_bf16 v[128:131], a[216:219], a[72:75], v[128:131]\n"
"v_mfma_f32_16x16x32_bf16 v[128:131], a[212:215], a[68:71], v[128:131]\n"
"v_mfma_f32_16x16x32_bf16 v[128:131], a[208:211], a[64:67], v[128:131]\n"
"s_waitcnt lgkmcnt(0)\n"
"v_mfma_f32_16x16x32_bf16 v[132:135], a[224:227], a[16:19], v[132:135]\n"
"v_mfma_f32_16x16x32_bf16 v[132:135], a[228:231], a[20:23], v[132:135]\n"
"v_mfma_f32_16x16x32_bf16 v[132:135], a[232:235], a[24:27], v[132:135]\n"
"v_mfma_f32_16x16x32_bf16 v[132:135], a[236:239], a[28:31], v[132:135]\n"
"v_mfma_f32_16x16x32_bf16 v[136:139], a[236:239], a[44:47], v[136:139]\n"
"v_mfma_f32_16x16x32_bf16 v[136:139], a[232:235], a[40:43], v[136:139]\n"
"v_mfma_f32_16x16x32_bf16 v[136:139], a[228:231], a[36:39], v[136:139]\n"
"v_mfma_f32_16x16x32_bf16 v[136:139], a[224:227], a[32:35], v[136:139]\n"
"v_mfma_f32_16x16x32_bf16 v[140:143], a[224:227], a[48:51], v[140:143]\n"
"v_mfma_f32_16x16x32_bf16 v[140:143], a[228:231], a[52:55], v[140:143]\n"
"v_mfma_f32_16x16x32_bf16 v[140:143], a[232:235], a[56:59], v[140:143]\n"
"v_mfma_f32_16x16x32_bf16 v[140:143], a[236:239], a[60:63], v[140:143]\n"
"v_mfma_f32_16x16x32_bf16 v[128:131], a[236:239], a[12:15], v[128:131]\n"
"v_mfma_f32_16x16x32_bf16 v[128:131], a[232:235], a[8:11], v[128:131]\n"
"v_mfma_f32_16x16x32_bf16 v[128:131], a[228:231], a[4:7], v[128:131]\n"
"v_mfma_f32_16x16x32_bf16 v[128:131], a[224:227], a[0:3], v[128:131]\n"
"s_branch L_end2%=\n"
"L_odd%=:\n"
"v_mfma_f32_16x16x32_bf16 v[0:3], a[192:195], v[144:147], 0\n"
"ds_read_b64_tr_b16 a[224:225], v216 offset:64\n"
"v_mfma_f32_16x16x32_bf16 v[0:3], a[196:199], v[148:151], v[0:3]\n"
"v_mfma_f32_16x16x32_bf16 v[0:3], a[200:203], v[152:155], v[0:3]\n"
"ds_read_b64_tr_b16 a[226:227], v217 offset:64\n"
"v_mfma_f32_16x16x32_bf16 v[0:3], a[204:207], v[156:159], v[0:3]\n"
"buffer_load_dwordx4 a[24:27], v232, s[52:55], s66 offen offset:2048 sc1\n"
"v_mfma_f32_16x16x32_bf16 v[4:7], a[204:207], v[172:175], 0\n"
"ds_read_b64_tr_b16 a[228:229], v216 offset:9280\n"
"v_mfma_f32_16x16x32_bf16 v[4:7], a[200:203], v[168:171], v[4:7]\n"
"v_mfma_f32_16x16x32_bf16 v[4:7], a[196:199], v[164:167], v[4:7]\n"
"ds_read_b64_tr_b16 a[230:231], v217 offset:9280\n"
"v_mfma_f32_16x16x32_bf16 v[4:7], a[192:195], v[160:163], v[4:7]\n"
"v_mfma_f32_16x16x32_bf16 v[8:11], a[192:195], v[176:179], 0\n"
"ds_read_b64_tr_b16 a[232:233], v216 offset:18496\n"
"v_mfma_f32_16x16x32_bf16 v[8:11], a[196:199], v[180:183], v[8:11]\n"
"buffer_load_dwordx4 a[28:31], v232, s[52:55], s66 offen offset:3072 sc1\n"
"v_mfma_f32_16x16x32_bf16 v[8:11], a[200:203], v[184:187], v[8:11]\n"
"ds_read_b64_tr_b16 a[234:235], v217 offset:18496\n"
"v_mfma_f32_16x16x32_bf16 v[8:11], a[204:207], v[188:191], v[8:11]\n"
"v_mfma_f32_16x16x32_bf16 v[12:15], a[204:207], v[204:207], 0\n"
"ds_read_b64_tr_b16 a[236:237], v216 offset:27712\n"
"v_mfma_f32_16x16x32_bf16 v[12:15], a[200:203], v[200:203], v[12:15]\n"
"v_mfma_f32_16x16x32_bf16 v[12:15], a[196:199], v[196:199], v[12:15]\n"
"s_add_u32 s61, s65, 12288\n"
"ds_read_b64_tr_b16 a[238:239], v217 offset:27712\n"
"v_mfma_f32_16x16x32_bf16 v[12:15], a[192:195], v[192:195], v[12:15]\n"
"s_waitcnt lgkmcnt(8)\n"
"buffer_store_dwordx4 v[192:195], v232, s[52:55], s61 offen sc1\n"
"v_mfma_f32_16x16x32_bf16 v[28:31], a[208:211], v[192:195], 0\n"
"s_add_u32 s66, s77, 8192\n"
"ds_read_b64_tr_b16 a[192:193], v216 offset:96\n"
"v_mfma_f32_16x16x32_bf16 v[28:31], a[212:215], v[196:199], v[28:31]\n"
"buffer_load_dwordx4 a[32:35], v232, s[52:55], s66 offen sc1\n"
"v_mfma_f32_16x16x32_bf16 v[28:31], a[216:219], v[200:203], v[28:31]\n"
"ds_read_b64_tr_b16 a[194:195], v217 offset:96\n"
"v_mfma_f32_16x16x32_bf16 v[28:31], a[220:223], v[204:207], v[28:31]\n"
"v_mfma_f32_16x16x32_bf16 v[24:27], a[220:223], v[188:191], 0\n"
"ds_read_b64_tr_b16 a[196:197], v216 offset:9312\n"
"v_mfma_f32_16x16x32_bf16 v[24:27], a[216:219], v[184:187], v[24:27]\n"
"buffer_load_dwordx4 a[36:39], v232, s[52:55], s66 offen offset:1024 sc1\n"
"v_mfma_f32_16x16x32_bf16 v[24:27], a[212:215], v[180:183], v[24:27]\n"
"ds_read_b64_tr_b16 a[198:199], v217 offset:9312\n"
"v_mfma_f32_16x16x32_bf16 v[24:27], a[208:211], v[176:179], v[24:27]\n"
"v_mfma_f32_16x16x32_bf16 v[20:23], a[208:211], v[160:163], 0\n"
"ds_read_b64_tr_b16 a[200:201], v216 offset:18528\n"
"v_mfma_f32_16x16x32_bf16 v[20:23], a[212:215], v[164:167], v[20:23]\n"
"v_mfma_f32_16x16x32_bf16 v[20:23], a[216:219], v[168:171], v[20:23]\n"
"ds_read_b64_tr_b16 a[202:203], v217 offset:18528\n"
"v_mfma_f32_16x16x32_bf16 v[20:23], a[220:223], v[172:175], v[20:23]\n"
"buffer_load_dwordx4 a[40:43], v232, s[52:55], s66 offen offset:2048 sc1\n"
"v_mfma_f32_16x16x32_bf16 v[16:19], a[220:223], v[156:159], 0\n"
"ds_read_b64_tr_b16 a[204:205], v216 offset:27744\n"
"v_mfma_f32_16x16x32_bf16 v[16:19], a[216:219], v[152:155], v[16:19]\n"
"v_mfma_f32_16x16x32_bf16 v[16:19], a[212:215], v[148:151], v[16:19]\n"
"ds_read_b64_tr_b16 a[206:207], v217 offset:27744\n"
"v_mfma_f32_16x16x32_bf16 v[16:19], a[208:211], v[144:147], v[16:19]\n"
"s_waitcnt lgkmcnt(8)\n"
"v_mfma_f32_16x16x32_bf16 v[32:35], a[224:227], v[144:147], 0\n"
"s_add_u32 s61, s65, 0\n"
"ds_read_b64_tr_b16 a[208:209], v216 offset:128\n"
"v_mfma_f32_16x16x32_bf16 v[32:35], a[228:231], v[148:151], v[32:35]\n"
"buffer_store_dwordx4 v[148:151], v232, s[52:55], s61 offen offset:1024 sc1\n"
"v_mfma_f32_16x16x32_bf16 v[32:35], a[232:235], v[152:155], v[32:35]\n"
"ds_read_b64_tr_b16 a[210:211], v217 offset:128\n"
"v_mfma_f32_16x16x32_bf16 v[32:35], a[236:239], v[156:159], v[32:35]\n"
"buffer_load_dwordx4 a[44:47], v232, s[52:55], s66 offen offset:3072 sc1\n"
"v_mfma_f32_16x16x32_bf16 v[36:39], a[236:239], v[172:175], 0\n"
"ds_read_b64_tr_b16 a[212:213], v216 offset:9344\n"
"v_mfma_f32_16x16x32_bf16 v[36:39], a[232:235], v[168:171], v[36:39]\n"
"v_mfma_f32_16x16x32_bf16 v[36:39], a[228:231], v[164:167], v[36:39]\n"
"s_add_u32 s66, s77, 12288\n"
"ds_read_b64_tr_b16 a[214:215], v217 offset:9344\n"
"v_mfma_f32_16x16x32_bf16 v[36:39], a[224:227], v[160:163], v[36:39]\n"
"buffer_load_dwordx4 a[48:51], v232, s[52:55], s66 offen sc1\n"
"v_mfma_f32_16x16x32_bf16 v[40:43], a[224:227], v[176:179], 0\n"
"ds_read_b64_tr_b16 a[216:217], v216 offset:18560\n"
"v_mfma_f32_16x16x32_bf16 v[40:43], a[228:231], v[180:183], v[40:43]\n"
"v_mfma_f32_16x16x32_bf16 v[40:43], a[232:235], v[184:187], v[40:43]\n"
"ds_read_b64_tr_b16 a[218:219], v217 offset:18560\n"
"v_mfma_f32_16x16x32_bf16 v[40:43], a[236:239], v[188:191], v[40:43]\n"
"v_mfma_f32_16x16x32_bf16 v[44:47], a[236:239], v[204:207], 0\n"
"ds_read_b64_tr_b16 a[220:221], v216 offset:27776\n"
"v_mfma_f32_16x16x32_bf16 v[44:47], a[232:235], v[200:203], v[44:47]\n"
"buffer_load_dwordx4 a[52:55], v232, s[52:55], s66 offen offset:1024 sc1\n"
"v_mfma_f32_16x16x32_bf16 v[44:47], a[228:231], v[196:199], v[44:47]\n"
"ds_read_b64_tr_b16 a[222:223], v217 offset:27776\n"
"v_mfma_f32_16x16x32_bf16 v[44:47], a[224:227], v[192:195], v[44:47]\n"
"s_waitcnt lgkmcnt(8)\n"
"v_mfma_f32_16x16x32_bf16 v[60:63], a[192:195], v[192:195], 0\n"
"ds_read_b64_tr_b16 a[224:225], v216 offset:160\n"
"v_mfma_f32_16x16x32_bf16 v[60:63], a[196:199], v[196:199], v[60:63]\n"
"v_mfma_f32_16x16x32_bf16 v[60:63], a[200:203], v[200:203], v[60:63]\n"
"s_add_u32 s61, s65, 4096\n"
"ds_read_b64_tr_b16 a[226:227], v217 offset:160\n"
"v_mfma_f32_16x16x32_bf16 v[60:63], a[204:207], v[204:207], v[60:63]\n"
"buffer_store_dwordx4 v[164:167], v232, s[52:55], s61 offen offset:1024 sc1\n"
"v_mfma_f32_16x16x32_bf16 v[56:59], a[204:207], v[188:191], 0\n"
"ds_read_b64_tr_b16 a[228:229], v216 offset:9376\n"
"v_mfma_f32_16x16x32_bf16 v[56:59], a[200:203], v[184:187], v[56:59]\n"
"buffer_load_dwordx4 a[56:59], v232, s[52:55], s66 offen offset:2048 sc1\n"
"v_mfma_f32_16x16x32_bf16 v[56:59], a[196:199], v[180:183], v[56:59]\n"
"ds_read_b64_tr_b16 a[230:231], v217 offset:9376\n"
"v_mfma_f32_16x16x32_bf16 v[56:59], a[192:195], v[176:179], v[56:59]\n"
"v_mfma_f32_16x16x32_bf16 v[52:55], a[192:195], v[160:163], 0\n"
"ds_read_b64_tr_b16 a[232:233], v216 offset:18592\n"
"v_mfma_f32_16x16x32_bf16 v[52:55], a[196:199], v[164:167], v[52:55]\n"
"buffer_load_dwordx4 a[60:63], v232, s[52:55], s66 offen offset:3072 sc1\n"
"v_mfma_f32_16x16x32_bf16 v[52:55], a[200:203], v[168:171], v[52:55]\n"
"ds_read_b64_tr_b16 a[234:235], v217 offset:18592\n"
"v_mfma_f32_16x16x32_bf16 v[52:55], a[204:207], v[172:175], v[52:55]\n"
"v_mfma_f32_16x16x32_bf16 v[48:51], a[204:207], v[156:159], 0\n"
"ds_read_b64_tr_b16 a[236:237], v216 offset:27808\n"
"v_mfma_f32_16x16x32_bf16 v[48:51], a[200:203], v[152:155], v[48:51]\n"
"v_mfma_f32_16x16x32_bf16 v[48:51], a[196:199], v[148:151], v[48:51]\n"
"ds_read_b64_tr_b16 a[238:239], v217 offset:27808\n"
"v_mfma_f32_16x16x32_bf16 v[48:51], a[192:195], v[144:147], v[48:51]\n"
"s_waitcnt lgkmcnt(8)\n"
"v_mfma_f32_16x16x32_bf16 v[64:67], a[208:211], v[144:147], 0\n"
"ds_read_b64_tr_b16 a[192:193], v216 offset:192\n"
"v_mfma_f32_16x16x32_bf16 v[64:67], a[212:215], v[148:151], v[64:67]\n"
"v_mfma_f32_16x16x32_bf16 v[64:67], a[216:219], v[152:155], v[64:67]\n"
"ds_read_b64_tr_b16 a[194:195], v217 offset:192\n"
"v_mfma_f32_16x16x32_bf16 v[64:67], a[220:223], v[156:159], v[64:67]\n"
"v_mfma_f32_16x16x32_bf16 v[68:71], a[220:223], v[172:175], 0\n"
"s_add_u32 s61, s65, 8192\n"
"ds_read_b64_tr_b16 a[196:197], v216 offset:9408\n"
"v_mfma_f32_16x16x32_bf16 v[68:71], a[216:219], v[168:171], v[68:71]\n"
"buffer_store_dwordx4 v[180:183], v232, s[52:55], s61 offen offset:1024 sc1\n"
"v_mfma_f32_16x16x32_bf16 v[68:71], a[212:215], v[164:167], v[68:71]\n"
"ds_read_b64_tr_b16 a[198:199], v217 offset:9408\n"
"v_mfma_f32_16x16x32_bf16 v[68:71], a[208:211], v[160:163], v[68:71]\n"
"v_mfma_f32_16x16x32_bf16 v[72:75], a[208:211], v[176:179], 0\n"
"ds_read_b64_tr_b16 a[200:201], v216 offset:18624\n"
"v_mfma_f32_16x16x32_bf16 v[72:75], a[212:215], v[180:183], v[72:75]\n"
"v_mfma_f32_16x16x32_bf16 v[72:75], a[216:219], v[184:187], v[72:75]\n"
"s_mov_b32 s98, s79\n"
"ds_read_b64_tr_b16 a[202:203], v217 offset:18624\n"
"v_mfma_f32_16x16x32_bf16 v[72:75], a[220:223], v[188:191], v[72:75]\n"
"s_waitcnt vmcnt(57)\n"
"buffer_store_dword v247, v248, s[96:99], 0 offen sc1\n"
"v_mfma_f32_16x16x32_bf16 v[76:79], a[220:223], v[204:207], 0\n"
"s_mov_b32 s98, 139264\n"
"ds_read_b64_tr_b16 a[204:205], v216 offset:27840\n"
"v_mfma_f32_16x16x32_bf16 v[76:79], a[216:219], v[200:203], v[76:79]\n"
"v_mfma_f32_16x16x32_bf16 v[76:79], a[212:215], v[196:199], v[76:79]\n"
"ds_read_b64_tr_b16 a[206:207], v217 offset:27840\n"
"v_mfma_f32_16x16x32_bf16 v[76:79], a[208:211], v[192:195], v[76:79]\n"
"s_waitcnt lgkmcnt(8)\n"
"v_mfma_f32_16x16x32_bf16 v[92:95], a[224:227], v[192:195], 0\n"
"ds_read_b64_tr_b16 a[208:209], v216 offset:224\n"
"v_mfma_f32_16x16x32_bf16 v[92:95], a[228:231], v[196:199], v[92:95]\n"
"v_mfma_f32_16x16x32_bf16 v[92:95], a[232:235], v[200:203], v[92:95]\n"
"ds_read_b64_tr_b16 a[210:211], v217 offset:224\n"
"v_mfma_f32_16x16x32_bf16 v[92:95], a[236:239], v[204:207], v[92:95]\n"
"v_mfma_f32_16x16x32_bf16 v[88:91], a[236:239], v[188:191], 0\n"
"ds_read_b64_tr_b16 a[212:213], v216 offset:9440\n"
"v_mfma_f32_16x16x32_bf16 v[88:91], a[232:235], v[184:187], v[88:91]\n"
"v_mfma_f32_16x16x32_bf16 v[88:91], a[228:231], v[180:183], v[88:91]\n"
"s_add_u32 s61, s65, 12288\n"
"ds_read_b64_tr_b16 a[214:215], v217 offset:9440\n"
"v_mfma_f32_16x16x32_bf16 v[88:91], a[224:227], v[176:179], v[88:91]\n"
"buffer_store_dwordx4 v[196:199], v232, s[52:55], s61 offen offset:1024 sc1\n"
"v_mfma_f32_16x16x32_bf16 v[84:87], a[224:227], v[160:163], 0\n"
"ds_read_b64_tr_b16 a[216:217], v216 offset:18656\n"
"v_mfma_f32_16x16x32_bf16 v[84:87], a[228:231], v[164:167], v[84:87]\n"
"v_mfma_f32_16x16x32_bf16 v[84:87], a[232:235], v[168:171], v[84:87]\n"
"ds_read_b64_tr_b16 a[218:219], v217 offset:18656\n"
"v_mfma_f32_16x16x32_bf16 v[84:87], a[236:239], v[172:175], v[84:87]\n"
"v_mfma_f32_16x16x32_bf16 v[80:83], a[236:239], v[156:159], 0\n"
"ds_read_b64_tr_b16 a[220:221], v216 offset:27872\n"
"v_mfma_f32_16x16x32_bf16 v[80:83], a[232:235], v[152:155], v[80:83]\n"
"v_mfma_f32_16x16x32_bf16 v[80:83], a[228:231], v[148:151], v[80:83]\n"
"ds_read_b64_tr_b16 a[222:223], v217 offset:27872\n"
"v_mfma_f32_16x16x32_bf16 v[80:83], a[224:227], v[144:147], v[80:83]\n"
"s_waitcnt lgkmcnt(8)\n"
"v_mfma_f32_16x16x32_bf16 v[96:99], a[192:195], v[144:147], 0\n"
"ds_read_b64_tr_b16 a[224:225], v216 offset:256\n"
"v_mfma_f32_16x16x32_bf16 v[96:99], a[196:199], v[148:151], v[96:99]\n"
"v_mfma_f32_16x16x32_bf16 v[96:99], a[200:203], v[152:155], v[96:99]\n"
"ds_read_b64_tr_b16 a[226:227], v217 offset:256\n"
"v_mfma_f32_16x16x32_bf16 v[96:99], a[204:207], v[156:159], v[96:99]\n"
"v_mfma_f32_16x16x32_bf16 v[100:103], a[204:207], v[172:175], 0\n"
"ds_read_b64_tr_b16 a[228:229], v216 offset:9472\n"
"v_mfma_f32_16x16x32_bf16 v[100:103], a[200:203], v[168:171], v[100:103]\n"
"v_mfma_f32_16x16x32_bf16 v[100:103], a[196:199], v[164:167], v[100:103]\n"
"ds_read_b64_tr_b16 a[230:231], v217 offset:9472\n"
"v_mfma_f32_16x16x32_bf16 v[100:103], a[192:195], v[160:163], v[100:103]\n"
"v_mfma_f32_16x16x32_bf16 v[104:107], a[192:195], v[176:179], 0\n"
"s_add_u32 s61, s65, 0\n"
"ds_read_b64_tr_b16 a[232:233], v216 offset:18688\n"
"v_mfma_f32_16x16x32_bf16 v[104:107], a[196:199], v[180:183], v[104:107]\n"
"buffer_store_dwordx4 v[152:155], v232, s[52:55], s61 offen offset:2048 sc1\n"
"v_mfma_f32_16x16x32_bf16 v[104:107], a[200:203], v[184:187], v[104:107]\n"
"ds_read_b64_tr_b16 a[234:235], v217 offset:18688\n"
"v_mfma_f32_16x16x32_bf16 v[104:107], a[204:207], v[188:191], v[104:107]\n"
"v_mfma_f32_16x16x32_bf16 v[108:111], a[204:207], v[204:207], 0\n"
"ds_read_b64_tr_b16 a[236:237], v216 offset:27904\n"
"v_mfma_f32_16x16x32_bf16 v[108:111], a[200:203], v[200:203], v[108:111]\n"
"v_mfma_f32_16x16x32_bf16 v[108:111], a[196:199], v[196:199], v[108:111]\n"
"ds_read_b64_tr_b16 a[238:239], v217 offset:27904\n"
"v_mfma_f32_16x16x32_bf16 v[108:111], a[192:195], v[192:195], v[108:111]\n"
"s_waitcnt lgkmcnt(8)\n"
"v_mfma_f32_16x16x32_bf16 v[124:127], a[208:211], v[192:195], 0\n"
"v_mfma_f32_16x16x32_bf16 v[124:127], a[212:215], v[196:199], v[124:127]\n"
"v_mfma_f32_16x16x32_bf16 v[124:127], a[216:219], v[200:203], v[124:127]\n"
"v_mfma_f32_16x16x32_bf16 v[124:127], a[220:223], v[204:207], v[124:127]\n"
"s_waitcnt vmcnt(43) lgkmcnt(0)\n"
"v_mfma_f32_16x16x32_bf16 v[120:123], a[220:223], v[188:191], 0\n"
"s_barrier\n"
"v_mfma_f32_16x16x32_bf16 v[120:123], a[216:219], v[184:187], v[120:123]\n"
"v_mfma_f32_16x16x32_bf16 v[120:123], a[212:215], v[180:183], v[120:123]\n"
"ds_read_b64_tr_b16 a[192:193], v216 offset:36864\n"
"ds_read_b64_tr_b16 a[194:195], v217 offset:36864\n"
"v_mfma_f32_16x16x32_bf16 v[120:123], a[208:211], v[176:179], v[120:123]\n"
"v_mfma_f32_16x16x32_bf16 v[116:119], a[208:211], v[160:163], 0\n"
"ds_read_b64_tr_b16 a[196:197], v216 offset:46080\n"
"ds_read_b64_tr_b16 a[198:199], v217 offset:46080\n"
"v_mfma_f32_16x16x32_bf16 v[116:119], a[212:215], v[164:167], v[116:119]\n"
"v_mfma_f32_16x16x32_bf16 v[116:119], a[216:219], v[168:171], v[116:119]\n"
"s_add_u32 s61, s65, 4096\n"
"ds_read_b64_tr_b16 a[200:201], v216 offset:55296\n"
"ds_read_b64_tr_b16 a[202:203], v217 offset:55296\n"
"v_mfma_f32_16x16x32_bf16 v[116:119], a[220:223], v[172:175], v[116:119]\n"
"buffer_store_dwordx4 v[168:171], v232, s[52:55], s61 offen offset:2048 sc1\n"
"v_mfma_f32_16x16x32_bf16 v[112:115], a[220:223], v[156:159], 0\n"
"ds_read_b64_tr_b16 a[204:205], v216 offset:64512\n"
"v_mfma_f32_16x16x32_bf16 v[112:115], a[216:219], v[152:155], v[112:115]\n"
"v_mfma_f32_16x16x32_bf16 v[112:115], a[212:215], v[148:151], v[112:115]\n"
"ds_read_b64_tr_b16 a[206:207], v217 offset:64512\n"
"v_mfma_f32_16x16x32_bf16 v[112:115], a[208:211], v[144:147], v[112:115]\n"
"v_mfma_f32_16x16x32_bf16 v[128:131], a[224:227], v[144:147], 0\n"
"ds_read_b64_tr_b16 a[208:209], v216 offset:36896\n"
"v_mfma_f32_16x16x32_bf16 v[128:131], a[228:231], v[148:151], v[128:131]\n"
"v_mfma_f32_16x16x32_bf16 v[128:131], a[232:235], v[152:155], v[128:131]\n"
"ds_read_b64_tr_b16 a[210:211], v217 offset:36896\n"
"v_mfma_f32_16x16x32_bf16 v[128:131], a[236:239], v[156:159], v[128:131]\n"
"v_mfma_f32_16x16x32_bf16 v[132:135], a[236:239], v[172:175], 0\n"
"ds_read_b64_tr_b16 a[212:213], v216 offset:46112\n"
"v_mfma_f32_16x16x32_bf16 v[132:135], a[232:235], v[168:171], v[132:135]\n"
"v_mfma_f32_16x16x32_bf16 v[132:135], a[228:231], v[164:167], v[132:135]\n"
"ds_read_b64_tr_b16 a[214:215], v217 offset:46112\n"
"v_mfma_f32_16x16x32_bf16 v[132:135], a[224:227], v[160:163], v[132:135]\n"
"v_mfma_f32_16x16x32_bf16 v[136:139], a[224:227], v[176:179], 0\n"
"ds_read_b64_tr_b16 a[216:217], v216 offset:55328\n"
"v_mfma_f32_16x16x32_bf16 v[136:139], a[228:231], v[180:183], v[136:139]\n"
"v_mfma_f32_16x16x32_bf16 v[136:139], a[232:235], v[184:187], v[136:139]\n"
"ds_read_b64_tr_b16 a[218:219], v217 offset:55328\n"
"v_mfma_f32_16x16x32_bf16 v[136:139], a[236:239], v[188:191], v[136:139]\n"
"v_mfma_f32_16x16x32_bf16 v[140:143], a[236:239], v[204:207], 0\n"
"s_add_u32 s61, s65, 8192\n"
"ds_read_b64_tr_b16 a[220:221], v216 offset:64544\n"
"v_mfma_f32_16x16x32_bf16 v[140:143], a[232:235], v[200:203], v[140:143]\n"
"buffer_store_dwordx4 v[184:187], v232, s[52:55], s61 offen offset:2048 sc1\n"
"v_mfma_f32_16x16x32_bf16 v[140:143], a[228:231], v[196:199], v[140:143]\n"
"ds_read_b64_tr_b16 a[222:223], v217 offset:64544\n"
"v_mfma_f32_16x16x32_bf16 v[140:143], a[224:227], v[192:195], v[140:143]\n"
"s_waitcnt vmcnt(30) lgkmcnt(8)\n"
"v_mfma_f32_16x16x32_bf16 v[12:15], a[192:195], a[176:179], v[12:15]\n"
"ds_read_b64_tr_b16 a[224:225], v216 offset:36928\n"
"v_mfma_f32_16x16x32_bf16 v[12:15], a[196:199], a[180:183], v[12:15]\n"
"v_mfma_f32_16x16x32_bf16 v[12:15], a[200:203], a[184:187], v[12:15]\n"
"ds_read_b64_tr_b16 a[226:227], v217 offset:36928\n"
"v_mfma_f32_16x16x32_bf16 v[12:15], a[204:207], a[188:191], v[12:15]\n"
"v_mfma_f32_16x16x32_bf16 v[8:11], a[204:207], a[172:175], v[8:11]\n"
"ds_read_b64_tr_b16 a[228:229], v216 offset:46144\n"
"v_mfma_f32_16x16x32_bf16 v[8:11], a[200:203], a[168:171], v[8:11]\n"
"v_mfma_f32_16x16x32_bf16 v[8:11], a[196:199], a[164:167], v[8:11]\n"
"ds_read_b64_tr_b16 a[230:231], v217 offset:46144\n"
"v_mfma_f32_16x16x32_bf16 v[8:11], a[192:195], a[160:163], v[8:11]\n"
"v_mfma_f32_16x16x32_bf16 v[4:7], a[192:195], a[144:147], v[4:7]\n"
"ds_read_b64_tr_b16 a[232:233], v216 offset:55360\n"
"v_mfma_f32_16x16x32_bf16 v[4:7], a[196:199], a[148:151], v[4:7]\n"
"v_mfma_f32_16x16x32_bf16 v[4:7], a[200:203], a[152:155], v[4:7]\n"
"ds_read_b64_tr_b16 a[234:235], v217 offset:55360\n"
"v_mfma_f32_16x16x32_bf16 v[4:7], a[204:207], a[156:159], v[4:7]\n"
"v_mfma_f32_16x16x32_bf16 v[0:3], a[204:207], a[140:143], v[0:3]\n"
"ds_read_b64_tr_b16 a[236:237], v216 offset:64576\n"
"v_mfma_f32_16x16x32_bf16 v[0:3], a[200:203], a[136:139], v[0:3]\n"
"v_mfma_f32_16x16x32_bf16 v[0:3], a[196:199], a[132:135], v[0:3]\n"
"s_add_u32 s61, s65, 12288\n"
"ds_read_b64_tr_b16 a[238:239], v217 offset:64576\n"
"v_mfma_f32_16x16x32_bf16 v[0:3], a[192:195], a[128:131], v[0:3]\n"
"s_waitcnt lgkmcnt(8)\n"
"buffer_store_dwordx4 v[200:203], v232, s[52:55], s61 offen offset:2048 sc1\n"
"v_mfma_f32_16x16x32_bf16 v[16:19], a[208:211], a[128:131], v[16:19]\n"
"ds_read_b64_tr_b16 a[192:193], v216 offset:36960\n"
"v_mfma_f32_16x16x32_bf16 v[16:19], a[212:215], a[132:135], v[16:19]\n"
"v_mfma_f32_16x16x32_bf16 v[16:19], a[216:219], a[136:139], v[16:19]\n"
"ds_read_b64_tr_b16 a[194:195], v217 offset:36960\n"
"v_mfma_f32_16x16x32_bf16 v[16:19], a[220:223], a[140:143], v[16:19]\n"
"v_mfma_f32_16x16x32_bf16 v[20:23], a[220:223], a[156:159], v[20:23]\n"
"ds_read_b64_tr_b16 a[196:197], v216 offset:46176\n"
"v_mfma_f32_16x16x32_bf16 v[20:23], a[216:219], a[152:155], v[20:23]\n"
"v_mfma_f32_16x16x32_bf16 v[20:23], a[212:215], a[148:151], v[20:23]\n"
"ds_read_b64_tr_b16 a[198:199], v217 offset:46176\n"
"v_mfma_f32_16x16x32_bf16 v[20:23], a[208:211], a[144:147], v[20:23]\n"
"v_mfma_f32_16x16x32_bf16 v[24:27], a[208:211], a[160:163], v[24:27]\n"
"ds_read_b64_tr_b16 a[200:201], v216 offset:55392\n"
"v_mfma_f32_16x16x32_bf16 v[24:27], a[212:215], a[164:167], v[24:27]\n"
"v_mfma_f32_16x16x32_bf16 v[24:27], a[216:219], a[168:171], v[24:27]\n"
"ds_read_b64_tr_b16 a[202:203], v217 offset:55392\n"
"v_mfma_f32_16x16x32_bf16 v[24:27], a[220:223], a[172:175], v[24:27]\n"
"v_mfma_f32_16x16x32_bf16 v[28:31], a[220:223], a[188:191], v[28:31]\n"
"ds_read_b64_tr_b16 a[204:205], v216 offset:64608\n"
"v_mfma_f32_16x16x32_bf16 v[28:31], a[216:219], a[184:187], v[28:31]\n"
"v_mfma_f32_16x16x32_bf16 v[28:31], a[212:215], a[180:183], v[28:31]\n"
"ds_read_b64_tr_b16 a[206:207], v217 offset:64608\n"
"v_mfma_f32_16x16x32_bf16 v[28:31], a[208:211], a[176:179], v[28:31]\n"
"s_waitcnt lgkmcnt(8)\n"
"v_mfma_f32_16x16x32_bf16 v[44:47], a[224:227], a[176:179], v[44:47]\n"
"s_add_u32 s61, s65, 0\n"
"ds_read_b64_tr_b16 a[208:209], v216 offset:36992\n"
"v_mfma_f32_16x16x32_bf16 v[44:47], a[228:231], a[180:183], v[44:47]\n"
"buffer_store_dwordx4 v[156:159], v232, s[52:55], s61 offen offset:3072 sc1\n"
"v_mfma_f32_16x16x32_bf16 v[44:47], a[232:235], a[184:187], v[44:47]\n"
"ds_read_b64_tr_b16 a[210:211], v217 offset:36992\n"
"v_mfma_f32_16x16x32_bf16 v[44:47], a[236:239], a[188:191], v[44:47]\n"
"v_mfma_f32_16x16x32_bf16 v[40:43], a[236:239], a[172:175], v[40:43]\n"
"ds_read_b64_tr_b16 a[212:213], v216 offset:46208\n"
"v_mfma_f32_16x16x32_bf16 v[40:43], a[232:235], a[168:171], v[40:43]\n"
"v_mfma_f32_16x16x32_bf16 v[40:43], a[228:231], a[164:167], v[40:43]\n"
"ds_read_b64_tr_b16 a[214:215], v217 offset:46208\n"
"v_mfma_f32_16x16x32_bf16 v[40:43], a[224:227], a[160:163], v[40:43]\n"
"v_mfma_f32_16x16x32_bf16 v[36:39], a[224:227], a[144:147], v[36:39]\n"
"ds_read_b64_tr_b16 a[216:217], v216 offset:55424\n"
"v_mfma_f32_16x16x32_bf16 v[36:39], a[228:231], a[148:151], v[36:39]\n"
"v_mfma_f32_16x16x32_bf16 v[36:39], a[232:235], a[152:155], v[36:39]\n"
"ds_read_b64_tr_b16 a[218:219], v217 offset:55424\n"
"v_mfma_f32_16x16x32_bf16 v[36:39], a[236:239], a[156:159], v[36:39]\n"
"v_mfma_f32_16x16x32_bf16 v[32:35], a[236:239], a[140:143], v[32:35]\n"
"ds_read_b64_tr_b16 a[220:221], v216 offset:64640\n"
"v_mfma_f32_16x16x32_bf16 v[32:35], a[232:235], a[136:139], v[32:35]\n"
"v_mfma_f32_16x16x32_bf16 v[32:35], a[228:231], a[132:135], v[32:35]\n"
"ds_read_b64_tr_b16 a[222:223], v217 offset:64640\n"
"v_mfma_f32_16x16x32_bf16 v[32:35], a[224:227], a[128:131], v[32:35]\n"
"s_waitcnt lgkmcnt(8)\n"
"v_mfma_f32_16x16x32_bf16 v[48:51], a[192:195], a[128:131], v[48:51]\n"
"ds_read_b64_tr_b16 a[224:225], v216 offset:37024\n"
"v_mfma_f32_16x16x32_bf16 v[48:51], a[196:199], a[132:135], v[48:51]\n"
"v_mfma_f32_16x16x32_bf16 v[48:51], a[200:203], a[136:139], v[48:51]\n"
"s_add_u32 s61, s65, 4096\n"
"ds_read_b64_tr_b16 a[226:227], v217 offset:37024\n"
"v_mfma_f32_16x16x32_bf16 v[48:51], a[204:207], a[140:143], v[48:51]\n"
"buffer_store_dwordx4 v[172:175], v232, s[52:55], s61 offen offset:3072 sc1\n"
"v_mfma_f32_16x16x32_bf16 v[52:55], a[204:207], a[156:159], v[52:55]\n"
"ds_read_b64_tr_b16 a[228:229], v216 offset:46240\n"
"v_mfma_f32_16x16x32_bf16 v[52:55], a[200:203], a[152:155], v[52:55]\n"
"v_mfma_f32_16x16x32_bf16 v[52:55], a[196:199], a[148:151], v[52:55]\n"
"ds_read_b64_tr_b16 a[230:231], v217 offset:46240\n"
"v_mfma_f32_16x16x32_bf16 v[52:55], a[192:195], a[144:147], v[52:55]\n"
"v_mfma_f32_16x16x32_bf16 v[56:59], a[192:195], a[160:163], v[56:59]\n"
"s_mov_b32 s72, 6\n"
"s_mul_i32 s58, s72, 147456\n"
"s_add_u32 m0, s60, 110592\n"
"ds_read_b64_tr_b16 a[232:233], v216 offset:55456\n"
"v_mfma_f32_16x16x32_bf16 v[56:59], a[196:199], a[164:167], v[56:59]\n"
"buffer_load_dwordx4 v221, s[92:95], s58 offen lds\n"
"v_mfma_f32_16x16x32_bf16 v[56:59], a[200:203], a[168:171], v[56:59]\n"
"ds_read_b64_tr_b16 a[234:235], v217 offset:55456\n"
"v_mfma_f32_16x16x32_bf16 v[56:59], a[204:207], a[172:175], v[56:59]\n"
"v_mfma_f32_16x16x32_bf16 v[60:63], a[204:207], a[188:191], v[60:63]\n"
"ds_read_b64_tr_b16 a[236:237], v216 offset:64672\n"
"v_mfma_f32_16x16x32_bf16 v[60:63], a[200:203], a[184:187], v[60:63]\n"
"buffer_load_dwordx4 v225, s[92:95], s58 offen offset:1024 lds\n"
"v_mfma_f32_16x16x32_bf16 v[60:63], a[196:199], a[180:183], v[60:63]\n"
"ds_read_b64_tr_b16 a[238:239], v217 offset:64672\n"
"v_mfma_f32_16x16x32_bf16 v[60:63], a[192:195], a[176:179], v[60:63]\n"
"s_waitcnt lgkmcnt(8)\n"
"v_mfma_f32_16x16x32_bf16 v[76:79], a[208:211], a[176:179], v[76:79]\n"
"ds_read_b64_tr_b16 a[192:193], v216 offset:37056\n"
"v_mfma_f32_16x16x32_bf16 v[76:79], a[212:215], a[180:183], v[76:79]\n"
"v_mfma_f32_16x16x32_bf16 v[76:79], a[216:219], a[184:187], v[76:79]\n"
"ds_read_b64_tr_b16 a[194:195], v217 offset:37056\n"
"v_mfma_f32_16x16x32_bf16 v[76:79], a[220:223], a[188:191], v[76:79]\n"
"buffer_load_dwordx4 v226, s[92:95], s58 offen offset:2048 lds\n"
"v_mfma_f32_16x16x32_bf16 v[72:75], a[220:223], a[172:175], v[72:75]\n"
"s_add_u32 s61, s65, 8192\n"
"ds_read_b64_tr_b16 a[196:197], v216 offset:46272\n"
"v_mfma_f32_16x16x32_bf16 v[72:75], a[216:219], a[168:171], v[72:75]\n"
"buffer_store_dwordx4 v[188:191], v232, s[52:55], s61 offen offset:3072 sc1\n"
"v_mfma_f32_16x16x32_bf16 v[72:75], a[212:215], a[164:167], v[72:75]\n"
"ds_read_b64_tr_b16 a[198:199], v217 offset:46272\n"
"v_mfma_f32_16x16x32_bf16 v[72:75], a[208:211], a[160:163], v[72:75]\n"
"v_mfma_f32_16x16x32_bf16 v[68:71], a[208:211], a[144:147], v[68:71]\n"
"ds_read_b64_tr_b16 a[200:201], v216 offset:55488\n"
"v_mfma_f32_16x16x32_bf16 v[68:71], a[212:215], a[148:151], v[68:71]\n"
"buffer_load_dwordx4 v227, s[92:95], s58 offen offset:3072 lds\n"
"v_mfma_f32_16x16x32_bf16 v[68:71], a[216:219], a[152:155], v[68:71]\n"
"ds_read_b64_tr_b16 a[202:203], v217 offset:55488\n"
"v_mfma_f32_16x16x32_bf16 v[68:71], a[220:223], a[156:159], v[68:71]\n"
"v_mfma_f32_16x16x32_bf16 v[64:67], a[220:223], a[140:143], v[64:67]\n"
"ds_read_b64_tr_b16 a[204:205], v216 offset:64704\n"
"v_mfma_f32_16x16x32_bf16 v[64:67], a[216:219], a[136:139], v[64:67]\n"
"v_mfma_f32_16x16x32_bf16 v[64:67], a[212:215], a[132:135], v[64:67]\n"
"s_add_u32 m0, s60, 114688\n"
"ds_read_b64_tr_b16 a[206:207], v217 offset:64704\n"
"v_mfma_f32_16x16x32_bf16 v[64:67], a[208:211], a[128:131], v[64:67]\n"
"s_waitcnt lgkmcnt(8)\n"
"buffer_load_dwordx4 v228, s[92:95], s58 offen lds\n"
"v_mfma_f32_16x16x32_bf16 v[80:83], a[224:227], a[128:131], v[80:83]\n"
"ds_read_b64_tr_b16 a[208:209], v216 offset:37088\n"
"v_mfma_f32_16x16x32_bf16 v[80:83], a[228:231], a[132:135], v[80:83]\n"
"v_mfma_f32_16x16x32_bf16 v[80:83], a[232:235], a[136:139], v[80:83]\n"
"ds_read_b64_tr_b16 a[210:211], v217 offset:37088\n"
"v_mfma_f32_16x16x32_bf16 v[80:83], a[236:239], a[140:143], v[80:83]\n"
"buffer_load_dwordx4 v229, s[92:95], s58 offen offset:1024 lds\n"
"v_mfma_f32_16x16x32_bf16 v[84:87], a[236:239], a[156:159], v[84:87]\n"
"ds_read_b64_tr_b16 a[212:213], v216 offset:46304\n"
"v_mfma_f32_16x16x32_bf16 v[84:87], a[232:235], a[152:155], v[84:87]\n"
"v_mfma_f32_16x16x32_bf16 v[84:87], a[228:231], a[148:151], v[84:87]\n"
"s_add_u32 s61, s65, 12288\n"
"ds_read_b64_tr_b16 a[214:215], v217 offset:46304\n"
"v_mfma_f32_16x16x32_bf16 v[84:87], a[224:227], a[144:147], v[84:87]\n"
"buffer_store_dwordx4 v[204:207], v232, s[52:55], s61 offen offset:3072 sc1\n"
"v_mfma_f32_16x16x32_bf16 v[88:91], a[224:227], a[160:163], v[88:91]\n"
"ds_read_b64_tr_b16 a[216:217], v216 offset:55520\n"
"v_mfma_f32_16x16x32_bf16 v[88:91], a[228:231], a[164:167], v[88:91]\n"
"buffer_load_dwordx4 v230, s[92:95], s58 offen offset:2048 lds\n"
"v_mfma_f32_16x16x32_bf16 v[88:91], a[232:235], a[168:171], v[88:91]\n"
"ds_read_b64_tr_b16 a[218:219], v217 offset:55520\n"
"v_mfma_f32_16x16x32_bf16 v[88:91], a[236:239], a[172:175], v[88:91]\n"
"v_mfma_f32_16x16x32_bf16 v[92:95], a[236:239], a[188:191], v[92:95]\n"
"ds_read_b64_tr_b16 a[220:221], v216 offset:64736\n"
"v_mfma_f32_16x16x32_bf16 v[92:95], a[232:235], a[184:187], v[92:95]\n"
"v_mfma_f32_16x16x32_bf16 v[92:95], a[228:231], a[180:183], v[92:95]\n"
"ds_read_b64_tr_b16 a[222:223], v217 offset:64736\n"
"v_mfma_f32_16x16x32_bf16 v[92:95], a[224:227], a[176:179], v[92:95]\n"
"s_waitcnt lgkmcnt(8)\n"
"buffer_load_dwordx4 v231, s[92:95], s58 offen offset:3072 lds\n"
"v_mfma_f32_16x16x32_bf16 v[108:111], a[192:195], a[176:179], v[108:111]\n"
"ds_read_b64_tr_b16 a[224:225], v216 offset:37120\n"
"v_mfma_f32_16x16x32_bf16 v[108:111], a[196:199], a[180:183], v[108:111]\n"
"v_mfma_f32_16x16x32_bf16 v[108:111], a[200:203], a[184:187], v[108:111]\n"
"ds_read_b64_tr_b16 a[226:227], v217 offset:37120\n"
"v_mfma_f32_16x16x32_bf16 v[108:111], a[204:207], a[188:191], v[108:111]\n"
"v_mfma_f32_16x16x32_bf16 v[104:107], a[204:207], a[172:175], v[104:107]\n"
"s_add_u32 m0, s60, 118784\n"
"ds_read_b64_tr_b16 a[228:229], v216 offset:46336\n"
"v_mfma_f32_16x16x32_bf16 v[104:107], a[200:203], a[168:171], v[104:107]\n"
"buffer_load_dwordx4 v235, s[92:95], s58 offen lds\n"
"v_mfma_f32_16x16x32_bf16 v[104:107], a[196:199], a[164:167], v[104:107]\n"
"ds_read_b64_tr_b16 a[230:231], v217 offset:46336\n"
"v_mfma_f32_16x16x32_bf16 v[104:107], a[192:195], a[160:163], v[104:107]\n"
"v_mfma_f32_16x16x32_bf16 v[100:103], a[192:195], a[144:147], v[100:103]\n"
"s_mov_b32 s72, 6\n"
"s_mul_i32 s72, s72, 65536\n"
"s_add_u32 s77, s76, s72\n"
"s_add_u32 s66, s77, 0\n"
"ds_read_b64_tr_b16 a[232:233], v216 offset:55552\n"
"v_mfma_f32_16x16x32_bf16 v[100:103], a[196:199], a[148:151], v[100:103]\n"
"buffer_load_dwordx4 a[64:67], v232, s[52:55], s66 offen sc1\n"
"v_mfma_f32_16x16x32_bf16 v[100:103], a[200:203], a[152:155], v[100:103]\n"
"ds_read_b64_tr_b16 a[234:235], v217 offset:55552\n"
"v_mfma_f32_16x16x32_bf16 v[100:103], a[204:207], a[156:159], v[100:103]\n"
"v_mfma_f32_16x16x32_bf16 v[96:99], a[204:207], a[140:143], v[96:99]\n"
"ds_read_b64_tr_b16 a[236:237], v216 offset:64768\n"
"v_mfma_f32_16x16x32_bf16 v[96:99], a[200:203], a[136:139], v[96:99]\n"
"v_mfma_f32_16x16x32_bf16 v[96:99], a[196:199], a[132:135], v[96:99]\n"
"ds_read_b64_tr_b16 a[238:239], v217 offset:64768\n"
"v_mfma_f32_16x16x32_bf16 v[96:99], a[192:195], a[128:131], v[96:99]\n"
"s_waitcnt lgkmcnt(8)\n"
"buffer_load_dwordx4 a[68:71], v232, s[52:55], s66 offen offset:1024 sc1\n"
"v_mfma_f32_16x16x32_bf16 v[112:115], a[208:211], a[128:131], v[112:115]\n"
"v_mfma_f32_16x16x32_bf16 v[112:115], a[212:215], a[132:135], v[112:115]\n"
"v_mfma_f32_16x16x32_bf16 v[112:115], a[216:219], a[136:139], v[112:115]\n"
"v_mfma_f32_16x16x32_bf16 v[112:115], a[220:223], a[140:143], v[112:115]\n"
"s_waitcnt vmcnt(43) lgkmcnt(0)\n"
"v_mfma_f32_16x16x32_bf16 v[116:119], a[220:223], a[156:159], v[116:119]\n"
"s_barrier\n"
"v_mfma_f32_16x16x32_bf16 v[116:119], a[216:219], a[152:155], v[116:119]\n"
"buffer_load_dwordx4 a[72:75], v232, s[52:55], s66 offen offset:2048 sc1\n"
"v_mfma_f32_16x16x32_bf16 v[116:119], a[212:215], a[148:151], v[116:119]\n"
"ds_read_b64_tr_b16 a[192:193], v218\n"
"ds_read_b64_tr_b16 a[194:195], v219\n"
"v_mfma_f32_16x16x32_bf16 v[116:119], a[208:211], a[144:147], v[116:119]\n"
"v_mfma_f32_16x16x32_bf16 v[120:123], a[208:211], a[160:163], v[120:123]\n"
"ds_read_b64_tr_b16 a[196:197], v218 offset:9216\n"
"ds_read_b64_tr_b16 a[198:199], v219 offset:9216\n"
"v_mfma_f32_16x16x32_bf16 v[120:123], a[212:215], a[164:167], v[120:123]\n"
"v_mfma_f32_16x16x32_bf16 v[120:123], a[216:219], a[168:171], v[120:123]\n"
"ds_read_b64_tr_b16 a[200:201], v218 offset:18432\n"
"ds_read_b64_tr_b16 a[202:203], v219 offset:18432\n"
"v_mfma_f32_16x16x32_bf16 v[120:123], a[220:223], a[172:175], v[120:123]\n"
"buffer_load_dwordx4 a[76:79], v232, s[52:55], s66 offen offset:3072 sc1\n"
"v_mfma_f32_16x16x32_bf16 v[124:127], a[220:223], a[188:191], v[124:127]\n"
"ds_read_b64_tr_b16 a[204:205], v218 offset:27648\n"
"v_mfma_f32_16x16x32_bf16 v[124:127], a[216:219], a[184:187], v[124:127]\n"
"v_mfma_f32_16x16x32_bf16 v[124:127], a[212:215], a[180:183], v[124:127]\n"
"s_add_u32 s66, s77, 4096\n"
"ds_read_b64_tr_b16 a[206:207], v219 offset:27648\n"
"v_mfma_f32_16x16x32_bf16 v[124:127], a[208:211], a[176:179], v[124:127]\n"
"buffer_load_dwordx4 a[80:83], v232, s[52:55], s66 offen sc1\n"
"v_mfma_f32_16x16x32_bf16 v[140:143], a[224:227], a[176:179], v[140:143]\n"
"ds_read_b64_tr_b16 a[208:209], v218 offset:32\n"
"v_mfma_f32_16x16x32_bf16 v[140:143], a[228:231], a[180:183], v[140:143]\n"
"v_mfma_f32_16x16x32_bf16 v[140:143], a[232:235], a[184:187], v[140:143]\n"
"ds_read_b64_tr_b16 a[210:211], v219 offset:32\n"
"v_mfma_f32_16x16x32_bf16 v[140:143], a[236:239], a[188:191], v[140:143]\n"
"v_mfma_f32_16x16x32_bf16 v[136:139], a[236:239], a[172:175], v[136:139]\n"
"ds_read_b64_tr_b16 a[212:213], v218 offset:9248\n"
"v_mfma_f32_16x16x32_bf16 v[136:139], a[232:235], a[168:171], v[136:139]\n"
"buffer_load_dwordx4 a[84:87], v232, s[52:55], s66 offen offset:1024 sc1\n"
"v_mfma_f32_16x16x32_bf16 v[136:139], a[228:231], a[164:167], v[136:139]\n"
"ds_read_b64_tr_b16 a[214:215], v219 offset:9248\n"
"v_mfma_f32_16x16x32_bf16 v[136:139], a[224:227], a[160:163], v[136:139]\n"
"v_mfma_f32_16x16x32_bf16 v[132:135], a[224:227], a[144:147], v[132:135]\n"
"ds_read_b64_tr_b16 a[216:217], v218 offset:18464\n"
"v_mfma_f32_16x16x32_bf16 v[132:135], a[228:231], a[148:151], v[132:135]\n"
"v_mfma_f32_16x16x32_bf16 v[132:135], a[232:235], a[152:155], v[132:135]\n"
"ds_read_b64_tr_b16 a[218:219], v219 offset:18464\n"
"v_mfma_f32_16x16x32_bf16 v[132:135], a[236:239], a[156:159], v[132:135]\n"
"buffer_load_dwordx4 a[88:91], v232, s[52:55], s66 offen offset:2048 sc1\n"
"v_mfma_f32_16x16x32_bf16 v[128:131], a[236:239], a[140:143], v[128:131]\n"
"ds_read_b64_tr_b16 a[220:221], v218 offset:27680\n"
"v_mfma_f32_16x16x32_bf16 v[128:131], a[232:235], a[136:139], v[128:131]\n"
"v_mfma_f32_16x16x32_bf16 v[128:131], a[228:231], a[132:135], v[128:131]\n"
"ds_read_b64_tr_b16 a[222:223], v219 offset:27680\n"
"v_mfma_f32_16x16x32_bf16 v[128:131], a[224:227], a[128:131], v[128:131]\n"
"s_waitcnt vmcnt(27) lgkmcnt(8)\n"
"v_mfma_f32_16x16x32_bf16 v[0:3], a[192:195], a[0:3], v[0:3]\n"
"ds_read_b64_tr_b16 a[224:225], v218 offset:64\n"
"v_mfma_f32_16x16x32_bf16 v[0:3], a[196:199], a[4:7], v[0:3]\n"
"buffer_load_dwordx4 a[92:95], v232, s[52:55], s66 offen offset:3072 sc1\n"
"v_mfma_f32_16x16x32_bf16 v[0:3], a[200:203], a[8:11], v[0:3]\n"
"ds_read_b64_tr_b16 a[226:227], v219 offset:64\n"
"v_mfma_f32_16x16x32_bf16 v[0:3], a[204:207], a[12:15], v[0:3]\n"
"v_mfma_f32_16x16x32_bf16 v[4:7], a[204:207], a[28:31], v[4:7]\n"
"s_add_u32 s66, s77, 8192\n"
"ds_read_b64_tr_b16 a[228:229], v218 offset:9280\n"
"v_mfma_f32_16x16x32_bf16 v[4:7], a[200:203], a[24:27], v[4:7]\n"
"buffer_load_dwordx4 a[96:99], v232, s[52:55], s66 offen sc1\n"
"v_mfma_f32_16x16x32_bf16 v[4:7], a[196:199], a[20:23], v[4:7]\n"
"ds_read_b64_tr_b16 a[230:231], v219 offset:9280\n"
"v_mfma_f32_16x16x32_bf16 v[4:7], a[192:195], a[16:19], v[4:7]\n"
"v_mfma_f32_16x16x32_bf16 v[8:11], a[192:195], a[32:35], v[8:11]\n"
"ds_read_b64_tr_b16 a[232:233], v218 offset:18496\n"
"v_mfma_f32_16x16x32_bf16 v[8:11], a[196:199], a[36:39], v[8:11]\n"
"v_mfma_f32_16x16x32_bf16 v[8:11], a[200:203], a[40:43], v[8:11]\n"
"ds_read_b64_tr_b16 a[234:235], v219 offset:18496\n"
"v_mfma_f32_16x16x32_bf16 v[8:11], a[204:207], a[44:47], v[8:11]\n"
"buffer_load_dwordx4 a[100:103], v232, s[52:55], s66 offen offset:1024 sc1\n"
"v_mfma_f32_16x16x32_bf16 v[12:15], a[204:207], a[60:63], v[12:15]\n"
"s_waitcnt vmcnt(13)\n"
"ds_read_b64_tr_b16 a[236:237], v218 offset:27712\n"
"v_mfma_f32_16x16x32_bf16 v[12:15], a[200:203], a[56:59], v[12:15]\n"
"buffer_store_dword v247, v222, s[96:99], 0 offen sc1\n"
"v_mfma_f32_16x16x32_bf16 v[12:15], a[196:199], a[52:55], v[12:15]\n"
"ds_read_b64_tr_b16 a[238:239], v219 offset:27712\n"
"v_mfma_f32_16x16x32_bf16 v[12:15], a[192:195], a[48:51], v[12:15]\n"
"s_waitcnt lgkmcnt(8)\n"
"v_mfma_f32_16x16x32_bf16 v[28:31], a[208:211], a[48:51], v[28:31]\n"
"ds_read_b64_tr_b16 a[192:193], v218 offset:96\n"
"v_mfma_f32_16x16x32_bf16 v[28:31], a[212:215], a[52:55], v[28:31]\n"
"buffer_load_dwordx4 a[104:107], v232, s[52:55], s66 offen offset:2048 sc1\n"
"v_mfma_f32_16x16x32_bf16 v[28:31], a[216:219], a[56:59], v[28:31]\n"
"ds_read_b64_tr_b16 a[194:195], v219 offset:96\n"
"v_mfma_f32_16x16x32_bf16 v[28:31], a[220:223], a[60:63], v[28:31]\n"
"v_mfma_f32_16x16x32_bf16 v[24:27], a[220:223], a[44:47], v[24:27]\n"
"ds_read_b64_tr_b16 a[196:197], v218 offset:9312\n"
"v_mfma_f32_16x16x32_bf16 v[24:27], a[216:219], a[40:43], v[24:27]\n"
"v_mfma_f32_16x16x32_bf16 v[24:27], a[212:215], a[36:39], v[24:27]\n"
"ds_read_b64_tr_b16 a[198:199], v219 offset:9312\n"
"v_mfma_f32_16x16x32_bf16 v[24:27], a[208:211], a[32:35], v[24:27]\n"
"buffer_load_dwordx4 a[108:111], v232, s[52:55], s66 offen offset:3072 sc1\n"
"v_mfma_f32_16x16x32_bf16 v[20:23], a[208:211], a[16:19], v[20:23]\n"
"ds_read_b64_tr_b16 a[200:201], v218 offset:18528\n"
"v_mfma_f32_16x16x32_bf16 v[20:23], a[212:215], a[20:23], v[20:23]\n"
"v_mfma_f32_16x16x32_bf16 v[20:23], a[216:219], a[24:27], v[20:23]\n"
"s_add_u32 s66, s77, 12288\n"
"ds_read_b64_tr_b16 a[202:203], v219 offset:18528\n"
"v_mfma_f32_16x16x32_bf16 v[20:23], a[220:223], a[28:31], v[20:23]\n"
"buffer_load_dwordx4 a[112:115], v232, s[52:55], s66 offen sc1\n"
"v_mfma_f32_16x16x32_bf16 v[16:19], a[220:223], a[12:15], v[16:19]\n"
"ds_read_b64_tr_b16 a[204:205], v218 offset:27744\n"
"v_mfma_f32_16x16x32_bf16 v[16:19], a[216:219], a[8:11], v[16:19]\n"
"v_mfma_f32_16x16x32_bf16 v[16:19], a[212:215], a[4:7], v[16:19]\n"
"ds_read_b64_tr_b16 a[206:207], v219 offset:27744\n"
"v_mfma_f32_16x16x32_bf16 v[16:19], a[208:211], a[0:3], v[16:19]\n"
"s_waitcnt lgkmcnt(8)\n"
"v_mfma_f32_16x16x32_bf16 v[32:35], a[224:227], a[0:3], v[32:35]\n"
"ds_read_b64_tr_b16 a[208:209], v218 offset:128\n"
"v_mfma_f32_16x16x32_bf16 v[32:35], a[228:231], a[4:7], v[32:35]\n"
"buffer_load_dwordx4 a[116:119], v232, s[52:55], s66 offen offset:1024 sc1\n"
"v_mfma_f32_16x16x32_bf16 v[32:35], a[232:235], a[8:11], v[32:35]\n"
"ds_read_b64_tr_b16 a[210:211], v219 offset:128\n"
"v_mfma_f32_16x16x32_bf16 v[32:35], a[236:239], a[12:15], v[32:35]\n"
"v_mfma_f32_16x16x32_bf16 v[36:39], a[236:239], a[28:31], v[36:39]\n"
"ds_read_b64_tr_b16 a[212:213], v218 offset:9344\n"
"v_mfma_f32_16x16x32_bf16 v[36:39], a[232:235], a[24:27], v[36:39]\n"
"v_mfma_f32_16x16x32_bf16 v[36:39], a[228:231], a[20:23], v[36:39]\n"
"ds_read_b64_tr_b16 a[214:215], v219 offset:9344\n"
"v_mfma_f32_16x16x32_bf16 v[36:39], a[224:227], a[16:19], v[36:39]\n"
"buffer_load_dwordx4 a[120:123], v232, s[52:55], s66 offen offset:2048 sc1\n"
"v_mfma_f32_16x16x32_bf16 v[40:43], a[224:227], a[32:35], v[40:43]\n"
"ds_read_b64_tr_b16 a[216:217], v218 offset:18560\n"
"v_mfma_f32_16x16x32_bf16 v[40:43], a[228:231], a[36:39], v[40:43]\n"
"v_mfma_f32_16x16x32_bf16 v[40:43], a[232:235], a[40:43], v[40:43]\n"
"ds_read_b64_tr_b16 a[218:219], v219 offset:18560\n"
"v_mfma_f32_16x16x32_bf16 v[40:43], a[236:239], a[44:47], v[40:43]\n"
"v_mfma_f32_16x16x32_bf16 v[44:47], a[236:239], a[60:63], v[44:47]\n"
"ds_read_b64_tr_b16 a[220:221], v218 offset:27776\n"
"v_mfma_f32_16x16x32_bf16 v[44:47], a[232:235], a[56:59], v[44:47]\n"
"buffer_load_dwordx4 a[124:127], v232, s[52:55], s66 offen offset:3072 sc1\n"
"v_mfma_f32_16x16x32_bf16 v[44:47], a[228:231], a[52:55], v[44:47]\n"
"ds_read_b64_tr_b16 a[222:223], v219 offset:27776\n"
"v_mfma_f32_16x16x32_bf16 v[44:47], a[224:227], a[48:51], v[44:47]\n"
"s_waitcnt lgkmcnt(8)\n"
"v_mfma_f32_16x16x32_bf16 v[60:63], a[192:195], a[48:51], v[60:63]\n"
"s_cmp_le_u32 s59, 0\n"
"s_cselect_b32 s72, 3, 1\n"
"s_mul_i32 s58, s72, 147456\n"
"s_add_u32 m0, s60, 0\n"
"ds_read_b64_tr_b16 a[224:225], v218 offset:160\n"
"v_mfma_f32_16x16x32_bf16 v[60:63], a[196:199], a[52:55], v[60:63]\n"
"buffer_load_dwordx4 v221, s[92:95], s58 offen lds\n"
"v_mfma_f32_16x16x32_bf16 v[60:63], a[200:203], a[56:59], v[60:63]\n"
"ds_read_b64_tr_b16 a[226:227], v219 offset:160\n"
"v_mfma_f32_16x16x32_bf16 v[60:63], a[204:207], a[60:63], v[60:63]\n"
"v_mfma_f32_16x16x32_bf16 v[56:59], a[204:207], a[44:47], v[56:59]\n"
"ds_read_b64_tr_b16 a[228:229], v218 offset:9376\n"
"v_mfma_f32_16x16x32_bf16 v[56:59], a[200:203], a[40:43], v[56:59]\n"
"v_mfma_f32_16x16x32_bf16 v[56:59], a[196:199], a[36:39], v[56:59]\n"
"ds_read_b64_tr_b16 a[230:231], v219 offset:9376\n"
"v_mfma_f32_16x16x32_bf16 v[56:59], a[192:195], a[32:35], v[56:59]\n"
"buffer_load_dwordx4 v225, s[92:95], s58 offen offset:1024 lds\n"
"v_mfma_f32_16x16x32_bf16 v[52:55], a[192:195], a[16:19], v[52:55]\n"
"ds_read_b64_tr_b16 a[232:233], v218 offset:18592\n"
"v_mfma_f32_16x16x32_bf16 v[52:55], a[196:199], a[20:23], v[52:55]\n"
"v_mfma_f32_16x16x32_bf16 v[52:55], a[200:203], a[24:27], v[52:55]\n"
"ds_read_b64_tr_b16 a[234:235], v219 offset:18592\n"
"v_mfma_f32_16x16x32_bf16 v[52:55], a[204:207], a[28:31], v[52:55]\n"
"v_mfma_f32_16x16x32_bf16 v[48:51], a[204:207], a[12:15], v[48:51]\n"
"ds_read_b64_tr_b16 a[236:237], v218 offset:27808\n"
"v_mfma_f32_16x16x32_bf16 v[48:51], a[200:203], a[8:11], v[48:51]\n"
"buffer_load_dwordx4 v226, s[92:95], s58 offen offset:2048 lds\n"
"v_mfma_f32_16x16x32_bf16 v[48:51], a[196:199], a[4:7], v[48:51]\n"
"ds_read_b64_tr_b16 a[238:239], v219 offset:27808\n"
"v_mfma_f32_16x16x32_bf16 v[48:51], a[192:195], a[0:3], v[48:51]\n"
"s_waitcnt lgkmcnt(8)\n"
"v_mfma_f32_16x16x32_bf16 v[64:67], a[208:211], a[0:3], v[64:67]\n"
"ds_read_b64_tr_b16 a[192:193], v218 offset:192\n"
"v_mfma_f32_16x16x32_bf16 v[64:67], a[212:215], a[4:7], v[64:67]\n"
"v_mfma_f32_16x16x32_bf16 v[64:67], a[216:219], a[8:11], v[64:67]\n"
"ds_read_b64_tr_b16 a[194:195], v219 offset:192\n"
"v_mfma_f32_16x16x32_bf16 v[64:67], a[220:223], a[12:15], v[64:67]\n"
"buffer_load_dwordx4 v227, s[92:95], s58 offen offset:3072 lds\n"
"v_mfma_f32_16x16x32_bf16 v[68:71], a[220:223], a[28:31], v[68:71]\n"
"ds_read_b64_tr_b16 a[196:197], v218 offset:9408\n"
"v_mfma_f32_16x16x32_bf16 v[68:71], a[216:219], a[24:27], v[68:71]\n"
"v_mfma_f32_16x16x32_bf16 v[68:71], a[212:215], a[20:23], v[68:71]\n"
"s_add_u32 m0, s60, 4096\n"
"ds_read_b64_tr_b16 a[198:199], v219 offset:9408\n"
"v_mfma_f32_16x16x32_bf16 v[68:71], a[208:211], a[16:19], v[68:71]\n"
"buffer_load_dwordx4 v228, s[92:95], s58 offen lds\n"
"v_mfma_f32_16x16x32_bf16 v[72:75], a[208:211], a[32:35], v[72:75]\n"
"ds_read_b64_tr_b16 a[200:201], v218 offset:18624\n"
"v_mfma_f32_16x16x32_bf16 v[72:75], a[212:215], a[36:39], v[72:75]\n"
"v_mfma_f32_16x16x32_bf16 v[72:75], a[216:219], a[40:43], v[72:75]\n"
"ds_read_b64_tr_b16 a[202:203], v219 offset:18624\n"
"v_mfma_f32_16x16x32_bf16 v[72:75], a[220:223], a[44:47], v[72:75]\n"
"v_mfma_f32_16x16x32_bf16 v[76:79], a[220:223], a[60:63], v[76:79]\n"
"ds_read_b64_tr_b16 a[204:205], v218 offset:27840\n"
"v_mfma_f32_16x16x32_bf16 v[76:79], a[216:219], a[56:59], v[76:79]\n"
"buffer_load_dwordx4 v229, s[92:95], s58 offen offset:1024 lds\n"
"v_mfma_f32_16x16x32_bf16 v[76:79], a[212:215], a[52:55], v[76:79]\n"
"ds_read_b64_tr_b16 a[206:207], v219 offset:27840\n"
"v_mfma_f32_16x16x32_bf16 v[76:79], a[208:211], a[48:51], v[76:79]\n"
"s_waitcnt lgkmcnt(8)\n"
"v_mfma_f32_16x16x32_bf16 v[92:95], a[224:227], a[48:51], v[92:95]\n"
"ds_read_b64_tr_b16 a[208:209], v218 offset:224\n"
"v_mfma_f32_16x16x32_bf16 v[92:95], a[228:231], a[52:55], v[92:95]\n"
"v_mfma_f32_16x16x32_bf16 v[92:95], a[232:235], a[56:59], v[92:95]\n"
"ds_read_b64_tr_b16 a[210:211], v219 offset:224\n"
"v_mfma_f32_16x16x32_bf16 v[92:95], a[236:239], a[60:63], v[92:95]\n"
"buffer_load_dwordx4 v230, s[92:95], s58 offen offset:2048 lds\n"
"v_mfma_f32_16x16x32_bf16 v[88:91], a[236:239], a[44:47], v[88:91]\n"
"ds_read_b64_tr_b16 a[212:213], v218 offset:9440\n"
"v_mfma_f32_16x16x32_bf16 v[88:91], a[232:235], a[40:43], v[88:91]\n"
"v_mfma_f32_16x16x32_bf16 v[88:91], a[228:231], a[36:39], v[88:91]\n"
"ds_read_b64_tr_b16 a[214:215], v219 offset:9440\n"
"v_mfma_f32_16x16x32_bf16 v[88:91], a[224:227], a[32:35], v[88:91]\n"
"v_mfma_f32_16x16x32_bf16 v[84:87], a[224:227], a[16:19], v[84:87]\n"
"ds_read_b64_tr_b16 a[216:217], v218 offset:18656\n"
"v_mfma_f32_16x16x32_bf16 v[84:87], a[228:231], a[20:23], v[84:87]\n"
"buffer_load_dwordx4 v231, s[92:95], s58 offen offset:3072 lds\n"
"v_mfma_f32_16x16x32_bf16 v[84:87], a[232:235], a[24:27], v[84:87]\n"
"ds_read_b64_tr_b16 a[218:219], v219 offset:18656\n"
"v_mfma_f32_16x16x32_bf16 v[84:87], a[236:239], a[28:31], v[84:87]\n"
"buffer_load_dword v251, v250, s[96:99], 0 offen sc1\n"
"v_mfma_f32_16x16x32_bf16 v[80:83], a[236:239], a[12:15], v[80:83]\n"
"s_add_u32 m0, s60, 8192\n"
"ds_read_b64_tr_b16 a[220:221], v218 offset:27872\n"
"v_mfma_f32_16x16x32_bf16 v[80:83], a[232:235], a[8:11], v[80:83]\n"
"buffer_load_dwordx4 v235, s[92:95], s58 offen lds\n"
"v_mfma_f32_16x16x32_bf16 v[80:83], a[228:231], a[4:7], v[80:83]\n"
"ds_read_b64_tr_b16 a[222:223], v219 offset:27872\n"
"v_mfma_f32_16x16x32_bf16 v[80:83], a[224:227], a[0:3], v[80:83]\n"
"s_waitcnt lgkmcnt(8)\n"
"v_mfma_f32_16x16x32_bf16 v[96:99], a[192:195], a[0:3], v[96:99]\n"
"ds_read_b64_tr_b16 a[224:225], v218 offset:256\n"
"v_mfma_f32_16x16x32_bf16 v[96:99], a[196:199], a[4:7], v[96:99]\n"
"v_mfma_f32_16x16x32_bf16 v[96:99], a[200:203], a[8:11], v[96:99]\n"
"s_cmp_le_u32 s59, 1\n"
"s_cselect_b32 s72, 5, 3\n"
"s_mul_i32 s58, s72, 147456\n"
"s_add_u32 m0, s60, 36864\n"
"ds_read_b64_tr_b16 a[226:227], v219 offset:256\n"
"v_mfma_f32_16x16x32_bf16 v[96:99], a[204:207], a[12:15], v[96:99]\n"
"buffer_load_dwordx4 v221, s[92:95], s58 offen lds\n"
"v_mfma_f32_16x16x32_bf16 v[100:103], a[204:207], a[28:31], v[100:103]\n"
"ds_read_b64_tr_b16 a[228:229], v218 offset:9472\n"
"v_mfma_f32_16x16x32_bf16 v[100:103], a[200:203], a[24:27], v[100:103]\n"
"v_mfma_f32_16x16x32_bf16 v[100:103], a[196:199], a[20:23], v[100:103]\n"
"ds_read_b64_tr_b16 a[230:231], v219 offset:9472\n"
"v_mfma_f32_16x16x32_bf16 v[100:103], a[192:195], a[16:19], v[100:103]\n"
"v_mfma_f32_16x16x32_bf16 v[104:107], a[192:195], a[32:35], v[104:107]\n"
"ds_read_b64_tr_b16 a[232:233], v218 offset:18688\n"
"v_mfma_f32_16x16x32_bf16 v[104:107], a[196:199], a[36:39], v[104:107]\n"
"buffer_load_dwordx4 v225, s[92:95], s58 offen offset:1024 lds\n"
"v_mfma_f32_16x16x32_bf16 v[104:107], a[200:203], a[40:43], v[104:107]\n"
"ds_read_b64_tr_b16 a[234:235], v219 offset:18688\n"
"v_mfma_f32_16x16x32_bf16 v[104:107], a[204:207], a[44:47], v[104:107]\n"
"v_mfma_f32_16x16x32_bf16 v[108:111], a[204:207], a[60:63], v[108:111]\n"
"ds_read_b64_tr_b16 a[236:237], v218 offset:27904\n"
"v_mfma_f32_16x16x32_bf16 v[108:111], a[200:203], a[56:59], v[108:111]\n"
"v_mfma_f32_16x16x32_bf16 v[108:111], a[196:199], a[52:55], v[108:111]\n"
"ds_read_b64_tr_b16 a[238:239], v219 offset:27904\n"
"v_mfma_f32_16x16x32_bf16 v[108:111], a[192:195], a[48:51], v[108:111]\n"
"s_waitcnt lgkmcnt(8)\n"
"buffer_load_dwordx4 v226, s[92:95], s58 offen offset:2048 lds\n"
"v_mfma_f32_16x16x32_bf16 v[124:127], a[208:211], a[48:51], v[124:127]\n"
"v_mfma_f32_16x16x32_bf16 v[124:127], a[212:215], a[52:55], v[124:127]\n"
"v_mfma_f32_16x16x32_bf16 v[124:127], a[216:219], a[56:59], v[124:127]\n"
"v_mfma_f32_16x16x32_bf16 v[124:127], a[220:223], a[60:63], v[124:127]\n"
"s_waitcnt vmcnt(30) lgkmcnt(0)\n"
"buffer_load_dwordx4 v227, s[92:95], s58 offen offset:3072 lds\n"
"v_mfma_f32_16x16x32_bf16 v[120:123], a[220:223], a[44:47], v[120:123]\n"
"s_barrier\n"
"v_mfma_f32_16x16x32_bf16 v[120:123], a[216:219], a[40:43], v[120:123]\n"
"v_mfma_f32_16x16x32_bf16 v[120:123], a[212:215], a[36:39], v[120:123]\n"
"ds_read_b64_tr_b16 a[192:193], v218 offset:36864\n"
"ds_read_b64_tr_b16 a[194:195], v219 offset:36864\n"
"v_mfma_f32_16x16x32_bf16 v[120:123], a[208:211], a[32:35], v[120:123]\n"
"v_mfma_f32_16x16x32_bf16 v[116:119], a[208:211], a[16:19], v[116:119]\n"
"s_add_u32 m0, s60, 40960\n"
"ds_read_b64_tr_b16 a[196:197], v218 offset:46080\n"
"ds_read_b64_tr_b16 a[198:199], v219 offset:46080\n"
"v_mfma_f32_16x16x32_bf16 v[116:119], a[212:215], a[20:23], v[116:119]\n"
"buffer_load_dwordx4 v228, s[92:95], s58 offen lds\n"
"v_mfma_f32_16x16x32_bf16 v[116:119], a[216:219], a[24:27], v[116:119]\n"
"ds_read_b64_tr_b16 a[200:201], v218 offset:55296\n"
"ds_read_b64_tr_b16 a[202:203], v219 offset:55296\n"
"v_mfma_f32_16x16x32_bf16 v[116:119], a[220:223], a[28:31], v[116:119]\n"
"v_mfma_f32_16x16x32_bf16 v[112:115], a[220:223], a[12:15], v[112:115]\n"
"ds_read_b64_tr_b16 a[204:205], v218 offset:64512\n"
"v_mfma_f32_16x16x32_bf16 v[112:115], a[216:219], a[8:11], v[112:115]\n"
"v_mfma_f32_16x16x32_bf16 v[112:115], a[212:215], a[4:7], v[112:115]\n"
"ds_read_b64_tr_b16 a[206:207], v219 offset:64512\n"
"v_mfma_f32_16x16x32_bf16 v[112:115], a[208:211], a[0:3], v[112:115]\n"
"buffer_load_dwordx4 v229, s[92:95], s58 offen offset:1024 lds\n"
"v_mfma_f32_16x16x32_bf16 v[128:131], a[224:227], a[0:3], v[128:131]\n"
"ds_read_b64_tr_b16 a[208:209], v218 offset:36896\n"
"v_mfma_f32_16x16x32_bf16 v[128:131], a[228:231], a[4:7], v[128:131]\n"
"v_mfma_f32_16x16x32_bf16 v[128:131], a[232:235], a[8:11], v[128:131]\n"
"ds_read_b64_tr_b16 a[210:211], v219 offset:36896\n"
"v_mfma_f32_16x16x32_bf16 v[128:131], a[236:239], a[12:15], v[128:131]\n"
"v_mfma_f32_16x16x32_bf16 v[132:135], a[236:239], a[28:31], v[132:135]\n"
"ds_read_b64_tr_b16 a[212:213], v218 offset:46112\n"
"v_mfma_f32_16x16x32_bf16 v[132:135], a[232:235], a[24:27], v[132:135]\n"
"buffer_load_dwordx4 v230, s[92:95], s58 offen offset:2048 lds\n"
"v_mfma_f32_16x16x32_bf16 v[132:135], a[228:231], a[20:23], v[132:135]\n"
"ds_read_b64_tr_b16 a[214:215], v219 offset:46112\n"
"v_mfma_f32_16x16x32_bf16 v[132:135], a[224:227], a[16:19], v[132:135]\n"
"v_mfma_f32_16x16x32_bf16 v[136:139], a[224:227], a[32:35], v[136:139]\n"
"ds_read_b64_tr_b16 a[216:217], v218 offset:55328\n"
"v_mfma_f32_16x16x32_bf16 v[136:139], a[228:231], a[36:39], v[136:139]\n"
"buffer_load_dwordx4 v231, s[92:95], s58 offen offset:3072 lds\n"
"v_mfma_f32_16x16x32_bf16 v[136:139], a[232:235], a[40:43], v[136:139]\n"
"ds_read_b64_tr_b16 a[218:219], v219 offset:55328\n"
"v_mfma_f32_16x16x32_bf16 v[136:139], a[236:239], a[44:47], v[136:139]\n"
"v_mfma_f32_16x16x32_bf16 v[140:143], a[236:239], a[60:63], v[140:143]\n"
"ds_read_b64_tr_b16 a[220:221], v218 offset:64544\n"
"v_mfma_f32_16x16x32_bf16 v[140:143], a[232:235], a[56:59], v[140:143]\n"
"v_mfma_f32_16x16x32_bf16 v[140:143], a[228:231], a[52:55], v[140:143]\n"
"s_add_u32 m0, s60, 45056\n"
"ds_read_b64_tr_b16 a[222:223], v219 offset:64544\n"
"v_mfma_f32_16x16x32_bf16 v[140:143], a[224:227], a[48:51], v[140:143]\n"
"s_waitcnt vmcnt(18) lgkmcnt(8)\n"
"buffer_load_dwordx4 v235, s[92:95], s58 offen lds\n"
"v_mfma_f32_16x16x32_bf16 v[12:15], a[192:195], a[112:115], v[12:15]\n"
"ds_read_b64_tr_b16 a[224:225], v218 offset:36928\n"
"v_mfma_f32_16x16x32_bf16 v[12:15], a[196:199], a[116:119], v[12:15]\n"
"v_mfma_f32_16x16x32_bf16 v[12:15], a[200:203], a[120:123], v[12:15]\n"
"ds_read_b64_tr_b16 a[226:227], v219 offset:36928\n"
"L_chk2o%=:\n"
"s_waitcnt vmcnt(10)\n"
"v_cmp_eq_u32 vcc, s78, v251\n"
"s_nop 3\n"
"s_lshr_b32 s72, vcc_lo, 12\n"
"s_and_b32 s72, s72, 15\n"
"s_cmp_eq_u32 s72, 15\n"
"s_cbranch_scc0 L_rt2o%=\n"
"L_ok2o%=:\n"
"v_mfma_f32_16x16x32_bf16 v[12:15], a[204:207], a[124:127], v[12:15]\n"
"v_mfma_f32_16x16x32_bf16 v[8:11], a[204:207], a[108:111], v[8:11]\n"
"s_cmp_le_u32 s59, 0\n"
"s_cselect_b32 s72, 3, 1\n"
"s_mul_i32 s72, s72, 65536\n"
"s_add_u32 s77, s76, s72\n"
"s_add_u32 s66, s77, 0\n"
"ds_read_b64_tr_b16 a[228:229], v218 offset:46144\n"
"v_mfma_f32_16x16x32_bf16 v[8:11], a[200:203], a[104:107], v[8:11]\n"
"buffer_load_dwordx4 a[128:131], v232, s[52:55], s66 offen sc1\n"
"v_mfma_f32_16x16x32_bf16 v[8:11], a[196:199], a[100:103], v[8:11]\n"
"ds_read_b64_tr_b16 a[230:231], v219 offset:46144\n"
"v_mfma_f32_16x16x32_bf16 v[8:11], a[192:195], a[96:99], v[8:11]\n"
"v_mfma_f32_16x16x32_bf16 v[4:7], a[192:195], a[80:83], v[4:7]\n"
"ds_read_b64_tr_b16 a[232:233], v218 offset:55360\n"
"v_mfma_f32_16x16x32_bf16 v[4:7], a[196:199], a[84:87], v[4:7]\n"
"v_mfma_f32_16x16x32_bf16 v[4:7], a[200:203], a[88:91], v[4:7]\n"
"ds_read_b64_tr_b16 a[234:235], v219 offset:55360\n"
"v_mfma_f32_16x16x32_bf16 v[4:7], a[204:207], a[92:95], v[4:7]\n"
"buffer_load_dwordx4 a[132:135], v232, s[52:55], s66 offen offset:1024 sc1\n"
"v_mfma_f32_16x16x32_bf16 v[0:3], a[204:207], a[76:79], v[0:3]\n"
"ds_read_b64_tr_b16 a[236:237], v218 offset:64576\n"
"v_mfma_f32_16x16x32_bf16 v[0:3], a[200:203], a[72:75], v[0:3]\n"
"v_mfma_f32_16x16x32_bf16 v[0:3], a[196:199], a[68:71], v[0:3]\n"
"ds_read_b64_tr_b16 a[238:239], v219 offset:64576\n"
"v_mfma_f32_16x16x32_bf16 v[0:3], a[192:195], a[64:67], v[0:3]\n"
"s_waitcnt lgkmcnt(8)\n"
"buffer_load_dwordx4 a[136:139], v232, s[52:55], s66 offen offset:2048 sc1\n"
"v_mfma_f32_16x16x32_bf16 v[16:19], a[208:211], a[64:67], v[16:19]\n"
"ds_read_b64_tr_b16 a[192:193], v218 offset:36960\n"
"v_mfma_f32_16x16x32_bf16 v[16:19], a[212:215], a[68:71], v[16:19]\n"
"v_mfma_f32_16x16x32_bf16 v[16:19], a[216:219], a[72:75], v[16:19]\n"
"ds_read_b64_tr_b16 a[194:195], v219 offset:36960\n"
"v_mfma_f32_16x16x32_bf16 v[16:19], a[220:223], a[76:79], v[16:19]\n"
"v_mfma_f32_16x16x32_bf16 v[20:23], a[220:223], a[92:95], v[20:23]\n"
"ds_read_b64_tr_b16 a[196:197], v218 offset:46176\n"
"v_mfma_f32_16x16x32_bf16 v[20:23], a[216:219], a[88:91], v[20:23]\n"
"buffer_load_dwordx4 a[140:143], v232, s[52:55], s66 offen offset:3072 sc1\n"
"v_mfma_f32_16x16x32_bf16 v[20:23], a[212:215], a[84:87], v[20:23]\n"
"ds_read_b64_tr_b16 a[198:199], v219 offset:46176\n"
"v_mfma_f32_16x16x32_bf16 v[20:23], a[208:211], a[80:83], v[20:23]\n"
"v_mfma_f32_16x16x32_bf16 v[24:27], a[208:211], a[96:99], v[24:27]\n"
"ds_read_b64_tr_b16 a[200:201], v218 offset:55392\n"
"v_mfma_f32_16x16x32_bf16 v[24:27], a[212:215], a[100:103], v[24:27]\n"
"v_mfma_f32_16x16x32_bf16 v[24:27], a[216:219], a[104:107], v[24:27]\n"
"s_add_u32 s66, s77, 4096\n"
"ds_read_b64_tr_b16 a[202:203], v219 offset:55392\n"
"v_mfma_f32_16x16x32_bf16 v[24:27], a[220:223], a[108:111], v[24:27]\n"
"buffer_load_dwordx4 a[144:147], v232, s[52:55], s66 offen sc1\n"
"v_mfma_f32_16x16x32_bf16 v[28:31], a[220:223], a[124:127], v[28:31]\n"
"ds_read_b64_tr_b16 a[204:205], v218 offset:64608\n"
"v_mfma_f32_16x16x32_bf16 v[28:31], a[216:219], a[120:123], v[28:31]\n"
"v_mfma_f32_16x16x32_bf16 v[28:31], a[212:215], a[116:119], v[28:31]\n"
"ds_read_b64_tr_b16 a[206:207], v219 offset:64608\n"
"v_mfma_f32_16x16x32_bf16 v[28:31], a[208:211], a[112:115], v[28:31]\n"
"s_waitcnt lgkmcnt(8)\n"
"v_mfma_f32_16x16x32_bf16 v[44:47], a[224:227], a[112:115], v[44:47]\n"
"ds_read_b64_tr_b16 a[208:209], v218 offset:36992\n"
"v_mfma_f32_16x16x32_bf16 v[44:47], a[228:231], a[116:119], v[44:47]\n"
"buffer_load_dwordx4 a[148:151], v232, s[52:55], s66 offen offset:1024 sc1\n"
"v_mfma_f32_16x16x32_bf16 v[44:47], a[232:235], a[120:123], v[44:47]\n"
"ds_read_b64_tr_b16 a[210:211], v219 offset:36992\n"
"v_mfma_f32_16x16x32_bf16 v[44:47], a[236:239], a[124:127], v[44:47]\n"
"v_mfma_f32_16x16x32_bf16 v[40:43], a[236:239], a[108:111], v[40:43]\n"
"ds_read_b64_tr_b16 a[212:213], v218 offset:46208\n"
"v_mfma_f32_16x16x32_bf16 v[40:43], a[232:235], a[104:107], v[40:43]\n"
"buffer_load_dwordx4 a[152:155], v232, s[52:55], s66 offen offset:2048 sc1\n"
"v_mfma_f32_16x16x32_bf16 v[40:43], a[228:231], a[100:103], v[40:43]\n"
"ds_read_b64_tr_b16 a[214:215], v219 offset:46208\n"
"v_mfma_f32_16x16x32_bf16 v[40:43], a[224:227], a[96:99], v[40:43]\n"
"v_mfma_f32_16x16x32_bf16 v[36:39], a[224:227], a[80:83], v[36:39]\n"
"ds_read_b64_tr_b16 a[216:217], v218 offset:55424\n"
"v_mfma_f32_16x16x32_bf16 v[36:39], a[228:231], a[84:87], v[36:39]\n"
"v_mfma_f32_16x16x32_bf16 v[36:39], a[232:235], a[88:91], v[36:39]\n"
"ds_read_b64_tr_b16 a[218:219], v219 offset:55424\n"
"v_mfma_f32_16x16x32_bf16 v[36:39], a[236:239], a[92:95], v[36:39]\n"
"buffer_load_dwordx4 a[156:159], v232, s[52:55], s66 offen offset:3072 sc1\n"
"v_mfma_f32_16x16x32_bf16 v[32:35], a[236:239], a[76:79], v[32:35]\n"
"ds_read_b64_tr_b16 a[220:221], v218 offset:64640\n"
"v_mfma_f32_16x16x32_bf16 v[32:35], a[232:235], a[72:75], v[32:35]\n"
"v_mfma_f32_16x16x32_bf16 v[32:35], a[228:231], a[68:71], v[32:35]\n"
"ds_read_b64_tr_b16 a[222:223], v219 offset:64640\n"
"v_mfma_f32_16x16x32_bf16 v[32:35], a[224:227], a[64:67], v[32:35]\n"
"s_waitcnt lgkmcnt(8)\n"
"v_mfma_f32_16x16x32_bf16 v[48:51], a[192:195], a[64:67], v[48:51]\n"
"s_add_u32 s66, s77, 8192\n"
"ds_read_b64_tr_b16 a[224:225], v218 offset:37024\n"
"v_mfma_f32_16x16x32_bf16 v[48:51], a[196:199], a[68:71], v[48:51]\n"
"buffer_load_dwordx4 a[160:163], v232, s[52:55], s66 offen sc1\n"
"v_mfma_f32_16x16x32_bf16 v[48:51], a[200:203], a[72:75], v[48:51]\n"
"ds_read_b64_tr_b16 a[226:227], v219 offset:37024\n"
"v_mfma_f32_16x16x32_bf16 v[48:51], a[204:207], a[76:79], v[48:51]\n"
"v_mfma_f32_16x16x32_bf16 v[52:55], a[204:207], a[92:95], v[52:55]\n"
"ds_read_b64_tr_b16 a[228:229], v218 offset:46240\n"
"v_mfma_f32_16x16x32_bf16 v[52:55], a[200:203], a[88:91], v[52:55]\n"
"v_mfma_f32_16x16x32_bf16 v[52:55], a[196:199], a[84:87], v[52:55]\n"
"ds_read_b64_tr_b16 a[230:231], v219 offset:46240\n"
"v_mfma_f32_16x16x32_bf16 v[52:55], a[192:195], a[80:83], v[52:55]\n"
"buffer_load_dwordx4 a[164:167], v232, s[52:55], s66 offen offset:1024 sc1\n"
"v_mfma_f32_16x16x32_bf16 v[56:59], a[192:195], a[96:99], v[56:59]\n"
"ds_read_b64_tr_b16 a[232:233], v218 offset:55456\n"
"v_mfma_f32_16x16x32_bf16 v[56:59], a[196:199], a[100:103], v[56:59]\n"
"v_mfma_f32_16x16x32_bf16 v[56:59], a[200:203], a[104:107], v[56:59]\n"
"ds_read_b64_tr_b16 a[234:235], v219 offset:55456\n"
"v_mfma_f32_16x16x32_bf16 v[56:59], a[204:207], a[108:111], v[56:59]\n"
"buffer_load_dwordx4 a[168:171], v232, s[52:55], s66 offen offset:2048 sc1\n"
"v_mfma_f32_16x16x32_bf16 v[60:63], a[204:207], a[124:127], v[60:63]\n"
"ds_read_b64_tr_b16 a[236:237], v218 offset:64672\n"
"v_mfma_f32_16x16x32_bf16 v[60:63], a[200:203], a[120:123], v[60:63]\n"
"v_mfma_f32_16x16x32_bf16 v[60:63], a[196:199], a[116:119], v[60:63]\n"
"ds_read_b64_tr_b16 a[238:239], v219 offset:64672\n"
"v_mfma_f32_16x16x32_bf16 v[60:63], a[192:195], a[112:115], v[60:63]\n"
"s_waitcnt lgkmcnt(8)\n"
"v_mfma_f32_16x16x32_bf16 v[76:79], a[208:211], a[112:115], v[76:79]\n"
"ds_read_b64_tr_b16 a[192:193], v218 offset:37056\n"
"v_mfma_f32_16x16x32_bf16 v[76:79], a[212:215], a[116:119], v[76:79]\n"
"buffer_load_dwordx4 a[172:175], v232, s[52:55], s66 offen offset:3072 sc1\n"
"v_mfma_f32_16x16x32_bf16 v[76:79], a[216:219], a[120:123], v[76:79]\n"
"ds_read_b64_tr_b16 a[194:195], v219 offset:37056\n"
"v_mfma_f32_16x16x32_bf16 v[76:79], a[220:223], a[124:127], v[76:79]\n"
"v_mfma_f32_16x16x32_bf16 v[72:75], a[220:223], a[108:111], v[72:75]\n"
"ds_read_b64_tr_b16 a[196:197], v218 offset:46272\n"
"v_mfma_f32_16x16x32_bf16 v[72:75], a[216:219], a[104:107], v[72:75]\n"
"v_mfma_f32_16x16x32_bf16 v[72:75], a[212:215], a[100:103], v[72:75]\n"
"s_add_u32 s66, s77, 12288\n"
"ds_read_b64_tr_b16 a[198:199], v219 offset:46272\n"
"v_mfma_f32_16x16x32_bf16 v[72:75], a[208:211], a[96:99], v[72:75]\n"
"buffer_load_dwordx4 a[176:179], v232, s[52:55], s66 offen sc1\n"
"v_mfma_f32_16x16x32_bf16 v[68:71], a[208:211], a[80:83], v[68:71]\n"
"ds_read_b64_tr_b16 a[200:201], v218 offset:55488\n"
"v_mfma_f32_16x16x32_bf16 v[68:71], a[212:215], a[84:87], v[68:71]\n"
"v_mfma_f32_16x16x32_bf16 v[68:71], a[216:219], a[88:91], v[68:71]\n"
"ds_read_b64_tr_b16 a[202:203], v219 offset:55488\n"
"v_mfma_f32_16x16x32_bf16 v[68:71], a[220:223], a[92:95], v[68:71]\n"
"v_mfma_f32_16x16x32_bf16 v[64:67], a[220:223], a[76:79], v[64:67]\n"
"ds_read_b64_tr_b16 a[204:205], v218 offset:64704\n"
"v_mfma_f32_16x16x32_bf16 v[64:67], a[216:219], a[72:75], v[64:67]\n"
"buffer_load_dwordx4 a[180:183], v232, s[52:55], s66 offen offset:1024 sc1\n"
"v_mfma_f32_16x16x32_bf16 v[64:67], a[212:215], a[68:71], v[64:67]\n"
"ds_read_b64_tr_b16 a[206:207], v219 offset:64704\n"
"v_mfma_f32_16x16x32_bf16 v[64:67], a[208:211], a[64:67], v[64:67]\n"
"s_waitcnt lgkmcnt(8)\n"
"v_mfma_f32_16x16x32_bf16 v[80:83], a[224:227], a[64:67], v[80:83]\n"
"ds_read_b64_tr_b16 a[208:209], v218 offset:37088\n"
"v_mfma_f32_16x16x32_bf16 v[80:83], a[228:231], a[68:71], v[80:83]\n"
"buffer_load_dwordx4 a[184:187], v232, s[52:55], s66 offen offset:2048 sc1\n"
"v_mfma_f32_16x16x32_bf16 v[80:83], a[232:235], a[72:75], v[80:83]\n"
"ds_read_b64_tr_b16 a[210:211], v219 offset:37088\n"
"v_mfma_f32_16x16x32_bf16 v[80:83], a[236:239], a[76:79], v[80:83]\n"
"v_mfma_f32_16x16x32_bf16 v[84:87], a[236:239], a[92:95], v[84:87]\n"
"ds_read_b64_tr_b16 a[212:213], v218 offset:46304\n"
"v_mfma_f32_16x16x32_bf16 v[84:87], a[232:235], a[88:91], v[84:87]\n"
"v_mfma_f32_16x16x32_bf16 v[84:87], a[228:231], a[84:87], v[84:87]\n"
"ds_read_b64_tr_b16 a[214:215], v219 offset:46304\n"
"v_mfma_f32_16x16x32_bf16 v[84:87], a[224:227], a[80:83], v[84:87]\n"
"buffer_load_dwordx4 a[188:191], v232, s[52:55], s66 offen offset:3072 sc1\n"
"v_mfma_f32_16x16x32_bf16 v[88:91], a[224:227], a[96:99], v[88:91]\n"
"ds_read_b64_tr_b16 a[216:217], v218 offset:55520\n"
"v_mfma_f32_16x16x32_bf16 v[88:91], a[228:231], a[100:103], v[88:91]\n"
"v_mfma_f32_16x16x32_bf16 v[88:91], a[232:235], a[104:107], v[88:91]\n"
"ds_read_b64_tr_b16 a[218:219], v219 offset:55520\n"
"v_mfma_f32_16x16x32_bf16 v[88:91], a[236:239], a[108:111], v[88:91]\n"
"v_mfma_f32_16x16x32_bf16 v[92:95], a[236:239], a[124:127], v[92:95]\n"
"s_cmp_le_u32 s59, 1\n"
"s_cselect_b32 s72, 5, 3\n"
"s_mul_i32 s72, s72, 65536\n"
"s_add_u32 s77, s76, s72\n"
"s_add_u32 s66, s77, 0\n"
"ds_read_b64_tr_b16 a[220:221], v218 offset:64736\n"
"v_mfma_f32_16x16x32_bf16 v[92:95], a[232:235], a[120:123], v[92:95]\n"
"buffer_load_dwordx4 a[0:3], v232, s[52:55], s66 offen sc1\n"
"v_mfma_f32_16x16x32_bf16 v[92:95], a[228:231], a[116:119], v[92:95]\n"
"ds_read_b64_tr_b16 a[222:223], v219 offset:64736\n"
"v_mfma_f32_16x16x32_bf16 v[92:95], a[224:227], a[112:115], v[92:95]\n"
"s_waitcnt lgkmcnt(8)\n"
"v_mfma_f32_16x16x32_bf16 v[108:111], a[192:195], a[112:115], v[108:111]\n"
"ds_read_b64_tr_b16 a[224:225], v218 offset:37120\n"
"v_mfma_f32_16x16x32_bf16 v[108:111], a[196:199], a[116:119], v[108:111]\n"
"v_mfma_f32_16x16x32_bf16 v[108:111], a[200:203], a[120:123], v[108:111]\n"
"ds_read_b64_tr_b16 a[226:227], v219 offset:37120\n"
"v_mfma_f32_16x16x32_bf16 v[108:111], a[204:207], a[124:127], v[108:111]\n"
"buffer_load_dwordx4 a[4:7], v232, s[52:55], s66 offen offset:1024 sc1\n"
"v_mfma_f32_16x16x32_bf16 v[104:107], a[204:207], a[108:111], v[104:107]\n"
"ds_read_b64_tr_b16 a[228:229], v218 offset:46336\n"
"v_mfma_f32_16x16x32_bf16 v[104:107], a[200:203], a[104:107], v[104:107]\n"
"v_mfma_f32_16x16x32_bf16 v[104:107], a[196:199], a[100:103], v[104:107]\n"
"ds_read_b64_tr_b16 a[230:231], v219 offset:46336\n"
"v_mfma_f32_16x16x32_bf16 v[104:107], a[192:195], a[96:99], v[104:107]\n"
"buffer_load_dwordx4 a[8:11], v232, s[52:55], s66 offen offset:2048 sc1\n"
"v_mfma_f32_16x16x32_bf16 v[100:103], a[192:195], a[80:83], v[100:103]\n"
"ds_read_b64_tr_b16 a[232:233], v218 offset:55552\n"
"v_mfma_f32_16x16x32_bf16 v[100:103], a[196:199], a[84:87], v[100:103]\n"
"v_mfma_f32_16x16x32_bf16 v[100:103], a[200:203], a[88:91], v[100:103]\n"
"ds_read_b64_tr_b16 a[234:235], v219 offset:55552\n"
"v_mfma_f32_16x16x32_bf16 v[100:103], a[204:207], a[92:95], v[100:103]\n"
"v_mfma_f32_16x16x32_bf16 v[96:99], a[204:207], a[76:79], v[96:99]\n"
"ds_read_b64_tr_b16 a[236:237], v218 offset:64768\n"
"v_mfma_f32_16x16x32_bf16 v[96:99], a[200:203], a[72:75], v[96:99]\n"
"buffer_load_dwordx4 a[12:15], v232, s[52:55], s66 offen offset:3072 sc1\n"
"v_mfma_f32_16x16x32_bf16 v[96:99], a[196:199], a[68:71], v[96:99]\n"
"ds_read_b64_tr_b16 a[238:239], v219 offset:64768\n"
"v_mfma_f32_16x16x32_bf16 v[96:99], a[192:195], a[64:67], v[96:99]\n"
"s_waitcnt lgkmcnt(8)\n"
"v_mfma_f32_16x16x32_bf16 v[112:115], a[208:211], a[64:67], v[112:115]\n"
"v_mfma_f32_16x16x32_bf16 v[112:115], a[212:215], a[68:71], v[112:115]\n"
"v_mfma_f32_16x16x32_bf16 v[112:115], a[216:219], a[72:75], v[112:115]\n"
"s_add_u32 s66, s77, 4096\n"
"v_mfma_f32_16x16x32_bf16 v[112:115], a[220:223], a[76:79], v[112:115]\n"
"s_waitcnt vmcnt(29) lgkmcnt(0)\n"
"buffer_load_dwordx4 a[16:19], v232, s[52:55], s66 offen sc1\n"
"v_mfma_f32_16x16x32_bf16 v[116:119], a[220:223], a[92:95], v[116:119]\n"
"s_barrier\n"
"v_mfma_f32_16x16x32_bf16 v[116:119], a[216:219], a[88:91], v[116:119]\n"
"v_mfma_f32_16x16x32_bf16 v[116:119], a[212:215], a[84:87], v[116:119]\n"
"ds_read_b64_tr_b16 a[192:193], v216\n"
"ds_read_b64_tr_b16 a[194:195], v217\n"
"v_mfma_f32_16x16x32_bf16 v[116:119], a[208:211], a[80:83], v[116:119]\n"
"v_mfma_f32_16x16x32_bf16 v[120:123], a[208:211], a[96:99], v[120:123]\n"
"ds_read_b64_tr_b16 a[196:197], v216 offset:9216\n"
"ds_read_b64_tr_b16 a[198:199], v217 offset:9216\n"
"v_mfma_f32_16x16x32_bf16 v[120:123], a[212:215], a[100:103], v[120:123]\n"
"buffer_load_dwordx4 a[20:23], v232, s[52:55], s66 offen offset:1024 sc1\n"
"v_mfma_f32_16x16x32_bf16 v[120:123], a[216:219], a[104:107], v[120:123]\n"
"ds_read_b64_tr_b16 a[200:201], v216 offset:18432\n"
"ds_read_b64_tr_b16 a[202:203], v217 offset:18432\n"
"v_mfma_f32_16x16x32_bf16 v[120:123], a[220:223], a[108:111], v[120:123]\n"
"v_mfma_f32_16x16x32_bf16 v[124:127], a[220:223], a[124:127], v[124:127]\n"
"ds_read_b64_tr_b16 a[204:205], v216 offset:27648\n"
"v_mfma_f32_16x16x32_bf16 v[124:127], a[216:219], a[120:123], v[124:127]\n"
"buffer_load_dwordx4 a[24:27], v232, s[52:55], s66 offen offset:2048 sc1\n"
"v_mfma_f32_16x16x32_bf16 v[124:127], a[212:215], a[116:119], v[124:127]\n"
"ds_read_b64_tr_b16 a[206:207], v217 offset:27648\n"
"v_mfma_f32_16x16x32_bf16 v[124:127], a[208:211], a[112:115], v[124:127]\n"
"v_mfma_f32_16x16x32_bf16 v[140:143], a[224:227], a[112:115], v[140:143]\n"
"ds_read_b64_tr_b16 a[208:209], v216 offset:32\n"
"v_mfma_f32_16x16x32_bf16 v[140:143], a[228:231], a[116:119], v[140:143]\n"
"v_mfma_f32_16x16x32_bf16 v[140:143], a[232:235], a[120:123], v[140:143]\n"
"ds_read_b64_tr_b16 a[210:211], v217 offset:32\n"
"v_mfma_f32_16x16x32_bf16 v[140:143], a[236:239], a[124:127], v[140:143]\n"
"buffer_load_dwordx4 a[28:31], v232, s[52:55], s66 offen offset:3072 sc1\n"
"v_mfma_f32_16x16x32_bf16 v[136:139], a[236:239], a[108:111], v[136:139]\n"
"ds_read_b64_tr_b16 a[212:213], v216 offset:9248\n"
"v_mfma_f32_16x16x32_bf16 v[136:139], a[232:235], a[104:107], v[136:139]\n"
"v_mfma_f32_16x16x32_bf16 v[136:139], a[228:231], a[100:103], v[136:139]\n"
"ds_read_b64_tr_b16 a[214:215], v217 offset:9248\n"
"v_mfma_f32_16x16x32_bf16 v[136:139], a[224:227], a[96:99], v[136:139]\n"
"v_mfma_f32_16x16x32_bf16 v[132:135], a[224:227], a[80:83], v[132:135]\n"
"s_add_u32 s66, s77, 8192\n"
"ds_read_b64_tr_b16 a[216:217], v216 offset:18464\n"
"v_mfma_f32_16x16x32_bf16 v[132:135], a[228:231], a[84:87], v[132:135]\n"
"buffer_load_dwordx4 a[32:35], v232, s[52:55], s66 offen sc1\n"
"v_mfma_f32_16x16x32_bf16 v[132:135], a[232:235], a[88:91], v[132:135]\n"
"ds_read_b64_tr_b16 a[218:219], v217 offset:18464\n"
"v_mfma_f32_16x16x32_bf16 v[132:135], a[236:239], a[92:95], v[132:135]\n"
"v_mfma_f32_16x16x32_bf16 v[128:131], a[236:239], a[76:79], v[128:131]\n"
"ds_read_b64_tr_b16 a[220:221], v216 offset:27680\n"
"v_mfma_f32_16x16x32_bf16 v[128:131], a[232:235], a[72:75], v[128:131]\n"
"v_mfma_f32_16x16x32_bf16 v[128:131], a[228:231], a[68:71], v[128:131]\n"
"ds_read_b64_tr_b16 a[222:223], v217 offset:27680\n"
"v_mfma_f32_16x16x32_bf16 v[128:131], a[224:227], a[64:67], v[128:131]\n"
"s_waitcnt vmcnt(9) lgkmcnt(8)\n"
"buffer_load_dwordx4 a[36:39], v232, s[52:55], s66 offen offset:1024 sc1\n"
"v_mfma_f32_16x16x32_bf16 v[0:3], a[192:195], a[128:131], v[0:3]\n"
"ds_read_b64_tr_b16 a[224:225], v216 offset:64\n"
"v_mfma_f32_16x16x32_bf16 v[0:3], a[196:199], a[132:135], v[0:3]\n"
"v_mfma_f32_16x16x32_bf16 v[0:3], a[200:203], a[136:139], v[0:3]\n"
"ds_read_b64_tr_b16 a[226:227], v217 offset:64\n"
"v_mfma_f32_16x16x32_bf16 v[0:3], a[204:207], a[140:143], v[0:3]\n"
"buffer_load_dwordx4 a[40:43], v232, s[52:55], s66 offen offset:2048 sc1\n"
"v_mfma_f32_16x16x32_bf16 v[4:7], a[204:207], a[156:159], v[4:7]\n"
"ds_read_b64_tr_b16 a[228:229], v216 offset:9280\n"
"v_mfma_f32_16x16x32_bf16 v[4:7], a[200:203], a[152:155], v[4:7]\n"
"v_mfma_f32_16x16x32_bf16 v[4:7], a[196:199], a[148:151], v[4:7]\n"
"ds_read_b64_tr_b16 a[230:231], v217 offset:9280\n"
"v_mfma_f32_16x16x32_bf16 v[4:7], a[192:195], a[144:147], v[4:7]\n"
"v_mfma_f32_16x16x32_bf16 v[8:11], a[192:195], a[160:163], v[8:11]\n"
"ds_read_b64_tr_b16 a[232:233], v216 offset:18496\n"
"v_mfma_f32_16x16x32_bf16 v[8:11], a[196:199], a[164:167], v[8:11]\n"
"buffer_load_dwordx4 a[44:47], v232, s[52:55], s66 offen offset:3072 sc1\n"
"v_mfma_f32_16x16x32_bf16 v[8:11], a[200:203], a[168:171], v[8:11]\n"
"ds_read_b64_tr_b16 a[234:235], v217 offset:18496\n"
"v_mfma_f32_16x16x32_bf16 v[8:11], a[204:207], a[172:175], v[8:11]\n"
"v_mfma_f32_16x16x32_bf16 v[12:15], a[204:207], a[188:191], v[12:15]\n"
"ds_read_b64_tr_b16 a[236:237], v216 offset:27712\n"
"v_mfma_f32_16x16x32_bf16 v[12:15], a[200:203], a[184:187], v[12:15]\n"
"v_mfma_f32_16x16x32_bf16 v[12:15], a[196:199], a[180:183], v[12:15]\n"
"s_add_u32 s66, s77, 12288\n"
"ds_read_b64_tr_b16 a[238:239], v217 offset:27712\n"
"v_mfma_f32_16x16x32_bf16 v[12:15], a[192:195], a[176:179], v[12:15]\n"
"s_waitcnt lgkmcnt(8)\n"
"buffer_load_dwordx4 a[48:51], v232, s[52:55], s66 offen sc1\n"
"v_mfma_f32_16x16x32_bf16 v[28:31], a[208:211], a[176:179], v[28:31]\n"
"ds_read_b64_tr_b16 a[192:193], v216 offset:96\n"
"v_mfma_f32_16x16x32_bf16 v[28:31], a[212:215], a[180:183], v[28:31]\n"
"v_mfma_f32_16x16x32_bf16 v[28:31], a[216:219], a[184:187], v[28:31]\n"
"ds_read_b64_tr_b16 a[194:195], v217 offset:96\n"
"v_mfma_f32_16x16x32_bf16 v[28:31], a[220:223], a[188:191], v[28:31]\n"
"v_mfma_f32_16x16x32_bf16 v[24:27], a[220:223], a[172:175], v[24:27]\n"
"ds_read_b64_tr_b16 a[196:197], v216 offset:9312\n"
"v_mfma_f32_16x16x32_bf16 v[24:27], a[216:219], a[168:171], v[24:27]\n"
"buffer_load_dwordx4 a[52:55], v232, s[52:55], s66 offen offset:1024 sc1\n"
"v_mfma_f32_16x16x32_bf16 v[24:27], a[212:215], a[164:167], v[24:27]\n"
"ds_read_b64_tr_b16 a[198:199], v217 offset:9312\n"
"v_mfma_f32_16x16x32_bf16 v[24:27], a[208:211], a[160:163], v[24:27]\n"
"v_mfma_f32_16x16x32_bf16 v[20:23], a[208:211], a[144:147], v[20:23]\n"
"ds_read_b64_tr_b16 a[200:201], v216 offset:18528\n"
"v_mfma_f32_16x16x32_bf16 v[20:23], a[212:215], a[148:151], v[20:23]\n"
"buffer_load_dwordx4 a[56:59], v232, s[52:55], s66 offen offset:2048 sc1\n"
"v_mfma_f32_16x16x32_bf16 v[20:23], a[216:219], a[152:155], v[20:23]\n"
"ds_read_b64_tr_b16 a[202:203], v217 offset:18528\n"
"v_mfma_f32_16x16x32_bf16 v[20:23], a[220:223], a[156:159], v[20:23]\n"
"v_mfma_f32_16x16x32_bf16 v[16:19], a[220:223], a[140:143], v[16:19]\n"
"ds_read_b64_tr_b16 a[204:205], v216 offset:27744\n"
"v_mfma_f32_16x16x32_bf16 v[16:19], a[216:219], a[136:139], v[16:19]\n"
"v_mfma_f32_16x16x32_bf16 v[16:19], a[212:215], a[132:135], v[16:19]\n"
"ds_read_b64_tr_b16 a[206:207], v217 offset:27744\n"
"v_mfma_f32_16x16x32_bf16 v[16:19], a[208:211], a[128:131], v[16:19]\n"
"s_waitcnt lgkmcnt(8)\n"
"buffer_load_dwordx4 a[60:63], v232, s[52:55], s66 offen offset:3072 sc1\n"
"v_mfma_f32_16x16x32_bf16 v[32:35], a[224:227], a[128:131], v[32:35]\n"
"ds_read_b64_tr_b16 a[208:209], v216 offset:128\n"
"v_mfma_f32_16x16x32_bf16 v[32:35], a[228:231], a[132:135], v[32:35]\n"
"v_mfma_f32_16x16x32_bf16 v[32:35], a[232:235], a[136:139], v[32:35]\n"
"ds_read_b64_tr_b16 a[210:211], v217 offset:128\n"
"v_mfma_f32_16x16x32_bf16 v[32:35], a[236:239], a[140:143], v[32:35]\n"
"v_mfma_f32_16x16x32_bf16 v[36:39], a[236:239], a[156:159], v[36:39]\n"
"s_cmp_le_u32 s59, 2\n"
"s_cselect_b32 s72, 7, 5\n"
"s_mul_i32 s58, s72, 147456\n"
"s_add_u32 m0, s60, 73728\n"
"ds_read_b64_tr_b16 a[212:213], v216 offset:9344\n"
"v_mfma_f32_16x16x32_bf16 v[36:39], a[232:235], a[152:155], v[36:39]\n"
"buffer_load_dwordx4 v221, s[92:95], s58 offen lds\n"
"v_mfma_f32_16x16x32_bf16 v[36:39], a[228:231], a[148:151], v[36:39]\n"
"s_add_u32 s74, s69, 128\n"
"ds_read_b64_tr_b16 a[214:215], v217 offset:9344\n"
"v_mfma_f32_16x16x32_bf16 v[36:39], a[224:227], a[144:147], v[36:39]\n"
"v_mfma_f32_16x16x32_bf16 v[40:43], a[224:227], a[160:163], v[40:43]\n"
"ds_read_b64_tr_b16 a[216:217], v216 offset:18560\n"
"v_mfma_f32_16x16x32_bf16 v[40:43], a[228:231], a[164:167], v[40:43]\n"
"v_mfma_f32_16x16x32_bf16 v[40:43], a[232:235], a[168:171], v[40:43]\n"
"s_add_u32 s84, s23, 0\n"
"ds_read_b64_tr_b16 a[218:219], v217 offset:18560\n"
"v_mfma_f32_16x16x32_bf16 v[40:43], a[236:239], a[172:175], v[40:43]\n"
"buffer_load_dwordx4 v225, s[92:95], s58 offen offset:1024 lds\n"
"v_mfma_f32_16x16x32_bf16 v[44:47], a[236:239], a[188:191], v[44:47]\n"
"ds_read_b64_tr_b16 a[220:221], v216 offset:27776\n"
"v_mfma_f32_16x16x32_bf16 v[44:47], a[232:235], a[184:187], v[44:47]\n"
"v_mfma_f32_16x16x32_bf16 v[44:47], a[228:231], a[180:183], v[44:47]\n"
"s_and_b32 s84, s84, 15\n"
"ds_read_b64_tr_b16 a[222:223], v217 offset:27776\n"
"v_mfma_f32_16x16x32_bf16 v[44:47], a[224:227], a[176:179], v[44:47]\n"
"s_waitcnt lgkmcnt(8)\n"
"buffer_load_dwordx4 v226, s[92:95], s58 offen offset:2048 lds\n"
"v_mfma_f32_16x16x32_bf16 v[60:63], a[192:195], a[176:179], v[60:63]\n"
"ds_read_b64_tr_b16 a[224:225], v216 offset:160\n"
"v_mfma_f32_16x16x32_bf16 v[60:63], a[196:199], a[180:183], v[60:63]\n"
"v_mfma_f32_16x16x32_bf16 v[60:63], a[200:203], a[184:187], v[60:63]\n"
"s_lshl2_add_u32 s84, s84, s74\n"
"ds_read_b64_tr_b16 a[226:227], v217 offset:160\n"
"v_mfma_f32_16x16x32_bf16 v[60:63], a[204:207], a[188:191], v[60:63]\n"
"v_mfma_f32_16x16x32_bf16 v[56:59], a[204:207], a[172:175], v[56:59]\n"
"ds_read_b64_tr_b16 a[228:229], v216 offset:9376\n"
"v_mfma_f32_16x16x32_bf16 v[56:59], a[200:203], a[168:171], v[56:59]\n"
"buffer_load_dwordx4 v227, s[92:95], s58 offen offset:3072 lds\n"
"v_mfma_f32_16x16x32_bf16 v[56:59], a[196:199], a[164:167], v[56:59]\n"
"s_mul_i32 s84, s84, 16384\n"
"ds_read_b64_tr_b16 a[230:231], v217 offset:9376\n"
"v_mfma_f32_16x16x32_bf16 v[56:59], a[192:195], a[160:163], v[56:59]\n"
"v_mfma_f32_16x16x32_bf16 v[52:55], a[192:195], a[144:147], v[52:55]\n"
"ds_read_b64_tr_b16 a[232:233], v216 offset:18592\n"
"v_mfma_f32_16x16x32_bf16 v[52:55], a[196:199], a[148:151], v[52:55]\n"
"s_add_u32 m0, s60, 77824\n"
"v_mfma_f32_16x16x32_bf16 v[52:55], a[200:203], a[152:155], v[52:55]\n"
"s_add_u32 s85, s23, 4\n"
"ds_read_b64_tr_b16 a[234:235], v217 offset:18592\n"
"v_mfma_f32_16x16x32_bf16 v[52:55], a[204:207], a[156:159], v[52:55]\n"
"buffer_load_dwordx4 v228, s[92:95], s58 offen lds\n"
"v_mfma_f32_16x16x32_bf16 v[48:51], a[204:207], a[140:143], v[48:51]\n"
"ds_read_b64_tr_b16 a[236:237], v216 offset:27808\n"
"v_mfma_f32_16x16x32_bf16 v[48:51], a[200:203], a[136:139], v[48:51]\n"
"v_mfma_f32_16x16x32_bf16 v[48:51], a[196:199], a[132:135], v[48:51]\n"
"s_and_b32 s85, s85, 15\n"
"ds_read_b64_tr_b16 a[238:239], v217 offset:27808\n"
"v_mfma_f32_16x16x32_bf16 v[48:51], a[192:195], a[128:131], v[48:51]\n"
"s_waitcnt lgkmcnt(8)\n"
"v_mfma_f32_16x16x32_bf16 v[64:67], a[208:211], a[128:131], v[64:67]\n"
"ds_read_b64_tr_b16 a[192:193], v216 offset:192\n"
"v_mfma_f32_16x16x32_bf16 v[64:67], a[212:215], a[132:135], v[64:67]\n"
"buffer_load_dwordx4 v229, s[92:95], s58 offen offset:1024 lds\n"
"v_mfma_f32_16x16x32_bf16 v[64:67], a[216:219], a[136:139], v[64:67]\n"
"s_lshl2_add_u32 s85, s85, s74\n"
"ds_read_b64_tr_b16 a[194:195], v217 offset:192\n"
"v_mfma_f32_16x16x32_bf16 v[64:67], a[220:223], a[140:143], v[64:67]\n"
"v_mfma_f32_16x16x32_bf16 v[68:71], a[220:223], a[156:159], v[68:71]\n"
"ds_read_b64_tr_b16 a[196:197], v216 offset:9408\n"
"v_mfma_f32_16x16x32_bf16 v[68:71], a[216:219], a[152:155], v[68:71]\n"
"buffer_load_dwordx4 v230, s[92:95], s58 offen offset:2048 lds\n"
"v_mfma_f32_16x16x32_bf16 v[68:71], a[212:215], a[148:151], v[68:71]\n"
"s_mul_i32 s85, s85, 16384\n"
"ds_read_b64_tr_b16 a[198:199], v217 offset:9408\n"
"v_mfma_f32_16x16x32_bf16 v[68:71], a[208:211], a[144:147], v[68:71]\n"
"v_mfma_f32_16x16x32_bf16 v[72:75], a[208:211], a[160:163], v[72:75]\n"
"ds_read_b64_tr_b16 a[200:201], v216 offset:18624\n"
"v_mfma_f32_16x16x32_bf16 v[72:75], a[212:215], a[164:167], v[72:75]\n"
"v_mfma_f32_16x16x32_bf16 v[72:75], a[216:219], a[168:171], v[72:75]\n"
"s_add_u32 s87, s23, 8\n"
"ds_read_b64_tr_b16 a[202:203], v217 offset:18624\n"
"v_mfma_f32_16x16x32_bf16 v[72:75], a[220:223], a[172:175], v[72:75]\n"
"buffer_load_dwordx4 v231, s[92:95], s58 offen offset:3072 lds\n"
"v_mfma_f32_16x16x32_bf16 v[76:79], a[220:223], a[188:191], v[76:79]\n"
"ds_read_b64_tr_b16 a[204:205], v216 offset:27840\n"
"v_mfma_f32_16x16x32_bf16 v[76:79], a[216:219], a[184:187], v[76:79]\n"
"v_mfma_f32_16x16x32_bf16 v[76:79], a[212:215], a[180:183], v[76:79]\n"
"s_and_b32 s87, s87, 15\n"
"ds_read_b64_tr_b16 a[206:207], v217 offset:27840\n"
"v_mfma_f32_16x16x32_bf16 v[76:79], a[208:211], a[176:179], v[76:79]\n"
"s_waitcnt lgkmcnt(8)\n"
"v_mfma_f32_16x16x32_bf16 v[92:95], a[224:227], a[176:179], v[92:95]\n"
"s_add_u32 m0, s60, 81920\n"
"ds_read_b64_tr_b16 a[208:209], v216 offset:224\n"
"v_mfma_f32_16x16x32_bf16 v[92:95], a[228:231], a[180:183], v[92:95]\n"
"buffer_load_dwordx4 v235, s[92:95], s58 offen lds\n"
"v_mfma_f32_16x16x32_bf16 v[92:95], a[232:235], a[184:187], v[92:95]\n"
"s_lshl2_add_u32 s87, s87, s74\n"
"ds_read_b64_tr_b16 a[210:211], v217 offset:224\n"
"v_mfma_f32_16x16x32_bf16 v[92:95], a[236:239], a[188:191], v[92:95]\n"
"v_mfma_f32_16x16x32_bf16 v[88:91], a[236:239], a[172:175], v[88:91]\n"
"ds_read_b64_tr_b16 a[212:213], v216 offset:9440\n"
"v_mfma_f32_16x16x32_bf16 v[88:91], a[232:235], a[168:171], v[88:91]\n"
"s_cmp_le_u32 s59, 2\n"
"s_cselect_b32 s72, 7, 5\n"
"s_mul_i32 s72, s72, 65536\n"
"s_add_u32 s77, s76, s72\n"
"s_add_u32 s66, s77, 0\n"
"v_mfma_f32_16x16x32_bf16 v[88:91], a[228:231], a[164:167], v[88:91]\n"
"s_mul_i32 s87, s87, 16384\n"
"ds_read_b64_tr_b16 a[214:215], v217 offset:9440\n"
"v_mfma_f32_16x16x32_bf16 v[88:91], a[224:227], a[160:163], v[88:91]\n"
"buffer_load_dwordx4 a[64:67], v232, s[52:55], s66 offen sc1\n"
"v_mfma_f32_16x16x32_bf16 v[84:87], a[224:227], a[144:147], v[84:87]\n"
"ds_read_b64_tr_b16 a[216:217], v216 offset:18656\n"
"v_mfma_f32_16x16x32_bf16 v[84:87], a[228:231], a[148:151], v[84:87]\n"
"v_mfma_f32_16x16x32_bf16 v[84:87], a[232:235], a[152:155], v[84:87]\n"
"s_add_u32 s68, s23, 12\n"
"ds_read_b64_tr_b16 a[218:219], v217 offset:18656\n"
"v_mfma_f32_16x16x32_bf16 v[84:87], a[236:239], a[156:159], v[84:87]\n"
"buffer_load_dwordx4 a[68:71], v232, s[52:55], s66 offen offset:1024 sc1\n"
"v_mfma_f32_16x16x32_bf16 v[80:83], a[236:239], a[140:143], v[80:83]\n"
"ds_read_b64_tr_b16 a[220:221], v216 offset:27872\n"
"v_mfma_f32_16x16x32_bf16 v[80:83], a[232:235], a[136:139], v[80:83]\n"
"v_mfma_f32_16x16x32_bf16 v[80:83], a[228:231], a[132:135], v[80:83]\n"
"s_and_b32 s68, s68, 15\n"
"ds_read_b64_tr_b16 a[222:223], v217 offset:27872\n"
"v_mfma_f32_16x16x32_bf16 v[80:83], a[224:227], a[128:131], v[80:83]\n"
"s_waitcnt lgkmcnt(8)\n"
"v_mfma_f32_16x16x32_bf16 v[96:99], a[192:195], a[128:131], v[96:99]\n"
"ds_read_b64_tr_b16 a[224:225], v216 offset:256\n"
"v_mfma_f32_16x16x32_bf16 v[96:99], a[196:199], a[132:135], v[96:99]\n"
"buffer_load_dwordx4 a[72:75], v232, s[52:55], s66 offen offset:2048 sc1\n"
"v_mfma_f32_16x16x32_bf16 v[96:99], a[200:203], a[136:139], v[96:99]\n"
"s_lshl2_add_u32 s68, s68, s74\n"
"ds_read_b64_tr_b16 a[226:227], v217 offset:256\n"
"v_mfma_f32_16x16x32_bf16 v[96:99], a[204:207], a[140:143], v[96:99]\n"
"v_mfma_f32_16x16x32_bf16 v[100:103], a[204:207], a[156:159], v[100:103]\n"
"ds_read_b64_tr_b16 a[228:229], v216 offset:9472\n"
"v_mfma_f32_16x16x32_bf16 v[100:103], a[200:203], a[152:155], v[100:103]\n"
"v_mfma_f32_16x16x32_bf16 v[100:103], a[196:199], a[148:151], v[100:103]\n"
"s_mul_i32 s68, s68, 16384\n"
"ds_read_b64_tr_b16 a[230:231], v217 offset:9472\n"
"v_mfma_f32_16x16x32_bf16 v[100:103], a[192:195], a[144:147], v[100:103]\n"
"buffer_load_dwordx4 a[76:79], v232, s[52:55], s66 offen offset:3072 sc1\n"
"v_mfma_f32_16x16x32_bf16 v[104:107], a[192:195], a[160:163], v[104:107]\n"
"ds_read_b64_tr_b16 a[232:233], v216 offset:18688\n"
"v_mfma_f32_16x16x32_bf16 v[104:107], a[196:199], a[164:167], v[104:107]\n"
"v_mfma_f32_16x16x32_bf16 v[104:107], a[200:203], a[168:171], v[104:107]\n"
"s_mov_b64 s[36:37], s[18:19]\n"
"ds_read_b64_tr_b16 a[234:235], v217 offset:18688\n"
"v_mfma_f32_16x16x32_bf16 v[104:107], a[204:207], a[172:175], v[104:107]\n"
"v_mfma_f32_16x16x32_bf16 v[108:111], a[204:207], a[188:191], v[108:111]\n"
"s_add_u32 s66, s77, 4096\n"
"ds_read_b64_tr_b16 a[236:237], v216 offset:27904\n"
"v_mfma_f32_16x16x32_bf16 v[108:111], a[200:203], a[184:187], v[108:111]\n"
"buffer_load_dwordx4 a[80:83], v232, s[52:55], s66 offen sc1\n"
"v_mfma_f32_16x16x32_bf16 v[108:111], a[196:199], a[180:183], v[108:111]\n"
"s_and_b32 s37, s37, 0xffff\n"
"ds_read_b64_tr_b16 a[238:239], v217 offset:27904\n"
"v_mfma_f32_16x16x32_bf16 v[108:111], a[192:195], a[176:179], v[108:111]\n"
"s_waitcnt lgkmcnt(8)\n"
"v_mfma_f32_16x16x32_bf16 v[124:127], a[208:211], a[176:179], v[124:127]\n"
"v_mfma_f32_16x16x32_bf16 v[124:127], a[212:215], a[180:183], v[124:127]\n"
"buffer_load_dwordx4 a[84:87], v232, s[52:55], s66 offen offset:1024 sc1\n"
"v_mfma_f32_16x16x32_bf16 v[124:127], a[216:219], a[184:187], v[124:127]\n"
"s_mov_b32 s38, 477216\n"
"v_mfma_f32_16x16x32_bf16 v[124:127], a[220:223], a[188:191], v[124:127]\n"
"s_waitcnt vmcnt(47) lgkmcnt(0)\n"
"v_mfma_f32_16x16x32_bf16 v[120:123], a[220:223], a[172:175], v[120:123]\n"
"s_barrier\n"
"v_mfma_f32_16x16x32_bf16 v[120:123], a[216:219], a[168:171], v[120:123]\n"
"v_mfma_f32_16x16x32_bf16 v[120:123], a[212:215], a[164:167], v[120:123]\n"
"s_mov_b64 s[40:41], s[52:53]\n"
"ds_read_b64_tr_b16 a[192:193], v216 offset:36864\n"
"ds_read_b64_tr_b16 a[194:195], v217 offset:36864\n"
"v_mfma_f32_16x16x32_bf16 v[120:123], a[208:211], a[160:163], v[120:123]\n"
"buffer_load_dwordx4 a[88:91], v232, s[52:55], s66 offen offset:2048 sc1\n"
"v_mfma_f32_16x16x32_bf16 v[116:119], a[208:211], a[144:147], v[116:119]\n"
"ds_read_b64_tr_b16 a[196:197], v216 offset:46080\n"
"ds_read_b64_tr_b16 a[198:199], v217 offset:46080\n"
"v_mfma_f32_16x16x32_bf16 v[116:119], a[212:215], a[148:151], v[116:119]\n"
"v_mfma_f32_16x16x32_bf16 v[116:119], a[216:219], a[152:155], v[116:119]\n"
"s_mov_b32 s62, 0xee\n"
"ds_read_b64_tr_b16 a[200:201], v216 offset:55296\n"
"ds_read_b64_tr_b16 a[202:203], v217 offset:55296\n"
"v_mfma_f32_16x16x32_bf16 v[116:119], a[220:223], a[156:159], v[116:119]\n"
"v_mfma_f32_16x16x32_bf16 v[112:115], a[220:223], a[140:143], v[112:115]\n"
"ds_read_b64_tr_b16 a[204:205], v216 offset:64512\n"
"v_mfma_f32_16x16x32_bf16 v[112:115], a[216:219], a[136:139], v[112:115]\n"
"buffer_load_dwordx4 a[92:95], v232, s[52:55], s66 offen offset:3072 sc1\n"
"v_mfma_f32_16x16x32_bf16 v[112:115], a[212:215], a[132:135], v[112:115]\n"
"ds_read_b64_tr_b16 a[206:207], v217 offset:64512\n"
"v_mfma_f32_16x16x32_bf16 v[112:115], a[208:211], a[128:131], v[112:115]\n"
"v_mfma_f32_16x16x32_bf16 v[128:131], a[224:227], a[128:131], v[128:131]\n"
"ds_read_b64_tr_b16 a[208:209], v216 offset:36896\n"
"v_mfma_f32_16x16x32_bf16 v[128:131], a[228:231], a[132:135], v[128:131]\n"
"v_mfma_f32_16x16x32_bf16 v[128:131], a[232:235], a[136:139], v[128:131]\n"
"s_add_u32 s66, s77, 8192\n"
"ds_read_b64_tr_b16 a[210:211], v217 offset:36896\n"
"v_mfma_f32_16x16x32_bf16 v[128:131], a[236:239], a[140:143], v[128:131]\n"
"buffer_load_dwordx4 a[96:99], v232, s[52:55], s66 offen sc1\n"
"v_mfma_f32_16x16x32_bf16 v[132:135], a[236:239], a[156:159], v[132:135]\n"
"ds_read_b64_tr_b16 a[212:213], v216 offset:46112\n"
"v_mfma_f32_16x16x32_bf16 v[132:135], a[232:235], a[152:155], v[132:135]\n"
"v_mfma_f32_16x16x32_bf16 v[132:135], a[228:231], a[148:151], v[132:135]\n"
"ds_read_b64_tr_b16 a[214:215], v217 offset:46112\n"
"v_mfma_f32_16x16x32_bf16 v[132:135], a[224:227], a[144:147], v[132:135]\n"
"buffer_load_dwordx4 a[100:103], v232, s[52:55], s66 offen offset:1024 sc1\n"
"v_mfma_f32_16x16x32_bf16 v[136:139], a[224:227], a[160:163], v[136:139]\n"
"ds_read_b64_tr_b16 a[216:217], v216 offset:55328\n"
"v_mfma_f32_16x16x32_bf16 v[136:139], a[228:231], a[164:167], v[136:139]\n"
"v_mfma_f32_16x16x32_bf16 v[136:139], a[232:235], a[168:171], v[136:139]\n"
"ds_read_b64_tr_b16 a[218:219], v217 offset:55328\n"
"v_mfma_f32_16x16x32_bf16 v[136:139], a[236:239], a[172:175], v[136:139]\n"
"v_mfma_f32_16x16x32_bf16 v[140:143], a[236:239], a[188:191], v[140:143]\n"
"ds_read_b64_tr_b16 a[220:221], v216 offset:64544\n"
"v_mfma_f32_16x16x32_bf16 v[140:143], a[232:235], a[184:187], v[140:143]\n"
"buffer_load_dwordx4 a[104:107], v232, s[52:55], s66 offen offset:2048 sc1\n"
"v_mfma_f32_16x16x32_bf16 v[140:143], a[228:231], a[180:183], v[140:143]\n"
"ds_read_b64_tr_b16 a[222:223], v217 offset:64544\n"
"v_mfma_f32_16x16x32_bf16 v[140:143], a[224:227], a[176:179], v[140:143]\n"
"s_waitcnt vmcnt(20) lgkmcnt(8)\n"
"v_mfma_f32_16x16x32_bf16 v[12:15], a[192:195], a[48:51], v[12:15]\n"
"ds_read_b64_tr_b16 a[224:225], v216 offset:36928\n"
"v_mfma_f32_16x16x32_bf16 v[12:15], a[196:199], a[52:55], v[12:15]\n"
"v_mfma_f32_16x16x32_bf16 v[12:15], a[200:203], a[56:59], v[12:15]\n"
"ds_read_b64_tr_b16 a[226:227], v217 offset:36928\n"
"v_mfma_f32_16x16x32_bf16 v[12:15], a[204:207], a[60:63], v[12:15]\n"
"buffer_load_dwordx4 a[108:111], v232, s[52:55], s66 offen offset:3072 sc1\n"
"v_mfma_f32_16x16x32_bf16 v[8:11], a[204:207], a[44:47], v[8:11]\n"
"ds_read_b64_tr_b16 a[228:229], v216 offset:46144\n"
"v_lshrrev_b32 v236, 4, v232\n"
"v_mfma_f32_16x16x32_bf16 v[8:11], a[200:203], a[40:43], v[8:11]\n"
"v_lshrrev_b32 v238, 4, v236\n"
"v_bfe_u32 v239, v236, 2, 2\n"
"v_mfma_f32_16x16x32_bf16 v[8:11], a[196:199], a[36:39], v[8:11]\n"
"ds_read_b64_tr_b16 a[230:231], v217 offset:46144\n"
"v_cmp_eq_u32 vcc, v238, v239\n"
"v_mfma_f32_16x16x32_bf16 v[8:11], a[192:195], a[32:35], v[8:11]\n"
"v_and_b32 v238, 1, v236\n"
"v_lshlrev_b32 v238, 4, v238\n"
"v_mfma_f32_16x16x32_bf16 v[4:7], a[192:195], a[16:19], v[4:7]\n"
"s_add_u32 s66, s77, 12288\n"
"ds_read_b64_tr_b16 a[232:233], v216 offset:55360\n"
"v_mov_b32 v239, 0x3f80\n"
"v_mfma_f32_16x16x32_bf16 v[4:7], a[196:199], a[20:23], v[4:7]\n"
"buffer_load_dwordx4 a[112:115], v232, s[52:55], s66 offen sc1\n"
"v_mfma_f32_16x16x32_bf16 v[4:7], a[200:203], a[24:27], v[4:7]\n"
"ds_read_b64_tr_b16 a[234:235], v217 offset:55360\n"
"v_lshlrev_b32 v238, v238, v239\n"
"v_mfma_f32_16x16x32_bf16 v[4:7], a[204:207], a[28:31], v[4:7]\n"
"v_cndmask_b32 v238, 0, v238, vcc\n"
"v_and_b32 v239, 2, v236\n"
"v_mfma_f32_16x16x32_bf16 v[0:3], a[204:207], a[12:15], v[0:3]\n"
"ds_read_b64_tr_b16 a[236:237], v216 offset:64576\n"
"v_cmp_ne_u32 vcc, 0, v239\n"
"v_mfma_f32_16x16x32_bf16 v[0:3], a[200:203], a[8:11], v[0:3]\n"
"buffer_load_dwordx4 a[116:119], v232, s[52:55], s66 offen offset:1024 sc1\n"
"v_mfma_f32_16x16x32_bf16 v[0:3], a[196:199], a[4:7], v[0:3]\n"
"ds_read_b64_tr_b16 a[238:239], v217 offset:64576\n"
"v_mov_b32 v208, 0\n"
"v_mfma_f32_16x16x32_bf16 v[0:3], a[192:195], a[0:3], v[0:3]\n"
"s_waitcnt lgkmcnt(8)\n"
"v_mov_b32 v209, 0\n"
"v_mov_b32 v212, 0\n"
"v_mfma_f32_16x16x32_bf16 v[16:19], a[208:211], a[0:3], v[16:19]\n"
"ds_read_b64_tr_b16 a[192:193], v216 offset:36960\n"
"v_mov_b32 v213, 0\n"
"v_mfma_f32_16x16x32_bf16 v[16:19], a[212:215], a[4:7], v[16:19]\n"
"v_lshrrev_b32 v223, 1, v232\n"
"v_and_b32 v250, 0xfffffbff, v250\n"
"v_mfma_f32_16x16x32_bf16 v[16:19], a[216:219], a[8:11], v[16:19]\n"
"ds_read_b64_tr_b16 a[194:195], v217 offset:36960\n"
"v_cndmask_b32 v211, 0, v238, vcc\n"
"v_mfma_f32_16x16x32_bf16 v[16:19], a[220:223], a[12:15], v[16:19]\n"
"buffer_load_dwordx4 a[120:123], v232, s[52:55], s66 offen offset:2048 sc1\n"
"v_mfma_f32_16x16x32_bf16 v[20:23], a[220:223], a[28:31], v[20:23]\n"
"ds_read_b64_tr_b16 a[196:197], v216 offset:46176\n"
"v_xor_b32 v210, v211, v238\n"
"v_mfma_f32_16x16x32_bf16 v[20:23], a[216:219], a[24:27], v[20:23]\n"
"v_accvgpr_write_b32 a146, 0\n"
"v_accvgpr_write_b32 a147, 0\n"
"v_mfma_f32_16x16x32_bf16 v[20:23], a[212:215], a[20:23], v[20:23]\n"
"ds_read_b64_tr_b16 a[198:199], v217 offset:46176\n"
"v_accvgpr_write_b32 a166, 0\n"
"v_mfma_f32_16x16x32_bf16 v[20:23], a[208:211], a[16:19], v[20:23]\n"
"v_accvgpr_write_b32 a167, 0\n"
"v_accvgpr_write_b32 a186, 0\n"
"v_mfma_f32_16x16x32_bf16 v[24:27], a[208:211], a[32:35], v[24:27]\n"
"ds_read_b64_tr_b16 a[200:201], v216 offset:55392\n"
"v_accvgpr_write_b32 a187, 0\n"
"v_mfma_f32_16x16x32_bf16 v[24:27], a[212:215], a[36:39], v[24:27]\n"
"buffer_load_dwordx4 a[124:127], v232, s[52:55], s66 offen offset:3072 sc1\n"
"v_mfma_f32_16x16x32_bf16 v[24:27], a[216:219], a[40:43], v[24:27]\n"
"ds_read_b64_tr_b16 a[202:203], v217 offset:55392\n"
"v_mfma_f32_16x16x32_bf16 v[24:27], a[220:223], a[44:47], v[24:27]\n"
"v_mfma_f32_16x16x32_bf16 v[28:31], a[220:223], a[60:63], v[28:31]\n"
"ds_read_b64_tr_b16 a[204:205], v216 offset:64608\n"
"v_mfma_f32_16x16x32_bf16 v[28:31], a[216:219], a[56:59], v[28:31]\n"
"v_mfma_f32_16x16x32_bf16 v[28:31], a[212:215], a[52:55], v[28:31]\n"
"ds_read_b64_tr_b16 a[206:207], v217 offset:64608\n"
"v_mfma_f32_16x16x32_bf16 v[28:31], a[208:211], a[48:51], v[28:31]\n"
"s_waitcnt lgkmcnt(8)\n"
"v_mfma_f32_16x16x32_bf16 v[44:47], a[224:227], a[48:51], v[44:47]\n"
"ds_read_b64_tr_b16 a[208:209], v216 offset:36992\n"
"v_mfma_f32_16x16x32_bf16 v[44:47], a[228:231], a[52:55], v[44:47]\n"
"v_mfma_f32_16x16x32_bf16 v[44:47], a[232:235], a[56:59], v[44:47]\n"
"ds_read_b64_tr_b16 a[210:211], v217 offset:36992\n"
"v_mfma_f32_16x16x32_bf16 v[44:47], a[236:239], a[60:63], v[44:47]\n"
"v_mfma_f32_16x16x32_bf16 v[40:43], a[236:239], a[44:47], v[40:43]\n"
"ds_read_b64_tr_b16 a[212:213], v216 offset:46208\n"
"v_mfma_f32_16x16x32_bf16 v[40:43], a[232:235], a[40:43], v[40:43]\n"
"v_mfma_f32_16x16x32_bf16 v[40:43], a[228:231], a[36:39], v[40:43]\n"
"ds_read_b64_tr_b16 a[214:215], v217 offset:46208\n"
"v_mfma_f32_16x16x32_bf16 v[40:43], a[224:227], a[32:35], v[40:43]\n"
"v_mfma_f32_16x16x32_bf16 v[36:39], a[224:227], a[16:19], v[36:39]\n"
"ds_read_b64_tr_b16 a[216:217], v216 offset:55424\n"
"v_mfma_f32_16x16x32_bf16 v[36:39], a[228:231], a[20:23], v[36:39]\n"
"v_mfma_f32_16x16x32_bf16 v[36:39], a[232:235], a[24:27], v[36:39]\n"
"ds_read_b64_tr_b16 a[218:219], v217 offset:55424\n"
"v_mfma_f32_16x16x32_bf16 v[36:39], a[236:239], a[28:31], v[36:39]\n"
"v_mfma_f32_16x16x32_bf16 v[32:35], a[236:239], a[12:15], v[32:35]\n"
"ds_read_b64_tr_b16 a[220:221], v216 offset:64640\n"
"v_mfma_f32_16x16x32_bf16 v[32:35], a[232:235], a[8:11], v[32:35]\n"
"v_mfma_f32_16x16x32_bf16 v[32:35], a[228:231], a[4:7], v[32:35]\n"
"ds_read_b64_tr_b16 a[222:223], v217 offset:64640\n"
"v_mfma_f32_16x16x32_bf16 v[32:35], a[224:227], a[0:3], v[32:35]\n"
"s_waitcnt lgkmcnt(8)\n"
"v_mfma_f32_16x16x32_bf16 v[48:51], a[192:195], a[0:3], v[48:51]\n"
"v_mfma_f32_16x16x32_bf16 v[48:51], a[196:199], a[4:7], v[48:51]\n"
"v_mfma_f32_16x16x32_bf16 v[48:51], a[200:203], a[8:11], v[48:51]\n"
"v_mfma_f32_16x16x32_bf16 v[48:51], a[204:207], a[12:15], v[48:51]\n"
"s_waitcnt vmcnt(16) lgkmcnt(0)\n"
"v_mfma_f32_16x16x32_bf16 v[52:55], a[204:207], a[28:31], v[52:55]\n"
"s_barrier\n"
"v_mfma_f32_16x16x32_bf16 v[52:55], a[200:203], a[24:27], v[52:55]\n"
"v_mfma_f32_16x16x32_bf16 v[52:55], a[196:199], a[20:23], v[52:55]\n"
"ds_read_b64_tr_b16 a[224:225], v218\n"
"ds_read_b64_tr_b16 a[226:227], v219\n"
"v_mfma_f32_16x16x32_bf16 v[52:55], a[192:195], a[16:19], v[52:55]\n"
"v_mfma_f32_16x16x32_bf16 v[56:59], a[192:195], a[32:35], v[56:59]\n"
"ds_read_b64_tr_b16 a[228:229], v218 offset:9216\n"
"ds_read_b64_tr_b16 a[230:231], v219 offset:9216\n"
"v_mfma_f32_16x16x32_bf16 v[56:59], a[196:199], a[36:39], v[56:59]\n"
"v_mfma_f32_16x16x32_bf16 v[56:59], a[200:203], a[40:43], v[56:59]\n"
"ds_read_b64_tr_b16 a[232:233], v218 offset:18432\n"
"ds_read_b64_tr_b16 a[234:235], v219 offset:18432\n"
"v_mfma_f32_16x16x32_bf16 v[56:59], a[204:207], a[44:47], v[56:59]\n"
"v_mfma_f32_16x16x32_bf16 v[60:63], a[204:207], a[60:63], v[60:63]\n"
"ds_read_b64_tr_b16 a[236:237], v218 offset:27648\n"
"v_mfma_f32_16x16x32_bf16 v[60:63], a[200:203], a[56:59], v[60:63]\n"
"v_mfma_f32_16x16x32_bf16 v[60:63], a[196:199], a[52:55], v[60:63]\n"
"ds_read_b64_tr_b16 a[238:239], v219 offset:27648\n"
"v_mfma_f32_16x16x32_bf16 v[60:63], a[192:195], a[48:51], v[60:63]\n"
"v_mfma_f32_16x16x32_bf16 v[76:79], a[208:211], a[48:51], v[76:79]\n"
"ds_read_b64_tr_b16 a[192:193], v218 offset:32\n"
"v_mfma_f32_16x16x32_bf16 v[76:79], a[212:215], a[52:55], v[76:79]\n"
"v_mfma_f32_16x16x32_bf16 v[76:79], a[216:219], a[56:59], v[76:79]\n"
"ds_read_b64_tr_b16 a[194:195], v219 offset:32\n"
"v_mfma_f32_16x16x32_bf16 v[76:79], a[220:223], a[60:63], v[76:79]\n"
"v_mfma_f32_16x16x32_bf16 v[72:75], a[220:223], a[44:47], v[72:75]\n"
"ds_read_b64_tr_b16 a[196:197], v218 offset:9248\n"
"v_mfma_f32_16x16x32_bf16 v[72:75], a[216:219], a[40:43], v[72:75]\n"
"v_mfma_f32_16x16x32_bf16 v[72:75], a[212:215], a[36:39], v[72:75]\n"
"ds_read_b64_tr_b16 a[198:199], v219 offset:9248\n"
"v_mfma_f32_16x16x32_bf16 v[72:75], a[208:211], a[32:35], v[72:75]\n"
"v_mfma_f32_16x16x32_bf16 v[68:71], a[208:211], a[16:19], v[68:71]\n"
"ds_read_b64_tr_b16 a[200:201], v218 offset:18464\n"
"v_mfma_f32_16x16x32_bf16 v[68:71], a[212:215], a[20:23], v[68:71]\n"
"v_mfma_f32_16x16x32_bf16 v[68:71], a[216:219], a[24:27], v[68:71]\n"
"ds_read_b64_tr_b16 a[202:203], v219 offset:18464\n"
"v_mfma_f32_16x16x32_bf16 v[68:71], a[220:223], a[28:31], v[68:71]\n"
"v_mfma_f32_16x16x32_bf16 v[64:67], a[220:223], a[12:15], v[64:67]\n"
"ds_read_b64_tr_b16 a[204:205], v218 offset:27680\n"
"v_mfma_f32_16x16x32_bf16 v[64:67], a[216:219], a[8:11], v[64:67]\n"
"v_mfma_f32_16x16x32_bf16 v[64:67], a[212:215], a[4:7], v[64:67]\n"
"ds_read_b64_tr_b16 a[206:207], v219 offset:27680\n"
"v_mfma_f32_16x16x32_bf16 v[64:67], a[208:211], a[0:3], v[64:67]\n"
"s_waitcnt vmcnt(0) lgkmcnt(8)\n"
"v_mfma_f32_16x16x32_bf16 v[4:7], a[224:227], a[80:83], v[4:7]\n"
"ds_read_b64_tr_b16 a[208:209], v218 offset:64\n"
"v_mfma_f32_16x16x32_bf16 v[4:7], a[228:231], a[84:87], v[4:7]\n"
"v_mfma_f32_16x16x32_bf16 v[4:7], a[232:235], a[88:91], v[4:7]\n"
"ds_read_b64_tr_b16 a[210:211], v219 offset:64\n"
"v_mfma_f32_16x16x32_bf16 v[4:7], a[236:239], a[92:95], v[4:7]\n"
"v_mfma_f32_16x16x32_bf16 v[8:11], a[236:239], a[108:111], v[8:11]\n"
"ds_read_b64_tr_b16 a[212:213], v218 offset:9280\n"
"v_mfma_f32_16x16x32_bf16 v[8:11], a[232:235], a[104:107], v[8:11]\n"
"v_mfma_f32_16x16x32_bf16 v[8:11], a[228:231], a[100:103], v[8:11]\n"
"ds_read_b64_tr_b16 a[214:215], v219 offset:9280\n"
"v_mfma_f32_16x16x32_bf16 v[8:11], a[224:227], a[96:99], v[8:11]\n"
"v_mfma_f32_16x16x32_bf16 v[12:15], a[224:227], a[112:115], v[12:15]\n"
"ds_read_b64_tr_b16 a[216:217], v218 offset:18496\n"
"v_mfma_f32_16x16x32_bf16 v[12:15], a[228:231], a[116:119], v[12:15]\n"
"v_mfma_f32_16x16x32_bf16 v[12:15], a[232:235], a[120:123], v[12:15]\n"
"ds_read_b64_tr_b16 a[218:219], v219 offset:18496\n"
"v_mfma_f32_16x16x32_bf16 v[12:15], a[236:239], a[124:127], v[12:15]\n"
"v_mfma_f32_16x16x32_bf16 v[0:3], a[236:239], a[76:79], v[0:3]\n"
"ds_read_b64_tr_b16 a[220:221], v218 offset:27712\n"
"v_mfma_f32_16x16x32_bf16 v[0:3], a[232:235], a[72:75], v[0:3]\n"
"v_mfma_f32_16x16x32_bf16 v[0:3], a[228:231], a[68:71], v[0:3]\n"
"ds_read_b64_tr_b16 a[222:223], v219 offset:27712\n"
"v_mfma_f32_16x16x32_bf16 v[0:3], a[224:227], a[64:67], v[0:3]\n"
"s_waitcnt lgkmcnt(8)\n"
"v_mfma_f32_16x16x32_bf16 v[20:23], a[192:195], a[80:83], v[20:23]\n"
"ds_read_b64_tr_b16 a[224:225], v218 offset:96\n"
"v_mfma_f32_16x16x32_bf16 v[20:23], a[196:199], a[84:87], v[20:23]\n"
"v_mfma_f32_16x16x32_bf16 v[20:23], a[200:203], a[88:91], v[20:23]\n"
"ds_read_b64_tr_b16 a[226:227], v219 offset:96\n"
"v_mfma_f32_16x16x32_bf16 v[20:23], a[204:207], a[92:95], v[20:23]\n"
"v_mfma_f32_16x16x32_bf16 v[24:27], a[204:207], a[108:111], v[24:27]\n"
"ds_read_b64_tr_b16 a[228:229], v218 offset:9312\n"
"v_mfma_f32_16x16x32_bf16 v[24:27], a[200:203], a[104:107], v[24:27]\n"
"v_mfma_f32_16x16x32_bf16 v[24:27], a[196:199], a[100:103], v[24:27]\n"
"ds_read_b64_tr_b16 a[230:231], v219 offset:9312\n"
"v_mfma_f32_16x16x32_bf16 v[24:27], a[192:195], a[96:99], v[24:27]\n"
"v_mfma_f32_16x16x32_bf16 v[28:31], a[192:195], a[112:115], v[28:31]\n"
"ds_read_b64_tr_b16 a[232:233], v218 offset:18528\n"
"v_mfma_f32_16x16x32_bf16 v[28:31], a[196:199], a[116:119], v[28:31]\n"
"v_mfma_f32_16x16x32_bf16 v[28:31], a[200:203], a[120:123], v[28:31]\n"
"ds_read_b64_tr_b16 a[234:235], v219 offset:18528\n"
"v_mfma_f32_16x16x32_bf16 v[28:31], a[204:207], a[124:127], v[28:31]\n"
"v_mfma_f32_16x16x32_bf16 v[16:19], a[204:207], a[76:79], v[16:19]\n"
"ds_read_b64_tr_b16 a[236:237], v218 offset:27744\n"
"v_cvt_pk_bf16_f32 v184, v4, v5\n"
"v_mfma_f32_16x16x32_bf16 v[16:19], a[200:203], a[72:75], v[16:19]\n"
"v_cvt_pk_bf16_f32 v185, v6, v7\n"
"v_cvt_pk_bf16_f32 v186, v20, v21\n"
"v_mfma_f32_16x16x32_bf16 v[16:19], a[196:199], a[68:71], v[16:19]\n"
"s_add_u32 s77, s85, 0\n"
"ds_read_b64_tr_b16 a[238:239], v219 offset:27744\n"
"v_cvt_pk_bf16_f32 v187, v22, v23\n"
"v_mfma_f32_16x16x32_bf16 v[16:19], a[192:195], a[64:67], v[16:19]\n"
"s_waitcnt lgkmcnt(8)\n"
"buffer_store_dwordx4 v[184:187], v232, s[52:55], s77 offen sc1\n"
"v_mfma_f32_16x16x32_bf16 v[36:39], a[208:211], a[80:83], v[36:39]\n"
"ds_read_b64_tr_b16 a[192:193], v218 offset:128\n"
"v_cvt_pk_bf16_f32 v188, v8, v9\n"
"v_mfma_f32_16x16x32_bf16 v[36:39], a[212:215], a[84:87], v[36:39]\n"
"v_cvt_pk_bf16_f32 v189, v10, v11\n"
"v_cvt_pk_bf16_f32 v190, v24, v25\n"
"v_mfma_f32_16x16x32_bf16 v[36:39], a[216:219], a[88:91], v[36:39]\n"
"s_add_u32 s77, s87, 0\n"
"ds_read_b64_tr_b16 a[194:195], v219 offset:128\n"
"v_cvt_pk_bf16_f32 v191, v26, v27\n"
"v_mfma_f32_16x16x32_bf16 v[36:39], a[220:223], a[92:95], v[36:39]\n"
"buffer_store_dwordx4 v[188:191], v232, s[52:55], s77 offen offset:1024 sc1\n"
"v_mfma_f32_16x16x32_bf16 v[40:43], a[220:223], a[108:111], v[40:43]\n"
"ds_read_b64_tr_b16 a[196:197], v218 offset:9344\n"
"v_cvt_pk_bf16_f32 v192, v12, v13\n"
"v_mfma_f32_16x16x32_bf16 v[40:43], a[216:219], a[104:107], v[40:43]\n"
"v_cvt_pk_bf16_f32 v193, v14, v15\n"
"v_cvt_pk_bf16_f32 v194, v28, v29\n"
"v_mfma_f32_16x16x32_bf16 v[40:43], a[212:215], a[100:103], v[40:43]\n"
"s_add_u32 s77, s68, 0\n"
"ds_read_b64_tr_b16 a[198:199], v219 offset:9344\n"
"v_cvt_pk_bf16_f32 v195, v30, v31\n"
"v_mfma_f32_16x16x32_bf16 v[40:43], a[208:211], a[96:99], v[40:43]\n"
"buffer_store_dwordx4 v[192:195], v232, s[52:55], s77 offen offset:2048 sc1\n"
"v_mfma_f32_16x16x32_bf16 v[44:47], a[208:211], a[112:115], v[44:47]\n"
"ds_read_b64_tr_b16 a[200:201], v218 offset:18560\n"
"v_mfma_f32_16x16x32_bf16 v[44:47], a[212:215], a[116:119], v[44:47]\n"
"v_mfma_f32_16x16x32_bf16 v[44:47], a[216:219], a[120:123], v[44:47]\n"
"ds_read_b64_tr_b16 a[202:203], v219 offset:18560\n"
"v_mfma_f32_16x16x32_bf16 v[44:47], a[220:223], a[124:127], v[44:47]\n"
"v_mfma_f32_16x16x32_bf16 v[32:35], a[220:223], a[76:79], v[32:35]\n"
"ds_read_b64_tr_b16 a[204:205], v218 offset:27776\n"
"v_mfma_f32_16x16x32_bf16 v[32:35], a[216:219], a[72:75], v[32:35]\n"
"v_mfma_f32_16x16x32_bf16 v[32:35], a[212:215], a[68:71], v[32:35]\n"
"ds_read_b64_tr_b16 a[206:207], v219 offset:27776\n"
"v_mfma_f32_16x16x32_bf16 v[32:35], a[208:211], a[64:67], v[32:35]\n"
"s_waitcnt lgkmcnt(8)\n"
"v_mfma_f32_16x16x32_bf16 v[52:55], a[224:227], a[80:83], v[52:55]\n"
"ds_read_b64_tr_b16 a[208:209], v216 offset:37024\n"
"v_mfma_f32_16x16x32_bf16 v[52:55], a[228:231], a[84:87], v[52:55]\n"
"v_mfma_f32_16x16x32_bf16 v[52:55], a[232:235], a[88:91], v[52:55]\n"
"ds_read_b64_tr_b16 a[210:211], v217 offset:37024\n"
"v_mfma_f32_16x16x32_bf16 v[52:55], a[236:239], a[92:95], v[52:55]\n"
"v_mfma_f32_16x16x32_bf16 v[56:59], a[236:239], a[108:111], v[56:59]\n"
"ds_read_b64_tr_b16 a[212:213], v216 offset:46240\n"
"v_mfma_f32_16x16x32_bf16 v[56:59], a[232:235], a[104:107], v[56:59]\n"
"v_mfma_f32_16x16x32_bf16 v[56:59], a[228:231], a[100:103], v[56:59]\n"
"ds_read_b64_tr_b16 a[214:215], v217 offset:46240\n"
"v_mfma_f32_16x16x32_bf16 v[56:59], a[224:227], a[96:99], v[56:59]\n"
"v_mfma_f32_16x16x32_bf16 v[60:63], a[224:227], a[112:115], v[60:63]\n"
"ds_read_b64_tr_b16 a[216:217], v216 offset:55456\n"
"v_mfma_f32_16x16x32_bf16 v[60:63], a[228:231], a[116:119], v[60:63]\n"
"v_mfma_f32_16x16x32_bf16 v[60:63], a[232:235], a[120:123], v[60:63]\n"
"ds_read_b64_tr_b16 a[218:219], v217 offset:55456\n"
"v_mfma_f32_16x16x32_bf16 v[60:63], a[236:239], a[124:127], v[60:63]\n"
"v_mfma_f32_16x16x32_bf16 v[48:51], a[236:239], a[76:79], v[48:51]\n"
"ds_read_b64_tr_b16 a[220:221], v216 offset:64672\n"
"v_cvt_pk_bf16_f32 v196, v36, v37\n"
"v_mfma_f32_16x16x32_bf16 v[48:51], a[232:235], a[72:75], v[48:51]\n"
"v_cvt_pk_bf16_f32 v197, v38, v39\n"
"v_cvt_pk_bf16_f32 v198, v52, v53\n"
"v_mfma_f32_16x16x32_bf16 v[48:51], a[228:231], a[68:71], v[48:51]\n"
"s_add_u32 s77, s85, 0\n"
"ds_read_b64_tr_b16 a[222:223], v217 offset:64672\n"
"v_cvt_pk_bf16_f32 v199, v54, v55\n"
"v_mfma_f32_16x16x32_bf16 v[48:51], a[224:227], a[64:67], v[48:51]\n"
"s_waitcnt lgkmcnt(8)\n"
"buffer_store_dwordx4 v[196:199], v232, s[52:55], s77 offen offset:3072 sc1\n"
"v_mfma_f32_16x16x32_bf16 v[68:71], a[192:195], a[80:83], v[68:71]\n"
"ds_read_b64_tr_b16 a[224:225], v218 offset:160\n"
"v_cvt_pk_bf16_f32 v200, v40, v41\n"
"v_mfma_f32_16x16x32_bf16 v[68:71], a[196:199], a[84:87], v[68:71]\n"
"v_cvt_pk_bf16_f32 v201, v42, v43\n"
"v_cvt_pk_bf16_f32 v202, v56, v57\n"
"v_mfma_f32_16x16x32_bf16 v[68:71], a[200:203], a[88:91], v[68:71]\n"
"s_add_u32 s77, s87, 4096\n"
"ds_read_b64_tr_b16 a[226:227], v219 offset:160\n"
"v_cvt_pk_bf16_f32 v203, v58, v59\n"
"v_mfma_f32_16x16x32_bf16 v[68:71], a[204:207], a[92:95], v[68:71]\n"
"buffer_store_dwordx4 v[200:203], v232, s[52:55], s77 offen sc1\n"
"v_mfma_f32_16x16x32_bf16 v[72:75], a[204:207], a[108:111], v[72:75]\n"
"ds_read_b64_tr_b16 a[228:229], v218 offset:9376\n"
"v_cvt_pk_bf16_f32 v180, v44, v45\n"
"v_mfma_f32_16x16x32_bf16 v[72:75], a[200:203], a[104:107], v[72:75]\n"
"v_cvt_pk_bf16_f32 v181, v46, v47\n"
"v_cvt_pk_bf16_f32 v182, v60, v61\n"
"v_mfma_f32_16x16x32_bf16 v[72:75], a[196:199], a[100:103], v[72:75]\n"
"s_add_u32 s77, s68, 4096\n"
"ds_read_b64_tr_b16 a[230:231], v219 offset:9376\n"
"v_cvt_pk_bf16_f32 v183, v62, v63\n"
"v_mfma_f32_16x16x32_bf16 v[72:75], a[192:195], a[96:99], v[72:75]\n"
"buffer_store_dwordx4 v[180:183], v232, s[52:55], s77 offen offset:1024 sc1\n"
"v_mfma_f32_16x16x32_bf16 v[76:79], a[192:195], a[112:115], v[76:79]\n"
"ds_read_b64_tr_b16 a[232:233], v218 offset:18592\n"
"v_mfma_f32_16x16x32_bf16 v[76:79], a[196:199], a[116:119], v[76:79]\n"
"v_mfma_f32_16x16x32_bf16 v[76:79], a[200:203], a[120:123], v[76:79]\n"
"ds_read_b64_tr_b16 a[234:235], v219 offset:18592\n"
"v_mfma_f32_16x16x32_bf16 v[76:79], a[204:207], a[124:127], v[76:79]\n"
"v_mfma_f32_16x16x32_bf16 v[64:67], a[204:207], a[76:79], v[64:67]\n"
"ds_read_b64_tr_b16 a[236:237], v218 offset:27808\n"
"v_mfma_f32_16x16x32_bf16 v[64:67], a[200:203], a[72:75], v[64:67]\n"
"v_mfma_f32_16x16x32_bf16 v[64:67], a[196:199], a[68:71], v[64:67]\n"
"ds_read_b64_tr_b16 a[238:239], v219 offset:27808\n"
"v_mfma_f32_16x16x32_bf16 v[64:67], a[192:195], a[64:67], v[64:67]\n"
"s_waitcnt lgkmcnt(8)\n"
"v_mfma_f32_16x16x32_bf16 v[92:95], a[208:211], a[48:51], v[92:95]\n"
"ds_read_b64_tr_b16 a[192:193], v216 offset:37056\n"
"v_mfma_f32_16x16x32_bf16 v[92:95], a[212:215], a[52:55], v[92:95]\n"
"v_mfma_f32_16x16x32_bf16 v[92:95], a[216:219], a[56:59], v[92:95]\n"
"ds_read_b64_tr_b16 a[194:195], v217 offset:37056\n"
"v_mfma_f32_16x16x32_bf16 v[92:95], a[220:223], a[60:63], v[92:95]\n"
"v_mfma_f32_16x16x32_bf16 v[88:91], a[220:223], a[44:47], v[88:91]\n"
"ds_read_b64_tr_b16 a[196:197], v216 offset:46272\n"
"v_mfma_f32_16x16x32_bf16 v[88:91], a[216:219], a[40:43], v[88:91]\n"
"v_mfma_f32_16x16x32_bf16 v[88:91], a[212:215], a[36:39], v[88:91]\n"
"ds_read_b64_tr_b16 a[198:199], v217 offset:46272\n"
"v_mfma_f32_16x16x32_bf16 v[88:91], a[208:211], a[32:35], v[88:91]\n"
"v_mfma_f32_16x16x32_bf16 v[84:87], a[208:211], a[16:19], v[84:87]\n"
"ds_read_b64_tr_b16 a[200:201], v216 offset:55488\n"
"v_mfma_f32_16x16x32_bf16 v[84:87], a[212:215], a[20:23], v[84:87]\n"
"v_mfma_f32_16x16x32_bf16 v[84:87], a[216:219], a[24:27], v[84:87]\n"
"ds_read_b64_tr_b16 a[202:203], v217 offset:55488\n"
"v_mfma_f32_16x16x32_bf16 v[84:87], a[220:223], a[28:31], v[84:87]\n"
"v_mfma_f32_16x16x32_bf16 v[80:83], a[220:223], a[12:15], v[80:83]\n"
"ds_read_b64_tr_b16 a[204:205], v216 offset:64704\n"
"v_mfma_f32_16x16x32_bf16 v[80:83], a[216:219], a[8:11], v[80:83]\n"
"v_mfma_f32_16x16x32_bf16 v[80:83], a[212:215], a[4:7], v[80:83]\n"
"ds_read_b64_tr_b16 a[206:207], v217 offset:64704\n"
"v_mfma_f32_16x16x32_bf16 v[80:83], a[208:211], a[0:3], v[80:83]\n"
"s_waitcnt lgkmcnt(8)\n"
"v_mfma_f32_16x16x32_bf16 v[84:87], a[224:227], a[80:83], v[84:87]\n"
"ds_read_b64_tr_b16 a[208:209], v218 offset:192\n"
"v_mfma_f32_16x16x32_bf16 v[84:87], a[228:231], a[84:87], v[84:87]\n"
"v_mfma_f32_16x16x32_bf16 v[84:87], a[232:235], a[88:91], v[84:87]\n"
"ds_read_b64_tr_b16 a[210:211], v219 offset:192\n"
"v_mfma_f32_16x16x32_bf16 v[84:87], a[236:239], a[92:95], v[84:87]\n"
"buffer_load_dword v251, v250, s[96:99], 0 offen sc1\n"
"v_mfma_f32_16x16x32_bf16 v[88:91], a[236:239], a[108:111], v[88:91]\n"
"ds_read_b64_tr_b16 a[212:213], v218 offset:9408\n"
"v_mfma_f32_16x16x32_bf16 v[88:91], a[232:235], a[104:107], v[88:91]\n"
"v_mfma_f32_16x16x32_bf16 v[88:91], a[228:231], a[100:103], v[88:91]\n"
"ds_read_b64_tr_b16 a[214:215], v219 offset:9408\n"
"v_mfma_f32_16x16x32_bf16 v[88:91], a[224:227], a[96:99], v[88:91]\n"
"v_mfma_f32_16x16x32_bf16 v[92:95], a[224:227], a[112:115], v[92:95]\n"
"ds_read_b64_tr_b16 a[216:217], v218 offset:18624\n"
"v_mfma_f32_16x16x32_bf16 v[92:95], a[228:231], a[116:119], v[92:95]\n"
"v_mfma_f32_16x16x32_bf16 v[92:95], a[232:235], a[120:123], v[92:95]\n"
"ds_read_b64_tr_b16 a[218:219], v219 offset:18624\n"
"v_mfma_f32_16x16x32_bf16 v[92:95], a[236:239], a[124:127], v[92:95]\n"
"v_mfma_f32_16x16x32_bf16 v[80:83], a[236:239], a[76:79], v[80:83]\n"
"ds_read_b64_tr_b16 a[220:221], v218 offset:27840\n"
"v_cvt_pk_bf16_f32 v184, v68, v69\n"
"v_mfma_f32_16x16x32_bf16 v[80:83], a[232:235], a[72:75], v[80:83]\n"
"v_cvt_pk_bf16_f32 v185, v70, v71\n"
"v_cvt_pk_bf16_f32 v186, v84, v85\n"
"v_mfma_f32_16x16x32_bf16 v[80:83], a[228:231], a[68:71], v[80:83]\n"
"s_add_u32 s77, s85, 4096\n"
"ds_read_b64_tr_b16 a[222:223], v219 offset:27840\n"
"v_cvt_pk_bf16_f32 v187, v86, v87\n"
"v_mfma_f32_16x16x32_bf16 v[80:83], a[224:227], a[64:67], v[80:83]\n"
"s_waitcnt lgkmcnt(8)\n"
"buffer_store_dwordx4 v[184:187], v232, s[52:55], s77 offen offset:2048 sc1\n"
"v_mfma_f32_16x16x32_bf16 v[108:111], a[192:195], a[48:51], v[108:111]\n"
"ds_read_b64_tr_b16 a[224:225], v216 offset:37088\n"
"v_cvt_pk_bf16_f32 v188, v72, v73\n"
"v_mfma_f32_16x16x32_bf16 v[108:111], a[196:199], a[52:55], v[108:111]\n"
"v_cvt_pk_bf16_f32 v189, v74, v75\n"
"v_cvt_pk_bf16_f32 v190, v88, v89\n"
"v_mfma_f32_16x16x32_bf16 v[108:111], a[200:203], a[56:59], v[108:111]\n"
"s_add_u32 s77, s87, 4096\n"
"ds_read_b64_tr_b16 a[226:227], v217 offset:37088\n"
"v_cvt_pk_bf16_f32 v191, v90, v91\n"
"v_mfma_f32_16x16x32_bf16 v[108:111], a[204:207], a[60:63], v[108:111]\n"
"buffer_store_dwordx4 v[188:191], v232, s[52:55], s77 offen offset:3072 sc1\n"
"v_mfma_f32_16x16x32_bf16 v[104:107], a[204:207], a[44:47], v[104:107]\n"
"ds_read_b64_tr_b16 a[228:229], v216 offset:46304\n"
"v_cvt_pk_bf16_f32 v192, v76, v77\n"
"v_mfma_f32_16x16x32_bf16 v[104:107], a[200:203], a[40:43], v[104:107]\n"
"v_cvt_pk_bf16_f32 v193, v78, v79\n"
"v_cvt_pk_bf16_f32 v194, v92, v93\n"
"v_mfma_f32_16x16x32_bf16 v[104:107], a[196:199], a[36:39], v[104:107]\n"
"s_add_u32 s77, s68, 8192\n"
"ds_read_b64_tr_b16 a[230:231], v217 offset:46304\n"
"v_cvt_pk_bf16_f32 v195, v94, v95\n"
"v_mfma_f32_16x16x32_bf16 v[104:107], a[192:195], a[32:35], v[104:107]\n"
"buffer_store_dwordx4 v[192:195], v232, s[52:55], s77 offen sc1\n"
"v_mfma_f32_16x16x32_bf16 v[100:103], a[192:195], a[16:19], v[100:103]\n"
"s_waitcnt vmcnt(4)\n"
"ds_read_b64_tr_b16 a[232:233], v216 offset:55520\n"
"v_mfma_f32_16x16x32_bf16 v[100:103], a[196:199], a[20:23], v[100:103]\n"
"buffer_store_dword v247, v224, s[96:99], 0 offen sc1\n"
"v_mfma_f32_16x16x32_bf16 v[100:103], a[200:203], a[24:27], v[100:103]\n"
"ds_read_b64_tr_b16 a[234:235], v217 offset:55520\n"
"v_mfma_f32_16x16x32_bf16 v[100:103], a[204:207], a[28:31], v[100:103]\n"
"v_mfma_f32_16x16x32_bf16 v[96:99], a[204:207], a[12:15], v[96:99]\n"
"ds_read_b64_tr_b16 a[236:237], v216 offset:64736\n"
"v_mfma_f32_16x16x32_bf16 v[96:99], a[200:203], a[8:11], v[96:99]\n"
"v_mfma_f32_16x16x32_bf16 v[96:99], a[196:199], a[4:7], v[96:99]\n"
"ds_read_b64_tr_b16 a[238:239], v217 offset:64736\n"
"v_mfma_f32_16x16x32_bf16 v[96:99], a[192:195], a[0:3], v[96:99]\n"
"s_waitcnt lgkmcnt(8)\n"
"v_mfma_f32_16x16x32_bf16 v[100:103], a[208:211], a[80:83], v[100:103]\n"
"ds_read_b64_tr_b16 a[192:193], v218 offset:224\n"
"v_mfma_f32_16x16x32_bf16 v[100:103], a[212:215], a[84:87], v[100:103]\n"
"v_mfma_f32_16x16x32_bf16 v[100:103], a[216:219], a[88:91], v[100:103]\n"
"ds_read_b64_tr_b16 a[194:195], v219 offset:224\n"
"v_mfma_f32_16x16x32_bf16 v[100:103], a[220:223], a[92:95], v[100:103]\n"
"v_mfma_f32_16x16x32_bf16 v[104:107], a[220:223], a[108:111], v[104:107]\n"
"ds_read_b64_tr_b16 a[196:197], v218 offset:9440\n"
"v_mfma_f32_16x16x32_bf16 v[104:107], a[216:219], a[104:107], v[104:107]\n"
"v_mfma_f32_16x16x32_bf16 v[104:107], a[212:215], a[100:103], v[104:107]\n"
"ds_read_b64_tr_b16 a[198:199], v219 offset:9440\n"
"v_mfma_f32_16x16x32_bf16 v[104:107], a[208:211], a[96:99], v[104:107]\n"
"v_mfma_f32_16x16x32_bf16 v[108:111], a[208:211], a[112:115], v[108:111]\n"
"ds_read_b64_tr_b16 a[200:201], v218 offset:18656\n"
"v_mfma_f32_16x16x32_bf16 v[108:111], a[212:215], a[116:119], v[108:111]\n"
"v_mfma_f32_16x16x32_bf16 v[108:111], a[216:219], a[120:123], v[108:111]\n"
"s_waitcnt vmcnt(4)\n"
"ds_read_b64_tr_b16 a[202:203], v219 offset:18656\n"
"v_cmp_eq_u32 vcc, s78, v251\n"
"s_nop 3\n"
"s_and_b32 s56, vcc_lo, 0xe\n"
"s_andn2_b32 s62, s62, s56\n"
"v_mfma_f32_16x16x32_bf16 v[108:111], a[220:223], a[124:127], v[108:111]\n"
"v_mfma_f32_16x16x32_bf16 v[96:99], a[220:223], a[76:79], v[96:99]\n"
"s_bitcmp1_b32 s56, 1\n"
"s_cselect_b32 s42, 3145728, 0\n"
"s_add_u32 s66, s84, 0\n"
"ds_read_b64_tr_b16 a[204:205], v218 offset:27872\n"
"v_mfma_f32_16x16x32_bf16 v[96:99], a[216:219], a[72:75], v[96:99]\n"
"buffer_load_dwordx4 a[128:131], v232, s[40:43], s66 offen sc1\n"
"v_mfma_f32_16x16x32_bf16 v[96:99], a[212:215], a[68:71], v[96:99]\n"
"ds_read_b64_tr_b16 a[206:207], v219 offset:27872\n"
"v_mfma_f32_16x16x32_bf16 v[96:99], a[208:211], a[64:67], v[96:99]\n"
"s_waitcnt lgkmcnt(8)\n"
"v_mfma_f32_16x16x32_bf16 v[124:127], a[224:227], a[48:51], v[124:127]\n"
"ds_read_b64_tr_b16 a[208:209], v216 offset:37120\n"
"v_mfma_f32_16x16x32_bf16 v[124:127], a[228:231], a[52:55], v[124:127]\n"
"v_mfma_f32_16x16x32_bf16 v[124:127], a[232:235], a[56:59], v[124:127]\n"
"s_add_u32 s66, s84, 0\n"
"ds_read_b64_tr_b16 a[210:211], v217 offset:37120\n"
"v_mfma_f32_16x16x32_bf16 v[124:127], a[236:239], a[60:63], v[124:127]\n"
"buffer_load_dwordx4 a[132:135], v232, s[40:43], s66 offen offset:3072 sc1\n"
"v_mfma_f32_16x16x32_bf16 v[120:123], a[236:239], a[44:47], v[120:123]\n"
"ds_read_b64_tr_b16 a[212:213], v216 offset:46336\n"
"v_mfma_f32_16x16x32_bf16 v[120:123], a[232:235], a[40:43], v[120:123]\n"
"v_mfma_f32_16x16x32_bf16 v[120:123], a[228:231], a[36:39], v[120:123]\n"
"ds_read_b64_tr_b16 a[214:215], v217 offset:46336\n"
"v_mfma_f32_16x16x32_bf16 v[120:123], a[224:227], a[32:35], v[120:123]\n"
"v_mfma_f32_16x16x32_bf16 v[116:119], a[224:227], a[16:19], v[116:119]\n"
"s_bitcmp1_b32 s56, 3\n"
"s_cselect_b32 s42, 3145728, 0\n"
"s_add_u32 s66, s84, 0\n"
"ds_read_b64_tr_b16 a[216:217], v216 offset:55552\n"
"v_mfma_f32_16x16x32_bf16 v[116:119], a[228:231], a[20:23], v[116:119]\n"
"buffer_load_dwordx4 a[168:171], v232, s[40:43], s66 offen offset:2048 sc1\n"
"v_mfma_f32_16x16x32_bf16 v[116:119], a[232:235], a[24:27], v[116:119]\n"
"ds_read_b64_tr_b16 a[218:219], v217 offset:55552\n"
"v_mfma_f32_16x16x32_bf16 v[116:119], a[236:239], a[28:31], v[116:119]\n"
"v_mfma_f32_16x16x32_bf16 v[112:115], a[236:239], a[12:15], v[112:115]\n"
"ds_read_b64_tr_b16 a[220:221], v216 offset:64768\n"
"v_mfma_f32_16x16x32_bf16 v[112:115], a[232:235], a[8:11], v[112:115]\n"
"v_mfma_f32_16x16x32_bf16 v[112:115], a[228:231], a[4:7], v[112:115]\n"
"ds_read_b64_tr_b16 a[222:223], v217 offset:64768\n"
"v_mfma_f32_16x16x32_bf16 v[112:115], a[224:227], a[0:3], v[112:115]\n"
"s_waitcnt lgkmcnt(8)\n"
"v_mfma_f32_16x16x32_bf16 v[116:119], a[192:195], a[80:83], v[116:119]\n"
"s_add_u32 s66, s84, 4096\n"
"ds_read_b64_tr_b16 a[224:225], v218 offset:256\n"
"v_mfma_f32_16x16x32_bf16 v[116:119], a[196:199], a[84:87], v[116:119]\n"
"buffer_load_dwordx4 a[172:175], v232, s[40:43], s66 offen offset:1024 sc1\n"
"v_mfma_f32_16x16x32_bf16 v[116:119], a[200:203], a[88:91], v[116:119]\n"
"ds_read_b64_tr_b16 a[226:227], v219 offset:256\n"
"v_mfma_f32_16x16x32_bf16 v[116:119], a[204:207], a[92:95], v[116:119]\n"
"v_mfma_f32_16x16x32_bf16 v[120:123], a[204:207], a[108:111], v[120:123]\n"
"ds_read_b64_tr_b16 a[228:229], v218 offset:9472\n"
"v_mfma_f32_16x16x32_bf16 v[120:123], a[200:203], a[104:107], v[120:123]\n"
"v_mfma_f32_16x16x32_bf16 v[120:123], a[196:199], a[100:103], v[120:123]\n"
"s_bitcmp1_b32 s56, 2\n"
"s_cselect_b32 s42, 3145728, 0\n"
"s_add_u32 s66, s84, 0\n"
"ds_read_b64_tr_b16 a[230:231], v219 offset:9472\n"
"v_mfma_f32_16x16x32_bf16 v[120:123], a[192:195], a[96:99], v[120:123]\n"
"buffer_load_dwordx4 a[148:151], v232, s[40:43], s66 offen offset:1024 sc1\n"
"v_mfma_f32_16x16x32_bf16 v[124:127], a[192:195], a[112:115], v[124:127]\n"
"ds_read_b64_tr_b16 a[232:233], v218 offset:18688\n"
"v_mfma_f32_16x16x32_bf16 v[124:127], a[196:199], a[116:119], v[124:127]\n"
"v_mfma_f32_16x16x32_bf16 v[124:127], a[200:203], a[120:123], v[124:127]\n"
"ds_read_b64_tr_b16 a[234:235], v219 offset:18688\n"
"v_mfma_f32_16x16x32_bf16 v[124:127], a[204:207], a[124:127], v[124:127]\n"
"v_mfma_f32_16x16x32_bf16 v[112:115], a[204:207], a[76:79], v[112:115]\n"
"ds_read_b64_tr_b16 a[236:237], v218 offset:27904\n"
"v_cvt_pk_bf16_f32 v196, v100, v101\n"
"v_mfma_f32_16x16x32_bf16 v[112:115], a[200:203], a[72:75], v[112:115]\n"
"v_cvt_pk_bf16_f32 v197, v102, v103\n"
"v_cvt_pk_bf16_f32 v198, v116, v117\n"
"v_mfma_f32_16x16x32_bf16 v[112:115], a[196:199], a[68:71], v[112:115]\n"
"s_add_u32 s77, s85, 8192\n"
"ds_read_b64_tr_b16 a[238:239], v219 offset:27904\n"
"v_cvt_pk_bf16_f32 v199, v118, v119\n"
"v_mfma_f32_16x16x32_bf16 v[112:115], a[192:195], a[64:67], v[112:115]\n"
"s_waitcnt lgkmcnt(8)\n"
"buffer_store_dwordx4 v[196:199], v232, s[52:55], s77 offen offset:1024 sc1\n"
"v_mfma_f32_16x16x32_bf16 v[140:143], a[208:211], a[48:51], v[140:143]\n"
"v_cvt_pk_bf16_f32 v200, v104, v105\n"
"v_cvt_pk_bf16_f32 v201, v106, v107\n"
"v_mfma_f32_16x16x32_bf16 v[140:143], a[212:215], a[52:55], v[140:143]\n"
"s_add_u32 s77, s87, 8192\n"
"v_cvt_pk_bf16_f32 v202, v120, v121\n"
"v_cvt_pk_bf16_f32 v203, v122, v123\n"
"v_mfma_f32_16x16x32_bf16 v[140:143], a[216:219], a[56:59], v[140:143]\n"
"buffer_store_dwordx4 v[200:203], v232, s[52:55], s77 offen offset:2048 sc1\n"
"v_mfma_f32_16x16x32_bf16 v[140:143], a[220:223], a[60:63], v[140:143]\n"
"v_cvt_pk_bf16_f32 v180, v108, v109\n"
"v_cvt_pk_bf16_f32 v181, v110, v111\n"
"v_mfma_f32_16x16x32_bf16 v[136:139], a[220:223], a[44:47], v[136:139]\n"
"s_add_u32 s77, s68, 8192\n"
"v_cvt_pk_bf16_f32 v182, v124, v125\n"
"v_cvt_pk_bf16_f32 v183, v126, v127\n"
"v_mfma_f32_16x16x32_bf16 v[136:139], a[216:219], a[40:43], v[136:139]\n"
"buffer_store_dwordx4 v[180:183], v232, s[52:55], s77 offen offset:3072 sc1\n"
"v_mfma_f32_16x16x32_bf16 v[136:139], a[212:215], a[36:39], v[136:139]\n"
"s_add_u32 s66, s84, 4096\n"
"v_mfma_f32_16x16x32_bf16 v[136:139], a[208:211], a[32:35], v[136:139]\n"
"buffer_load_dwordx4 a[152:155], v232, s[40:43], s66 offen sc1\n"
"v_mfma_f32_16x16x32_bf16 v[132:135], a[208:211], a[16:19], v[132:135]\n"
"v_mfma_f32_16x16x32_bf16 v[132:135], a[212:215], a[20:23], v[132:135]\n"
"v_mfma_f32_16x16x32_bf16 v[132:135], a[216:219], a[24:27], v[132:135]\n"
"v_mfma_f32_16x16x32_bf16 v[132:135], a[220:223], a[28:31], v[132:135]\n"
"v_mfma_f32_16x16x32_bf16 v[128:131], a[220:223], a[12:15], v[128:131]\n"
"v_mfma_f32_16x16x32_bf16 v[128:131], a[216:219], a[8:11], v[128:131]\n"
"v_mfma_f32_16x16x32_bf16 v[128:131], a[212:215], a[4:7], v[128:131]\n"
"v_mfma_f32_16x16x32_bf16 v[128:131], a[208:211], a[0:3], v[128:131]\n"
"s_waitcnt lgkmcnt(0)\n"
"v_mfma_f32_16x16x32_bf16 v[132:135], a[224:227], a[80:83], v[132:135]\n"
"v_mfma_f32_16x16x32_bf16 v[132:135], a[228:231], a[84:87], v[132:135]\n"
"v_mfma_f32_16x16x32_bf16 v[132:135], a[232:235], a[88:91], v[132:135]\n"
"v_mfma_f32_16x16x32_bf16 v[132:135], a[236:239], a[92:95], v[132:135]\n"
"v_mfma_f32_16x16x32_bf16 v[136:139], a[236:239], a[108:111], v[136:139]\n"
"v_mfma_f32_16x16x32_bf16 v[136:139], a[232:235], a[104:107], v[136:139]\n"
"v_mfma_f32_16x16x32_bf16 v[136:139], a[228:231], a[100:103], v[136:139]\n"
"v_mfma_f32_16x16x32_bf16 v[136:139], a[224:227], a[96:99], v[136:139]\n"
"v_mfma_f32_16x16x32_bf16 v[140:143], a[224:227], a[112:115], v[140:143]\n"
"v_mfma_f32_16x16x32_bf16 v[140:143], a[228:231], a[116:119], v[140:143]\n"
"v_mfma_f32_16x16x32_bf16 v[140:143], a[232:235], a[120:123], v[140:143]\n"
"v_mfma_f32_16x16x32_bf16 v[140:143], a[236:239], a[124:127], v[140:143]\n"
"v_mfma_f32_16x16x32_bf16 v[128:131], a[236:239], a[76:79], v[128:131]\n"
"v_mfma_f32_16x16x32_bf16 v[128:131], a[232:235], a[72:75], v[128:131]\n"
"v_mfma_f32_16x16x32_bf16 v[128:131], a[228:231], a[68:71], v[128:131]\n"
"v_mfma_f32_16x16x32_bf16 v[128:131], a[224:227], a[64:67], v[128:131]\n"
"L_end2%=:\n"
"s_add_u32 s77, s85, 12288\n"
"v_cvt_pk_bf16_f32 v184, v132, v133\n"
"v_cvt_pk_bf16_f32 v185, v134, v135\n"
"buffer_store_dwordx2 v[184:185], v223, s[52:55], s77 offen sc1\n"
"s_add_u32 s77, s87, 12288\n"
"v_cvt_pk_bf16_f32 v188, v136, v137\n"
"v_cvt_pk_bf16_f32 v189, v138, v139\n"
"buffer_store_dwordx2 v[188:189], v223, s[52:55], s77 offen offset:512 sc1\n"
"s_add_u32 s77, s68, 12288\n"
"v_cvt_pk_bf16_f32 v192, v140, v141\n"
"v_cvt_pk_bf16_f32 v193, v142, v143\n"
"buffer_store_dwordx2 v[192:193], v223, s[52:55], s77 offen offset:1024 sc1\n"
"buffer_load_dword v251, v250, s[96:99], 0 offen sc1\n"
"s_waitcnt vmcnt(1)\n"
"buffer_store_dword v247, v224, s[96:99], 0 offen offset:512 sc1\n"
"s_waitcnt vmcnt(1)\n"
"s_cmp_eq_u32 s62, 0\n"
"s_cbranch_scc1 L_got%=\n"
"L_col%=:\n"
"v_cmp_eq_u32 vcc, s78, v251\n"
"s_nop 3\n"
"s_and_b32 s56, vcc_lo, s62\n"
"s_cbranch_scc0 L_again%=\n"
"s_andn2_b32 s62, s62, s56\n"
"s_bitcmp1_b32 s56, 1\n"
"s_cbranch_scc0 L_sk01%=\n"
"s_add_u32 s66, s84, 0\n"
"buffer_load_dwordx4 a[128:131], v232, s[52:55], s66 offen sc1\n"
"s_add_u32 s66, s84, 0\n"
"buffer_load_dwordx4 a[132:135], v232, s[52:55], s66 offen offset:3072 sc1\n"
"L_sk01%=:\n"
"s_bitcmp1_b32 s56, 2\n"
"s_cbranch_scc0 L_sk02%=\n"
"s_add_u32 s66, s84, 0\n"
"buffer_load_dwordx4 a[148:151], v232, s[52:55], s66 offen offset:1024 sc1\n"
"s_add_u32 s66, s84, 4096\n"
"buffer_load_dwordx4 a[152:155], v232, s[52:55], s66 offen sc1\n"
"L_sk02%=:\n"
"s_bitcmp1_b32 s56, 3\n"
"s_cbranch_scc0 L_sk03%=\n"
"s_add_u32 s66, s84, 0\n"
"buffer_load_dwordx4 a[168:171], v232, s[52:55], s66 offen offset:2048 sc1\n"
"s_add_u32 s66, s84, 4096\n"
"buffer_load_dwordx4 a[172:175], v232, s[52:55], s66 offen offset:1024 sc1\n"
"L_sk03%=:\n"
"s_bitcmp1_b32 s56, 5\n"
"s_cbranch_scc0 L_sk11%=\n"
"s_add_u32 s66, s84, 4096\n"
"buffer_load_dwordx4 a[136:139], v232, s[52:55], s66 offen offset:2048 sc1\n"
"s_add_u32 s66, s84, 8192\n"
"buffer_load_dwordx4 a[140:143], v232, s[52:55], s66 offen offset:1024 sc1\n"
"s_add_u32 s66, s84, 12288\n"
"buffer_load_dwordx2 a[144:145], v223, s[52:55], s66 offen sc1\n"
"L_sk11%=:\n"
"s_bitcmp1_b32 s56, 6\n"
"s_cbranch_scc0 L_sk12%=\n"
"s_add_u32 s66, s84, 4096\n"
"buffer_load_dwordx4 a[156:159], v232, s[52:55], s66 offen offset:3072 sc1\n"
"s_add_u32 s66, s84, 8192\n"
"buffer_load_dwordx4 a[160:163], v232, s[52:55], s66 offen offset:2048 sc1\n"
"s_add_u32 s66, s84, 12288\n"
"buffer_load_dwordx2 a[164:165], v223, s[52:55], s66 offen offset:512 sc1\n"
"L_sk12%=:\n"
"s_bitcmp1_b32 s56, 7\n"
"s_cbranch_scc0 L_sk13%=\n"
"s_add_u32 s66, s84, 8192\n"
"buffer_load_dwordx4 a[176:179], v232, s[52:55], s66 offen sc1\n"
"s_add_u32 s66, s84, 8192\n"
"buffer_load_dwordx4 a[180:183], v232, s[52:55], s66 offen offset:3072 sc1\n"
"s_add_u32 s66, s84, 12288\n"
"buffer_load_dwordx2 a[184:185], v223, s[52:55], s66 offen offset:1024 sc1\n"
"L_sk13%=:\n"
"s_cmp_eq_u32 s62, 0\n"
"s_cbranch_scc1 L_gw%=\n"
"L_again%=:\n"
"s_sub_u32 s64, s64, 1\n"
"s_cbranch_scc1 L_gw%=\n"
"buffer_load_dword v251, v250, s[96:99], 0 offen sc1\n"
"s_waitcnt vmcnt(0)\n"
"s_branch L_col%=\n"
"L_gw%=:\n"
"s_and_b32 s72, s56, 0xe\n"
"s_cbranch_scc0 L_got%=\n"
"s_waitcnt vmcnt(0)\n"
"L_got%=:\n"
"v_mfma_f32_16x16x32_bf16 v[0:3], v[210:213], a[128:131], v[0:3]\n"
"v_mfma_f32_16x16x32_bf16 v[0:3], v[210:213], a[148:151], v[0:3]\n"
"v_mfma_f32_16x16x32_bf16 v[0:3], v[210:213], a[168:171], v[0:3]\n"
"v_mfma_f32_16x16x32_bf16 v[16:19], v[208:211], a[128:131], v[16:19]\n"
"v_mfma_f32_16x16x32_bf16 v[16:19], v[208:211], a[148:151], v[16:19]\n"
"v_mfma_f32_16x16x32_bf16 v[16:19], v[208:211], a[168:171], v[16:19]\n"
"v_mfma_f32_16x16x32_bf16 v[32:35], v[210:213], a[132:135], v[32:35]\n"
"v_mfma_f32_16x16x32_bf16 v[32:35], v[210:213], a[152:155], v[32:35]\n"
"v_mfma_f32_16x16x32_bf16 v[32:35], v[210:213], a[172:175], v[32:35]\n"
"v_mfma_f32_16x16x32_bf16 v[48:51], v[208:211], a[132:135], v[48:51]\n"
"s_nop 0\n"
"v_cvt_pk_bf16_f32 v0, v0, v1\n"
"v_cvt_pk_bf16_f32 v1, v2, v3\n"
"v_mfma_f32_16x16x32_bf16 v[48:51], v[208:211], a[152:155], v[48:51]\n"
"buffer_store_dwordx2 v[0:1], v220, s[36:39], 0 offen\n"
"v_mfma_f32_16x16x32_bf16 v[48:51], v[208:211], a[172:175], v[48:51]\n"
"v_cvt_pk_bf16_f32 v16, v16, v17\n"
"v_cvt_pk_bf16_f32 v17, v18, v19\n"
"buffer_store_dwordx2 v[16:17], v220, s[36:39], 0 offen offset:32\n"
"v_cvt_pk_bf16_f32 v32, v32, v33\n"
"v_cvt_pk_bf16_f32 v33, v34, v35\n"
"buffer_store_dwordx2 v[32:33], v220, s[36:39], 0 offen offset:64\n"
"s_nop 1\n"
"v_cvt_pk_bf16_f32 v48, v48, v49\n"
"v_cvt_pk_bf16_f32 v49, v50, v51\n"
"buffer_store_dwordx2 v[48:49], v220, s[36:39], 0 offen offset:96\n"
"s_waitcnt vmcnt(4)\n"
"v_mfma_f32_16x16x32_bf16 v[64:67], v[210:213], a[136:139], v[64:67]\n"
"v_mfma_f32_16x16x32_bf16 v[64:67], v[210:213], a[156:159], v[64:67]\n"
"v_mfma_f32_16x16x32_bf16 v[64:67], v[210:213], a[176:179], v[64:67]\n"
"v_mfma_f32_16x16x32_bf16 v[80:83], v[208:211], a[136:139], v[80:83]\n"
"v_mfma_f32_16x16x32_bf16 v[80:83], v[208:211], a[156:159], v[80:83]\n"
"v_mfma_f32_16x16x32_bf16 v[80:83], v[208:211], a[176:179], v[80:83]\n"
"v_mfma_f32_16x16x32_bf16 v[96:99], v[210:213], a[140:143], v[96:99]\n"
"v_mfma_f32_16x16x32_bf16 v[96:99], v[210:213], a[160:163], v[96:99]\n"
"v_mfma_f32_16x16x32_bf16 v[96:99], v[210:213], a[180:183], v[96:99]\n"
"v_mfma_f32_16x16x32_bf16 v[112:115], v[208:211], a[140:143], v[112:115]\n"
"s_nop 0\n"
"v_cvt_pk_bf16_f32 v64, v64, v65\n"
"v_cvt_pk_bf16_f32 v65, v66, v67\n"
"v_mfma_f32_16x16x32_bf16 v[112:115], v[208:211], a[160:163], v[112:115]\n"
"buffer_store_dwordx2 v[64:65], v220, s[36:39], 0 offen offset:128\n"
"v_mfma_f32_16x16x32_bf16 v[112:115], v[208:211], a[180:183], v[112:115]\n"
"v_mfma_f32_16x16x32_bf16 v[128:131], v[210:213], a[144:147], v[128:131]\n"
"v_cvt_pk_bf16_f32 v80, v80, v81\n"
"v_cvt_pk_bf16_f32 v81, v82, v83\n"
"v_mfma_f32_16x16x32_bf16 v[128:131], v[210:213], a[164:167], v[128:131]\n"
"buffer_store_dwordx2 v[80:81], v220, s[36:39], 0 offen offset:160\n"
"v_mfma_f32_16x16x32_bf16 v[128:131], v[210:213], a[184:187], v[128:131]\n"
"v_cvt_pk_bf16_f32 v96, v96, v97\n"
"v_cvt_pk_bf16_f32 v97, v98, v99\n"
"buffer_store_dwordx2 v[96:97], v220, s[36:39], 0 offen offset:192\n"
"v_cvt_pk_bf16_f32 v112, v112, v113\n"
"v_cvt_pk_bf16_f32 v113, v114, v115\n"
"buffer_store_dwordx2 v[112:113], v220, s[36:39], 0 offen offset:224\n"
"s_nop 1\n"
"v_cvt_pk_bf16_f32 v128, v128, v129\n"
"v_cvt_pk_bf16_f32 v129, v130, v131\n"
"buffer_store_dwordx2 v[128:129], v220, s[36:39], 0 offen offset:256\n"
"s_branch L_end%=\n"
"L_wL0%=:\n"
"s_memrealtime s[34:35]\n"
"s_waitcnt lgkmcnt(0)\n"
"s_sub_u32 s35, s34, s73\n"
"s_cmp_lt_i32 s35, 0\n"
"s_cbranch_scc1 L_wL0%=\n"
"s_sub_u32 s66, s66, s76\n"
"s_memtime s[76:77]\n"
"s_waitcnt lgkmcnt(0)\n"
"s_add_u32 s66, s66, s76\n"
"s_branch L_wbL0%=\n"
"L_wL1%=:\n"
"s_memrealtime s[34:35]\n"
"s_waitcnt lgkmcnt(0)\n"
"s_sub_u32 s35, s34, s73\n"
"s_cmp_lt_i32 s35, 0\n"
"s_cbranch_scc1 L_wL1%=\n"
"s_sub_u32 s66, s66, s76\n"
"s_memtime s[76:77]\n"
"s_waitcnt lgkmcnt(0)\n"
"s_add_u32 s66, s66, s76\n"
"s_branch L_wbL1%=\n"
"L_wL2%=:\n"
"s_memrealtime s[34:35]\n"
"s_waitcnt lgkmcnt(0)\n"
"s_sub_u32 s35, s34, s73\n"
"s_cmp_lt_i32 s35, 0\n"
"s_cbranch_scc1 L_wL2%=\n"
"s_sub_u32 s66, s66, s76\n"
"s_memtime s[76:77]\n"
"s_waitcnt lgkmcnt(0)\n"
"s_add_u32 s66, s66, s76\n"
"s_branch L_wbL2%=\n"
"L_wL3%=:\n"
"s_memrealtime s[34:35]\n"
"s_waitcnt lgkmcnt(0)\n"
"s_sub_u32 s35, s34, s73\n"
"s_cmp_lt_i32 s35, 0\n"
"s_cbranch_scc1 L_wL3%=\n"
"s_sub_u32 s66, s66, s76\n"
"s_memtime s[76:77]\n"
"s_waitcnt lgkmcnt(0)\n"
"s_add_u32 s66, s66, s76\n"
"s_branch L_wbL3%=\n"
"L_rt1%=:\n"
"s_sub_u32 s64, s64, 1\n"
"s_cbranch_scc1 L_to1%=\n"
"s_sleep 1\n"
"v_lshrrev_b32 v247, 2, v232\n"
"buffer_load_dword v247, v247, s[96:99], 0 offen sc1\n"
"s_mov_b32 s98, s79\n"
"s_waitcnt vmcnt(0)\n"
"buffer_store_dword v247, v248, s[96:99], 0 offen sc1\n"
"s_mov_b32 s98, 139264\n"
"buffer_load_dword v251, v250, s[96:99], 0 offen sc1\n"
"s_waitcnt vmcnt(0)\n"
"s_branch L_chk1%=\n"
"L_to1%=:\n"
"s_mov_b32 s64, 0\n"
"s_branch L_ok1%=\n"
"L_rt2e%=:\n"
"s_sub_u32 s64, s64, 1\n"
"s_cbranch_scc1 L_to2e%=\n"
"s_sleep 1\n"
"buffer_load_dword v251, v250, s[96:99], 0 offen sc1\n"
"s_waitcnt vmcnt(0)\n"
"s_branch L_chk2e%=\n"
"L_to2e%=:\n"
"s_mov_b32 s64, 0\n"
"s_branch L_ok2e%=\n"
"L_rt2o%=:\n"
"s_sub_u32 s64, s64, 1\n"
"s_cbranch_scc1 L_to2o%=\n"
"s_sleep 1\n"
"buffer_load_dword v251, v250, s[96:99], 0 offen sc1\n"
"s_waitcnt vmcnt(0)\n"
"s_branch L_chk2o%=\n"
"L_to2o%=:\n"
"s_mov_b32 s64, 0\n"
"s_branch L_ok2o%=\n"
"L_slow%=:\n"
"s_waitcnt vmcnt(0) lgkmcnt(0)\n"
"s_barrier\n"
"s_mov_b32 s73, 16\n"
"v_mov_b32 v236, 0xff800000\n"
"v_mov_b32 v237, 0xff800000\n"
"v_mov_b32 v238, 0xff800000\n"
"v_mov_b32 v239, 0xff800000\n"
"L_sl1%=:\n"
"s_add_u32 m0, s61, 0\n"
"s_nop 0\n"
"buffer_load_dwordx4 v222, s[36:39], s56 offen nt lds\n"
"buffer_load_dwordx4 v222, s[36:39], s56 offen offset:1024 nt lds\n"
"buffer_load_dwordx4 v223, s[36:39], s56 offen offset:2048 nt lds\n"
"buffer_load_dwordx4 v223, s[36:39], s56 offen offset:3072 nt lds\n"
"s_sub_u32 s56, s56, 159744\n"
"s_and_b32 s56, s56, 159744\n"
"s_waitcnt vmcnt(0)\n"
"s_barrier\n"
"ds_read_b128 a[128:131], v216\n"
"ds_read_b128 a[132:135], v217\n"
"ds_read_b128 a[136:139], v218\n"
"ds_read_b128 a[140:143], v219\n"
"s_waitcnt lgkmcnt(3)\n"
"v_mfma_f32_16x16x32_bf16 v[0:3], a[128:131], v[64:67], 0\n"
"s_waitcnt lgkmcnt(2)\n"
"v_mfma_f32_16x16x32_bf16 v[0:3], a[132:135], v[68:71], v[0:3]\n"
"s_waitcnt lgkmcnt(1)\n"
"v_mfma_f32_16x16x32_bf16 v[0:3], a[136:139], v[72:75], v[0:3]\n"
"s_waitcnt lgkmcnt(0)\n"
"v_mfma_f32_16x16x32_bf16 v[0:3], a[140:143], v[76:79], v[0:3]\n"
"v_mfma_f32_16x16x32_bf16 v[4:7], a[128:131], v[80:83], 0\n"
"v_mfma_f32_16x16x32_bf16 v[4:7], a[132:135], v[84:87], v[4:7]\n"
"v_mfma_f32_16x16x32_bf16 v[4:7], a[136:139], v[88:91], v[4:7]\n"
"v_mfma_f32_16x16x32_bf16 v[4:7], a[140:143], v[92:95], v[4:7]\n"
"v_mfma_f32_16x16x32_bf16 v[8:11], a[128:131], v[96:99], 0\n"
"v_mfma_f32_16x16x32_bf16 v[8:11], a[132:135], v[100:103], v[8:11]\n"
"v_mfma_f32_16x16x32_bf16 v[8:11], a[136:139], v[104:107], v[8:11]\n"
"v_mfma_f32_16x16x32_bf16 v[8:11], a[140:143], v[108:111], v[8:11]\n"
"v_mfma_f32_16x16x32_bf16 v[12:15], a[128:131], v[112:115], 0\n"
"v_mfma_f32_16x16x32_bf16 v[12:15], a[132:135], v[116:119], v[12:15]\n"
"v_mfma_f32_16x16x32_bf16 v[12:15], a[136:139], v[120:123], v[12:15]\n"
"v_mfma_f32_16x16x32_bf16 v[12:15], a[140:143], v[124:127], v[12:15]\n"
"ds_read_b128 a[144:147], v216 offset:1024\n"
"ds_read_b128 a[148:151], v217 offset:1024\n"
"ds_read_b128 a[152:155], v218 offset:1024\n"
"ds_read_b128 a[156:159], v219 offset:1024\n"
"s_waitcnt lgkmcnt(3)\n"
"v_mfma_f32_16x16x32_bf16 v[16:19], a[144:147], v[64:67], 0\n"
"s_waitcnt lgkmcnt(2)\n"
"v_mfma_f32_16x16x32_bf16 v[16:19], a[148:151], v[68:71], v[16:19]\n"
"s_waitcnt lgkmcnt(1)\n"
"v_mfma_f32_16x16x32_bf16 v[16:19], a[152:155], v[72:75], v[16:19]\n"
"s_waitcnt lgkmcnt(0)\n"
"v_mfma_f32_16x16x32_bf16 v[16:19], a[156:159], v[76:79], v[16:19]\n"
"v_mfma_f32_16x16x32_bf16 v[20:23], a[144:147], v[80:83], 0\n"
"v_mfma_f32_16x16x32_bf16 v[20:23], a[148:151], v[84:87], v[20:23]\n"
"v_mfma_f32_16x16x32_bf16 v[20:23], a[152:155], v[88:91], v[20:23]\n"
"v_mfma_f32_16x16x32_bf16 v[20:23], a[156:159], v[92:95], v[20:23]\n"
"v_mfma_f32_16x16x32_bf16 v[24:27], a[144:147], v[96:99], 0\n"
"v_mfma_f32_16x16x32_bf16 v[24:27], a[148:151], v[100:103], v[24:27]\n"
"v_mfma_f32_16x16x32_bf16 v[24:27], a[152:155], v[104:107], v[24:27]\n"
"v_mfma_f32_16x16x32_bf16 v[24:27], a[156:159], v[108:111], v[24:27]\n"
"v_mfma_f32_16x16x32_bf16 v[28:31], a[144:147], v[112:115], 0\n"
"v_mfma_f32_16x16x32_bf16 v[28:31], a[148:151], v[116:119], v[28:31]\n"
"v_mfma_f32_16x16x32_bf16 v[28:31], a[152:155], v[120:123], v[28:31]\n"
"v_mfma_f32_16x16x32_bf16 v[28:31], a[156:159], v[124:127], v[28:31]\n"
"ds_read_b128 a[128:131], v216 offset:8192\n"
"ds_read_b128 a[132:135], v217 offset:8192\n"
"ds_read_b128 a[136:139], v218 offset:8192\n"
"ds_read_b128 a[140:143], v219 offset:8192\n"
"s_waitcnt lgkmcnt(3)\n"
"v_mfma_f32_16x16x32_bf16 v[32:35], a[128:131], v[64:67], 0\n"
"s_waitcnt lgkmcnt(2)\n"
"v_mfma_f32_16x16x32_bf16 v[32:35], a[132:135], v[68:71], v[32:35]\n"
"s_waitcnt lgkmcnt(1)\n"
"v_mfma_f32_16x16x32_bf16 v[32:35], a[136:139], v[72:75], v[32:35]\n"
"s_waitcnt lgkmcnt(0)\n"
"v_mfma_f32_16x16x32_bf16 v[32:35], a[140:143], v[76:79], v[32:35]\n"
"v_mfma_f32_16x16x32_bf16 v[36:39], a[128:131], v[80:83], 0\n"
"v_mfma_f32_16x16x32_bf16 v[36:39], a[132:135], v[84:87], v[36:39]\n"
"v_mfma_f32_16x16x32_bf16 v[36:39], a[136:139], v[88:91], v[36:39]\n"
"v_mfma_f32_16x16x32_bf16 v[36:39], a[140:143], v[92:95], v[36:39]\n"
"v_mfma_f32_16x16x32_bf16 v[40:43], a[128:131], v[96:99], 0\n"
"v_mfma_f32_16x16x32_bf16 v[40:43], a[132:135], v[100:103], v[40:43]\n"
"v_mfma_f32_16x16x32_bf16 v[40:43], a[136:139], v[104:107], v[40:43]\n"
"v_mfma_f32_16x16x32_bf16 v[40:43], a[140:143], v[108:111], v[40:43]\n"
"v_mfma_f32_16x16x32_bf16 v[44:47], a[128:131], v[112:115], 0\n"
"v_mfma_f32_16x16x32_bf16 v[44:47], a[132:135], v[116:119], v[44:47]\n"
"v_mfma_f32_16x16x32_bf16 v[44:47], a[136:139], v[120:123], v[44:47]\n"
"v_mfma_f32_16x16x32_bf16 v[44:47], a[140:143], v[124:127], v[44:47]\n"
"ds_read_b128 a[144:147], v216 offset:9216\n"
"ds_read_b128 a[148:151], v217 offset:9216\n"
"ds_read_b128 a[152:155], v218 offset:9216\n"
"ds_read_b128 a[156:159], v219 offset:9216\n"
"s_waitcnt lgkmcnt(3)\n"
"v_mfma_f32_16x16x32_bf16 v[48:51], a[144:147], v[64:67], 0\n"
"s_waitcnt lgkmcnt(2)\n"
"v_mfma_f32_16x16x32_bf16 v[48:51], a[148:151], v[68:71], v[48:51]\n"
"s_waitcnt lgkmcnt(1)\n"
"v_mfma_f32_16x16x32_bf16 v[48:51], a[152:155], v[72:75], v[48:51]\n"
"s_waitcnt lgkmcnt(0)\n"
"v_mfma_f32_16x16x32_bf16 v[48:51], a[156:159], v[76:79], v[48:51]\n"
"v_mfma_f32_16x16x32_bf16 v[52:55], a[144:147], v[80:83], 0\n"
"v_mfma_f32_16x16x32_bf16 v[52:55], a[148:151], v[84:87], v[52:55]\n"
"v_mfma_f32_16x16x32_bf16 v[52:55], a[152:155], v[88:91], v[52:55]\n"
"v_mfma_f32_16x16x32_bf16 v[52:55], a[156:159], v[92:95], v[52:55]\n"
"v_mfma_f32_16x16x32_bf16 v[56:59], a[144:147], v[96:99], 0\n"
"v_mfma_f32_16x16x32_bf16 v[56:59], a[148:151], v[100:103], v[56:59]\n"
"v_mfma_f32_16x16x32_bf16 v[56:59], a[152:155], v[104:107], v[56:59]\n"
"v_mfma_f32_16x16x32_bf16 v[56:59], a[156:159], v[108:111], v[56:59]\n"
"v_mfma_f32_16x16x32_bf16 v[60:63], a[144:147], v[112:115], 0\n"
"v_mfma_f32_16x16x32_bf16 v[60:63], a[148:151], v[116:119], v[60:63]\n"
"v_mfma_f32_16x16x32_bf16 v[60:63], a[152:155], v[120:123], v[60:63]\n"
"v_mfma_f32_16x16x32_bf16 v[60:63], a[156:159], v[124:127], v[60:63]\n"
"v_max3_f32 v236, v236, v0, v1\n"
"v_max3_f32 v236, v236, v2, v3\n"
"v_max3_f32 v237, v237, v4, v5\n"
"v_max3_f32 v237, v237, v6, v7\n"
"v_max3_f32 v238, v238, v8, v9\n"
"v_max3_f32 v238, v238, v10, v11\n"
"v_max3_f32 v239, v239, v12, v13\n"
"v_max3_f32 v239, v239, v14, v15\n"
"v_max3_f32 v236, v236, v16, v17\n"
"v_max3_f32 v236, v236, v18, v19\n"
"v_max3_f32 v237, v237, v20, v21\n"
"v_max3_f32 v237, v237, v22, v23\n"
"v_max3_f32 v238, v238, v24, v25\n"
"v_max3_f32 v238, v238, v26, v27\n"
"v_max3_f32 v239, v239, v28, v29\n"
"v_max3_f32 v239, v239, v30, v31\n"
"v_max3_f32 v236, v236, v32, v33\n"
"v_max3_f32 v236, v236, v34, v35\n"
"v_max3_f32 v237, v237, v36, v37\n"
"v_max3_f32 v237, v237, v38, v39\n"
"v_max3_f32 v238, v238, v40, v41\n"
"v_max3_f32 v238, v238, v42, v43\n"
"v_max3_f32 v239, v239, v44, v45\n"
"v_max3_f32 v239, v239, v46, v47\n"
"v_max3_f32 v236, v236, v48, v49\n"
"v_max3_f32 v236, v236, v50, v51\n"
"v_max3_f32 v237, v237, v52, v53\n"
"v_max3_f32 v237, v237, v54, v55\n"
"v_max3_f32 v238, v238, v56, v57\n"
"v_max3_f32 v238, v238, v58, v59\n"
"v_max3_f32 v239, v239, v60, v61\n"
"v_max3_f32 v239, v239, v62, v63\n"
"s_waitcnt lgkmcnt(0)\n"
"s_barrier\n"
"s_sub_u32 s73, s73, 1\n"
"s_cmp_lg_u32 s73, 0\n"
"s_cbranch_scc1 L_sl1%=\n"
"s_mov_b32 s73, 16\n"
"v_mov_b32 v240, v236\n"
"s_nop 1\n"
"v_permlane32_swap_b32 v236, v240\n"
"v_max_f32 v236, v236, v240\n"
"v_mov_b32 v240, v236\n"
"s_nop 1\n"
"v_permlane16_swap_b32 v236, v240\n"
"v_max_f32 v236, v236, v240\n"
"v_mul_f32 v192, -1.0, v236\n"
"v_mul_f32 v193, -1.0, v236\n"
"v_mul_f32 v194, -1.0, v236\n"
"v_mul_f32 v195, -1.0, v236\n"
"v_mov_b32 v241, v237\n"
"s_nop 1\n"
"v_permlane32_swap_b32 v237, v241\n"
"v_max_f32 v237, v237, v241\n"
"v_mov_b32 v241, v237\n"
"s_nop 1\n"
"v_permlane16_swap_b32 v237, v241\n"
"v_max_f32 v237, v237, v241\n"
"v_mul_f32 v196, -1.0, v237\n"
"v_mul_f32 v197, -1.0, v237\n"
"v_mul_f32 v198, -1.0, v237\n"
"v_mul_f32 v199, -1.0, v237\n"
"v_mov_b32 v242, v238\n"
"s_nop 1\n"
"v_permlane32_swap_b32 v238, v242\n"
"v_max_f32 v238, v238, v242\n"
"v_mov_b32 v242, v238\n"
"s_nop 1\n"
"v_permlane16_swap_b32 v238, v242\n"
"v_max_f32 v238, v238, v242\n"
"v_mul_f32 v200, -1.0, v238\n"
"v_mul_f32 v201, -1.0, v238\n"
"v_mul_f32 v202, -1.0, v238\n"
"v_mul_f32 v203, -1.0, v238\n"
"v_mov_b32 v243, v239\n"
"s_nop 1\n"
"v_permlane32_swap_b32 v239, v243\n"
"v_max_f32 v239, v239, v243\n"
"v_mov_b32 v243, v239\n"
"s_nop 1\n"
"v_permlane16_swap_b32 v239, v243\n"
"v_max_f32 v239, v239, v243\n"
"v_mul_f32 v204, -1.0, v239\n"
"v_mul_f32 v205, -1.0, v239\n"
"v_mul_f32 v206, -1.0, v239\n"
"v_mul_f32 v207, -1.0, v239\n"
"v_accvgpr_write_b32 a0, 0\n"
"v_accvgpr_write_b32 a1, 0\n"
"v_accvgpr_write_b32 a2, 0\n"
"v_accvgpr_write_b32 a3, 0\n"
"v_accvgpr_write_b32 a4, 0\n"
"v_accvgpr_write_b32 a5, 0\n"
"v_accvgpr_write_b32 a6, 0\n"
"v_accvgpr_write_b32 a7, 0\n"
"v_accvgpr_write_b32 a8, 0\n"
"v_accvgpr_write_b32 a9, 0\n"
"v_accvgpr_write_b32 a10, 0\n"
"v_accvgpr_write_b32 a11, 0\n"
"v_accvgpr_write_b32 a12, 0\n"
"v_accvgpr_write_b32 a13, 0\n"
"v_accvgpr_write_b32 a14, 0\n"
"v_accvgpr_write_b32 a15, 0\n"
"v_accvgpr_write_b32 a16, 0\n"
"v_accvgpr_write_b32 a17, 0\n"
"v_accvgpr_write_b32 a18, 0\n"
"v_accvgpr_write_b32 a19, 0\n"
"v_accvgpr_write_b32 a20, 0\n"
"v_accvgpr_write_b32 a21, 0\n"
"v_accvgpr_write_b32 a22, 0\n"
"v_accvgpr_write_b32 a23, 0\n"
"v_accvgpr_write_b32 a24, 0\n"
"v_accvgpr_write_b32 a25, 0\n"
"v_accvgpr_write_b32 a26, 0\n"
"v_accvgpr_write_b32 a27, 0\n"
"v_accvgpr_write_b32 a28, 0\n"
"v_accvgpr_write_b32 a29, 0\n"
"v_accvgpr_write_b32 a30, 0\n"
"v_accvgpr_write_b32 a31, 0\n"
"v_accvgpr_write_b32 a32, 0\n"
"v_accvgpr_write_b32 a33, 0\n"
"v_accvgpr_write_b32 a34, 0\n"
"v_accvgpr_write_b32 a35, 0\n"
"v_accvgpr_write_b32 a36, 0\n"
"v_accvgpr_write_b32 a37, 0\n"
"v_accvgpr_write_b32 a38, 0\n"
"v_accvgpr_write_b32 a39, 0\n"
"v_accvgpr_write_b32 a40, 0\n"
"v_accvgpr_write_b32 a41, 0\n"
"v_accvgpr_write_b32 a42, 0\n"
"v_accvgpr_write_b32 a43, 0\n"
"v_accvgpr_write_b32 a44, 0\n"
"v_accvgpr_write_b32 a45, 0\n"
"v_accvgpr_write_b32 a46, 0\n"
"v_accvgpr_write_b32 a47, 0\n"
"v_accvgpr_write_b32 a48, 0\n"
"v_accvgpr_write_b32 a49, 0\n"
"v_accvgpr_write_b32 a50, 0\n"
"v_accvgpr_write_b32 a51, 0\n"
"v_accvgpr_write_b32 a52, 0\n"
"v_accvgpr_write_b32 a53, 0\n"
"v_accvgpr_write_b32 a54, 0\n"
"v_accvgpr_write_b32 a55, 0\n"
"v_accvgpr_write_b32 a56, 0\n"
"v_accvgpr_write_b32 a57, 0\n"
"v_accvgpr_write_b32 a58, 0\n"
"v_accvgpr_write_b32 a59, 0\n"
"v_accvgpr_write_b32 a60, 0\n"
"v_accvgpr_write_b32 a61, 0\n"
"v_accvgpr_write_b32 a62, 0\n"
"v_accvgpr_write_b32 a63, 0\n"
"v_accvgpr_write_b32 a64, 0\n"
"v_accvgpr_write_b32 a65, 0\n"
"v_accvgpr_write_b32 a66, 0\n"
"v_accvgpr_write_b32 a67, 0\n"
"v_accvgpr_write_b32 a68, 0\n"
"v_accvgpr_write_b32 a69, 0\n"
"v_accvgpr_write_b32 a70, 0\n"
"v_accvgpr_write_b32 a71, 0\n"
"v_accvgpr_write_b32 a72, 0\n"
"v_accvgpr_write_b32 a73, 0\n"
"v_accvgpr_write_b32 a74, 0\n"
"v_accvgpr_write_b32 a75, 0\n"
"v_accvgpr_write_b32 a76, 0\n"
"v_accvgpr_write_b32 a77, 0\n"
"v_accvgpr_write_b32 a78, 0\n"
"v_accvgpr_write_b32 a79, 0\n"
"v_accvgpr_write_b32 a80, 0\n"
"v_accvgpr_write_b32 a81, 0\n"
"v_accvgpr_write_b32 a82, 0\n"
"v_accvgpr_write_b32 a83, 0\n"
"v_accvgpr_write_b32 a84, 0\n"
"v_accvgpr_write_b32 a85, 0\n"
"v_accvgpr_write_b32 a86, 0\n"
"v_accvgpr_write_b32 a87, 0\n"
"v_accvgpr_write_b32 a88, 0\n"
"v_accvgpr_write_b32 a89, 0\n"
"v_accvgpr_write_b32 a90, 0\n"
"v_accvgpr_write_b32 a91, 0\n"
"v_accvgpr_write_b32 a92, 0\n"
"v_accvgpr_write_b32 a93, 0\n"
"v_accvgpr_write_b32 a94, 0\n"
"v_accvgpr_write_b32 a95, 0\n"
"v_accvgpr_write_b32 a96, 0\n"
"v_accvgpr_write_b32 a97, 0\n"
"v_accvgpr_write_b32 a98, 0\n"
"v_accvgpr_write_b32 a99, 0\n"
"v_accvgpr_write_b32 a100, 0\n"
"v_accvgpr_write_b32 a101, 0\n"
"v_accvgpr_write_b32 a102, 0\n"
"v_accvgpr_write_b32 a103, 0\n"
"v_accvgpr_write_b32 a104, 0\n"
"v_accvgpr_write_b32 a105, 0\n"
"v_accvgpr_write_b32 a106, 0\n"
"v_accvgpr_write_b32 a107, 0\n"
"v_accvgpr_write_b32 a108, 0\n"
"v_accvgpr_write_b32 a109, 0\n"
"v_accvgpr_write_b32 a110, 0\n"
"v_accvgpr_write_b32 a111, 0\n"
"v_accvgpr_write_b32 a112, 0\n"
"v_accvgpr_write_b32 a113, 0\n"
"v_accvgpr_write_b32 a114, 0\n"
"v_accvgpr_write_b32 a115, 0\n"
"v_accvgpr_write_b32 a116, 0\n"
"v_accvgpr_write_b32 a117, 0\n"
"v_accvgpr_write_b32 a118, 0\n"
"v_accvgpr_write_b32 a119, 0\n"
"v_accvgpr_write_b32 a120, 0\n"
"v_accvgpr_write_b32 a121, 0\n"
"v_accvgpr_write_b32 a122, 0\n"
"v_accvgpr_write_b32 a123, 0\n"
"v_accvgpr_write_b32 a124, 0\n"
"v_accvgpr_write_b32 a125, 0\n"
"v_accvgpr_write_b32 a126, 0\n"
"v_accvgpr_write_b32 a127, 0\n"
"v_accvgpr_write_b32 a228, 0\n"
"v_accvgpr_write_b32 a229, 0\n"
"v_accvgpr_write_b32 a230, 0\n"
"v_accvgpr_write_b32 a231, 0\n"
"v_accvgpr_write_b32 a232, 0\n"
"v_accvgpr_write_b32 a233, 0\n"
"v_accvgpr_write_b32 a234, 0\n"
"v_accvgpr_write_b32 a235, 0\n"
"v_accvgpr_write_b32 a236, 0\n"
"v_accvgpr_write_b32 a237, 0\n"
"v_accvgpr_write_b32 a238, 0\n"
"v_accvgpr_write_b32 a239, 0\n"
"v_accvgpr_write_b32 a240, 0\n"
"v_accvgpr_write_b32 a241, 0\n"
"v_accvgpr_write_b32 a242, 0\n"
"v_accvgpr_write_b32 a243, 0\n"
"L_sl3%=:\n"
"s_add_u32 m0, s61, 0\n"
"s_nop 0\n"
"buffer_load_dwordx4 v222, s[36:39], s56 offen nt lds\n"
"buffer_load_dwordx4 v222, s[36:39], s56 offen offset:1024 nt lds\n"
"buffer_load_dwordx4 v223, s[36:39], s56 offen offset:2048 nt lds\n"
"buffer_load_dwordx4 v223, s[36:39], s56 offen offset:3072 nt lds\n"
"s_sub_u32 s56, s56, 159744\n"
"s_and_b32 s56, s56, 159744\n"
"s_add_u32 m0, s62, 0\n"
"s_nop 0\n"
"buffer_load_dwordx4 v224, s[40:43], s57 offen nt lds\n"
"buffer_load_dwordx4 v224, s[40:43], s57 offen offset:1024 nt lds\n"
"s_add_u32 m0, s62, 8\n"
"s_nop 0\n"
"buffer_load_dwordx4 v224, s[40:43], s57 offen offset:2048 nt lds\n"
"buffer_load_dwordx4 v224, s[40:43], s57 offen offset:3072 nt lds\n"
"s_sub_u32 s57, s57, 159744\n"
"s_and_b32 s57, s57, 159744\n"
"s_waitcnt vmcnt(0)\n"
"s_barrier\n"
"ds_read_b128 a[128:131], v216\n"
"ds_read_b128 a[132:135], v217\n"
"ds_read_b128 a[136:139], v218\n"
"ds_read_b128 a[140:143], v219\n"
"s_waitcnt lgkmcnt(3)\n"
"v_mfma_f32_16x16x32_bf16 v[0:3], a[128:131], v[64:67], v[192:195]\n"
"s_waitcnt lgkmcnt(2)\n"
"v_mfma_f32_16x16x32_bf16 v[0:3], a[132:135], v[68:71], v[0:3]\n"
"s_waitcnt lgkmcnt(1)\n"
"v_mfma_f32_16x16x32_bf16 v[0:3], a[136:139], v[72:75], v[0:3]\n"
"s_waitcnt lgkmcnt(0)\n"
"v_mfma_f32_16x16x32_bf16 v[0:3], a[140:143], v[76:79], v[0:3]\n"
"v_mfma_f32_16x16x32_bf16 v[4:7], a[128:131], v[80:83], v[196:199]\n"
"v_mfma_f32_16x16x32_bf16 v[4:7], a[132:135], v[84:87], v[4:7]\n"
"v_mfma_f32_16x16x32_bf16 v[4:7], a[136:139], v[88:91], v[4:7]\n"
"v_mfma_f32_16x16x32_bf16 v[4:7], a[140:143], v[92:95], v[4:7]\n"
"v_mfma_f32_16x16x32_bf16 v[8:11], a[128:131], v[96:99], v[200:203]\n"
"v_mfma_f32_16x16x32_bf16 v[8:11], a[132:135], v[100:103], v[8:11]\n"
"v_mfma_f32_16x16x32_bf16 v[8:11], a[136:139], v[104:107], v[8:11]\n"
"v_mfma_f32_16x16x32_bf16 v[8:11], a[140:143], v[108:111], v[8:11]\n"
"v_mfma_f32_16x16x32_bf16 v[12:15], a[128:131], v[112:115], v[204:207]\n"
"v_mfma_f32_16x16x32_bf16 v[12:15], a[132:135], v[116:119], v[12:15]\n"
"v_mfma_f32_16x16x32_bf16 v[12:15], a[136:139], v[120:123], v[12:15]\n"
"v_mfma_f32_16x16x32_bf16 v[12:15], a[140:143], v[124:127], v[12:15]\n"
"ds_read_b128 a[144:147], v216 offset:1024\n"
"ds_read_b128 a[148:151], v217 offset:1024\n"
"ds_read_b128 a[152:155], v218 offset:1024\n"
"ds_read_b128 a[156:159], v219 offset:1024\n"
"s_waitcnt lgkmcnt(3)\n"
"v_mfma_f32_16x16x32_bf16 v[16:19], a[144:147], v[64:67], v[192:195]\n"
"s_waitcnt lgkmcnt(2)\n"
"v_mfma_f32_16x16x32_bf16 v[16:19], a[148:151], v[68:71], v[16:19]\n"
"s_waitcnt lgkmcnt(1)\n"
"v_mfma_f32_16x16x32_bf16 v[16:19], a[152:155], v[72:75], v[16:19]\n"
"s_waitcnt lgkmcnt(0)\n"
"v_mfma_f32_16x16x32_bf16 v[16:19], a[156:159], v[76:79], v[16:19]\n"
"v_mfma_f32_16x16x32_bf16 v[20:23], a[144:147], v[80:83], v[196:199]\n"
"v_mfma_f32_16x16x32_bf16 v[20:23], a[148:151], v[84:87], v[20:23]\n"
"v_mfma_f32_16x16x32_bf16 v[20:23], a[152:155], v[88:91], v[20:23]\n"
"v_mfma_f32_16x16x32_bf16 v[20:23], a[156:159], v[92:95], v[20:23]\n"
"v_mfma_f32_16x16x32_bf16 v[24:27], a[144:147], v[96:99], v[200:203]\n"
"v_mfma_f32_16x16x32_bf16 v[24:27], a[148:151], v[100:103], v[24:27]\n"
"v_mfma_f32_16x16x32_bf16 v[24:27], a[152:155], v[104:107], v[24:27]\n"
"v_mfma_f32_16x16x32_bf16 v[24:27], a[156:159], v[108:111], v[24:27]\n"
"v_mfma_f32_16x16x32_bf16 v[28:31], a[144:147], v[112:115], v[204:207]\n"
"v_mfma_f32_16x16x32_bf16 v[28:31], a[148:151], v[116:119], v[28:31]\n"
"v_mfma_f32_16x16x32_bf16 v[28:31], a[152:155], v[120:123], v[28:31]\n"
"v_mfma_f32_16x16x32_bf16 v[28:31], a[156:159], v[124:127], v[28:31]\n"
"ds_read_b128 a[128:131], v216 offset:8192\n"
"ds_read_b128 a[132:135], v217 offset:8192\n"
"ds_read_b128 a[136:139], v218 offset:8192\n"
"ds_read_b128 a[140:143], v219 offset:8192\n"
"s_waitcnt lgkmcnt(3)\n"
"v_mfma_f32_16x16x32_bf16 v[32:35], a[128:131], v[64:67], v[192:195]\n"
"s_waitcnt lgkmcnt(2)\n"
"v_mfma_f32_16x16x32_bf16 v[32:35], a[132:135], v[68:71], v[32:35]\n"
"s_waitcnt lgkmcnt(1)\n"
"v_mfma_f32_16x16x32_bf16 v[32:35], a[136:139], v[72:75], v[32:35]\n"
"s_waitcnt lgkmcnt(0)\n"
"v_mfma_f32_16x16x32_bf16 v[32:35], a[140:143], v[76:79], v[32:35]\n"
"v_mfma_f32_16x16x32_bf16 v[36:39], a[128:131], v[80:83], v[196:199]\n"
"v_mfma_f32_16x16x32_bf16 v[36:39], a[132:135], v[84:87], v[36:39]\n"
"v_mfma_f32_16x16x32_bf16 v[36:39], a[136:139], v[88:91], v[36:39]\n"
"v_mfma_f32_16x16x32_bf16 v[36:39], a[140:143], v[92:95], v[36:39]\n"
"v_mfma_f32_16x16x32_bf16 v[40:43], a[128:131], v[96:99], v[200:203]\n"
"v_mfma_f32_16x16x32_bf16 v[40:43], a[132:135], v[100:103], v[40:43]\n"
"v_mfma_f32_16x16x32_bf16 v[40:43], a[136:139], v[104:107], v[40:43]\n"
"v_mfma_f32_16x16x32_bf16 v[40:43], a[140:143], v[108:111], v[40:43]\n"
"v_mfma_f32_16x16x32_bf16 v[44:47], a[128:131], v[112:115], v[204:207]\n"
"v_mfma_f32_16x16x32_bf16 v[44:47], a[132:135], v[116:119], v[44:47]\n"
"v_mfma_f32_16x16x32_bf16 v[44:47], a[136:139], v[120:123], v[44:47]\n"
"v_mfma_f32_16x16x32_bf16 v[44:47], a[140:143], v[124:127], v[44:47]\n"
"ds_read_b128 a[144:147], v216 offset:9216\n"
"ds_read_b128 a[148:151], v217 offset:9216\n"
"ds_read_b128 a[152:155], v218 offset:9216\n"
"ds_read_b128 a[156:159], v219 offset:9216\n"
"s_waitcnt lgkmcnt(3)\n"
"v_mfma_f32_16x16x32_bf16 v[48:51], a[144:147], v[64:67], v[192:195]\n"
"s_waitcnt lgkmcnt(2)\n"
"v_mfma_f32_16x16x32_bf16 v[48:51], a[148:151], v[68:71], v[48:51]\n"
"s_waitcnt lgkmcnt(1)\n"
"v_mfma_f32_16x16x32_bf16 v[48:51], a[152:155], v[72:75], v[48:51]\n"
"s_waitcnt lgkmcnt(0)\n"
"v_mfma_f32_16x16x32_bf16 v[48:51], a[156:159], v[76:79], v[48:51]\n"
"v_mfma_f32_16x16x32_bf16 v[52:55], a[144:147], v[80:83], v[196:199]\n"
"v_mfma_f32_16x16x32_bf16 v[52:55], a[148:151], v[84:87], v[52:55]\n"
"v_mfma_f32_16x16x32_bf16 v[52:55], a[152:155], v[88:91], v[52:55]\n"
"v_mfma_f32_16x16x32_bf16 v[52:55], a[156:159], v[92:95], v[52:55]\n"
"v_mfma_f32_16x16x32_bf16 v[56:59], a[144:147], v[96:99], v[200:203]\n"
"v_mfma_f32_16x16x32_bf16 v[56:59], a[148:151], v[100:103], v[56:59]\n"
"v_mfma_f32_16x16x32_bf16 v[56:59], a[152:155], v[104:107], v[56:59]\n"
"v_mfma_f32_16x16x32_bf16 v[56:59], a[156:159], v[108:111], v[56:59]\n"
"v_mfma_f32_16x16x32_bf16 v[60:63], a[144:147], v[112:115], v[204:207]\n"
"v_mfma_f32_16x16x32_bf16 v[60:63], a[148:151], v[116:119], v[60:63]\n"
"v_mfma_f32_16x16x32_bf16 v[60:63], a[152:155], v[120:123], v[60:63]\n"
"v_mfma_f32_16x16x32_bf16 v[60:63], a[156:159], v[124:127], v[60:63]\n"
"v_exp_f32 v0, v0\n"
"v_exp_f32 v1, v1\n"
"v_exp_f32 v2, v2\n"
"v_exp_f32 v3, v3\n"
"v_exp_f32 v4, v4\n"
"v_exp_f32 v5, v5\n"
"v_exp_f32 v6, v6\n"
"v_exp_f32 v7, v7\n"
"v_exp_f32 v8, v8\n"
"v_exp_f32 v9, v9\n"
"v_exp_f32 v10, v10\n"
"v_exp_f32 v11, v11\n"
"v_exp_f32 v12, v12\n"
"v_exp_f32 v13, v13\n"
"v_exp_f32 v14, v14\n"
"v_exp_f32 v15, v15\n"
"v_exp_f32 v16, v16\n"
"v_exp_f32 v17, v17\n"
"v_exp_f32 v18, v18\n"
"v_exp_f32 v19, v19\n"
"v_exp_f32 v20, v20\n"
"v_exp_f32 v21, v21\n"
"v_exp_f32 v22, v22\n"
"v_exp_f32 v23, v23\n"
"v_exp_f32 v24, v24\n"
"v_exp_f32 v25, v25\n"
"v_exp_f32 v26, v26\n"
"v_exp_f32 v27, v27\n"
"v_exp_f32 v28, v28\n"
"v_exp_f32 v29, v29\n"
"v_exp_f32 v30, v30\n"
"v_exp_f32 v31, v31\n"
"v_exp_f32 v32, v32\n"
"v_exp_f32 v33, v33\n"
"v_exp_f32 v34, v34\n"
"v_exp_f32 v35, v35\n"
"v_exp_f32 v36, v36\n"
"v_exp_f32 v37, v37\n"
"v_exp_f32 v38, v38\n"
"v_exp_f32 v39, v39\n"
"v_exp_f32 v40, v40\n"
"v_exp_f32 v41, v41\n"
"v_exp_f32 v42, v42\n"
"v_exp_f32 v43, v43\n"
"v_exp_f32 v44, v44\n"
"v_exp_f32 v45, v45\n"
"v_exp_f32 v46, v46\n"
"v_exp_f32 v47, v47\n"
"v_exp_f32 v48, v48\n"
"v_exp_f32 v49, v49\n"
"v_exp_f32 v50, v50\n"
"v_exp_f32 v51, v51\n"
"v_exp_f32 v52, v52\n"
"v_exp_f32 v53, v53\n"
"v_exp_f32 v54, v54\n"
"v_exp_f32 v55, v55\n"
"v_exp_f32 v56, v56\n"
"v_exp_f32 v57, v57\n"
"v_exp_f32 v58, v58\n"
"v_exp_f32 v59, v59\n"
"v_exp_f32 v60, v60\n"
"v_exp_f32 v61, v61\n"
"v_exp_f32 v62, v62\n"
"v_exp_f32 v63, v63\n"
"v_cvt_pk_bf16_f32 v128, v0, v1\n"
"v_cvt_pk_bf16_f32 v129, v2, v3\n"
"v_cvt_pk_bf16_f32 v136, v4, v5\n"
"v_cvt_pk_bf16_f32 v137, v6, v7\n"
"v_cvt_pk_bf16_f32 v144, v8, v9\n"
"v_cvt_pk_bf16_f32 v145, v10, v11\n"
"v_cvt_pk_bf16_f32 v152, v12, v13\n"
"v_cvt_pk_bf16_f32 v153, v14, v15\n"
"v_cvt_pk_bf16_f32 v130, v16, v17\n"
"v_cvt_pk_bf16_f32 v131, v18, v19\n"
"v_cvt_pk_bf16_f32 v138, v20, v21\n"
"v_cvt_pk_bf16_f32 v139, v22, v23\n"
"v_cvt_pk_bf16_f32 v146, v24, v25\n"
"v_cvt_pk_bf16_f32 v147, v26, v27\n"
"v_cvt_pk_bf16_f32 v154, v28, v29\n"
"v_cvt_pk_bf16_f32 v155, v30, v31\n"
"v_cvt_pk_bf16_f32 v132, v32, v33\n"
"v_cvt_pk_bf16_f32 v133, v34, v35\n"
"v_cvt_pk_bf16_f32 v140, v36, v37\n"
"v_cvt_pk_bf16_f32 v141, v38, v39\n"
"v_cvt_pk_bf16_f32 v148, v40, v41\n"
"v_cvt_pk_bf16_f32 v149, v42, v43\n"
"v_cvt_pk_bf16_f32 v156, v44, v45\n"
"v_cvt_pk_bf16_f32 v157, v46, v47\n"
"v_cvt_pk_bf16_f32 v134, v48, v49\n"
"v_cvt_pk_bf16_f32 v135, v50, v51\n"
"v_cvt_pk_bf16_f32 v142, v52, v53\n"
"v_cvt_pk_bf16_f32 v143, v54, v55\n"
"v_cvt_pk_bf16_f32 v150, v56, v57\n"
"v_cvt_pk_bf16_f32 v151, v58, v59\n"
"v_cvt_pk_bf16_f32 v158, v60, v61\n"
"v_cvt_pk_bf16_f32 v159, v62, v63\n"
"v_smfmac_f32_16x16x64_bf16 a[228:231], a[200:203], v[128:135], v208\n"
"v_smfmac_f32_16x16x64_bf16 a[232:235], a[200:203], v[136:143], v208\n"
"v_smfmac_f32_16x16x64_bf16 a[236:239], a[200:203], v[144:151], v208\n"
"v_smfmac_f32_16x16x64_bf16 a[240:243], a[200:203], v[152:159], v208\n"
"ds_read_b64_tr_b16 a[160:161], v220\n"
"ds_read_b64_tr_b16 a[162:163], v220 offset:1024\n"
"ds_read_b64_tr_b16 a[164:165], v220 offset:8224\n"
"ds_read_b64_tr_b16 a[166:167], v220 offset:9248\n"
"s_waitcnt lgkmcnt(2)\n"
"v_mfma_f32_16x16x32_bf16 a[0:3], a[160:163], v[128:131], a[0:3]\n"
"s_waitcnt lgkmcnt(0)\n"
"v_mfma_f32_16x16x32_bf16 a[0:3], a[164:167], v[132:135], a[0:3]\n"
"v_mfma_f32_16x16x32_bf16 a[4:7], a[164:167], v[140:143], a[4:7]\n"
"v_mfma_f32_16x16x32_bf16 a[4:7], a[160:163], v[136:139], a[4:7]\n"
"v_mfma_f32_16x16x32_bf16 a[8:11], a[160:163], v[144:147], a[8:11]\n"
"v_mfma_f32_16x16x32_bf16 a[8:11], a[164:167], v[148:151], a[8:11]\n"
"v_mfma_f32_16x16x32_bf16 a[12:15], a[164:167], v[156:159], a[12:15]\n"
"v_mfma_f32_16x16x32_bf16 a[12:15], a[160:163], v[152:155], a[12:15]\n"
"ds_read_b64_tr_b16 a[168:169], v220 offset:8\n"
"ds_read_b64_tr_b16 a[170:171], v220 offset:1032\n"
"ds_read_b64_tr_b16 a[172:173], v220 offset:8232\n"
"ds_read_b64_tr_b16 a[174:175], v220 offset:9256\n"
"s_waitcnt lgkmcnt(2)\n"
"v_mfma_f32_16x16x32_bf16 a[28:31], a[168:171], v[152:155], a[28:31]\n"
"s_waitcnt lgkmcnt(0)\n"
"v_mfma_f32_16x16x32_bf16 a[28:31], a[172:175], v[156:159], a[28:31]\n"
"v_mfma_f32_16x16x32_bf16 a[24:27], a[172:175], v[148:151], a[24:27]\n"
"v_mfma_f32_16x16x32_bf16 a[24:27], a[168:171], v[144:147], a[24:27]\n"
"v_mfma_f32_16x16x32_bf16 a[20:23], a[168:171], v[136:139], a[20:23]\n"
"v_mfma_f32_16x16x32_bf16 a[20:23], a[172:175], v[140:143], a[20:23]\n"
"v_mfma_f32_16x16x32_bf16 a[16:19], a[172:175], v[132:135], a[16:19]\n"
"v_mfma_f32_16x16x32_bf16 a[16:19], a[168:171], v[128:131], a[16:19]\n"
"ds_read_b64_tr_b16 a[176:177], v220 offset:256\n"
"ds_read_b64_tr_b16 a[178:179], v220 offset:1280\n"
"ds_read_b64_tr_b16 a[180:181], v220 offset:8480\n"
"ds_read_b64_tr_b16 a[182:183], v220 offset:9504\n"
"s_waitcnt lgkmcnt(2)\n"
"v_mfma_f32_16x16x32_bf16 a[32:35], a[176:179], v[128:131], a[32:35]\n"
"s_waitcnt lgkmcnt(0)\n"
"v_mfma_f32_16x16x32_bf16 a[32:35], a[180:183], v[132:135], a[32:35]\n"
"v_mfma_f32_16x16x32_bf16 a[36:39], a[180:183], v[140:143], a[36:39]\n"
"v_mfma_f32_16x16x32_bf16 a[36:39], a[176:179], v[136:139], a[36:39]\n"
"v_mfma_f32_16x16x32_bf16 a[40:43], a[176:179], v[144:147], a[40:43]\n"
"v_mfma_f32_16x16x32_bf16 a[40:43], a[180:183], v[148:151], a[40:43]\n"
"v_mfma_f32_16x16x32_bf16 a[44:47], a[180:183], v[156:159], a[44:47]\n"
"v_mfma_f32_16x16x32_bf16 a[44:47], a[176:179], v[152:155], a[44:47]\n"
"ds_read_b64_tr_b16 a[184:185], v220 offset:264\n"
"ds_read_b64_tr_b16 a[186:187], v220 offset:1288\n"
"ds_read_b64_tr_b16 a[188:189], v220 offset:8488\n"
"ds_read_b64_tr_b16 a[190:191], v220 offset:9512\n"
"s_waitcnt lgkmcnt(2)\n"
"v_mfma_f32_16x16x32_bf16 a[60:63], a[184:187], v[152:155], a[60:63]\n"
"s_waitcnt lgkmcnt(0)\n"
"v_mfma_f32_16x16x32_bf16 a[60:63], a[188:191], v[156:159], a[60:63]\n"
"v_mfma_f32_16x16x32_bf16 a[56:59], a[188:191], v[148:151], a[56:59]\n"
"v_mfma_f32_16x16x32_bf16 a[56:59], a[184:187], v[144:147], a[56:59]\n"
"v_mfma_f32_16x16x32_bf16 a[52:55], a[184:187], v[136:139], a[52:55]\n"
"v_mfma_f32_16x16x32_bf16 a[52:55], a[188:191], v[140:143], a[52:55]\n"
"v_mfma_f32_16x16x32_bf16 a[48:51], a[188:191], v[132:135], a[48:51]\n"
"v_mfma_f32_16x16x32_bf16 a[48:51], a[184:187], v[128:131], a[48:51]\n"
"ds_read_b64_tr_b16 a[160:161], v220 offset:512\n"
"ds_read_b64_tr_b16 a[162:163], v220 offset:1536\n"
"ds_read_b64_tr_b16 a[164:165], v220 offset:8736\n"
"ds_read_b64_tr_b16 a[166:167], v220 offset:9760\n"
"s_waitcnt lgkmcnt(2)\n"
"v_mfma_f32_16x16x32_bf16 a[64:67], a[160:163], v[128:131], a[64:67]\n"
"s_waitcnt lgkmcnt(0)\n"
"v_mfma_f32_16x16x32_bf16 a[64:67], a[164:167], v[132:135], a[64:67]\n"
"v_mfma_f32_16x16x32_bf16 a[68:71], a[164:167], v[140:143], a[68:71]\n"
"v_mfma_f32_16x16x32_bf16 a[68:71], a[160:163], v[136:139], a[68:71]\n"
"v_mfma_f32_16x16x32_bf16 a[72:75], a[160:163], v[144:147], a[72:75]\n"
"v_mfma_f32_16x16x32_bf16 a[72:75], a[164:167], v[148:151], a[72:75]\n"
"v_mfma_f32_16x16x32_bf16 a[76:79], a[164:167], v[156:159], a[76:79]\n"
"v_mfma_f32_16x16x32_bf16 a[76:79], a[160:163], v[152:155], a[76:79]\n"
"ds_read_b64_tr_b16 a[168:169], v220 offset:520\n"
"ds_read_b64_tr_b16 a[170:171], v220 offset:1544\n"
"ds_read_b64_tr_b16 a[172:173], v220 offset:8744\n"
"ds_read_b64_tr_b16 a[174:175], v220 offset:9768\n"
"s_waitcnt lgkmcnt(2)\n"
"v_mfma_f32_16x16x32_bf16 a[92:95], a[168:171], v[152:155], a[92:95]\n"
"s_waitcnt lgkmcnt(0)\n"
"v_mfma_f32_16x16x32_bf16 a[92:95], a[172:175], v[156:159], a[92:95]\n"
"v_mfma_f32_16x16x32_bf16 a[88:91], a[172:175], v[148:151], a[88:91]\n"
"v_mfma_f32_16x16x32_bf16 a[88:91], a[168:171], v[144:147], a[88:91]\n"
"v_mfma_f32_16x16x32_bf16 a[84:87], a[168:171], v[136:139], a[84:87]\n"
"v_mfma_f32_16x16x32_bf16 a[84:87], a[172:175], v[140:143], a[84:87]\n"
"v_mfma_f32_16x16x32_bf16 a[80:83], a[172:175], v[132:135], a[80:83]\n"
"v_mfma_f32_16x16x32_bf16 a[80:83], a[168:171], v[128:131], a[80:83]\n"
"ds_read_b64_tr_b16 a[176:177], v220 offset:768\n"
"ds_read_b64_tr_b16 a[178:179], v220 offset:1792\n"
"ds_read_b64_tr_b16 a[180:181], v220 offset:8992\n"
"ds_read_b64_tr_b16 a[182:183], v220 offset:10016\n"
"s_waitcnt lgkmcnt(2)\n"
"v_mfma_f32_16x16x32_bf16 a[96:99], a[176:179], v[128:131], a[96:99]\n"
"s_waitcnt lgkmcnt(0)\n"
"v_mfma_f32_16x16x32_bf16 a[96:99], a[180:183], v[132:135], a[96:99]\n"
"v_mfma_f32_16x16x32_bf16 a[100:103], a[180:183], v[140:143], a[100:103]\n"
"v_mfma_f32_16x16x32_bf16 a[100:103], a[176:179], v[136:139], a[100:103]\n"
"v_mfma_f32_16x16x32_bf16 a[104:107], a[176:179], v[144:147], a[104:107]\n"
"v_mfma_f32_16x16x32_bf16 a[104:107], a[180:183], v[148:151], a[104:107]\n"
"v_mfma_f32_16x16x32_bf16 a[108:111], a[180:183], v[156:159], a[108:111]\n"
"v_mfma_f32_16x16x32_bf16 a[108:111], a[176:179], v[152:155], a[108:111]\n"
"ds_read_b64_tr_b16 a[184:185], v220 offset:776\n"
"ds_read_b64_tr_b16 a[186:187], v220 offset:1800\n"
"ds_read_b64_tr_b16 a[188:189], v220 offset:9000\n"
"ds_read_b64_tr_b16 a[190:191], v220 offset:10024\n"
"s_waitcnt lgkmcnt(2)\n"
"v_mfma_f32_16x16x32_bf16 a[124:127], a[184:187], v[152:155], a[124:127]\n"
"s_waitcnt lgkmcnt(0)\n"
"v_mfma_f32_16x16x32_bf16 a[124:127], a[188:191], v[156:159], a[124:127]\n"
"v_mfma_f32_16x16x32_bf16 a[120:123], a[188:191], v[148:151], a[120:123]\n"
"v_mfma_f32_16x16x32_bf16 a[120:123], a[184:187], v[144:147], a[120:123]\n"
"v_mfma_f32_16x16x32_bf16 a[116:119], a[184:187], v[136:139], a[116:119]\n"
"v_mfma_f32_16x16x32_bf16 a[116:119], a[188:191], v[140:143], a[116:119]\n"
"v_mfma_f32_16x16x32_bf16 a[112:115], a[188:191], v[132:135], a[112:115]\n"
"v_mfma_f32_16x16x32_bf16 a[112:115], a[184:187], v[128:131], a[112:115]\n"
"s_waitcnt lgkmcnt(0)\n"
"s_barrier\n"
"s_sub_u32 s73, s73, 1\n"
"s_cmp_lg_u32 s73, 0\n"
"s_cbranch_scc1 L_sl3%=\n"
"v_accvgpr_read_b32 v212, a228\n"
"v_accvgpr_read_b32 v213, a232\n"
"v_accvgpr_read_b32 v214, a236\n"
"v_accvgpr_read_b32 v215, a240\n"
"v_accvgpr_read_b32 v240, a229\n"
"v_accvgpr_read_b32 v241, a233\n"
"v_accvgpr_read_b32 v242, a237\n"
"v_accvgpr_read_b32 v243, a241\n"
"v_add_f32 v212, v212, v240\n"
"v_add_f32 v213, v213, v241\n"
"v_add_f32 v214, v214, v242\n"
"v_add_f32 v215, v215, v243\n"
"v_mov_b32 v240, v212\n"
"v_mov_b32 v241, v213\n"
"v_mov_b32 v242, v214\n"
"v_mov_b32 v243, v215\n"
"v_permlane32_swap_b32 v212, v240\n"
"v_permlane32_swap_b32 v213, v241\n"
"v_permlane32_swap_b32 v214, v242\n"
"v_permlane32_swap_b32 v215, v243\n"
"v_mov_b32 v240, v212\n"
"v_mov_b32 v241, v213\n"
"v_mov_b32 v242, v214\n"
"v_mov_b32 v243, v215\n"
"v_permlane16_swap_b32 v212, v240\n"
"v_permlane16_swap_b32 v213, v241\n"
"v_permlane16_swap_b32 v214, v242\n"
"v_permlane16_swap_b32 v215, v243\n"
"v_rcp_f32 v212, v212\n"
"v_rcp_f32 v213, v213\n"
"v_rcp_f32 v214, v214\n"
"v_rcp_f32 v215, v215\n"
"s_cmp_eq_u32 s63, 0\n"
"s_nop 7\n"
"s_cbranch_scc0 L_sl5%=\n"
"s_mov_b32 s63, 1\n"
"s_add_u32 m0, s61, 65536\n"
"s_add_u32 s58, s84, 0\n"
"buffer_load_dwordx4 v226, s[44:47], s58 offen lds\n"
"s_add_u32 m0, s61, 66560\n"
"s_nop 0\n"
"buffer_load_dwordx4 v227, s[44:47], s58 offen lds\n"
"s_add_u32 m0, s61, 67584\n"
"s_add_u32 s58, s85, 0\n"
"buffer_load_dwordx4 v226, s[44:47], s58 offen lds\n"
"s_add_u32 m0, s61, 68608\n"
"s_nop 0\n"
"buffer_load_dwordx4 v227, s[44:47], s58 offen lds\n"
"s_add_u32 m0, s61, 81984\n"
"s_add_u32 s58, s87, 0\n"
"buffer_load_dwordx4 v226, s[44:47], s58 offen lds\n"
"s_add_u32 m0, s61, 83008\n"
"s_nop 0\n"
"buffer_load_dwordx4 v227, s[44:47], s58 offen lds\n"
"s_add_u32 m0, s61, 84032\n"
"s_add_u32 s58, s68, 0\n"
"buffer_load_dwordx4 v226, s[44:47], s58 offen lds\n"
"s_add_u32 m0, s61, 85056\n"
"s_nop 0\n"
"buffer_load_dwordx4 v227, s[44:47], s58 offen lds\n"
"s_add_u32 m0, s62, 65792\n"
"s_add_u32 s60, s59, 0\n"
"buffer_load_dwordx4 v225, s[48:51], s60 offen lds\n"
"s_add_u32 m0, s62, 66816\n"
"s_add_u32 s60, s59, 32768\n"
"buffer_load_dwordx4 v225, s[48:51], s60 offen lds\n"
"s_add_u32 m0, s62, 67848\n"
"s_add_u32 s60, s59, 65536\n"
"buffer_load_dwordx4 v225, s[48:51], s60 offen lds\n"
"s_add_u32 m0, s62, 68872\n"
"s_add_u32 s60, s59, 98304\n"
"buffer_load_dwordx4 v225, s[48:51], s60 offen lds\n"
"s_add_u32 m0, s61, 0\n"
"s_add_u32 s58, s84, 128\n"
"buffer_load_dwordx4 v226, s[44:47], s58 offen lds\n"
"s_add_u32 m0, s61, 1024\n"
"s_nop 0\n"
"buffer_load_dwordx4 v227, s[44:47], s58 offen lds\n"
"s_add_u32 m0, s61, 2048\n"
"s_add_u32 s58, s85, 128\n"
"buffer_load_dwordx4 v226, s[44:47], s58 offen lds\n"
"s_add_u32 m0, s62, 32896\n"
"s_add_u32 s60, s59, 524288\n"
"buffer_load_dwordx4 v225, s[48:51], s60 offen lds\n"
"s_add_u32 m0, s61, 3072\n"
"s_nop 0\n"
"buffer_load_dwordx4 v227, s[44:47], s58 offen lds\n"
"s_add_u32 m0, s62, 33920\n"
"s_add_u32 s60, s59, 557056\n"
"buffer_load_dwordx4 v225, s[48:51], s60 offen lds\n"
"s_add_u32 m0, s61, 16384\n"
"s_add_u32 s58, s87, 128\n"
"buffer_load_dwordx4 v226, s[44:47], s58 offen lds\n"
"s_add_u32 m0, s62, 34952\n"
"s_add_u32 s60, s59, 589824\n"
"buffer_load_dwordx4 v225, s[48:51], s60 offen lds\n"
"s_add_u32 m0, s61, 17408\n"
"s_nop 0\n"
"buffer_load_dwordx4 v227, s[44:47], s58 offen lds\n"
"s_add_u32 m0, s62, 35976\n"
"s_add_u32 s60, s59, 622592\n"
"buffer_load_dwordx4 v225, s[48:51], s60 offen lds\n"
"s_add_u32 m0, s61, 18432\n"
"s_add_u32 s58, s68, 128\n"
"buffer_load_dwordx4 v226, s[44:47], s58 offen lds\n"
"s_add_u32 m0, s61, 19456\n"
"s_nop 0\n"
"buffer_load_dwordx4 v227, s[44:47], s58 offen lds\n"
"s_waitcnt vmcnt(16)\n"
"ds_read_b128 v[128:131], v230\n"
"ds_read_b128 v[132:135], v231\n"
"ds_read_b128 v[136:139], v230 offset:2048\n"
"ds_read_b128 v[140:143], v231 offset:2048\n"
"ds_read_b128 v[144:147], v230 offset:16448\n"
"ds_read_b128 v[148:151], v231 offset:16448\n"
"ds_read_b128 v[152:155], v230 offset:18496\n"
"ds_read_b128 v[156:159], v231 offset:18496\n"
"s_add_u32 m0, s61, 32768\n"
"s_add_u32 s58, s84, 256\n"
"buffer_load_dwordx4 v226, s[44:47], s58 offen lds\n"
"s_add_u32 m0, s61, 33792\n"
"s_nop 0\n"
"buffer_load_dwordx4 v227, s[44:47], s58 offen lds\n"
"s_add_u32 m0, s61, 34816\n"
"s_add_u32 s58, s85, 256\n"
"buffer_load_dwordx4 v226, s[44:47], s58 offen lds\n"
"s_add_u32 m0, s62, 49344\n"
"s_add_u32 s60, s59, 1048576\n"
"buffer_load_dwordx4 v225, s[48:51], s60 offen lds\n"
"s_add_u32 m0, s61, 35840\n"
"s_nop 0\n"
"buffer_load_dwordx4 v227, s[44:47], s58 offen lds\n"
"s_add_u32 m0, s62, 50368\n"
"s_add_u32 s60, s59, 1081344\n"
"buffer_load_dwordx4 v225, s[48:51], s60 offen lds\n"
"s_add_u32 m0, s61, 49152\n"
"s_add_u32 s58, s87, 256\n"
"buffer_load_dwordx4 v226, s[44:47], s58 offen lds\n"
"s_add_u32 m0, s62, 51400\n"
"s_add_u32 s60, s59, 1114112\n"
"buffer_load_dwordx4 v225, s[48:51], s60 offen lds\n"
"s_add_u32 m0, s61, 50176\n"
"s_nop 0\n"
"buffer_load_dwordx4 v227, s[44:47], s58 offen lds\n"
"s_add_u32 m0, s62, 52424\n"
"s_add_u32 s60, s59, 1146880\n"
"buffer_load_dwordx4 v225, s[48:51], s60 offen lds\n"
"s_add_u32 m0, s61, 51200\n"
"s_add_u32 s58, s68, 256\n"
"buffer_load_dwordx4 v226, s[44:47], s58 offen lds\n"
"s_add_u32 m0, s61, 52224\n"
"s_nop 0\n"
"buffer_load_dwordx4 v227, s[44:47], s58 offen lds\n"
"s_waitcnt vmcnt(24) lgkmcnt(0)\n"
"s_barrier\n"
"ds_read_b64_tr_b16 a[160:161], v221 offset:33024\n"
"ds_read_b64_tr_b16 a[162:163], v221 offset:34048\n"
"ds_read_b64_tr_b16 a[164:165], v221 offset:41248\n"
"ds_read_b64_tr_b16 a[166:167], v221 offset:42272\n"
"ds_read_b64_tr_b16 a[168:169], v221 offset:33032\n"
"ds_read_b64_tr_b16 a[170:171], v221 offset:34056\n"
"ds_read_b64_tr_b16 a[172:173], v221 offset:41256\n"
"ds_read_b64_tr_b16 a[174:175], v221 offset:42280\n"
"ds_read_b64_tr_b16 a[176:177], v221 offset:33280\n"
"ds_read_b64_tr_b16 a[178:179], v221 offset:34304\n"
"ds_read_b64_tr_b16 a[180:181], v221 offset:41504\n"
"ds_read_b64_tr_b16 a[182:183], v221 offset:42528\n"
"s_waitcnt lgkmcnt(8)\n"
"s_waitcnt vmcnt(0) lgkmcnt(0)\n"
"s_branch L_head%=\n"
"L_sl5%=:\n"
"s_lshl_b32 s72, s59, 1\n"
"s_add_u32 s72, s72, 1\n"
"s_mul_i32 s58, s72, 147456\n"
"s_add_u32 m0, s60, 0\n"
"s_nop 0\n"
"buffer_load_dwordx4 v221, s[92:95], s58 offen lds\n"
"buffer_load_dwordx4 v225, s[92:95], s58 offen offset:1024 lds\n"
"buffer_load_dwordx4 v226, s[92:95], s58 offen offset:2048 lds\n"
"buffer_load_dwordx4 v227, s[92:95], s58 offen offset:3072 lds\n"
"s_add_u32 m0, s60, 4096\n"
"s_nop 0\n"
"buffer_load_dwordx4 v228, s[92:95], s58 offen lds\n"
"buffer_load_dwordx4 v229, s[92:95], s58 offen offset:1024 lds\n"
"buffer_load_dwordx4 v230, s[92:95], s58 offen offset:2048 lds\n"
"buffer_load_dwordx4 v231, s[92:95], s58 offen offset:3072 lds\n"
"s_add_u32 m0, s60, 8192\n"
"s_nop 0\n"
"buffer_load_dwordx4 v235, s[92:95], s58 offen lds\n"
"s_mov_b32 s72, 2\n"
"s_mul_i32 s58, s72, 147456\n"
"s_add_u32 m0, s60, 36864\n"
"s_nop 0\n"
"buffer_load_dwordx4 v221, s[92:95], s58 offen lds\n"
"buffer_load_dwordx4 v225, s[92:95], s58 offen offset:1024 lds\n"
"buffer_load_dwordx4 v226, s[92:95], s58 offen offset:2048 lds\n"
"buffer_load_dwordx4 v227, s[92:95], s58 offen offset:3072 lds\n"
"s_add_u32 m0, s60, 40960\n"
"s_nop 0\n"
"buffer_load_dwordx4 v228, s[92:95], s58 offen lds\n"
"buffer_load_dwordx4 v229, s[92:95], s58 offen offset:1024 lds\n"
"buffer_load_dwordx4 v230, s[92:95], s58 offen offset:2048 lds\n"
"buffer_load_dwordx4 v231, s[92:95], s58 offen offset:3072 lds\n"
"s_add_u32 m0, s60, 45056\n"
"s_nop 0\n"
"buffer_load_dwordx4 v235, s[92:95], s58 offen lds\n"
"s_mov_b32 s72, 4\n"
"s_mul_i32 s58, s72, 147456\n"
"s_add_u32 m0, s60, 73728\n"
"s_nop 0\n"
"buffer_load_dwordx4 v221, s[92:95], s58 offen lds\n"
"buffer_load_dwordx4 v225, s[92:95], s58 offen offset:1024 lds\n"
"buffer_load_dwordx4 v226, s[92:95], s58 offen offset:2048 lds\n"
"buffer_load_dwordx4 v227, s[92:95], s58 offen offset:3072 lds\n"
"s_add_u32 m0, s60, 77824\n"
"s_nop 0\n"
"buffer_load_dwordx4 v228, s[92:95], s58 offen lds\n"
"buffer_load_dwordx4 v229, s[92:95], s58 offen offset:1024 lds\n"
"buffer_load_dwordx4 v230, s[92:95], s58 offen offset:2048 lds\n"
"s_add_u32 s66, s77, 0\n"
"buffer_load_dwordx4 a[128:131], v232, s[52:55], s66 offen sc1\n"
"buffer_load_dwordx4 a[132:135], v232, s[52:55], s66 offen offset:1024 sc1\n"
"buffer_load_dwordx4 a[136:139], v232, s[52:55], s66 offen offset:2048 sc1\n"
"buffer_load_dwordx4 a[140:143], v232, s[52:55], s66 offen offset:3072 sc1\n"
"s_add_u32 s66, s77, 4096\n"
"buffer_load_dwordx4 a[144:147], v232, s[52:55], s66 offen sc1\n"
"buffer_load_dwordx4 a[148:151], v232, s[52:55], s66 offen offset:1024 sc1\n"
"buffer_load_dwordx4 a[152:155], v232, s[52:55], s66 offen offset:2048 sc1\n"
"buffer_load_dwordx4 a[156:159], v232, s[52:55], s66 offen offset:3072 sc1\n"
"s_waitcnt vmcnt(0) lgkmcnt(0)\n"
"s_branch L_fin%=\n"
"L_end%=:\n"
    :: "s"(__builtin_amdgcn_kernarg_segment_ptr()), "s"(blockIdx.x), "s"(w), "s"((unsigned)(size_t)lds)
    : "v0", "v1", "v2", "v3", "v4", "v5", "v6", "v7", "v8", "v9", "v10", "v11", "v12", "v13", "v14", "v15", "v16", "v17", "v18", "v19", "v20", "v21", "v22",
      "v23", "v24", "v25", "v26", "v27", "v28", "v29", "v30", "v31", "v32", "v33", "v34", "v35", "v36", "v37", "v38", "v39", "v40", "v41", "v42", "v43",
      "v44", "v45", "v46", "v47", "v48", "v49", "v50", "v51", "v52", "v53", "v54", "v55", "v56", "v57", "v58", "v59", "v60", "v61", "v62", "v63", "v64",
      "v65", "v66", "v67", "v68", "v69", "v70", "v71", "v72", "v73", "v74", "v75", "v76", "v77", "v78", "v79", "v80", "v81", "v82", "v83", "v84", "v85",
      "v86", "v87", "v88", "v89", "v90", "v91", "v92", "v93", "v94", "v95", "v96", "v97", "v98", "v99", "v100", "v101", "v102", "v103", "v104", "v105",
      "v106", "v107", "v108", "v109", "v110", "v111", "v112", "v113", "v114", "v115", "v116", "v117", "v118", "v119", "v120", "v121", "v122", "v123",
      "v124", "v125", "v126", "v127", "v128", "v129", "v130", "v131", "v132", "v133", "v134", "v135", "v136", "v137", "v138", "v139", "v140", "v141",
      "v142", "v143", "v144", "v145", "v146", "v147", "v148", "v149", "v150", "v151", "v152", "v153", "v154", "v155", "v156", "v157", "v158", "v159",
      "v160", "v161", "v162", "v163", "v164", "v165", "v166", "v167", "v168", "v169", "v170", "v171", "v172", "v173", "v174", "v175", "v176", "v177",
      "v178", "v179", "v180", "v181", "v182", "v183", "v184", "v185", "v186", "v187", "v188", "v189", "v190", "v191", "v192", "v193", "v194", "v195",
      "v196", "v197", "v198", "v199", "v200", "v201", "v202", "v203", "v204", "v205", "v206", "v207", "v208", "v209", "v210", "v211", "v212", "v213",
      "v214", "v215", "v216", "v217", "v218", "v219", "v220", "v221", "v222", "v223", "v224", "v225", "v226", "v227", "v228", "v229", "v230", "v231",
      "v232", "v233", "v234", "v235", "v236", "v237", "v238", "v239", "v240", "v241", "v242", "v243", "v244", "v245", "v246", "v247", "v248", "v249",
      "v250", "v251", "v252", "v253", "v254", "v255", "a0", "a1", "a2", "a3", "a4", "a5", "a6", "a7", "a8", "a9", "a10", "a11", "a12", "a13", "a14", "a15",
      "a16", "a17", "a18", "a19", "a20", "a21", "a22", "a23", "a24", "a25", "a26", "a27", "a28", "a29", "a30", "a31", "a32", "a33", "a34", "a35", "a36",
      "a37", "a38", "a39", "a40", "a41", "a42", "a43", "a44", "a45", "a46", "a47", "a48", "a49", "a50", "a51", "a52", "a53", "a54", "a55", "a56", "a57",
      "a58", "a59", "a60", "a61", "a62", "a63", "a64", "a65", "a66", "a67", "a68", "a69", "a70", "a71", "a72", "a73", "a74", "a75", "a76", "a77", "a78",
      "a79", "a80", "a81", "a82", "a83", "a84", "a85", "a86", "a87", "a88", "a89", "a90", "a91", "a92", "a93", "a94", "a95", "a96", "a97", "a98", "a99",
      "a100", "a101", "a102", "a103", "a104", "a105", "a106", "a107", "a108", "a109", "a110", "a111", "a112", "a113", "a114", "a115", "a116", "a117",
      "a118", "a119", "a120", "a121", "a122", "a123", "a124", "a125", "a126", "a127", "a128", "a129", "a130", "a131", "a132", "a133", "a134", "a135",
      "a136", "a137", "a138", "a139", "a140", "a141", "a142", "a143", "a144", "a145", "a146", "a147", "a148", "a149", "a150", "a151", "a152", "a153",
      "a154", "a155", "a156", "a157", "a158", "a159", "a160", "a161", "a162", "a163", "a164", "a165", "a166", "a167", "a168", "a169", "a170", "a171",
      "a172", "a173", "a174", "a175", "a176", "a177", "a178", "a179", "a180", "a181", "a182", "a183", "a184", "a185", "a186", "a187", "a188", "a189",
      "a190", "a191", "a192", "a193", "a194", "a195", "a196", "a197", "a198", "a199", "a200", "a201", "a202", "a203", "a204", "a205", "a206", "a207",
      "a208", "a209", "a210", "a211", "a212", "a213", "a214", "a215", "a216", "a217", "a218", "a219", "a220", "a221", "a222", "a223", "a224", "a225",
      "a226", "a227", "a228", "a229", "a230", "a231", "a232", "a233", "a234", "a235", "a236", "a237", "a238", "a239", "a240", "a241", "a242", "a243",
      "a244", "a245", "a246", "a247", "a248", "a249", "a250", "a251", "a252", "a253", "a254", "a255", "s4", "s5", "s6", "s7", "s8", "s9", "s10", "s11",
      "s12", "s13", "s14", "s15", "s16", "s17", "s18", "s19", "s20", "s21", "s22", "s23", "s24", "s25", "s26", "s27", "s34", "s35", "s36", "s37", "s38",
      "s39", "s40", "s41", "s42", "s43", "s44", "s45", "s46", "s47", "s48", "s49", "s50", "s51", "s52", "s53", "s54", "s55", "s56", "s57", "s58", "s59",
      "s60", "s61", "s62", "s63", "s64", "s65", "s66", "s67", "s68", "s69", "s70", "s71", "s72", "s73", "s74", "s75", "s76", "s77", "s78", "s79", "s80",
      "s81", "s82", "s83", "s84", "s85", "s86", "s87", "s88", "s89", "s90", "s91", "s92", "s93", "s94", "s95", "s96", "s97", "s98", "s99", "vcc", "scc",
      "memory");
}

extern "C" void kernel_launch(void* const* d_in, const int* in_sizes, int n_in, void* d_out, int out_size, void* d_ws, size_t ws_size,
                              hipStream_t stream) {
  Args p;
  for (int i = 0; i < 10; ++i) p.in[i] = (const char*)d_in[i];
  p.ws = (char*)d_ws;
  p.out = (char*)d_out;
  mha2_fused<<<256, 256, 0, stream>>>(p);
}
```
